```python
import torch

def _softmax(x, axis=-1):
    return torch.softmax(x, dim=axis)
def _gelu(x):
    return torch.nn.functional.gelu(x, approximate="tanh")
def _silu(x):
    return torch.nn.functional.silu(x)
def _sigmoid(x):
    return torch.sigmoid(x)
def _layernorm(x, axis=-1, eps=1e-5):
    m = x.mean(dim=axis, keepdim=True)
    v = x.var(dim=axis, keepdim=True, unbiased=False)
    return (x - m) / torch.sqrt(v + eps)
def _scatter_add(tgt, idx, src, axis=0):
    return tgt.clone().index_add_(axis, idx, src)
def _segment_sum(src, seg, n):
    out = torch.zeros((n,) + src.shape[1:], dtype=src.dtype)
    return out.index_add_(0, seg, src)
def _pad(x, pw):
    flat = tuple(v for pair in reversed(pw) for v in pair)
    return torch.nn.functional.pad(x, flat)

kernel_name = 'parallel_sum__ssm_conv_s5__glu_gated_bilinear__prompt'

def setup_inputs():

    _w = (lambda a: (lambda t: t.to(torch.bfloat16) if t.is_floating_point() else t)(torch.as_tensor(a)))
    def _A_setup():
        B, S, D = 1, 2048, 512
        P = D
        Lambda_re = -0.5 * (lambda _x: torch.exp(torch.as_tensor(_x)))((lambda *s: torch.randn(*s, dtype=torch.bfloat16) * 0.2)(P))
        Lambda_im = (lambda *s: torch.randn(*s, dtype=torch.bfloat16) * 0.8)(P) * 1.0
        B_re = (lambda *s: torch.randn(*s, dtype=torch.bfloat16) * 0.2)(P, D) * (D ** -0.5)
        B_im = (lambda *s: torch.randn(*s, dtype=torch.bfloat16) * 0.2)(P, D) * (D ** -0.5)
        C_re = (lambda *s: torch.randn(*s, dtype=torch.bfloat16) * 0.2)(D, P) * (P ** -0.5)
        C_im = (lambda *s: torch.randn(*s, dtype=torch.bfloat16) * 0.2)(D, P) * (P ** -0.5)
        log_dt = (lambda *s: torch.randn(*s, dtype=torch.bfloat16) * 0.2)(P) * 0.3 - 2.0
        Dskip = (lambda *s: torch.randn(*s, dtype=torch.bfloat16) * 0.05)(D)
        return {
            "x": _w((lambda *s: torch.randn(*s, dtype=torch.bfloat16) * 0.05)(B, S, D)),
            "Lambda_re": _w(Lambda_re),
            "Lambda_im": _w(Lambda_im),
            "B_re": _w(B_re),
            "B_im": _w(B_im),
            "C_re": _w(C_re),
            "C_im": _w(C_im),
            "log_dt": _w(log_dt),
            "Dskip": _w(Dskip),
        }
    _A_in = _A_setup()

    def _B_setup():
        B, S, D, F = 1, 2048, 512, 1792
        return {
            "x": _w((lambda *s: torch.randn(*s, dtype=torch.bfloat16) * 0.05)(B, S, D)),
            "w_gate": _w((lambda *s: torch.randn(*s, dtype=torch.bfloat16) * 0.4)(D, F) * (D ** -0.5)),
            "w_up": _w((lambda *s: torch.randn(*s, dtype=torch.bfloat16) * 0.4)(D, F) * (D ** -0.5)),
            "w_down": _w((lambda *s: torch.randn(*s, dtype=torch.bfloat16) * 0.4)(F, D) * (F ** -0.5)),
        }
    _B_in = _B_setup()

    return {
        "A_x": _A_in["x"],
        "A_Lambda_re": _A_in["Lambda_re"],
        "A_Lambda_im": _A_in["Lambda_im"],
        "A_B_re": _A_in["B_re"],
        "A_B_im": _A_in["B_im"],
        "A_C_re": _A_in["C_re"],
        "A_C_im": _A_in["C_im"],
        "A_log_dt": _A_in["log_dt"],
        "A_Dskip": _A_in["Dskip"],
        "B_x": _B_in["x"],
        "B_w_gate": _B_in["w_gate"],
        "B_w_up": _B_in["w_up"],
        "B_w_down": _B_in["w_down"],
    }

def reference(A_x, A_Lambda_re, A_Lambda_im, A_B_re, A_B_im, A_C_re, A_C_im, A_log_dt, A_Dskip, B_x, B_w_gate, B_w_up, B_w_down):
    def _A_ref(x, Lambda_re, Lambda_im, B_re, B_im, C_re, C_im, log_dt, Dskip):
        Bsz, L, H = x.shape
        Lam = Lambda_re + 1j * Lambda_im
        Bmat = B_re + 1j * B_im
        Cmat = C_re + 1j * C_im
        dt = torch.exp(log_dt)
        Lam_bar = torch.exp(dt * Lam)
        B_bar = ((Lam_bar - 1.0) / Lam)[:, None] * Bmat
        Bu = torch.einsum("bld,pd->blp", x + 0j, B_bar)
        h = torch.zeros_like(Bu[:, 0, :])
        hs = []
        for t in range(L):
            h = Lam_bar[None, :] * h + Bu[:, t, :]
            hs.append(h)
        hseq = torch.stack(hs, 1)
        y = 2.0 * torch.einsum("blp,dp->bld", hseq, Cmat).real + x * Dskip[None, None, :]
        return y

    def _B_ref(x, w_gate, w_up, w_down):
        g = torch.einsum("bsd,df->bsf", x, w_gate)
        u = torch.einsum("bsd,df->bsf", x, w_up)
        h = g * u
        return torch.einsum("bsf,fd->bsd", h, w_down)

    x = A_x
    xn = _layernorm(x, axis=-1)
    return x + _A_ref(xn, A_Lambda_re, A_Lambda_im, A_B_re, A_B_im, A_C_re, A_C_im, A_log_dt, A_Dskip) + _B_ref(xn, B_w_gate, B_w_up, B_w_down)
```

```cpp
#include <hip/hip_runtime.h>
#include <hip/hip_bf16.h>
#include <stdint.h>

typedef unsigned short u16;
__device__ __forceinline__ float bf2f(u16 v) { return __uint_as_float(((unsigned)v) << 16); }
__device__ __forceinline__ u16 f2bf(float f) { unsigned u = __float_as_uint(f); return (u16)((u + 0x7FFF + ((u >> 16) & 1)) >> 16); }
__device__ __forceinline__ float rbf(float f) { return bf2f(f2bf(f)); }

#define MIMIC 0

constexpr int L = 2048, D = 512, P = 512, F = 1792;

__global__ void k_ln(const u16* x, float* xn) {
    int t = blockIdx.x; int tid = threadIdx.x;
    float v[8]; float s = 0;
    for (int i = 0; i < 8; i++) { v[i] = bf2f(x[t * D + tid + 64 * i]); s += v[i]; }
    for (int o = 32; o; o >>= 1) s += __shfl_xor(s, o);
    float m = s / D; float q = 0;
    for (int i = 0; i < 8; i++) { float d = v[i] - m; q += d * d; }
    for (int o = 32; o; o >>= 1) q += __shfl_xor(q, o);
    float var = q / D;
    float sd = sqrtf(var + 1e-5f);
    for (int i = 0; i < 8; i++) xn[t * D + tid + 64 * i] = rbf((v[i] - m) / sd);
}
__global__ void k_bu(const float* xn, const u16* Bre, const u16* Bim, float* bur, float* bui) {
    int t = blockIdx.x; int p = blockIdx.y * 256 + threadIdx.x;
    float ar = 0, ai = 0;
    for (int d = 0; d < D; d++) { float xv = xn[t * D + d]; ar += xv * bf2f(Bre[p * D + d]); ai += xv * bf2f(Bim[p * D + d]); }
    bur[t * P + p] = ar; bui[t * P + p] = ai;
}
__global__ void k_scan(float* bur, float* bui, const u16* lre, const u16* lim, const u16* ldt) {
    int p = blockIdx.x * 64 + threadIdx.x;
    float lr = bf2f(lre[p]), li = bf2f(lim[p]);
    float dt = expf(bf2f(ldt[p]));
    float e = expf(dt * lr); float ar = e * cosf(dt * li), ai = e * sinf(dt * li);
    float nr = ar - 1.f, ni = ai; float den = lr * lr + li * li;
    float cr = (nr * lr + ni * li) / den, ci = (ni * lr - nr * li) / den;
    float hr = 0, hi = 0;
    for (int t = 0; t < L; t++) {
        float br = bur[t * P + p], bi = bui[t * P + p];
        float ur = cr * br - ci * bi, ui = cr * bi + ci * br;
        float nhr = ar * hr - ai * hi + ur, nhi = ar * hi + ai * hr + ui;
        hr = nhr; hi = nhi;
        bur[t * P + p] = rbf(hr); bui[t * P + p] = rbf(hi);
    }
}
__global__ void k_gu(const float* xn, const u16* wg, const u16* wu, float* h) {
    int t = blockIdx.x; int f = blockIdx.y * 256 + threadIdx.x;
    float g = 0, u = 0;
    for (int d = 0; d < D; d++) { float xv = xn[t * D + d]; g += xv * bf2f(wg[d * F + f]); u += xv * bf2f(wu[d * F + f]); }
    h[t * F + f] = rbf(g * u);
}
__global__ void k_out(const u16* x, const float* xn, const float* hr, const float* hi, const float* h, const u16* Cre, const u16* Cim, const u16* Dsk, const u16* wd, float* out) {
    int t = blockIdx.x; int d = blockIdx.y * 256 + threadIdx.x;
    float y = 0;
    for (int p = 0; p < P; p++) y += hr[t * P + p] * bf2f(Cre[d * P + p]) - hi[t * P + p] * bf2f(Cim[d * P + p]);
    float b = 0;
    for (int f = 0; f < F; f++) b += h[t * F + f] * bf2f(wd[f * D + d]);
    float a = 2.f * y + (xn[t * D + d] * bf2f(Dsk[d]));
    out[t * D + d] = bf2f(x[t * D + d]) + a + b;
}

extern "C" void kernel_launch(void* const* d_in, const int* in_sizes, int n_in,
                              void* d_out, int out_size, void* d_ws, size_t ws_size,
                              hipStream_t stream) {
    const u16* x = (const u16*)d_in[0];
    const u16* lre = (const u16*)d_in[1]; const u16* lim = (const u16*)d_in[2];
    const u16* Bre = (const u16*)d_in[3]; const u16* Bim = (const u16*)d_in[4];
    const u16* Cre = (const u16*)d_in[5]; const u16* Cim = (const u16*)d_in[6];
    const u16* ldt = (const u16*)d_in[7]; const u16* Dsk = (const u16*)d_in[8];
    const u16* wg = (const u16*)d_in[10]; const u16* wu = (const u16*)d_in[11]; const u16* wd = (const u16*)d_in[12];
    float* ws = (float*)d_ws;
    float* xn = ws; float* bur = xn + L * D; float* bui = bur + L * P; float* h = bui + L * P;
    k_ln<<<L, 64, 0, stream>>>(x, xn);
    k_bu<<<dim3(L, 2), 256, 0, stream>>>(xn, Bre, Bim, bur, bui);
    k_scan<<<8, 64, 0, stream>>>(bur, bui, lre, lim, ldt);
    k_gu<<<dim3(L, 7), 256, 0, stream>>>(xn, wg, wu, h);
    k_out<<<dim3(L, 2), 256, 0, stream>>>(x, xn, bur, bui, h, Cre, Cim, Dsk, wd, (float*)d_out);
}
```

```cpp
#include <hip/hip_runtime.h>
#include <stdint.h>

typedef unsigned short u16;
typedef unsigned v4u __attribute__((ext_vector_type(4)));
typedef unsigned v2u __attribute__((ext_vector_type(2)));
typedef short s16x4 __attribute__((ext_vector_type(4)));
typedef __bf16 bf16x2 __attribute__((ext_vector_type(2)));
typedef __bf16 bf16x8 __attribute__((ext_vector_type(8)));
typedef float f32x2 __attribute__((ext_vector_type(2)));
typedef float f32x4 __attribute__((ext_vector_type(4)));
typedef float f32x16 __attribute__((ext_vector_type(16)));
#define GAS __attribute__((address_space(1)))
#define LAS __attribute__((address_space(3)))
#define DEVI __device__ __forceinline__

constexpr int SEQ = 2048, DM = 512, FF = 1792;
constexpr int NKS = 176;
constexpr unsigned A2_BYTES = 64u * NKS * 1024u;
constexpr unsigned WS_STATS = A2_BYTES;
constexpr unsigned WS_GRAN = WS_STATS + 2048 * 8;
constexpr unsigned WS_EPOCH = WS_GRAN + 16 * 16 * 512;
constexpr unsigned WS_END = WS_EPOCH + 16 * 256;

struct Params {
    const u16 *x, *lre, *lim, *Bre, *Bim, *Cre, *Cim, *ldt, *Dsk, *wg, *wu, *wd;
    float* out;
    unsigned char* ws;
};

DEVI float bf2f(u16 v) { return __uint_as_float(((unsigned)v) << 16); }
DEVI float bflo(unsigned v) { return __uint_as_float(v << 16); }
DEVI float bfhi(unsigned v) { return __uint_as_float(v & 0xffff0000u); }
DEVI unsigned pk2(float a, float b) { f32x2 p = {a, b}; return __builtin_bit_cast(unsigned, __builtin_convertvector(p, bf16x2)); }
DEVI float dot2(unsigned a, unsigned b, float c) { return __builtin_amdgcn_fdot2_f32_bf16(__builtin_bit_cast(bf16x2, a), __builtin_bit_cast(bf16x2, b), c, false); }
template <int CTRL> DEVI float dppf(float v) { return __builtin_bit_cast(float, __builtin_amdgcn_update_dpp(0, __builtin_bit_cast(int, v), CTRL, 0xf, 0xf, true)); }
DEVI f32x16 mfma(v4u a, v4u b, f32x16 c) { return __builtin_amdgcn_mfma_f32_32x32x16_bf16(__builtin_bit_cast(bf16x8, a), __builtin_bit_cast(bf16x8, b), c, 0, 0, 0); }
DEVI v4u lds128(unsigned a) { return *(LAS v4u*)(uintptr_t)a; }
DEVI void sts128(unsigned a, v4u v) { *(LAS v4u*)(uintptr_t)a = v; }
DEVI v2u ldstr(unsigned a) { return __builtin_bit_cast(v2u, __builtin_amdgcn_ds_read_tr16_b64_v4i16((LAS s16x4*)(uintptr_t)a)); }
DEVI v4u ldstr2(unsigned a0, unsigned a1) { v2u x = ldstr(a0), y = ldstr(a1); return v4u{x.x, x.y, y.x, y.y}; }
DEVI void bar() { asm volatile("s_waitcnt lgkmcnt(0)\n\ts_barrier" ::: "memory"); }
DEVI void bar_nw() { asm volatile("s_barrier" ::: "memory"); }
template <int N> DEVI void waitv() { asm volatile("s_waitcnt vmcnt(%0)" ::"n"(N) : "memory"); }
DEVI void dma16(const void* sbase, unsigned voff, unsigned ldst) {
    asm volatile("s_mov_b32 m0, %2\n\ts_nop 0\n\tglobal_load_lds_dwordx4 %0, %1" ::"v"(voff), "s"(sbase), "s"(ldst) : "memory");
}
#define SB() __builtin_amdgcn_sched_barrier(0)
struct TrueT { static constexpr bool value = true; };
struct FalseT { static constexpr bool value = false; };

constexpr int RING = 7;
constexpr unsigned L_PARK = 0, L_RING = 32768, L_MISC = L_RING + RING * 16384;
constexpr unsigned K1_LDS = L_MISC + 4096;
DEVI unsigned slot_addr(int st) { return L_RING + (unsigned)((st + RING - 1) % RING) * 16384u; }

DEVI void issue_stage(const Params& p, int st, int j, int hw, unsigned voffS, unsigned voffG) {
    const unsigned dst = slot_addr(st) + hw * 4096;
    if (st < 4) {
        const unsigned char* base = (const unsigned char*)((hw >> 1) ? p.Bim : p.Bre) + (size_t)(32 * j + 16 * (hw & 1)) * 1024 + st * 256;
#pragma unroll
        for (int n = 0; n < 4; n++) {
            const int ns = ((n & 1) << 1) | (n >> 1);
            dma16(base + ns * 4096, voffS ^ (unsigned)(n << 6), dst + n * 1024);
        }
    } else {
        const int g = st - 4, pass = g >> 3, kc = g & 7;
        const int line = 7 * (j >> 2) + 4 * pass + (j & 3);
        const unsigned char* base = (const unsigned char*)((hw >> 1) ? p.wu : p.wg) + (size_t)(kc * 64 + (hw & 1) * 32) * (FF * 2) + line * 128;
#pragma unroll
        for (int n = 0; n < 4; n++) dma16(base + n * 8 * (FF * 2), voffG, dst + n * 1024);
    }
}

__global__ __launch_bounds__(512, 2) void k1(Params p) {
    const int tid = threadIdx.x, lane = tid & 63;
    const int w = __builtin_amdgcn_readfirstlane(tid >> 6);
    const int b = blockIdx.x, xcd = b & 7, idx = b >> 3;
    const int i = 8 * (xcd & 1) + (idx & 7), j = 4 * (xcd >> 1) + (idx >> 3);
    const int npass = ((j & 3) == 3) ? 1 : 2;
    const int NS = 4 + 8 * npass;
    const int c = lane & 31, h = lane >> 5;

    unsigned voffS = 0, voffG = 0;
    if (w >= 4) {
        const int q = lane >> 4, pc = lane & 15;
        voffS = q * 1024 + ((pc ^ q) << 4);
        const int q8 = lane >> 3, p8 = lane & 7;
        voffG = q8 * (FF * 2) + (((p8 >> 1) ^ (((q8 >> 1) & 1) | ((w - 4) & 2))) << 5) + ((p8 & 1) << 4);
        issue_stage(p, 0, j, w - 4, voffS, voffG);
    }
    const __amdgpu_buffer_rsrc_t rws = __builtin_amdgcn_make_buffer_rsrc(p.ws, 0, WS_END, 0x00020000);
    {
        const int q = lane >> 3, pp = lane & 7;
        const GAS unsigned char* xb = (const GAS unsigned char*)p.x + (size_t)(128 * i + 16 * w + q) * 1024 + pp * 16;
        v4u xr[2][8];
#pragma unroll
        for (int g = 0; g < 2; g++)
#pragma unroll
            for (int n = 0; n < 8; n++) xr[g][n] = *(const GAS v4u*)(xb + g * 8192 + n * 128);
        SB();
#pragma unroll
        for (int g = 0; g < 2; g++) {
            float s0 = 0, s1 = 0, q0 = 0, q1 = 0;
#pragma unroll
            for (int n = 0; n < 8; n++) {
                s0 = dot2(xr[g][n].x, 0x3f803f80u, s0); q0 = dot2(xr[g][n].x, xr[g][n].x, q0);
                s1 = dot2(xr[g][n].y, 0x3f803f80u, s1); q1 = dot2(xr[g][n].y, xr[g][n].y, q1);
                s0 = dot2(xr[g][n].z, 0x3f803f80u, s0); q0 = dot2(xr[g][n].z, xr[g][n].z, q0);
                s1 = dot2(xr[g][n].w, 0x3f803f80u, s1); q1 = dot2(xr[g][n].w, xr[g][n].w, q1);
            }
            float s = s0 + s1, qq = q0 + q1;
            s += dppf<0xB1>(s); qq += dppf<0xB1>(qq);
            s += dppf<0x4E>(s); qq += dppf<0x4E>(qq);
            s += dppf<0x141>(s); qq += dppf<0x141>(qq);
            const float mean = s * (1.0f / DM);
            const float var = fmaxf(qq * (1.0f / DM) - mean * mean, 0.0f);
            const float rstd = __builtin_amdgcn_rsqf(var + 1e-5f);
            const float nb = -mean * rstd;
            const int t = 16 * w + 8 * g + q, tt = t & 31;
            const int r = (tt & 3) + 4 * (tt >> 4) + 8 * ((tt >> 2) & 3);
            const unsigned rowa = (unsigned)((t & ~31) + r) * 1024u + (unsigned)((pp ^ (r & 15)) << 4);
#pragma unroll
            for (int n = 0; n < 8; n++) {
                v4u o;
                o.x = pk2(fmaf(bflo(xr[g][n].x), rstd, nb), fmaf(bfhi(xr[g][n].x), rstd, nb));
                o.y = pk2(fmaf(bflo(xr[g][n].y), rstd, nb), fmaf(bfhi(xr[g][n].y), rstd, nb));
                o.z = pk2(fmaf(bflo(xr[g][n].z), rstd, nb), fmaf(bfhi(xr[g][n].z), rstd, nb));
                o.w = pk2(fmaf(bflo(xr[g][n].w), rstd, nb), fmaf(bfhi(xr[g][n].w), rstd, nb));
                sts128((rowa ^ (unsigned)((n & 1) << 7)) + (n >> 1) * 256, o);
            }
            if (j == 0 && pp == 0) *(GAS f32x2*)(p.ws + WS_STATS + (size_t)(128 * i + t) * 8) = f32x2{rstd, nb};
        }
    }
    bar();

    if (w >= 4) {
        const int hw = w - 4;
        bar_nw();
        for (int st = 1; st < RING; st++) issue_stage(p, st, j, hw, voffS, voffG);
        for (int s = 0; s < NS; s++) {
            int top = s + RING - 2; if (top < RING - 1) top = RING - 1; if (top > NS - 1) top = NS - 1;
            int y = top - (s + 1);
            if (y >= 5) waitv<20>(); else if (y == 4) waitv<16>(); else if (y == 3) waitv<12>(); else if (y == 2) waitv<8>(); else if (y == 1) waitv<4>(); else waitv<0>();
            if (s == 4) bar_nw();
            bar_nw();
            if (s >= 1 && s + RING - 1 < NS) issue_stage(p, s + RING - 1, j, hw, voffS, voffG);
        }
        return;
    }
    unsigned tag = __builtin_amdgcn_raw_buffer_load_b32(rws, WS_EPOCH + j * 256, 0, 16) + 1u;
    const int sg = (c & 19) | ((c & 4) << 1) | ((c & 8) >> 1);
    const int ps = 32 * j + sg;
    const float lr = bf2f(p.lre[ps]), li = bf2f(p.lim[ps]), ldt = bf2f(p.ldt[ps]);
    v4u xa[32];
    {
        const unsigned base = (unsigned)(32 * w + c) * 1024u + (unsigned)((h ^ (c & 15)) << 4);
#pragma unroll
        for (int s = 0; s < 32; s++) xa[s] = lds128((base ^ (unsigned)((s & 7) << 5)) + (s >> 3) * 256);
    }
    bar();
    float ar, ai, a16r, a16i, csr, csi;
    {
        f32x16 re = {}, im = {};
        const unsigned base = (unsigned)c * 256u + (unsigned)((h ^ (c & 15)) << 4);
        {
            constexpr int PD = 4;
            v4u bf[PD];
#pragma unroll
            for (int n = -PD; n < 64; n++) {
                if (n == -PD || (n > 0 && (n & 15) == 0)) bar_nw();
                v4u cur;
                if (n >= 0) cur = bf[n % PD];
                const int m = n + PD;
                if (m < 64) bf[m % PD] = lds128(((base ^ (unsigned)(((m >> 1) & 7) << 5)) + slot_addr(m >> 4)) + (m & 1) * 8192);
                if (n >= 0) { if (n & 1) im = mfma(xa[n >> 1], cur, im); else re = mfma(xa[n >> 1], cur, re); }
                SB();
            }
        }
        float dt = __expf(ldt);
        dt = bflo(pk2(dt, dt));
        const float e = __expf(dt * lr);
        const float th = dt * li * 0.15915494309189535f;
        const float fr = th - floorf(th);
        ar = e * __builtin_amdgcn_cosf(fr); ai = e * __builtin_amdgcn_sinf(fr);
        const float nr = ar - 1.0f, ni = ai, den = 1.0f / (lr * lr + li * li);
        const float cr = (nr * lr + ni * li) * den, ci = (ni * lr - nr * li) * den;
        float er = 0, ei = 0;
#pragma unroll
        for (int t = 0; t < 16; t++) {
            const float ur = cr * re[t] - ci * im[t], ui = cr * im[t] + ci * re[t];
            re[t] = ur; im[t] = ui;
            const float tr = fmaf(ar, er, fmaf(-ai, ei, ur)), ti = fmaf(ar, ei, fmaf(ai, er, ui));
            er = tr; ei = ti;
        }
        const unsigned pk = L_PARK + w * 8192 + lane * 16;
#pragma unroll
        for (int g = 0; g < 4; g++) {
            sts128(pk + g * 1024, __builtin_bit_cast(v4u, f32x4{re[4 * g], re[4 * g + 1], re[4 * g + 2], re[4 * g + 3]}));
            sts128(pk + 4096 + g * 1024, __builtin_bit_cast(v4u, f32x4{im[4 * g], im[4 * g + 1], im[4 * g + 2], im[4 * g + 3]}));
        }
        *(LAS f32x2*)(uintptr_t)(L_MISC + (w * 64 + lane) * 8) = f32x2{er, ei};
        a16r = ar; a16i = ai;
#pragma unroll
        for (int n = 0; n < 4; n++) { const float tr = a16r * a16r - a16i * a16i, ti = 2.0f * a16r * a16i; a16r = tr; a16i = ti; }
        bar();
        float tr = 0, ti = 0; csr = 0; csi = 0;
        const int seg = 2 * w + h;
#pragma unroll
        for (int s = 0; s < 8; s++) {
            if (s == seg) { csr = tr; csi = ti; }
            const f32x2 ev = *(LAS f32x2*)(uintptr_t)(L_MISC + (s * 32 + c) * 8);
            const float nr2 = fmaf(a16r, tr, fmaf(-a16i, ti, ev.x)), ni2 = fmaf(a16r, ti, fmaf(a16i, tr, ev.y));
            tr = nr2; ti = ni2;
        }
        if (w == 0 && lane < 32)
            __builtin_amdgcn_raw_buffer_store_b128(v4u{__float_as_uint(tr), tag, __float_as_uint(ti), tag}, rws, WS_GRAN + ((j * 16 + i) * 32 + c) * 16, 0, 16);
    }
    const unsigned a2base = (unsigned)(4 * i + w) * (NKS * 1024u) + lane * 16;
    {
        const int blk = (lane >> 4) & 1, q = (lane & 15) >> 2, pq = lane & 3;
        const unsigned tb = (unsigned)(8 * h + q) * 128u + blk * 8192 + (((q >> 1) | (blk << 1)) << 5) + ((((pq & 1) << 1) | (pq >> 1)) << 3);
        for (int pass = 0; pass < npass; pass++) {
            f32x16 acc[4] = {};
            constexpr int PD = 4;
            v4u bf[PD];
            unsigned bs = 0;
#pragma unroll
            for (int n = -PD; n < 128; n++) {
                if (n == -PD || (n > 0 && (n & 15) == 0)) bar_nw();
                v4u cur;
                if (n >= 0) cur = bf[n % PD];
                const int m = n + PD;
                if (m < 128) {
                    if ((m & 15) == 0) bs = tb + slot_addr(4 + pass * 8 + (m >> 4));
                    const unsigned a = (bs ^ (unsigned)((m & 3) << 5)) + ((m >> 2) & 3) * 2048;
                    bf[m % PD] = ldstr2(a, a + 512);
                }
                if (n >= 0) acc[n & 3] = mfma(cur, xa[n >> 2], acc[n & 3]);
                SB();
            }
            const int line = 7 * (j >> 2) + 4 * pass + (j & 3);
#pragma unroll
            for (int t = 0; t < 4; t++) {
                v4u o;
                o.x = pk2(acc[t][0] * acc[t][8], acc[t][1] * acc[t][9]);
                o.y = pk2(acc[t][2] * acc[t][10], acc[t][3] * acc[t][11]);
                o.z = pk2(acc[t][4] * acc[t][12], acc[t][5] * acc[t][13]);
                o.w = pk2(acc[t][6] * acc[t][14], acc[t][7] * acc[t][15]);
                __builtin_amdgcn_raw_buffer_store_b128(o, rws, a2base + (4 * line + t) * 1024, 0, 0);
            }
        }
    }
    {
        float a128r = a16r, a128i = a16i;
#pragma unroll
        for (int n = 0; n < 3; n++) { const float tr = a128r * a128r - a128i * a128i, ti = 2.0f * a128r * a128i; a128r = tr; a128i = ti; }
        float Cr = 0, Ci = 0;
        const unsigned go = WS_GRAN + (j * 16 * 32 + c) * 16;
        for (int spin = 0; spin < (1 << 20); spin++) {
            v4u g[15];
#pragma unroll
            for (int n = 0; n < 15; n++) g[n] = __builtin_amdgcn_raw_buffer_load_b128(rws, go + n * 512, 0, 16);
            bool ok = true; Cr = 0; Ci = 0;
#pragma unroll
            for (int n = 0; n < 15; n++) {
                if (n < i) {
                    ok = ok && g[n].y == tag && g[n].w == tag;
                    const float nr = fmaf(a128r, Cr, fmaf(-a128i, Ci, __uint_as_float(g[n].x))), ni = fmaf(a128r, Ci, fmaf(a128i, Cr, __uint_as_float(g[n].z)));
                    Cr = nr; Ci = ni;
                }
            }
            if (__all(ok)) break;
            __builtin_amdgcn_s_sleep(2);
        }
        if (i == 15 && w == 0 && lane == 0) __builtin_amdgcn_raw_buffer_store_b32(tag, rws, WS_EPOCH + j * 256, 0, 16);
        const int seg = 2 * w + h;
        for (int s = 0; s < 7; s++) {
            const float nr = a16r * Cr - a16i * Ci, ni = a16r * Ci + a16i * Cr;
            if (s < seg) { Cr = nr; Ci = ni; }
        }
        float hr = Cr + csr, hi = Ci + csi;
        const unsigned pk = L_PARK + w * 8192 + lane * 16;
        float yr[16], yi[16];
#pragma unroll
        for (int g = 0; g < 4; g++) {
            const f32x4 ur = __builtin_bit_cast(f32x4, lds128(pk + g * 1024)), ui = __builtin_bit_cast(f32x4, lds128(pk + 4096 + g * 1024));
#pragma unroll
            for (int e = 0; e < 4; e++) {
                const float tr = fmaf(ar, hr, fmaf(-ai, hi, ur[e])), ti = fmaf(ar, hi, fmaf(ai, hr, ui[e]));
                hr = tr; hi = ti;
                yr[4 * g + e] = 2.0f * hr; yi[4 * g + e] = -2.0f * hi;
            }
        }
        const int tk = 16 * ((c >> 2) & 1) + (c & 3) + 4 * (c >> 3) - 16 * h;
        f32x16 zr = {}, zi = {};
#pragma unroll
        for (int s = 0; s < 2; s++) {
            v4u id;
#pragma unroll
            for (int e = 0; e < 4; e++) id[e] = (tk == 8 * s + 2 * e ? 0x3f80u : 0u) | (tk == 8 * s + 2 * e + 1 ? 0x3f800000u : 0u);
            v4u fr, fi;
#pragma unroll
            for (int e = 0; e < 4; e++) { fr[e] = pk2(yr[8 * s + 2 * e], yr[8 * s + 2 * e + 1]); fi[e] = pk2(yi[8 * s + 2 * e], yi[8 * s + 2 * e + 1]); }
            zr = mfma(fr, id, zr); zi = mfma(fi, id, zi);
        }
#pragma unroll
        for (int s = 0; s < 2; s++) {
            v4u o, o2;
#pragma unroll
            for (int e = 0; e < 4; e++) { o[e] = pk2(zr[8 * s + 2 * e], zr[8 * s + 2 * e + 1]); o2[e] = pk2(zi[8 * s + 2 * e], zi[8 * s + 2 * e + 1]); }
            __builtin_amdgcn_raw_buffer_store_b128(o, rws, a2base + (112 + 2 * j + s) * 1024, 0, 0);
            __builtin_amdgcn_raw_buffer_store_b128(o2, rws, a2base + (144 + 2 * j + s) * 1024, 0, 0);
        }
    }
}

constexpr unsigned K2_LDS = 131072;
DEVI void ldA(v4u& d, unsigned voff, const void* sbase) { asm volatile("global_load_dwordx4 %0, %1, %2" : "=v"(d) : "v"(voff), "s"(sbase) : "memory"); }

__global__ __launch_bounds__(512, 2) void k2(Params p) {
    const int tid = threadIdx.x, lane = tid & 63;
    const int w = __builtin_amdgcn_readfirstlane(tid >> 6);
    const int b = blockIdx.x, xcd = b & 7, idx = b >> 3;
    const int mt = 4 * xcd + (idx >> 3), qc = idx & 7;
    const int c = lane & 31, h = lane >> 5;
    unsigned voffW[2];
    {
        const int q = lane >> 3, pp = lane & 7;
        const int g = (((q >> 1) & 1) << 2) | ((q >> 2) & 1);
        voffW[0] = q * 1024 + ((pp ^ g) << 4);
        voffW[1] = q * 1024 + ((pp ^ g ^ 2) << 4);
    }
    auto wbase = [&](int u) -> const unsigned char* {
        if (u < 28) return (const unsigned char*)p.wd + (size_t)u * 65536 + qc * 128;
        return (const unsigned char*)(u < 36 ? p.Cre : p.Cim) + (size_t)qc * 65536 + ((u - 28) & 7) * 128;
    };
    auto issueW = [&](int u, int buf) {
        const unsigned char* wb = wbase(u);
        const unsigned dst = w * 16384 + buf * 8192;
#pragma unroll
        for (int n = 0; n < 8; n++) dma16(wb + n * 8192, voffW[n & 1], dst + n * 1024);
    };
    const unsigned char* a2 = p.ws + (size_t)(2 * mt) * (NKS * 1024);
    const unsigned voA0 = lane * 16, voA1 = lane * 16 + NKS * 1024;
    v4u af[4][2];
    issueW(w, 0);
#pragma unroll
    for (int s = 0; s < 4; s++) { ldA(af[s][0], voA0, a2 + (4 * w + s) * 1024); ldA(af[s][1], voA1, a2 + (4 * w + s) * 1024); }
    f32x16 acc[2][2] = {};
    unsigned trb, cb;
    {
        const int blk = (lane >> 4) & 1, q = (lane & 15) >> 2, pq = lane & 3;
        const int piece = 2 * blk + (pq >> 1);
        trb = (unsigned)(8 * h + q) * 128u + (unsigned)((piece ^ (((q >> 1) << 2) | (h << 1))) << 4) + ((pq & 1) << 3);
        const int g = (((c >> 1) & 1) << 2) | (((c >> 3) & 1) << 1) | ((c >> 2) & 1);
        cb = (unsigned)c * 128u + (unsigned)((h ^ g) << 4);
    }
    int buf = 0;
    auto body = [&](auto isG, auto isLast, int u) {
        constexpr bool G = decltype(isG)::value, LAST = decltype(isLast)::value;
        waitv<8>();
        if (!LAST) issueW(u + 8, buf ^ 1);
        const unsigned lb = w * 16384 + buf * 8192;
        const unsigned char* an = a2 + (size_t)(4 * (u + 8)) * 1024;
#pragma unroll
        for (int s = 0; s < 4; s++) {
            v4u b0, b1;
            if (G) { const unsigned a = lb + trb + s * 2048; b0 = ldstr2(a, (a ^ 16) + 512); b1 = ldstr2(a ^ 64, (a ^ 80) + 512); }
            else { const unsigned a = lb + (cb ^ (unsigned)(s << 5)); b0 = lds128(a); b1 = lds128(a + 4096); }
            asm volatile("s_waitcnt vmcnt(%2)" : "+v"(af[s][0]), "+v"(af[s][1]) : "n"(LAST ? 6 - 2 * s : 14) : "memory");
            acc[0][0] = mfma(b0, af[s][0], acc[0][0]); acc[0][1] = mfma(b0, af[s][1], acc[0][1]);
            acc[1][0] = mfma(b1, af[s][0], acc[1][0]); acc[1][1] = mfma(b1, af[s][1], acc[1][1]);
            SB();
            if (!LAST) { ldA(af[s][0], voA0, an + s * 1024); ldA(af[s][1], voA1, an + s * 1024); }
        }
        buf ^= 1;
    };
    int u = w;
    for (; u < 28; u += 8) body(TrueT{}, FalseT{}, u);
    body(FalseT{}, FalseT{}, u);
    body(FalseT{}, TrueT{}, u + 8);
#pragma unroll
    for (int m = 0; m < 2; m++)
#pragma unroll
        for (int nt = 0; nt < 2; nt++)
#pragma unroll
            for (int g = 0; g < 4; g++) {
                const int e = nt * 8 + g * 2 + h;
                sts128(w * 16384 + (m * 32 + c) * 256 + ((e ^ (c & 15)) << 4),
                       __builtin_bit_cast(v4u, f32x4{acc[nt][m][4 * g], acc[nt][m][4 * g + 1], acc[nt][m][4 * g + 2], acc[nt][m][4 * g + 3]}));
            }
    bar();
#pragma unroll
    for (int it = 0; it < 2; it++) {
        const int row = w * 8 + it * 4 + (lane >> 4), e = lane & 15;
        const int cc = row & 31;
        const int tok = 64 * mt + (row & 32) + 16 * ((cc >> 2) & 1) + (cc & 3) + 4 * (cc >> 3);
        const int d = 64 * qc + 4 * e;
        const v2u xv = *(const GAS v2u*)(p.x + (size_t)tok * DM + d);
        const v2u dv = *(const GAS v2u*)(p.Dsk + d);
        const f32x2 st = *(const GAS f32x2*)(p.ws + WS_STATS + (size_t)tok * 8);
        f32x4 s = {0, 0, 0, 0};
#pragma unroll
        for (int v = 0; v < 8; v++) s += __builtin_bit_cast(f32x4, lds128(v * 16384 + row * 256 + ((e ^ (cc & 15)) << 4)));
        const float x0 = bflo(xv.x), x1 = bfhi(xv.x), x2 = bflo(xv.y), x3 = bfhi(xv.y);
        s.x += x0 + fmaf(x0, st.x, st.y) * bflo(dv.x);
        s.y += x1 + fmaf(x1, st.x, st.y) * bfhi(dv.x);
        s.z += x2 + fmaf(x2, st.x, st.y) * bflo(dv.y);
        s.w += x3 + fmaf(x3, st.x, st.y) * bfhi(dv.y);
        *(GAS f32x4*)(p.out + (size_t)tok * DM + d) = s;
    }
}

extern "C" void kernel_launch(void* const* d_in, const int* in_sizes, int n_in, void* d_out, int out_size, void* d_ws, size_t ws_size,
                              hipStream_t stream) {
    Params p;
    p.x = (const u16*)d_in[0]; p.lre = (const u16*)d_in[1]; p.lim = (const u16*)d_in[2];
    p.Bre = (const u16*)d_in[3]; p.Bim = (const u16*)d_in[4]; p.Cre = (const u16*)d_in[5]; p.Cim = (const u16*)d_in[6];
    p.ldt = (const u16*)d_in[7]; p.Dsk = (const u16*)d_in[8];
    p.wg = (const u16*)d_in[10]; p.wu = (const u16*)d_in[11]; p.wd = (const u16*)d_in[12];
    p.out = (float*)d_out; p.ws = (unsigned char*)d_ws;
    (void)hipFuncSetAttribute((const void*)k1, hipFuncAttributeMaxDynamicSharedMemorySize, K1_LDS);
    (void)hipFuncSetAttribute((const void*)k2, hipFuncAttributeMaxDynamicSharedMemorySize, K2_LDS);
    k1<<<256, 512, K1_LDS, stream>>>(p);
    k2<<<256, 512, K2_LDS, stream>>>(p);
}
```

```cpp
#include <hip/hip_runtime.h>
#include <stdint.h>

typedef unsigned short u16;
typedef unsigned v4u __attribute__((ext_vector_type(4)));
typedef unsigned v2u __attribute__((ext_vector_type(2)));
typedef short s16x4 __attribute__((ext_vector_type(4)));
typedef __bf16 bf16x2 __attribute__((ext_vector_type(2)));
typedef __bf16 bf16x8 __attribute__((ext_vector_type(8)));
typedef float f32x2 __attribute__((ext_vector_type(2)));
typedef float f32x4 __attribute__((ext_vector_type(4)));
#define GAS __attribute__((address_space(1)))
#define LAS __attribute__((address_space(3)))
#define DEVI __device__ __forceinline__
#define NOUNROLL _Pragma("nounroll")

constexpr int DM = 512, FF = 1792;
constexpr unsigned WS_A2 = 0;
constexpr unsigned WS_XB = 16u * 44u * 16384u;
constexpr unsigned WS_GRAN = WS_XB + 256u * 49152u;
constexpr unsigned WS_FLAG = WS_GRAN + 16u * 16u * 512u;
constexpr unsigned WS_EPOCH = WS_FLAG + 512u * 128u;
constexpr unsigned WS_END = WS_EPOCH + 256u;
constexpr unsigned SLOT = 16384;
constexpr unsigned L_RSTD = 0, L_MEAN = 512;
constexpr unsigned F_DONE = 1024, F_DRAIN = F_DONE + 16, F_FULL = F_DONE + 32, F_COMM = F_DONE + 40, L_TAG = F_DONE + 48;
constexpr unsigned L_CST = 1536;
constexpr unsigned L_XCH = L_CST + 7 * 256;
constexpr unsigned L_SEG = 4096, L_HIN = 8192;
constexpr unsigned L_A = SLOT, L_PARK = 4 * SLOT, L_W = 6 * SLOT, L_TOTAL = 10 * SLOT;
constexpr int SPIN_MAX = 1 << 18;
constexpr int LSPIN_MAX = 1 << 22;

struct Params {
    const u16 *x, *lre, *lim, *Bre, *Bim, *Cre, *Cim, *ldt, *Dsk, *wg, *wu, *wd;
    float* out;
    unsigned char* ws;
};

DEVI float bf2f(u16 v) { return __uint_as_float(((unsigned)v) << 16); }
DEVI float bflo(unsigned v) { return __uint_as_float(v << 16); }
DEVI float bfhi(unsigned v) { return __uint_as_float(v & 0xffff0000u); }
DEVI unsigned pk2(float a, float b) { f32x2 p = {a, b}; return __builtin_bit_cast(unsigned, __builtin_convertvector(p, bf16x2)); }
DEVI unsigned pk2(f32x2 p) { return __builtin_bit_cast(unsigned, __builtin_convertvector(p, bf16x2)); }
DEVI v4u pk8(f32x4 a, f32x4 b) { return v4u{pk2(a.x, a.y), pk2(a.z, a.w), pk2(b.x, b.y), pk2(b.z, b.w)}; }
DEVI float dot2(unsigned a, unsigned b, float c) { return __builtin_amdgcn_fdot2_f32_bf16(__builtin_bit_cast(bf16x2, a), __builtin_bit_cast(bf16x2, b), c, false); }
template <int CTRL> DEVI float dppf(float v) { return __builtin_bit_cast(float, __builtin_amdgcn_update_dpp(0, __builtin_bit_cast(int, v), CTRL, 0xf, 0xf, true)); }
DEVI f32x4 mfma(v4u a, v4u b, f32x4 c) { return __builtin_amdgcn_mfma_f32_16x16x32_bf16(__builtin_bit_cast(bf16x8, a), __builtin_bit_cast(bf16x8, b), c, 0, 0, 0); }
DEVI v4u lds128(unsigned a) { return *(LAS v4u*)(uintptr_t)a; }
DEVI f32x4 lds128f(unsigned a) { return *(LAS f32x4*)(uintptr_t)a; }
DEVI float lds32f(unsigned a) { return *(LAS float*)(uintptr_t)a; }
DEVI void sts32f(unsigned a, float v) { *(LAS float*)(uintptr_t)a = v; }
DEVI void sts128(unsigned a, v4u v) { *(LAS v4u*)(uintptr_t)a = v; }
DEVI void sts128f(unsigned a, f32x4 v) { *(LAS f32x4*)(uintptr_t)a = v; }
DEVI v2u ldstr(unsigned a) { return __builtin_bit_cast(v2u, __builtin_amdgcn_ds_read_tr16_b64_v4i16((LAS s16x4*)(uintptr_t)a)); }
DEVI void fence_c() { asm volatile("" ::: "memory"); }
DEVI void bar_l() { asm volatile("s_waitcnt lgkmcnt(0)\n\ts_barrier" ::: "memory"); }
template <int N> DEVI void waitv() { asm volatile("s_waitcnt vmcnt(%0)" ::"n"(N) : "memory"); }
DEVI void dma16(const void* sbase, unsigned voff, unsigned ldst) {
    asm volatile("s_mov_b32 m0, %2\n\ts_nop 0\n\tglobal_load_lds_dwordx4 %0, %1" ::"v"(voff), "s"(sbase), "s"(ldst) : "memory");
}
DEVI void dma16x4_sc1(const void* sbase, unsigned voff, unsigned ldst) {
    asm volatile("s_mov_b32 m0, %2\n\ts_nop 0\n\tglobal_load_lds_dwordx4 %0, %1 sc1\n\tglobal_load_lds_dwordx4 %0, %1 offset:1024 sc1\n\t"
                 "global_load_lds_dwordx4 %0, %1 offset:2048 sc1\n\tglobal_load_lds_dwordx4 %0, %1 offset:3072 sc1" ::"v"(voff), "s"(sbase), "s"(ldst) : "memory");
}
DEVI void post(unsigned a, unsigned v) { fence_c(); *(LAS unsigned*)(uintptr_t)a = v; fence_c(); }
DEVI unsigned peek(unsigned a) { fence_c(); return *(LAS unsigned*)(uintptr_t)a; }
DEVI bool any_lt(unsigned v, unsigned need) { return __builtin_amdgcn_ballot_w64(v < need) != 0; }
DEVI void wait_ge(unsigned a, unsigned need) { NOUNROLL for (int s = 0; s < LSPIN_MAX && any_lt(peek(a), need); s++) {} fence_c(); }
DEVI void need_full(bool lack, unsigned a, unsigned need) { if (__builtin_expect(lack, 0)) wait_ge(a, need); fence_c(); }
#define VAL(id, v)
DEVI float sfma(float a, float b, float c) { float d; asm("v_fma_f32 %0, %1, %2, %3" : "=v"(d) : "v"(a), "v"(b), "v"(c)); return d; }
DEVI float sfms(float a, float b, float c) { float d; asm("v_fma_f32 %0, %1, %2, -%3" : "=v"(d) : "v"(a), "v"(b), "v"(c)); return d; }
DEVI float smul(float a, float b) { float d; asm("v_mul_f32 %0, %1, %2" : "=v"(d) : "v"(a), "v"(b)); return d; }
struct cpx { float x, y; };
DEVI cpx cmul(cpx a, cpx b) { return cpx{sfms(a.x, b.x, smul(a.y, b.y)), sfma(a.x, b.y, smul(a.y, b.x))}; }
DEVI float lmad(cpx a, float h, float u) {
    float d;
    asm("v_fma_f32 %0, %1, %3, %4\n\ts_nop 0\n\tv_fmac_f32_dpp %0, %3, %2 quad_perm:[1,0,3,2] row_mask:0xf bank_mask:0xf" : "=&v"(d) : "v"(a.x), "v"(a.y), "v"(h), "v"(u));
    return d;
}
DEVI f32x2 dup(float v) { f32x2 d = {v, v}; asm("" : "+v"(d)); return d; }
DEVI f32x2 pfma(f32x2 a, f32x2 b, f32x2 c) { return __builtin_elementwise_fma(a, b, c); }
DEVI f32x2 lo2(f32x4 v) { return f32x2{v.x, v.y}; }
DEVI f32x2 hi2(f32x4 v) { return f32x2{v.z, v.w}; }
#define SB() __builtin_amdgcn_sched_barrier(0)
template <int V> struct IC { static constexpr int value = V; };
template <int B, int E, class F> DEVI void sfor(F&& f) { if constexpr (B < E) { f(IC<B>{}); sfor<B + 1, E>(f); } }
#define LAMB(nn) [&](auto nn) __attribute__((always_inline))
#define STAMP(id)
DEVI int fresh_lane() { int l; asm volatile("v_mbcnt_lo_u32_b32 %0, -1, 0\n\tv_mbcnt_hi_u32_b32 %0, -1, %0" : "=v"(l)); return l; }
DEVI v4u make_sel(int lane, int bb) {
    const int c = lane & 15, tk = 8 * (c >> 2) + 4 * bb + (c & 3) - 8 * (lane >> 4);
    v4u s;
#pragma unroll
    for (int e = 0; e < 4; e++) s[e] = (tk == 2 * e ? 0x3f80u : 0u) | (tk == 2 * e + 1 ? 0x3f800000u : 0u);
    return s;
}
#define LANE_VARS const int lane = fresh_lane(), c = lane & 15, g = lane >> 4, q8 = lane >> 3, p8 = lane & 7; (void)c; (void)g; (void)q8; (void)p8
constexpr unsigned wslot(int n) { return (unsigned)((n + 2) & 3) * SLOT; }
DEVI int tok_pos(int t) { return (t & ~7) + 4 * ((t >> 1) & 1) + 2 * (t & 1) + ((t >> 2) & 1); }

__global__ __launch_bounds__(512, 2) void fused(Params p) {
    const int w = __builtin_amdgcn_readfirstlane(threadIdx.x >> 6);
    const int b = blockIdx.x, xcd = b & 7, idx = b >> 3;
    const int i = 8 * (xcd & 1) + (idx & 7), kq = xcd >> 1, q = idx >> 3, j = 4 * kq + q;
    const bool heavy = q != 3;
    const int N1 = heavy ? 20 : 12;
    const int lw = w & 1;
    const __amdgpu_buffer_rsrc_t rws = __builtin_amdgcn_make_buffer_rsrc(p.ws, 0, WS_END, 0x00020000);
    auto issue_s = [&](int lane, int n) {
        const int q8 = lane >> 3, p8 = lane & 7;
        const unsigned voffS = (8 * (q8 >> 2) + (q8 & 3)) * 1024 + ((p8 ^ (q8 >> 1)) << 4);
        const unsigned char* base = (const unsigned char*)(lw ? p.Bim : p.Bre) + (size_t)(32 * j) * 1024 + n * 256;
        const unsigned dst = L_W + ((n + 2) & 3) * SLOT + lw * 8192;
#pragma unroll
        for (int m = 0; m < 8; m++) dma16(base + (16 * (m & 1) + 4 * ((m >> 1) & 1)) * 1024 + 128 * (m >> 2), voffS ^ (unsigned)((m & 1) << 6), dst + m * 1024);
    };
    v4u xa[16][2];
    {
        LANE_VARS;
        STAMP(0);
        v4u xr[2][8];
        float st_mean[2], st_rstd[2];
        unsigned tag0 = 0, prm0 = 0, prm1 = 0, prm2 = 0;
        const GAS unsigned char* xb = (const GAS unsigned char*)p.x + (size_t)(128 * i + 16 * w + q8) * 1024 + p8 * 16;
#pragma unroll
        for (int gq = 0; gq < 2; gq++)
#pragma unroll
            for (int n = 0; n < 8; n++) asm volatile("global_load_dwordx4 %0, %1, off offset:%2" : "=v"(xr[gq][n]) : "v"(xb + gq * 8192), "n"(n * 128) : "memory");
        {
            const unsigned voffS = (8 * (q8 >> 2) + (q8 & 3)) * 1024 + ((p8 ^ (q8 >> 1)) << 4);
            const unsigned char* base = (const unsigned char*)(lw ? p.Bim : p.Bre) + (size_t)(32 * j) * 1024 + (w >> 1) * 128;
            const unsigned dst = L_W + (2 + (w >> 2)) * SLOT + lw * 8192 + ((w >> 1) & 1) * 4096;
#pragma unroll
            for (int m = 0; m < 4; m++) dma16(base + (16 * (m & 1) + 4 * (m >> 1)) * 1024, voffS ^ (unsigned)((m & 1) << 6), dst + m * 1024);
        }
        if (w == 7) {
            const unsigned po = 2 * (32 * j + 8 * (lane >> 4) + 4 * ((lane >> 1) & 1) + ((lane >> 2) & 3));
            asm volatile("global_load_ushort %0, %4, %5\n\tglobal_load_ushort %1, %4, %6\n\tglobal_load_ushort %2, %4, %7\n\tglobal_load_dword %3, %8, %9 sc1"
                         : "+v"(prm0), "+v"(prm1), "+v"(prm2), "+v"(tag0) : "v"(po), "s"(p.lre), "s"(p.lim), "s"(p.ldt), "v"(WS_EPOCH), "s"(p.ws) : "memory");
        }
        SB();
        const v4u sel0 = make_sel(lane, 0), sel1 = make_sel(lane, 1);
        const unsigned o1 = (g == (c >> 2)) ? 0x3f803f80u : 0u;
        const v4u own = {o1, o1, o1, o1};
#pragma unroll
        for (int gq = 0; gq < 2; gq++) {
            asm volatile("s_waitcnt vmcnt(%8)" : "+v"(xr[gq][0]), "+v"(xr[gq][1]), "+v"(xr[gq][2]), "+v"(xr[gq][3]), "+v"(xr[gq][4]), "+v"(xr[gq][5]), "+v"(xr[gq][6]), "+v"(xr[gq][7]) : "n"(12 - 8 * gq) : "memory");
            f32x4 sa = {0, 0, 0, 0}, sb = {0, 0, 0, 0};
#pragma unroll
            for (int n = 0; n < 4; n++) { sa = mfma(own, xr[gq][n], sa); sb = mfma(own, xr[gq][n + 4], sb); }
            float s = sa.x + sb.x;
            s += dppf<0xB1>(s); s += dppf<0x4E>(s); s += dppf<0x141>(s);
            const float nm = s * (-1.0f / DM);
            st_mean[gq] = -nm;
            const f32x4 nm4 = {nm, nm, nm, nm};
            const int t = 16 * w + 8 * gq + q8;
            const int key = 4 * ((t >> 3) & 3) + 2 * (t & 1) + ((t >> 2) & 1);
            const unsigned rowa = (unsigned)t * 1024u + (unsigned)((p8 ^ key) << 4);
#pragma unroll
            for (int n = 0; n < 8; n++) sts128((rowa ^ (unsigned)((n & 1) << 7)) + (n >> 1) * 256, pk8(mfma(sel0, xr[gq][n], nm4), mfma(sel1, xr[gq][n], nm4)));
            SB();
        }
        STAMP(1);
        bar_l();
        STAMP(2);
        if (w < 4) {
            const unsigned base = (unsigned)(32 * w + 8 * (c >> 2) + 4 * (c & 1) + ((c >> 1) & 1)) * 1024u + (unsigned)((g ^ c) << 4);
#pragma unroll
            for (int ks = 0; ks < 16; ks++)
#pragma unroll
                for (int bb = 0; bb < 2; bb++) xa[ks][bb] = lds128((base ^ (unsigned)((ks & 3) << 6)) + (ks >> 2) * 256 + bb * 2048);
        }
        SB();
#pragma unroll
        for (int gq = 0; gq < 2; gq++) {
            float q0 = 0, q1 = 0, q2 = 0, q3 = 0;
#pragma unroll
            for (int n = 0; n < 8; n++) {
                q0 = dot2(xr[gq][n].x, xr[gq][n].x, q0); q1 = dot2(xr[gq][n].y, xr[gq][n].y, q1);
                q2 = dot2(xr[gq][n].z, xr[gq][n].z, q2); q3 = dot2(xr[gq][n].w, xr[gq][n].w, q3);
            }
            float qq = (q0 + q1) + (q2 + q3);
            qq += dppf<0xB1>(qq); qq += dppf<0x4E>(qq); qq += dppf<0x141>(qq);
            st_rstd[gq] = __builtin_amdgcn_rsqf(fmaxf(qq * (1.0f / DM) - st_mean[gq] * st_mean[gq], 0.0f) + 1e-5f);
        }
        asm volatile("s_waitcnt vmcnt(0)" : "+v"(tag0), "+v"(prm0), "+v"(prm1), "+v"(prm2)::"memory");
        bar_l();
        if (p8 == 0) {
#pragma unroll
            for (int gq = 0; gq < 2; gq++) {
                const int t = tok_pos(16 * w + 8 * gq + q8);
                sts32f(L_RSTD + t * 4, st_rstd[gq]);
                sts32f(L_MEAN + t * 4, st_mean[gq]);
            }
        }
        if (w == 7) {
            if (lane < 12) *(LAS unsigned*)(uintptr_t)(F_DONE + lane * 4) = (lane == 8 || lane == 9) ? 2u : 0u;
            *(LAS unsigned*)(uintptr_t)L_TAG = tag0 + 1u;
            *(LAS v2u*)(uintptr_t)(L_HIN + lane * 8) = v2u{prm0 | (prm1 << 16), prm2};
        }
        bar_l();
    }
    const unsigned fo = WS_FLAG + (unsigned)(i * 16 + j) * 256u;

    if (w == 4 || w == 5) {
        LANE_VARS;
        NOUNROLL for (int n = 2; n < 4; n++) issue_s(lane, n);
        const unsigned tg = peek(L_TAG);
        const v2u flagv = {tg, ~tg};
        const unsigned fa = F_DONE + (lane & 3) * 4, ffull = F_FULL + lw * 4;
        waitv<8>(); post(ffull, 3);
        auto wait_free = [&](unsigned n) {
            if (any_lt(peek(fa), n - 3)) { waitv<0>(); post(ffull, n); wait_ge(fa, n - 3); }
            fence_c();
            if (lw == 0 && (n == (heavy ? 17u : 15u) || n == 25u) && lane == 0) __builtin_amdgcn_raw_buffer_store_b64(flagv, rws, fo + (n == 25u ? 128 : 0), 0, 16);
        };
        {
            const unsigned voffG = q8 * (FF * 2) + (((p8 >> 1) ^ ((q8 >> 1) & 1)) << 5) + ((p8 & 1) << 4);
            NOUNROLL for (int n = 4; n < N1; n++) {
                const int t = n - 4, line = 7 * kq + 4 * (t >> 3) + q;
                const unsigned char* base = (const unsigned char*)(lw ? p.wu : p.wg) + (size_t)((t & 7) * 64) * (FF * 2) + line * 128;
                const unsigned dst = L_W + ((n + 2) & 3) * SLOT + lw * 8192;
                wait_free(n);
#pragma unroll
                for (int m = 0; m < 8; m++) dma16(base + m * 8 * (FF * 2), voffG ^ (unsigned)((m & 1) << 6), dst + m * 1024);
                waitv<8>();
                post(ffull, n);
            }
        }
        STAMP(3);
        if (lw == 0) {
            const unsigned voffC = q8 * 1024 + ((p8 ^ (q8 >> 1)) << 4);
            const unsigned voffD = q8 * 1024 + (((p8 >> 1) ^ (q8 >> 1) ^ (2 * (kq & 1))) << 5) + ((p8 & 1) << 4);
            NOUNROLL for (int v = 0; v < 11; v++) {
                const unsigned dst = L_W + ((v + 2) & 3) * SLOT;
                wait_free(N1 + v);
                if (v < 4) {
                    const unsigned char* base = (const unsigned char*)((v & 2) ? p.Cim : p.Cre) + (size_t)(128 * q) * 1024 + (2 * kq + (v & 1)) * 128;
#pragma unroll
                    for (int m = 0; m < 16; m++) dma16(base + ((m ^ (4 * kq)) * 8192), voffC ^ (unsigned)((m & 1) << 6), dst + m * 1024);
                } else {
                    const unsigned char* base = (const unsigned char*)p.wd + (size_t)(64 * (7 * kq + v - 4)) * 1024 + 256 * q;
#pragma unroll
                    for (int m = 0; m < 16; m++) dma16(base + (m & 7) * 8192 + (((m >> 3) ^ (kq >> 1)) * 128), voffD, dst + m * 1024);
                }
                waitv<16>();
                post(ffull, N1 + v);
            }
            waitv<0>(); post(ffull, N1 + 11);
        } else {
            NOUNROLL for (int v = 0; v < 11; v++) {
                if ((v & 7) == 0) {
                    STAMP(4 + (v >> 2));
                    const unsigned po = WS_FLAG + (unsigned)(i * 16 + 4 * kq + (lane & 3)) * 256u + v * 16;
                    NOUNROLL for (int spin = 0; spin < SPIN_MAX; spin++) {
                        fence_c();
                        const v2u f = __builtin_amdgcn_raw_buffer_load_b64(rws, po, 0, 16);
                        const unsigned okm = (unsigned)__builtin_amdgcn_ballot_w64(f.x == flagv.x && f.y == flagv.y) | (v ? 8u : 0u);
                        if (spin == 0) { waitv<0>(); post(ffull, N1 + v); }
                        if ((okm & 0xfu) == 0xfu) break;
                    }
                    fence_c();
                    STAMP(5 + (v >> 2));
                }
                if (v >= 4) wait_free(N1 + v);
                const int unit = v < 4 ? 28 + 8 * (v >> 1) + 2 * kq + (v & 1) : 7 * kq + v - 4;
                const unsigned char* base = p.ws + WS_A2 + (size_t)(i * 44 + unit) * 16384;
                const unsigned dst = L_A + (v & 3) * SLOT;
#pragma unroll
                for (int m = 0; m < 4; m++) dma16x4_sc1(base + m * 4096, lane * 16, dst + m * 4096);
                waitv<16>(); post(ffull, N1 + v);
            }
            waitv<0>(); post(ffull, N1 + 11);
        }
        STAMP(10);
        return;
    }
    if (w >= 6) {
        LANE_VARS;
        const unsigned tg = peek(L_TAG);
        const bool odd = lane & 1;
        const unsigned la = lane * 4;
        const v2u prm = *(LAS v2u*)(uintptr_t)(L_HIN + lane * 8);
        const float lr = bflo(prm.x), li = bfhi(prm.x);
        float dt = __expf(bflo(prm.y));
        dt = bflo(pk2(dt, dt));
        const float ex = __expf(dt * lr);
        const float fr = __builtin_amdgcn_fractf(dt * li * 0.15915494309189535f);
        const float ar = smul(ex, __builtin_amdgcn_cosf(fr)), ai = smul(ex, __builtin_amdgcn_sinf(fr));
        const cpx a1 = {ar, ai};
        const cpx a2 = cmul(a1, a1), a4 = cmul(a2, a2);
        if (lw == 0) { sts32f(L_CST + la, odd ? a1.y : a1.x); sts32f(L_CST + 256 + la, odd ? a4.y : a4.x); }
        post(F_COMM + lw * 4, 1);
        SB();
        cpx pw[8];
        pw[1] = cmul(a4, a4);
        const unsigned sg = odd ? 0u : 0x80000000u;
        auto sgn = [&](cpx v) { return cpx{v.x, __uint_as_float(__float_as_uint(v.y) ^ sg)}; };
        const cpx a8 = sgn(pw[1]);
        pw[2] = cmul(pw[1], pw[1]); pw[4] = cmul(pw[2], pw[2]);
        const cpx r64 = cmul(pw[4], pw[4]);
        const cpx a64 = sgn(r64);
        SB();
        STAMP(3);
        wait_ge(F_DONE + lw * 8 + (lane & 1) * 4, 4);
        STAMP(4);
        float P[8], h;
        {
            float e[8];
#pragma unroll
            for (int s = 0; s < 8; s++) e[s] = lds32f(L_SEG + (8 * lw + s) * 256 + la);
            h = e[0];
#pragma unroll
            for (int s = 1; s < 8; s++) { P[s] = h; h = lmad(a8, h, e[s]); }
        }
        float elo = 0;
        if (lw == 0) { sts32f(L_XCH + la, h); post(F_COMM, 2); }
        else {
            wait_ge(F_COMM, 2);
            elo = lds32f(L_XCH + la);
            __builtin_amdgcn_raw_buffer_store_b64(v2u{__float_as_uint(lmad(a64, elo, h)), tg}, rws, WS_GRAN + (j * 16 + i) * 512 + lane * 8, 0, 16);
        }
        SB();
        STAMP(5);
        const cpx r128 = cmul(r64, r64);
        const cpx a128 = sgn(r128), a256 = sgn(cmul(r128, r128));
        if (lw == 0) {
            const float nr = ar - 1.0f, den = 2.0f * __builtin_amdgcn_rcpf(sfma(lr, lr, smul(li, li)));
            cpx co = {smul(sfma(nr, lr, smul(ai, li)), den), smul(sfms(ai, lr, smul(nr, li)), den)};
#pragma unroll
            for (int t = 0; t < 5; t++) { sts32f(L_CST + (2 + t) * 256 + la, odd ? co.y : co.x); if (t < 4) co = cmul(co, a1); }
        }
        pw[3] = cmul(pw[1], pw[2]); pw[5] = cmul(pw[1], pw[4]); pw[6] = cmul(pw[2], pw[4]); pw[7] = cmul(pw[3], pw[4]);
#pragma unroll
        for (int s = 1; s < 8; s++) pw[s] = sgn(pw[s]);
        SB();
        STAMP(6);
        {
            const int top = i - 1 - lw;
            v2u gr[8];
            NOUNROLL for (int spin = 0; spin < SPIN_MAX; spin++) {
                fence_c();
#pragma unroll
                for (int n = 0; n < 8; n++) gr[n] = __builtin_amdgcn_raw_buffer_load_b64(rws, WS_GRAN + j * 8192 + lane * 8, ((top - 2 * n) & 15) * 512, 16);
                unsigned long long bad = 0;
#pragma unroll
                for (int n = 0; n < 8; n++) if (top - 2 * n >= 0) bad |= __builtin_amdgcn_ballot_w64(gr[n].y != tg);
                if (bad == 0) break;
            }
            h = 0;
#pragma unroll
            for (int n = 7; n >= 0; n--) if (top - 2 * n >= 0) h = lmad(a256, h, __uint_as_float(gr[n].x));
        }
        STAMP(7);
        sts32f(L_XCH + 256 + lw * 256 + la, h);
        post(F_COMM + lw * 4, 3);
        wait_ge(F_COMM + (lw ^ 1) * 4, 3);
        const float ho = lds32f(L_XCH + 256 + (lw ^ 1) * 256 + la);
        float C = lw ? lmad(a64, lmad(a128, h, ho), elo) : lmad(a128, ho, h);
        sts32f(L_HIN + (8 * lw) * 256 + la, C);
#pragma unroll
        for (int s = 1; s < 8; s++) sts32f(L_HIN + (8 * lw + s) * 256 + la, lmad(pw[s], C, P[s]));
        post(F_COMM + lw * 4, 4);
        STAMP(8);
        return;
    }
#define PD1 4
    unsigned it = 4;
    unsigned stall = 0; (void)stall;
    {
        constexpr int PD = PD1;
        LANE_VARS;
        const unsigned fdone = F_DONE + w * 4, ffl = F_FULL + (lane & 1) * 4;
        STAMP(3);
        {
            unsigned cb[2];
            cb[0] = L_W + (unsigned)c * 128u + (unsigned)((g ^ (c >> 1)) << 4); cb[1] = cb[0] ^ 64u;
            f32x4 acc[4][2] = {};
            v4u bf[PD];
            unsigned fl = 0; bool lack = false;
            sfor<-PD, 64>(LAMB(nn) {
                constexpr int n = decltype(nn)::value, m = n + PD;
                v4u cur;
                if constexpr (n >= 0) { cur = bf[n % PD]; acc[n & 3][0] = mfma(xa[n >> 2][0], cur, acc[n & 3][0]); SB(); }
                if constexpr (m > 0 && (m & 15) == 0) need_full(lack, ffl, (m >> 4) + 1);
                if constexpr (m < 64) bf[m % PD] = lds128(cb[(m >> 2) & 1] + wslot(m >> 4) + (((m >> 1) & 1) * 4 + ((m >> 3) & 1) * 2 + (m & 1)) * 2048);
                SB();
                if constexpr (n >= 0) { acc[n & 3][1] = mfma(xa[n >> 2][1], cur, acc[n & 3][1]); SB(); }
                if constexpr ((m & 15) == 10) fl = peek(ffl);
                if constexpr ((m & 15) == 14) lack = any_lt(fl, (m >> 4) + 2);
                if constexpr ((m & 15) == 15 && m < 63) post(fdone, (m >> 4) + 1);
                SB();
            });
            STAMP(4); VAL(24, stall); stall = 0;
            wait_ge(F_COMM, 1);
            const f32x4 ca = lds128f(L_CST + c * 16), c4 = lds128f(L_CST + 256 + c * 16);
            const f32x4 rs0 = lds128f(L_RSTD + (32 * w + 8 * g) * 4), rs1 = lds128f(L_RSTD + (32 * w + 8 * g + 4) * 4);
            f32x4 ev;
#pragma unroll
            for (int st = 0; st < 2; st++) {
                const f32x2 ar = dup(ca[2 * st]), ai = dup(ca[2 * st + 1]);
                const f32x4 r0 = acc[st][0] * rs0, r1 = acc[st][1] * rs1, i0 = acc[2 + st][0] * rs0, i1 = acc[2 + st][1] * rs1;
                f32x2 sr[4] = {lo2(r0), hi2(r0), lo2(r1), hi2(r1)}, si[4] = {lo2(i0), hi2(i0), lo2(i1), hi2(i1)};
#pragma unroll
                for (int t = 1; t < 4; t++) {
                    sr[t] = pfma(ar, sr[t - 1], pfma(-ai, si[t - 1], sr[t]));
                    si[t] = pfma(ar, si[t - 1], pfma(ai, sr[t - 1], si[t]));
                }
                ev[2 * st] = sfma(c4[2 * st], sr[3].x, sfma(-c4[2 * st + 1], si[3].x, sr[3].y));
                ev[2 * st + 1] = sfma(c4[2 * st], si[3].x, sfma(c4[2 * st + 1], sr[3].x, si[3].y));
                const unsigned pk = L_PARK + w * 8192 + st * 4096 + lane * 16;
                sts128f(pk, f32x4{sr[0].x, sr[0].y, sr[1].x, sr[1].y}); sts128f(pk + 1024, f32x4{sr[2].x, sr[2].y, sr[3].x, sr[3].y});
                sts128f(pk + 2048, f32x4{si[0].x, si[0].y, si[1].x, si[1].y}); sts128f(pk + 3072, f32x4{si[2].x, si[2].y, si[3].x, si[3].y});
            }
            sts128f(L_SEG + (w * 64 + lane) * 16, ev);
            post(fdone, 4);
        }
        STAMP(5);
        unsigned tb[4];
        {
            const int q4 = c >> 2, p4 = c & 3;
            const unsigned tb1 = L_W + (unsigned)(8 * g + q4) * 128u + (unsigned)((((q4 >> 1) & 1) | ((g & 1) << 1)) << 5) + p4 * 8;
#pragma unroll
            for (int t = 0; t < 4; t++) { tb[t] = tb1 ^ (unsigned)(t << 5); asm volatile("" : "+v"(tb[t])); }
        }
        const int npass = heavy ? 2 : 1;
        NOUNROLL for (int pass = 0; pass < npass; pass++) {
            f32x4 acc[4][2][2] = {};
            v4u bf[PD];
            unsigned fl = 0; bool lack = false;
            sfor<-PD, 128>(LAMB(nn) {
                constexpr int n = decltype(nn)::value, m = n + PD;
                constexpr unsigned off = wslot(m >> 4) + (m & 1) * 8192 + ((m >> 3) & 1) * 4096;
                v4u cur; v2u lo, hi;
                if constexpr (n >= 0) { cur = bf[n % PD]; acc[(n >> 1) & 3][n & 1][0] = mfma(cur, xa[n >> 3][0], acc[(n >> 1) & 3][n & 1][0]); SB(); }
                if constexpr (m > 0 && (m & 15) == 0 && m < 128) need_full(lack, ffl, it + 1);
                if constexpr (m < 128) lo = ldstr(tb[(m >> 1) & 3] + off);
                if constexpr ((m & 15) == 10 && m < 128) fl = peek(ffl);
                if constexpr ((m & 15) == 14) it++;
                if constexpr ((m & 15) == 15 && m < 128) lack = any_lt(fl, it + 1);
                if constexpr (m == 33) waitv<0>();
                if constexpr (m > 16 && m < 128 && (m & 15) == 1) post(fdone, it);
                if constexpr (m == 129) post(fdone, heavy ? it : it - 1);
                SB();
                if constexpr (n >= 0) { acc[(n >> 1) & 3][n & 1][1] = mfma(cur, xa[n >> 3][1], acc[(n >> 1) & 3][n & 1][1]); SB(); }
                if constexpr (m < 128) { hi = ldstr(tb[(m >> 1) & 3] + off + 512); bf[m % PD] = v4u{lo.x, lo.y, hi.x, hi.y}; }
                SB();
            });
            STAMP(6 + 2 * pass); VAL(25 + pass, stall); stall = 0;
            LANE_VARS;
            const int line = 7 * kq + 4 * pass + q;
            const unsigned ao = WS_A2 + (unsigned)i * (44u * 16384u) + w * 4096 + lane * 16;
#pragma unroll
            for (int bb = 0; bb < 2; bb++) {
                const float r1 = lds32f(L_RSTD + (32 * w + 8 * (c >> 2) + 4 * bb + (c & 3)) * 4);
                const f32x2 r2 = dup(r1 * r1);
                const f32x4 r4 = {r2.x, r2.y, r2.x, r2.y};
#pragma unroll
                for (int uu = 0; uu < 2; uu++)
                    __builtin_amdgcn_raw_buffer_store_b128(pk8(acc[2 * uu][0][bb] * acc[2 * uu][1][bb] * r4, acc[2 * uu + 1][0][bb] * acc[2 * uu + 1][1][bb] * r4), rws, ao + line * 16384 + (2 * uu + bb) * 1024, 0, 16);
            }
            SB();
            if (pass == 0) {
                wait_ge(F_COMM + (lane & 1) * 4, 4);
                const f32x4 hin = lds128f(L_HIN + (w * 64 + lane) * 16), c4 = lds128f(L_CST + 256 + c * 16), co = lds128f(L_CST + 512 + c * 16);
                f32x4 cw[4];
#pragma unroll
                for (int t = 0; t < 4; t++) cw[t] = lds128f(L_CST + (3 + t) * 256 + c * 16);
                v4u fr[2], fi[2];
#pragma unroll
                for (int st = 0; st < 2; st++) {
                    const unsigned pk = L_PARK + w * 8192 + st * 4096 + lane * 16;
                    const f32x4 r0 = lds128f(pk), r1 = lds128f(pk + 1024), i0 = lds128f(pk + 2048), i1 = lds128f(pk + 3072);
                    const f32x2 sr[4] = {lo2(r0), hi2(r0), lo2(r1), hi2(r1)}, si[4] = {lo2(i0), hi2(i0), lo2(i1), hi2(i1)};
                    const float hx = hin[2 * st], hy = hin[2 * st + 1];
                    f32x2 hr = {hx, sfma(c4[2 * st], hx, sfma(-c4[2 * st + 1], hy, sr[3].x))};
                    f32x2 hi = {hy, sfma(c4[2 * st], hy, sfma(c4[2 * st + 1], hx, si[3].x))};
                    asm("" : "+v"(hr), "+v"(hi));
                    const f32x2 cr = dup(co[2 * st]), ci = dup(co[2 * st + 1]);
#pragma unroll
                    for (int t = 0; t < 4; t++) {
                        const f32x2 wr = dup(cw[t][2 * st]), wi = dup(cw[t][2 * st + 1]);
                        fr[st][t] = pk2(pfma(wr, hr, pfma(-wi, hi, pfma(cr, sr[t], -ci * si[t]))));
                        fi[st][t] = pk2(pfma(-wr, hi, pfma(-wi, hr, pfma(-cr, si[t], -ci * sr[t]))));
                    }
                }
                const unsigned so = (28 + (j >> 1)) * 16384 + (j & 1) * 2048;
#pragma unroll
                for (int bb = 0; bb < 2; bb++) {
                    const f32x4 z = {0, 0, 0, 0};
                    const v4u sel = make_sel(lane, bb);
                    const f32x4 r0 = mfma(fr[0], sel, z), r1 = mfma(fr[1], sel, z), i0 = mfma(fi[0], sel, z), i1 = mfma(fi[1], sel, z);
                    __builtin_amdgcn_raw_buffer_store_b128(pk8(r0, r1), rws, ao + so + bb * 1024, 0, 16);
                    __builtin_amdgcn_raw_buffer_store_b128(pk8(i0, i1), rws, ao + so + 8 * 16384 + bb * 1024, 0, 16);
                }
                if (!heavy) { waitv<0>(); post(fdone, it); }
                STAMP(7);
            }
            need_full(lack, ffl, it + 1);
        }
    }
    LANE_VARS;
    STAMP(9); VAL(27, stall); stall = 0;
    constexpr int PD = 4;
    f32x4 acc[8][2] = {};
    v2u xv[4], dv;
    {
        const unsigned fdone = F_DONE + w * 4, ffl = F_FULL + (lane & 1) * 4;
        const unsigned ab = L_A + w * 4096 + lane * 16;
        v4u af[4], bf[PD];
#pragma unroll
        for (int f = 0; f < 4; f++) af[f] = lds128(ab + f * 1024);
        auto stage = [&](auto isG, int v, unsigned (&base)[4], unsigned base0) __attribute__((always_inline)) {
            constexpr bool G = decltype(isG)::value;
            v4u a2n, a3n;
            unsigned fl = 0, an = 0; bool lack = false;
            const unsigned nsl = (unsigned)((v + 3) & 3) * SLOT;
            sfor<0, 16>(LAMB(nn) {
                constexpr int n = decltype(nn)::value, m = (n + PD) & 15;
                const v4u cur = bf[n % PD];
                const v4u x0 = af[(n >> 3) * 2], x1 = af[(n >> 3) * 2 + 1];
                v2u lo, hi;
                acc[n & 7][0] = mfma(cur, x0, acc[n & 7][0]);
                SB();
                if constexpr (n == 12) need_full(lack, ffl, it + 1);
                if constexpr (G) lo = ldstr(base[m & 3] + ((m >> 2) & 1) * 8192 + (m >> 3) * 4096);
                else bf[n % PD] = lds128(base[m >> 3] + (m & 7) * 2048);
                if constexpr (n == 6) fl = peek(ffl);
                if constexpr (n == 3) an = ab + ((v + 1) & 3) * SLOT;
                if constexpr (n == 11) { it++; lack = any_lt(fl, v == 10 ? 0 : it + 1); }
                if constexpr (n == 12) af[0] = lds128(an);
                if constexpr (n == 13) af[1] = lds128(an + 1024);
                if constexpr (n == 14) a2n = lds128(an + 2048);
                if constexpr (n == 15) a3n = lds128(an + 3072);
                SB();
                acc[n & 7][1] = mfma(cur, x1, acc[n & 7][1]);
                SB();
                if constexpr (G) { hi = ldstr(base[m & 3] + ((m >> 2) & 1) * 8192 + (m >> 3) * 4096 + 2048); bf[n % PD] = v4u{lo.x, lo.y, hi.x, hi.y}; }
                if constexpr (G && n >= 8 && n < 12) base[n & 3] = (base0 ^ (unsigned)((n & 3) << 5)) + nsl;
                if constexpr (!G && n == 4) base[0] = base0 + nsl;
                if constexpr (!G && n == 11) base[1] = (base0 ^ 64u) + nsl;
                if constexpr (!G && n == 12) { if (v == 1) waitv<0>(); }
                if constexpr (n == 12) post(fdone, it);
                SB();
            });
            af[2] = a2n; af[3] = a3n;
        };
        unsigned base[4];
        const unsigned cb0 = L_W + (unsigned)c * 128u + (unsigned)((g ^ (c >> 1)) << 4);
        base[0] = cb0 + wslot(0); base[1] = (cb0 ^ 64u) + wslot(0); base[2] = base[3] = 0;
#pragma unroll
        for (int m = 0; m < PD; m++) bf[m] = lds128(base[0] + m * 2048);
        NOUNROLL for (int v = 0; v < 4; v++) {
            stage(IC<0>{}, v, base, cb0);
        }
        STAMP(10); VAL(28, stall); stall = 0;
        {
            const int d = 128 * q + 32 * kq + 4 * p8;
#pragma unroll
            for (int e = 0; e < 4; e++) xv[e] = *(const GAS v2u*)(p.x + (size_t)(128 * i + 32 * w + 8 * e + q8) * DM + d);
            dv = *(const GAS v2u*)(p.Dsk + d);
        }
        const int q4 = c >> 2, p4 = c & 3;
        const unsigned tb2 = L_W + (unsigned)(4 * g + q4) * 128u + (unsigned)((2 * (g & 1) + (q4 >> 1)) << 5) + p4 * 8;
#pragma unroll
        for (int m = 0; m < PD; m++) { base[m] = (tb2 ^ (unsigned)(m << 5)) + wslot(0); const v2u lo = ldstr(base[m]), hi = ldstr(base[m] + 2048); bf[m] = v4u{lo.x, lo.y, hi.x, hi.y}; }
        NOUNROLL for (int v = 4; v < 11; v++) stage(IC<1>{}, v, base, tb2);
    }
    STAMP(11); VAL(29, stall);
    {
        const unsigned tg = peek(L_TAG);
        const unsigned xo = WS_XB + (unsigned)((i * 4 + q) * 4) * 49152u + w * 4096 + lane * 16;
#pragma unroll
        for (int s = 1; s < 4; s++)
#pragma unroll
            for (int e = 0; e < 4; e++) {
                const f32x4 v = acc[2 * s + (e >> 1)][e & 1];
                __builtin_amdgcn_raw_buffer_store_b128(v4u{pk2(v.x, v.y), tg, pk2(v.z, v.w), tg}, rws, xo + (kq ^ s) * 49152u + (s - 1) * 16384 + e * 1024, 0, 16);
            }
        SB();
        f32x4 base[4];
        const f32x4 df = {bflo(dv.x), bfhi(dv.x), bflo(dv.y), bfhi(dv.y)};
#pragma unroll
        for (int e = 0; e < 4; e++) {
            const int tl = tok_pos(32 * w + 8 * e + q8);
            const f32x2 rs = dup(lds32f(L_RSTD + tl * 4)), mn = dup(lds32f(L_MEAN + tl * 4));
            const f32x4 xf = {bflo(xv[e].x), bfhi(xv[e].x), bflo(xv[e].y), bfhi(xv[e].y)};
            base[e] = xf + (xf - f32x4{mn.x, mn.y, mn.x, mn.y}) * f32x4{rs.x, rs.y, rs.x, rs.y} * df;
        }
        STAMP(12);
        v4u in[12];
        NOUNROLL for (int spin = 0; spin < SPIN_MAX; spin++) {
            fence_c();
#pragma unroll
            for (int n = 0; n < 12; n++) in[n] = __builtin_amdgcn_raw_buffer_load_b128(rws, xo + (n & 3) * 1024, kq * 49152u + (n >> 2) * 16384, 16);
            unsigned bad = 0;
#pragma unroll
            for (int n = 0; n < 12; n++) bad |= (in[n].y ^ tg) | (in[n].w ^ tg);
            if (__builtin_amdgcn_ballot_w64(bad != 0) == 0) break;
        }
        STAMP(13);
        const unsigned tb = L_A + w * 4096;
#pragma unroll
        for (int e = 0; e < 4; e++) {
            f32x4 o = acc[e >> 1][e & 1];
#pragma unroll
            for (int s = 0; s < 3; s++) { const v4u t = in[4 * s + e]; o += f32x4{bflo(t.x), bfhi(t.x), bflo(t.z), bfhi(t.z)}; }
            const int tl = 8 * (c >> 2) + 4 * (c & 1) + 2 * (e & 1) + ((c >> 1) & 1);
            sts128f(tb + tl * 128 + (((4 * (e >> 1) + g) ^ (c & 7)) << 4), o);
        }
        fence_c();
#pragma unroll
        for (int e = 0; e < 4; e++) {
            const int key = 4 * (e & 1) + 2 * (q8 & 1) + (q8 >> 2);
            const f32x4 o = lds128f(tb + (8 * e + q8) * 128 + ((p8 ^ key) << 4)) + base[e];
            *(GAS f32x4*)(p.out + (size_t)(128 * i + 32 * w + 8 * e + q8) * DM + 128 * q + 32 * kq + 4 * p8) = o;
        }
        if (i == 15 && j == 0 && w == 0 && lane == 0) __builtin_amdgcn_raw_buffer_store_b32(tg, rws, WS_EPOCH, 0, 16);
    }
    STAMP(14);
}

extern "C" void kernel_launch(void* const* d_in, const int* in_sizes, int n_in, void* d_out, int out_size, void* d_ws, size_t ws_size,
                              hipStream_t stream) {
    Params p;
    p.x = (const u16*)d_in[0]; p.lre = (const u16*)d_in[1]; p.lim = (const u16*)d_in[2];
    p.Bre = (const u16*)d_in[3]; p.Bim = (const u16*)d_in[4]; p.Cre = (const u16*)d_in[5]; p.Cim = (const u16*)d_in[6];
    p.ldt = (const u16*)d_in[7]; p.Dsk = (const u16*)d_in[8];
    p.wg = (const u16*)d_in[10]; p.wu = (const u16*)d_in[11]; p.wd = (const u16*)d_in[12];
    p.out = (float*)d_out; p.ws = (unsigned char*)d_ws;
    (void)hipFuncSetAttribute((const void*)fused, hipFuncAttributeMaxDynamicSharedMemorySize, L_TOTAL);
    fused<<<256, 512, L_TOTAL, stream>>>(p);
}
```

```cpp
#define WARM
#include <hip/hip_runtime.h>
#include <stdint.h>

typedef unsigned short u16;
typedef unsigned v4u __attribute__((ext_vector_type(4)));
typedef unsigned v2u __attribute__((ext_vector_type(2)));
typedef short s16x4 __attribute__((ext_vector_type(4)));
typedef __bf16 bf16x2 __attribute__((ext_vector_type(2)));
typedef __bf16 bf16x8 __attribute__((ext_vector_type(8)));
typedef float f32x2 __attribute__((ext_vector_type(2)));
typedef float f32x4 __attribute__((ext_vector_type(4)));
#define GAS __attribute__((address_space(1)))
#define LAS __attribute__((address_space(3)))
#define DEVI __device__ __forceinline__
#define NOUNROLL _Pragma("nounroll")

#define WARMN 1
constexpr int DM = 512, FF = 1792;
constexpr unsigned WS_A2 = 0;
constexpr unsigned WS_XB = 16u * 44u * 16384u;
constexpr unsigned WS_GRAN = WS_XB + 256u * 49152u;
constexpr unsigned WS_FLAG = WS_GRAN + 16u * 16u * 512u;
constexpr unsigned WS_EPOCH = WS_FLAG + 512u * 128u;
constexpr unsigned WS_END = WS_EPOCH + 256u;
constexpr unsigned SLOT = 16384;
constexpr unsigned L_RSTD = 0, L_MEAN = 512;
constexpr unsigned F_DONE = 1024, F_DRAIN = F_DONE + 16, F_FULL = F_DONE + 32, F_COMM = F_DONE + 40, L_TAG = F_DONE + 48;
constexpr unsigned L_CST = 1536;
constexpr unsigned L_XCH = L_CST + 7 * 256;
constexpr unsigned L_SEG = 4096, L_HIN = 8192;
constexpr unsigned L_A = SLOT, L_PARK = 4 * SLOT, L_W = 6 * SLOT, L_TOTAL = 10 * SLOT;
constexpr int SPIN_MAX = 1 << 18;
constexpr int LSPIN_MAX = 1 << 22;

struct Params {
    const u16 *x, *lre, *lim, *Bre, *Bim, *Cre, *Cim, *ldt, *Dsk, *wg, *wu, *wd;
    float* out;
    unsigned char* ws;
};

DEVI float bf2f(u16 v) { return __uint_as_float(((unsigned)v) << 16); }
DEVI float bflo(unsigned v) { return __uint_as_float(v << 16); }
DEVI float bfhi(unsigned v) { return __uint_as_float(v & 0xffff0000u); }
DEVI unsigned pk2(float a, float b) { f32x2 p = {a, b}; return __builtin_bit_cast(unsigned, __builtin_convertvector(p, bf16x2)); }
DEVI unsigned pk2(f32x2 p) { return __builtin_bit_cast(unsigned, __builtin_convertvector(p, bf16x2)); }
DEVI v4u pk8(f32x4 a, f32x4 b) { return v4u{pk2(a.x, a.y), pk2(a.z, a.w), pk2(b.x, b.y), pk2(b.z, b.w)}; }
DEVI float dot2(unsigned a, unsigned b, float c) { return __builtin_amdgcn_fdot2_f32_bf16(__builtin_bit_cast(bf16x2, a), __builtin_bit_cast(bf16x2, b), c, false); }
template <int CTRL> DEVI float dppf(float v) { return __builtin_bit_cast(float, __builtin_amdgcn_update_dpp(0, __builtin_bit_cast(int, v), CTRL, 0xf, 0xf, true)); }
DEVI f32x4 mfma(v4u a, v4u b, f32x4 c) { return __builtin_amdgcn_mfma_f32_16x16x32_bf16(__builtin_bit_cast(bf16x8, a), __builtin_bit_cast(bf16x8, b), c, 0, 0, 0); }
DEVI v4u lds128(unsigned a) { return *(LAS v4u*)(uintptr_t)a; }
DEVI f32x4 lds128f(unsigned a) { return *(LAS f32x4*)(uintptr_t)a; }
DEVI float lds32f(unsigned a) { return *(LAS float*)(uintptr_t)a; }
DEVI void sts32f(unsigned a, float v) { *(LAS float*)(uintptr_t)a = v; }
DEVI void sts128(unsigned a, v4u v) { *(LAS v4u*)(uintptr_t)a = v; }
DEVI void sts128f(unsigned a, f32x4 v) { *(LAS f32x4*)(uintptr_t)a = v; }
DEVI v2u ldstr(unsigned a) { return __builtin_bit_cast(v2u, __builtin_amdgcn_ds_read_tr16_b64_v4i16((LAS s16x4*)(uintptr_t)a)); }
DEVI void fence_c() { asm volatile("" ::: "memory"); }
DEVI void bar_l() { asm volatile("s_waitcnt lgkmcnt(0)\n\ts_barrier" ::: "memory"); }
template <int N> DEVI void waitv() { asm volatile("s_waitcnt vmcnt(%0)" ::"n"(N) : "memory"); }
DEVI void dma16(const void* sbase, unsigned voff, unsigned ldst) {
    asm volatile("s_mov_b32 m0, %2\n\ts_nop 0\n\tglobal_load_lds_dwordx4 %0, %1" ::"v"(voff), "s"(sbase), "s"(ldst) : "memory");
}
DEVI void dma16x4_sc1(const void* sbase, unsigned voff, unsigned ldst) {
    asm volatile("s_mov_b32 m0, %2\n\ts_nop 0\n\tglobal_load_lds_dwordx4 %0, %1 sc1\n\tglobal_load_lds_dwordx4 %0, %1 offset:1024 sc1\n\t"
                 "global_load_lds_dwordx4 %0, %1 offset:2048 sc1\n\tglobal_load_lds_dwordx4 %0, %1 offset:3072 sc1" ::"v"(voff), "s"(sbase), "s"(ldst) : "memory");
}
DEVI void post(unsigned a, unsigned v) { fence_c(); *(LAS unsigned*)(uintptr_t)a = v; fence_c(); }
DEVI unsigned peek(unsigned a) { fence_c(); return *(LAS unsigned*)(uintptr_t)a; }
DEVI bool any_lt(unsigned v, unsigned need) { return __builtin_amdgcn_ballot_w64(v < need) != 0; }
DEVI void wait_ge(unsigned a, unsigned need) { NOUNROLL for (int s = 0; s < LSPIN_MAX && any_lt(peek(a), need); s++) {} fence_c(); }
DEVI void need_full(bool lack, unsigned a, unsigned need) { if (__builtin_expect(lack, 0)) wait_ge(a, need); fence_c(); }
#define VAL(id, v)
DEVI float sfma(float a, float b, float c) { float d; asm("v_fma_f32 %0, %1, %2, %3" : "=v"(d) : "v"(a), "v"(b), "v"(c)); return d; }
DEVI float sfms(float a, float b, float c) { float d; asm("v_fma_f32 %0, %1, %2, -%3" : "=v"(d) : "v"(a), "v"(b), "v"(c)); return d; }
DEVI float smul(float a, float b) { float d; asm("v_mul_f32 %0, %1, %2" : "=v"(d) : "v"(a), "v"(b)); return d; }
DEVI float smul_t(float a, float b) { float d; asm("s_nop 0\n\tv_mul_f32 %0, %1, %2" : "=v"(d) : "v"(a), "v"(b)); return d; }
struct cpx { float x, y; };
DEVI cpx cmul(cpx a, cpx b) { return cpx{sfms(a.x, b.x, smul(a.y, b.y)), sfma(a.x, b.y, smul(a.y, b.x))}; }
DEVI float lmad(cpx a, float h, float u) {
    float d;
    asm("v_fma_f32 %0, %1, %3, %4\n\ts_nop 0\n\tv_fmac_f32_dpp %0, %3, %2 quad_perm:[1,0,3,2] row_mask:0xf bank_mask:0xf" : "=&v"(d) : "v"(a.x), "v"(a.y), "v"(h), "v"(u));
    return d;
}
DEVI f32x2 dup(float v) { f32x2 d = {v, v}; asm("" : "+v"(d)); return d; }
DEVI f32x2 pfma(f32x2 a, f32x2 b, f32x2 c) { return __builtin_elementwise_fma(a, b, c); }
DEVI f32x2 lo2(f32x4 v) { return f32x2{v.x, v.y}; }
DEVI f32x2 hi2(f32x4 v) { return f32x2{v.z, v.w}; }
#define SB() __builtin_amdgcn_sched_barrier(0)
template <int V> struct IC { static constexpr int value = V; };
template <int B, int E, class F> DEVI void sfor(F&& f) { if constexpr (B < E) { f(IC<B>{}); sfor<B + 1, E>(f); } }
#define LAMB(nn) [&](auto nn) __attribute__((always_inline))
#define STAMP(id)
#define STAMP_DUMP()
DEVI int fresh_lane() { int l; asm volatile("v_mbcnt_lo_u32_b32 %0, -1, 0\n\tv_mbcnt_hi_u32_b32 %0, -1, %0" : "=v"(l)); return l; }
DEVI v4u make_sel(int lane, int bb) {
    const int c = lane & 15, tk = 8 * (c >> 2) + 4 * bb + (c & 3) - 8 * (lane >> 4);
    v4u s;
#pragma unroll
    for (int e = 0; e < 4; e++) s[e] = (tk == 2 * e ? 0x3f80u : 0u) | (tk == 2 * e + 1 ? 0x3f800000u : 0u);
    return s;
}
#define LANE_VARS const int lane = fresh_lane(), c = lane & 15, g = lane >> 4, q8 = lane >> 3, p8 = lane & 7; (void)c; (void)g; (void)q8; (void)p8
constexpr unsigned wslot(int n) { return (unsigned)((n + 2) & 3) * SLOT; }
DEVI int tok_pos(int t) { return (t & ~7) + 4 * ((t >> 1) & 1) + 2 * (t & 1) + ((t >> 2) & 1); }

__global__ __launch_bounds__(512, 2) void fused(Params p) {
    const int w = __builtin_amdgcn_readfirstlane(threadIdx.x >> 6);
    const int b = blockIdx.x, xcd = b & 7, idx = b >> 3;
    const int i = 8 * (xcd & 1) + (idx & 7), kq = xcd >> 1, q = idx >> 3, j = 4 * kq + q;
    const bool heavy = q != 3;
    const int N1 = heavy ? 20 : 12;
    const int lw = w & 1;
    const __amdgpu_buffer_rsrc_t rws = __builtin_amdgcn_make_buffer_rsrc(p.ws, 0, WS_END, 0x00020000);
    auto issue_s = [&](int lane, int n) {
        const int q8 = lane >> 3, p8 = lane & 7;
        const unsigned voffS = (8 * (q8 >> 2) + (q8 & 3)) * 1024 + ((p8 ^ (q8 >> 1)) << 4);
        const unsigned char* base = (const unsigned char*)(lw ? p.Bim : p.Bre) + (size_t)(32 * j) * 1024 + n * 256;
        const unsigned dst = L_W + ((n + 2) & 3) * SLOT + lw * 8192;
#pragma unroll
        for (int m = 0; m < 8; m++) dma16(base + (16 * (m & 1) + 4 * ((m >> 1) & 1)) * 1024 + 128 * (m >> 2), voffS ^ (unsigned)((m & 1) << 6), dst + m * 1024);
    };
    v4u xa[16][2];
    unsigned stT = 0, stC = 0; (void)stT; (void)stC;
    {
        LANE_VARS;
        STAMP(0);
        v4u xr[2][8];
        float st_mean[2], st_rstd[2];
        unsigned tag0 = 0, prm0 = 0, prm1 = 0, prm2 = 0;
        const GAS unsigned char* xb = (const GAS unsigned char*)p.x + (size_t)(128 * i + 16 * w + q8) * 1024 + p8 * 16;
#pragma unroll
        for (int gq = 0; gq < 2; gq++)
#pragma unroll
            for (int n = 0; n < 8; n++) asm volatile("global_load_dwordx4 %0, %1, off offset:%2" : "=v"(xr[gq][n]) : "v"(xb + gq * 8192), "n"(n * 128) : "memory");
        {
            const unsigned voffS = (8 * (q8 >> 2) + (q8 & 3)) * 1024 + ((p8 ^ (q8 >> 1)) << 4);
            const unsigned char* base = (const unsigned char*)(lw ? p.Bim : p.Bre) + (size_t)(32 * j) * 1024 + (w >> 1) * 128;
            const unsigned dst = L_W + (2 + (w >> 2)) * SLOT + lw * 8192 + ((w >> 1) & 1) * 4096;
#pragma unroll
            for (int m = 0; m < 4; m++) dma16(base + (16 * (m & 1) + 4 * (m >> 1)) * 1024, voffS ^ (unsigned)((m & 1) << 6), dst + m * 1024);
        }
        unsigned tch = 0;
        {
            const unsigned char* tp; unsigned to;
            if (w < 3) { tp = (const unsigned char*)(w == 0 ? p.wg : w == 1 ? p.wu : p.wd); to = (b * 56 + min(lane, 55)) * 128; }
            else if (w < 5) { tp = (const unsigned char*)(w == 3 ? p.Cre : p.Cim); to = (b * 16 + (lane & 15)) * 128; }
            else if (w < 7) { tp = (const unsigned char*)(w == 5 ? p.Bre : p.Bim); to = (b * 16 + (lane & 15)) * 128; }
            else { tp = p.ws; to = min((unsigned)lane * 0x80000u + (b & 31) * 0x4000u, WS_END - 4); }
            asm volatile("global_load_dword %0, %1, %2" : "+v"(tch) : "v"(to), "s"(tp) : "memory");
        }
        if (w == 7) {
            const unsigned po = 2 * (32 * j + 8 * (lane >> 4) + 4 * ((lane >> 1) & 1) + ((lane >> 2) & 3));
            asm volatile("global_load_ushort %0, %4, %5\n\tglobal_load_ushort %1, %4, %6\n\tglobal_load_ushort %2, %4, %7\n\tglobal_load_dword %3, %8, %9 sc1"
                         : "+v"(prm0), "+v"(prm1), "+v"(prm2), "+v"(tag0) : "v"(po), "s"(p.lre), "s"(p.lim), "s"(p.ldt), "v"(WS_EPOCH), "s"(p.ws) : "memory");
        }
        SB();
        const v4u sel0 = make_sel(lane, 0), sel1 = make_sel(lane, 1);
        const unsigned o1 = (g == (c >> 2)) ? 0x3f803f80u : 0u;
        const v4u own = {o1, o1, o1, o1};
#pragma unroll
        for (int gq = 0; gq < 2; gq++) {
            asm volatile("s_waitcnt vmcnt(%8)" : "+v"(xr[gq][0]), "+v"(xr[gq][1]), "+v"(xr[gq][2]), "+v"(xr[gq][3]), "+v"(xr[gq][4]), "+v"(xr[gq][5]), "+v"(xr[gq][6]), "+v"(xr[gq][7]) : "n"(12 - 8 * gq + WARMN) : "memory");
            f32x4 sa = {0, 0, 0, 0}, sb = {0, 0, 0, 0};
#pragma unroll
            for (int n = 0; n < 4; n++) { sa = mfma(own, xr[gq][n], sa); sb = mfma(own, xr[gq][n + 4], sb); }
            float s = sa.x + sb.x;
            s += dppf<0xB1>(s); s += dppf<0x4E>(s); s += dppf<0x141>(s);
            const float nm = s * (-1.0f / DM);
            st_mean[gq] = -nm;
            const f32x4 nm4 = {nm, nm, nm, nm};
            const int t = 16 * w + 8 * gq + q8;
            const int key = 4 * ((t >> 3) & 3) + 2 * (t & 1) + ((t >> 2) & 1);
            const unsigned rowa = (unsigned)t * 1024u + (unsigned)((p8 ^ key) << 4);
#pragma unroll
            for (int n = 0; n < 8; n++) sts128((rowa ^ (unsigned)((n & 1) << 7)) + (n >> 1) * 256, pk8(mfma(sel0, xr[gq][n], nm4), mfma(sel1, xr[gq][n], nm4)));
            SB();
        }
        STAMP(1);
        bar_l();
        STAMP(2);
        if (w < 4) {
            const unsigned base = (unsigned)(32 * w + 8 * (c >> 2) + 4 * (c & 1) + ((c >> 1) & 1)) * 1024u + (unsigned)((g ^ c) << 4);
#pragma unroll
            for (int ks = 0; ks < 16; ks++)
#pragma unroll
                for (int bb = 0; bb < 2; bb++) xa[ks][bb] = lds128((base ^ (unsigned)((ks & 3) << 6)) + (ks >> 2) * 256 + bb * 2048);
        }
        SB();
#pragma unroll
        for (int gq = 0; gq < 2; gq++) {
            float q0 = 0, q1 = 0, q2 = 0, q3 = 0;
#pragma unroll
            for (int n = 0; n < 8; n++) {
                q0 = dot2(xr[gq][n].x, xr[gq][n].x, q0); q1 = dot2(xr[gq][n].y, xr[gq][n].y, q1);
                q2 = dot2(xr[gq][n].z, xr[gq][n].z, q2); q3 = dot2(xr[gq][n].w, xr[gq][n].w, q3);
            }
            float qq = (q0 + q1) + (q2 + q3);
            qq += dppf<0xB1>(qq); qq += dppf<0x4E>(qq); qq += dppf<0x141>(qq);
            st_rstd[gq] = __builtin_amdgcn_rsqf(fmaxf(qq * (1.0f / DM) - st_mean[gq] * st_mean[gq], 0.0f) + 1e-5f);
        }
        asm volatile("s_waitcnt vmcnt(0)" : "+v"(tag0), "+v"(prm0), "+v"(prm1), "+v"(prm2)::"memory");
        asm volatile("" :: "v"(tch));
        bar_l();
        if (p8 == 0) {
#pragma unroll
            for (int gq = 0; gq < 2; gq++) {
                const int t = tok_pos(16 * w + 8 * gq + q8);
                sts32f(L_RSTD + t * 4, st_rstd[gq]);
                sts32f(L_MEAN + t * 4, st_mean[gq]);
            }
        }
        if (w == 7) {
            if (lane < 12) *(LAS unsigned*)(uintptr_t)(F_DONE + lane * 4) = (lane == 8 || lane == 9) ? 2u : 0u;
            *(LAS unsigned*)(uintptr_t)L_TAG = tag0 + 1u;
            *(LAS v2u*)(uintptr_t)(L_HIN + lane * 8) = v2u{prm0 | (prm1 << 16), prm2};
        }
        bar_l();
    }
    const unsigned fo = WS_FLAG + (unsigned)(i * 16 + j) * 256u;

    if (w == 4 || w == 5) {
        LANE_VARS;
        NOUNROLL for (int n = 2; n < 4; n++) issue_s(lane, n);
        const unsigned tg = peek(L_TAG);
        const v2u flagv = {tg, ~tg};
        const unsigned fa = F_DONE + (lane & 3) * 4, ffull = F_FULL + lw * 4;
        waitv<8>(); post(ffull, 3);
        auto wait_free = [&](unsigned n) {
            if (any_lt(peek(fa), n - 3)) { waitv<0>(); post(ffull, n); wait_ge(fa, n - 3); }
            fence_c();
            if (lw == 0 && (n == (heavy ? 17u : 15u) || n == 25u) && lane == 0) __builtin_amdgcn_raw_buffer_store_b64(flagv, rws, fo + (n == 25u ? 128 : 0), 0, 16);
        };
        {
            const unsigned voffG = q8 * (FF * 2) + (((p8 >> 1) ^ ((q8 >> 1) & 1)) << 5) + ((p8 & 1) << 4);
            NOUNROLL for (int n = 4; n < N1; n++) {
                const int t = n - 4, line = 7 * kq + 4 * (t >> 3) + q;
                const unsigned char* base = (const unsigned char*)(lw ? p.wu : p.wg) + (size_t)((t & 7) * 64) * (FF * 2) + line * 128;
                const unsigned dst = L_W + ((n + 2) & 3) * SLOT + lw * 8192;
                wait_free(n);
#pragma unroll
                for (int m = 0; m < 8; m++) dma16(base + m * 8 * (FF * 2), voffG ^ (unsigned)((m & 1) << 6), dst + m * 1024);
                waitv<8>();
                post(ffull, n);
            }
        }
        STAMP(3);
        if (lw == 0) {
            const unsigned voffC = q8 * 1024 + ((p8 ^ (q8 >> 1)) << 4);
            const unsigned voffD = q8 * 1024 + (((p8 >> 1) ^ (q8 >> 1) ^ (2 * (kq & 1))) << 5) + ((p8 & 1) << 4);
            NOUNROLL for (int v = 0; v < 11; v++) {
                const unsigned dst = L_W + ((v + 2) & 3) * SLOT;
                wait_free(N1 + v);
                if (v < 4) {
                    const unsigned char* base = (const unsigned char*)((v & 2) ? p.Cim : p.Cre) + (size_t)(128 * q) * 1024 + (2 * kq + (v & 1)) * 128;
#pragma unroll
                    for (int m = 0; m < 16; m++) dma16(base + ((m ^ (4 * kq)) * 8192), voffC ^ (unsigned)((m & 1) << 6), dst + m * 1024);
                } else {
                    const unsigned char* base = (const unsigned char*)p.wd + (size_t)(64 * (7 * kq + v - 4)) * 1024 + 256 * q;
#pragma unroll
                    for (int m = 0; m < 16; m++) dma16(base + (m & 7) * 8192 + (((m >> 3) ^ (kq >> 1)) * 128), voffD, dst + m * 1024);
                }
                waitv<16>();
                post(ffull, N1 + v);
            }
            waitv<0>(); post(ffull, N1 + 11);
        } else {
            NOUNROLL for (int v = 0; v < 11; v++) {
                if ((v & 7) == 0) {
                    if (v) STAMP(6); else STAMP(4);
                    const unsigned po = WS_FLAG + (unsigned)(i * 16 + 4 * kq + (lane & 3)) * 256u + v * 16;
                    NOUNROLL for (int spin = 0; spin < SPIN_MAX; spin++) {
                        fence_c();
                        const v2u f = __builtin_amdgcn_raw_buffer_load_b64(rws, po, 0, 16);
                        const unsigned okm = (unsigned)__builtin_amdgcn_ballot_w64(f.x == flagv.x && f.y == flagv.y) | (v ? 8u : 0u);
                        if (spin == 0) { waitv<0>(); post(ffull, N1 + v); }
                        if ((okm & 0xfu) == 0xfu) break;
                    }
                    fence_c();
                    if (v) STAMP(7); else STAMP(5);
                }
                if (v >= 4) wait_free(N1 + v);
                const int unit = v < 4 ? 28 + 8 * (v >> 1) + 2 * kq + (v & 1) : 7 * kq + v - 4;
                const unsigned char* base = p.ws + WS_A2 + (size_t)(i * 44 + unit) * 16384;
                const unsigned dst = L_A + (v & 3) * SLOT;
#pragma unroll
                for (int m = 0; m < 4; m++) dma16x4_sc1(base + m * 4096, lane * 16, dst + m * 4096);
                waitv<16>(); post(ffull, N1 + v);
            }
            waitv<0>(); post(ffull, N1 + 11);
        }
        STAMP(10);
        STAMP_DUMP();
        return;
    }
    if (w >= 6) {
        LANE_VARS;
        const unsigned tg = peek(L_TAG);
        const bool odd = lane & 1;
        const unsigned la = lane * 4;
        const v2u prm = *(LAS v2u*)(uintptr_t)(L_HIN + lane * 8);
        const float lr = bflo(prm.x), li = bfhi(prm.x);
        float dt = __expf(bflo(prm.y));
        dt = bflo(pk2(dt, dt));
        const float ex = __expf(dt * lr);
        const float fr = __builtin_amdgcn_fractf(dt * li * 0.15915494309189535f);
        const float ar = smul_t(ex, __builtin_amdgcn_cosf(fr)), ai = smul_t(ex, __builtin_amdgcn_sinf(fr));
        const cpx a1 = {ar, ai};
        const cpx a2 = cmul(a1, a1), a4 = cmul(a2, a2);
        if (lw == 0) { sts32f(L_CST + la, odd ? a1.y : a1.x); sts32f(L_CST + 256 + la, odd ? a4.y : a4.x); }
        post(F_COMM + lw * 4, 1);
        SB();
        cpx pw[8];
        pw[1] = cmul(a4, a4);
        const unsigned sg = odd ? 0u : 0x80000000u;
        auto sgn = [&](cpx v) { return cpx{v.x, __uint_as_float(__float_as_uint(v.y) ^ sg)}; };
        const cpx a8 = sgn(pw[1]);
        pw[2] = cmul(pw[1], pw[1]); pw[4] = cmul(pw[2], pw[2]);
        const cpx r64 = cmul(pw[4], pw[4]);
        const cpx a64 = sgn(r64);
        SB();
        STAMP(3);
        wait_ge(F_DONE + lw * 8 + (lane & 1) * 4, 4);
        STAMP(4);
        float P[8], h;
        {
            float e[8];
#pragma unroll
            for (int s = 0; s < 8; s++) e[s] = lds32f(L_SEG + (8 * lw + s) * 256 + la);
            h = e[0];
#pragma unroll
            for (int s = 1; s < 8; s++) { P[s] = h; h = lmad(a8, h, e[s]); }
        }
        float elo = 0;
        if (lw == 0) { sts32f(L_XCH + la, h); post(F_COMM, 2); }
        else {
            wait_ge(F_COMM, 2);
            elo = lds32f(L_XCH + la);
            __builtin_amdgcn_raw_buffer_store_b64(v2u{__float_as_uint(lmad(a64, elo, h)), tg}, rws, WS_GRAN + (j * 16 + i) * 512 + lane * 8, 0, 16);
        }
        SB();
        STAMP(5);
        const cpx r128 = cmul(r64, r64);
        const cpx a128 = sgn(r128), a256 = sgn(cmul(r128, r128));
        if (lw == 0) {
            const float nr = ar - 1.0f, den = 2.0f * __builtin_amdgcn_rcpf(sfma(lr, lr, smul(li, li)));
            cpx co = {smul(sfma(nr, lr, smul(ai, li)), den), smul(sfms(ai, lr, smul(nr, li)), den)};
#pragma unroll
            for (int t = 0; t < 5; t++) { sts32f(L_CST + (2 + t) * 256 + la, odd ? co.y : co.x); if (t < 4) co = cmul(co, a1); }
        }
        pw[3] = cmul(pw[1], pw[2]); pw[5] = cmul(pw[1], pw[4]); pw[6] = cmul(pw[2], pw[4]); pw[7] = cmul(pw[3], pw[4]);
#pragma unroll
        for (int s = 1; s < 8; s++) pw[s] = sgn(pw[s]);
        SB();
        STAMP(6);
        {
            const int top = i - 1 - lw;
            v2u gr[8];
            NOUNROLL for (int spin = 0; spin < SPIN_MAX; spin++) {
                fence_c();
#pragma unroll
                for (int n = 0; n < 8; n++) gr[n] = __builtin_amdgcn_raw_buffer_load_b64(rws, WS_GRAN + j * 8192 + lane * 8, ((top - 2 * n) & 15) * 512, 16);
                unsigned long long bad = 0;
#pragma unroll
                for (int n = 0; n < 8; n++) if (top - 2 * n >= 0) bad |= __builtin_amdgcn_ballot_w64(gr[n].y != tg);
                if (bad == 0) break;
            }
            h = 0;
#pragma unroll
            for (int n = 7; n >= 0; n--) if (top - 2 * n >= 0) h = lmad(a256, h, __uint_as_float(gr[n].x));
        }
        STAMP(7);
        sts32f(L_XCH + 256 + lw * 256 + la, h);
        post(F_COMM + lw * 4, 3);
        wait_ge(F_COMM + (lw ^ 1) * 4, 3);
        const float ho = lds32f(L_XCH + 256 + (lw ^ 1) * 256 + la);
        float C = lw ? lmad(a64, lmad(a128, h, ho), elo) : lmad(a128, ho, h);
        sts32f(L_HIN + (8 * lw) * 256 + la, C);
#pragma unroll
        for (int s = 1; s < 8; s++) sts32f(L_HIN + (8 * lw + s) * 256 + la, lmad(pw[s], C, P[s]));
        post(F_COMM + lw * 4, 4);
        STAMP(8);
        STAMP_DUMP();
        return;
    }
#define PD1 4
    unsigned it = 4;
    unsigned stall = 0; (void)stall;
    {
        constexpr int PD = PD1;
        LANE_VARS;
        const unsigned fdone = F_DONE + w * 4, ffl = F_FULL + (lane & 1) * 4;
        STAMP(3);
        {
            unsigned cb[2];
            cb[0] = L_W + (unsigned)c * 128u + (unsigned)((g ^ (c >> 1)) << 4); cb[1] = cb[0] ^ 64u;
            f32x4 acc[4][2] = {};
            v4u bf[PD];
            unsigned fl = 0; bool lack = false;
            sfor<-PD, 64>(LAMB(nn) {
                constexpr int n = decltype(nn)::value, m = n + PD;
                v4u cur;
                if constexpr (n >= 0) { cur = bf[n % PD]; acc[n & 3][0] = mfma(xa[n >> 2][0], cur, acc[n & 3][0]); SB(); }
                if constexpr (m > 0 && (m & 15) == 0) need_full(lack, ffl, (m >> 4) + 1);
                if constexpr (m < 64) bf[m % PD] = lds128(cb[(m >> 2) & 1] + wslot(m >> 4) + (((m >> 1) & 1) * 4 + ((m >> 3) & 1) * 2 + (m & 1)) * 2048);
                SB();
                if constexpr (n >= 0) { acc[n & 3][1] = mfma(xa[n >> 2][1], cur, acc[n & 3][1]); SB(); }
                if constexpr ((m & 15) == 10) fl = peek(ffl);
                if constexpr ((m & 15) == 14) lack = any_lt(fl, (m >> 4) + 2);
                if constexpr ((m & 15) == 15 && m < 63) post(fdone, (m >> 4) + 1);
                SB();
            });
            STAMP(4); VAL(24, stall); stall = 0;
            wait_ge(F_COMM, 1);
            const f32x4 ca = lds128f(L_CST + c * 16), c4 = lds128f(L_CST + 256 + c * 16);
            const f32x4 rs0 = lds128f(L_RSTD + (32 * w + 8 * g) * 4), rs1 = lds128f(L_RSTD + (32 * w + 8 * g + 4) * 4);
            f32x4 ev;
#pragma unroll
            for (int st = 0; st < 2; st++) {
                const f32x2 ar = dup(ca[2 * st]), ai = dup(ca[2 * st + 1]);
                const f32x4 r0 = acc[st][0] * rs0, r1 = acc[st][1] * rs1, i0 = acc[2 + st][0] * rs0, i1 = acc[2 + st][1] * rs1;
                f32x2 sr[4] = {lo2(r0), hi2(r0), lo2(r1), hi2(r1)}, si[4] = {lo2(i0), hi2(i0), lo2(i1), hi2(i1)};
#pragma unroll
                for (int t = 1; t < 4; t++) {
                    sr[t] = pfma(ar, sr[t - 1], pfma(-ai, si[t - 1], sr[t]));
                    si[t] = pfma(ar, si[t - 1], pfma(ai, sr[t - 1], si[t]));
                }
                ev[2 * st] = sfma(c4[2 * st], sr[3].x, sfma(-c4[2 * st + 1], si[3].x, sr[3].y));
                ev[2 * st + 1] = sfma(c4[2 * st], si[3].x, sfma(c4[2 * st + 1], sr[3].x, si[3].y));
                const unsigned pk = L_PARK + w * 8192 + st * 4096 + lane * 16;
                sts128f(pk, f32x4{sr[0].x, sr[0].y, sr[1].x, sr[1].y}); sts128f(pk + 1024, f32x4{sr[2].x, sr[2].y, sr[3].x, sr[3].y});
                sts128f(pk + 2048, f32x4{si[0].x, si[0].y, si[1].x, si[1].y}); sts128f(pk + 3072, f32x4{si[2].x, si[2].y, si[3].x, si[3].y});
            }
            sts128f(L_SEG + (w * 64 + lane) * 16, ev);
            post(fdone, 4);
        }
        STAMP(5);
        unsigned tb[4];
        {
            const int q4 = c >> 2, p4 = c & 3;
            const unsigned tb1 = L_W + (unsigned)(8 * g + q4) * 128u + (unsigned)((((q4 >> 1) & 1) | ((g & 1) << 1)) << 5) + p4 * 8;
#pragma unroll
            for (int t = 0; t < 4; t++) { tb[t] = tb1 ^ (unsigned)(t << 5); asm volatile("" : "+v"(tb[t])); }
        }
        const int npass = heavy ? 2 : 1;
        NOUNROLL for (int pass = 0; pass < npass; pass++) {
            f32x4 acc[4][2][2] = {};
            v4u bf[PD];
            unsigned fl = 0; bool lack = false;
            sfor<-PD, 128>(LAMB(nn) {
                constexpr int n = decltype(nn)::value, m = n + PD;
                constexpr unsigned off = wslot(m >> 4) + (m & 1) * 8192 + ((m >> 3) & 1) * 4096;
                v4u cur; v2u lo, hi;
                if constexpr (n >= 0) { cur = bf[n % PD]; acc[(n >> 1) & 3][n & 1][0] = mfma(cur, xa[n >> 3][0], acc[(n >> 1) & 3][n & 1][0]); SB(); }
                if constexpr (m > 0 && (m & 15) == 0 && m < 128) need_full(lack, ffl, it + 1);
                if constexpr (m < 128) lo = ldstr(tb[(m >> 1) & 3] + off);
                if constexpr ((m & 15) == 10 && m < 128) fl = peek(ffl);
                if constexpr ((m & 15) == 14) it++;
                if constexpr ((m & 15) == 15 && m < 128) lack = any_lt(fl, it + 1);
                if constexpr (m == 33) waitv<0>();
                if constexpr (m > 16 && m < 128 && (m & 15) == 1) post(fdone, it);
                if constexpr (m == 129) post(fdone, heavy ? it : it - 1);
                SB();
                if constexpr (n >= 0) { acc[(n >> 1) & 3][n & 1][1] = mfma(cur, xa[n >> 3][1], acc[(n >> 1) & 3][n & 1][1]); SB(); }
                if constexpr (m < 128) { hi = ldstr(tb[(m >> 1) & 3] + off + 512); bf[m % PD] = v4u{lo.x, lo.y, hi.x, hi.y}; }
                SB();
            });
            if (pass) STAMP(8); else STAMP(6); VAL(25 + pass, stall); stall = 0;
            LANE_VARS;
            const int line = 7 * kq + 4 * pass + q;
            const unsigned ao = WS_A2 + (unsigned)i * (44u * 16384u) + w * 4096 + lane * 16;
#pragma unroll
            for (int bb = 0; bb < 2; bb++) {
                const float r1 = lds32f(L_RSTD + (32 * w + 8 * (c >> 2) + 4 * bb + (c & 3)) * 4);
                const f32x2 r2 = dup(r1 * r1);
                const f32x4 r4 = {r2.x, r2.y, r2.x, r2.y};
#pragma unroll
                for (int uu = 0; uu < 2; uu++)
                    __builtin_amdgcn_raw_buffer_store_b128(pk8(acc[2 * uu][0][bb] * acc[2 * uu][1][bb] * r4, acc[2 * uu + 1][0][bb] * acc[2 * uu + 1][1][bb] * r4), rws, ao + line * 16384 + (2 * uu + bb) * 1024, 0, 16);
            }
            SB();
            if (pass == 0) {
                wait_ge(F_COMM + (lane & 1) * 4, 4);
                const f32x4 hin = lds128f(L_HIN + (w * 64 + lane) * 16), c4 = lds128f(L_CST + 256 + c * 16), co = lds128f(L_CST + 512 + c * 16);
                f32x4 cw[4];
#pragma unroll
                for (int t = 0; t < 4; t++) cw[t] = lds128f(L_CST + (3 + t) * 256 + c * 16);
                v4u fr[2], fi[2];
#pragma unroll
                for (int st = 0; st < 2; st++) {
                    const unsigned pk = L_PARK + w * 8192 + st * 4096 + lane * 16;
                    const f32x4 r0 = lds128f(pk), r1 = lds128f(pk + 1024), i0 = lds128f(pk + 2048), i1 = lds128f(pk + 3072);
                    const f32x2 sr[4] = {lo2(r0), hi2(r0), lo2(r1), hi2(r1)}, si[4] = {lo2(i0), hi2(i0), lo2(i1), hi2(i1)};
                    const float hx = hin[2 * st], hy = hin[2 * st + 1];
                    f32x2 hr = {hx, sfma(c4[2 * st], hx, sfma(-c4[2 * st + 1], hy, sr[3].x))};
                    f32x2 hi = {hy, sfma(c4[2 * st], hy, sfma(c4[2 * st + 1], hx, si[3].x))};
                    asm("" : "+v"(hr), "+v"(hi));
                    const f32x2 cr = dup(co[2 * st]), ci = dup(co[2 * st + 1]);
#pragma unroll
                    for (int t = 0; t < 4; t++) {
                        const f32x2 wr = dup(cw[t][2 * st]), wi = dup(cw[t][2 * st + 1]);
                        fr[st][t] = pk2(pfma(wr, hr, pfma(-wi, hi, pfma(cr, sr[t], -ci * si[t]))));
                        fi[st][t] = pk2(pfma(-wr, hi, pfma(-wi, hr, pfma(-cr, si[t], -ci * sr[t]))));
                    }
                }
                const unsigned so = (28 + (j >> 1)) * 16384 + (j & 1) * 2048;
#pragma unroll
                for (int bb = 0; bb < 2; bb++) {
                    const f32x4 z = {0, 0, 0, 0};
                    const v4u sel = make_sel(lane, bb);
                    const f32x4 r0 = mfma(fr[0], sel, z), r1 = mfma(fr[1], sel, z), i0 = mfma(fi[0], sel, z), i1 = mfma(fi[1], sel, z);
                    __builtin_amdgcn_raw_buffer_store_b128(pk8(r0, r1), rws, ao + so + bb * 1024, 0, 16);
                    __builtin_amdgcn_raw_buffer_store_b128(pk8(i0, i1), rws, ao + so + 8 * 16384 + bb * 1024, 0, 16);
                }
                if (!heavy) { waitv<0>(); post(fdone, it); }
                STAMP(7);
            }
            need_full(lack, ffl, it + 1);
        }
    }
    LANE_VARS;
    STAMP(9); VAL(27, stall); stall = 0;
    constexpr int PD = 4;
    f32x4 acc[8][2] = {};
    v2u xv[4], dv;
    {
        const unsigned fdone = F_DONE + w * 4, ffl = F_FULL + (lane & 1) * 4;
        const unsigned ab = L_A + w * 4096 + lane * 16;
        v4u af[4], bf[PD];
#pragma unroll
        for (int f = 0; f < 4; f++) af[f] = lds128(ab + f * 1024);
        auto stage = [&](auto isG, int v, unsigned (&base)[4], unsigned base0) __attribute__((always_inline)) {
            constexpr bool G = decltype(isG)::value;
            v4u a2n, a3n;
            unsigned fl = 0, an = 0; bool lack = false;
            const unsigned nsl = (unsigned)((v + 3) & 3) * SLOT;
            sfor<0, 16>(LAMB(nn) {
                constexpr int n = decltype(nn)::value, m = (n + PD) & 15;
                const v4u cur = bf[n % PD];
                const v4u x0 = af[(n >> 3) * 2], x1 = af[(n >> 3) * 2 + 1];
                v2u lo, hi;
                acc[n & 7][0] = mfma(cur, x0, acc[n & 7][0]);
                SB();
                if constexpr (n == 12) need_full(lack, ffl, it + 1);
                if constexpr (G) lo = ldstr(base[m & 3] + ((m >> 2) & 1) * 8192 + (m >> 3) * 4096);
                else bf[n % PD] = lds128(base[m >> 3] + (m & 7) * 2048);
                if constexpr (n == 6) fl = peek(ffl);
                if constexpr (n == 3) an = ab + ((v + 1) & 3) * SLOT;
                if constexpr (n == 11) { it++; lack = any_lt(fl, v == 10 ? 0 : it + 1); }
                if constexpr (n == 12) af[0] = lds128(an);
                if constexpr (n == 13) af[1] = lds128(an + 1024);
                if constexpr (n == 14) a2n = lds128(an + 2048);
                if constexpr (n == 15) a3n = lds128(an + 3072);
                SB();
                acc[n & 7][1] = mfma(cur, x1, acc[n & 7][1]);
                SB();
                if constexpr (G) { hi = ldstr(base[m & 3] + ((m >> 2) & 1) * 8192 + (m >> 3) * 4096 + 2048); bf[n % PD] = v4u{lo.x, lo.y, hi.x, hi.y}; }
                if constexpr (G && n >= 8 && n < 12) base[n & 3] = (base0 ^ (unsigned)((n & 3) << 5)) + nsl;
                if constexpr (!G && n == 4) base[0] = base0 + nsl;
                if constexpr (!G && n == 11) base[1] = (base0 ^ 64u) + nsl;
                if constexpr (!G && n == 12) { if (v == 1) waitv<0>(); }
                if constexpr (n == 12) post(fdone, it);
                SB();
            });
            af[2] = a2n; af[3] = a3n;
        };
        unsigned base[4];
        const unsigned cb0 = L_W + (unsigned)c * 128u + (unsigned)((g ^ (c >> 1)) << 4);
        base[0] = cb0 + wslot(0); base[1] = (cb0 ^ 64u) + wslot(0); base[2] = base[3] = 0;
#pragma unroll
        for (int m = 0; m < PD; m++) bf[m] = lds128(base[0] + m * 2048);
        NOUNROLL for (int v = 0; v < 4; v++) {
            stage(IC<0>{}, v, base, cb0);
        }
        STAMP(10); VAL(28, stall); stall = 0;
        {
            const int d = 128 * q + 32 * kq + 4 * p8;
#pragma unroll
            for (int e = 0; e < 4; e++) xv[e] = *(const GAS v2u*)(p.x + (size_t)(128 * i + 32 * w + 8 * e + q8) * DM + d);
            dv = *(const GAS v2u*)(p.Dsk + d);
        }
        const int q4 = c >> 2, p4 = c & 3;
        const unsigned tb2 = L_W + (unsigned)(4 * g + q4) * 128u + (unsigned)((2 * (g & 1) + (q4 >> 1)) << 5) + p4 * 8;
#pragma unroll
        for (int m = 0; m < PD; m++) { base[m] = (tb2 ^ (unsigned)(m << 5)) + wslot(0); const v2u lo = ldstr(base[m]), hi = ldstr(base[m] + 2048); bf[m] = v4u{lo.x, lo.y, hi.x, hi.y}; }
        NOUNROLL for (int v = 4; v < 11; v++) stage(IC<1>{}, v, base, tb2);
    }
    STAMP(11); VAL(29, stall);
    {
        const unsigned tg = peek(L_TAG);
        const unsigned xo = WS_XB + (unsigned)((i * 4 + q) * 4) * 49152u + w * 4096 + lane * 16;
#pragma unroll
        for (int s = 1; s < 4; s++)
#pragma unroll
            for (int e = 0; e < 4; e++) {
                const f32x4 v = acc[2 * s + (e >> 1)][e & 1];
                __builtin_amdgcn_raw_buffer_store_b128(v4u{pk2(v.x, v.y), tg, pk2(v.z, v.w), tg}, rws, xo + (kq ^ s) * 49152u + (s - 1) * 16384 + e * 1024, 0, 16);
            }
        SB();
        f32x4 base[4];
        const f32x4 df = {bflo(dv.x), bfhi(dv.x), bflo(dv.y), bfhi(dv.y)};
#pragma unroll
        for (int e = 0; e < 4; e++) {
            const int tl = tok_pos(32 * w + 8 * e + q8);
            const f32x2 rs = dup(lds32f(L_RSTD + tl * 4)), mn = dup(lds32f(L_MEAN + tl * 4));
            const f32x4 xf = {bflo(xv[e].x), bfhi(xv[e].x), bflo(xv[e].y), bfhi(xv[e].y)};
            base[e] = xf + (xf - f32x4{mn.x, mn.y, mn.x, mn.y}) * f32x4{rs.x, rs.y, rs.x, rs.y} * df;
        }
        STAMP(12);
        v4u in[12];
        NOUNROLL for (int spin = 0; spin < SPIN_MAX; spin++) {
            fence_c();
#pragma unroll
            for (int n = 0; n < 12; n++) in[n] = __builtin_amdgcn_raw_buffer_load_b128(rws, xo + (n & 3) * 1024, kq * 49152u + (n >> 2) * 16384, 16);
            unsigned bad = 0;
#pragma unroll
            for (int n = 0; n < 12; n++) bad |= (in[n].y ^ tg) | (in[n].w ^ tg);
            if (__builtin_amdgcn_ballot_w64(bad != 0) == 0) break;
        }
        STAMP(13);
        const unsigned tb = L_A + w * 4096;
#pragma unroll
        for (int e = 0; e < 4; e++) {
            f32x4 o = acc[e >> 1][e & 1];
#pragma unroll
            for (int s = 0; s < 3; s++) { const v4u t = in[4 * s + e]; o += f32x4{bflo(t.x), bfhi(t.x), bflo(t.z), bfhi(t.z)}; }
            const int tl = 8 * (c >> 2) + 4 * (c & 1) + 2 * (e & 1) + ((c >> 1) & 1);
            sts128f(tb + tl * 128 + (((4 * (e >> 1) + g) ^ (c & 7)) << 4), o);
        }
        fence_c();
#pragma unroll
        for (int e = 0; e < 4; e++) {
            const int key = 4 * (e & 1) + 2 * (q8 & 1) + (q8 >> 2);
            const f32x4 o = lds128f(tb + (8 * e + q8) * 128 + ((p8 ^ key) << 4)) + base[e];
            *(GAS f32x4*)(p.out + (size_t)(128 * i + 32 * w + 8 * e + q8) * DM + 128 * q + 32 * kq + 4 * p8) = o;
        }
        if (i == 15 && j == 0 && w == 0 && lane == 0) __builtin_amdgcn_raw_buffer_store_b32(tg, rws, WS_EPOCH, 0, 16);
    }
    STAMP(14);
}

extern "C" void kernel_launch(void* const* d_in, const int* in_sizes, int n_in, void* d_out, int out_size, void* d_ws, size_t ws_size,
                              hipStream_t stream) {
    Params p;
    p.x = (const u16*)d_in[0]; p.lre = (const u16*)d_in[1]; p.lim = (const u16*)d_in[2];
    p.Bre = (const u16*)d_in[3]; p.Bim = (const u16*)d_in[4]; p.Cre = (const u16*)d_in[5]; p.Cim = (const u16*)d_in[6];
    p.ldt = (const u16*)d_in[7]; p.Dsk = (const u16*)d_in[8];
    p.wg = (const u16*)d_in[10]; p.wu = (const u16*)d_in[11]; p.wd = (const u16*)d_in[12];
    p.out = (float*)d_out; p.ws = (unsigned char*)d_ws;
    (void)hipFuncSetAttribute((const void*)fused, hipFuncAttributeMaxDynamicSharedMemorySize, L_TOTAL);
    fused<<<256, 512, L_TOTAL, stream>>>(p);
}
```

```cpp
#define OUT_POL "sc1"
#include <hip/hip_runtime.h>
#include <stdint.h>

typedef unsigned short u16;
typedef unsigned v4u __attribute__((ext_vector_type(4)));
typedef unsigned v2u __attribute__((ext_vector_type(2)));
typedef short s16x4 __attribute__((ext_vector_type(4)));
typedef __bf16 bf16x2 __attribute__((ext_vector_type(2)));
typedef __bf16 bf16x8 __attribute__((ext_vector_type(8)));
typedef float f32x2 __attribute__((ext_vector_type(2)));
typedef float f32x4 __attribute__((ext_vector_type(4)));
#define GAS __attribute__((address_space(1)))
#define LAS __attribute__((address_space(3)))
#define DEVI __device__ __forceinline__
#define NOUNROLL _Pragma("nounroll")

constexpr int DM = 512, FF = 1792;
constexpr unsigned WS_A2 = 0;
constexpr unsigned WS_STAT = 16u * 44u * 16384u;
constexpr unsigned WS_GRAN = WS_STAT + 64u * 1024u;
constexpr unsigned WS_FLAG = WS_GRAN + 16u * 16u * 512u;
constexpr unsigned WS_EPOCH = WS_FLAG + 512u * 128u;
constexpr unsigned WS_END = WS_EPOCH + 256u;
constexpr unsigned SLOT = 16384;
constexpr unsigned L_RSTD = 0;
constexpr unsigned F_DONE = 1024, F_FUL2 = F_DONE + 16, F_FULL = F_DONE + 32, F_COMM = F_DONE + 40, F_RDY = F_DONE + 48, F_OUT = F_DONE + 64, L_TAG = F_DONE + 80;
constexpr unsigned L_CST = 1536;
constexpr unsigned L_XCH = L_CST + 7 * 256;
constexpr unsigned L_SEG = 4096, L_HIN = 8192, L_SEL = 12288;
constexpr unsigned L_P = SLOT, L_O = 5 * SLOT, L_W = 6 * SLOT, L_TOTAL = 10 * SLOT;
constexpr int SPIN_MAX = 1 << 18;
constexpr int LSPIN_MAX = 1 << 22;

struct Params {
    const u16 *x, *lre, *lim, *Bre, *Bim, *Cre, *Cim, *ldt, *Dsk, *wg, *wu, *wd;
    float* out;
    unsigned char* ws;
};

DEVI float bf2f(u16 v) { return __uint_as_float(((unsigned)v) << 16); }
DEVI float bflo(unsigned v) { return __uint_as_float(v << 16); }
DEVI float bfhi(unsigned v) { return __uint_as_float(v & 0xffff0000u); }
DEVI unsigned pk2(float a, float b) { f32x2 p = {a, b}; return __builtin_bit_cast(unsigned, __builtin_convertvector(p, bf16x2)); }
DEVI unsigned pk2(f32x2 p) { return __builtin_bit_cast(unsigned, __builtin_convertvector(p, bf16x2)); }
DEVI v4u pk8(f32x4 a, f32x4 b) { return v4u{pk2(a.x, a.y), pk2(a.z, a.w), pk2(b.x, b.y), pk2(b.z, b.w)}; }
DEVI float dot2(unsigned a, unsigned b, float c) { return __builtin_amdgcn_fdot2_f32_bf16(__builtin_bit_cast(bf16x2, a), __builtin_bit_cast(bf16x2, b), c, false); }
template <int CTRL> DEVI float dppf(float v) { return __builtin_bit_cast(float, __builtin_amdgcn_update_dpp(0, __builtin_bit_cast(int, v), CTRL, 0xf, 0xf, true)); }
DEVI f32x4 mfma(v4u a, v4u b, f32x4 c) { return __builtin_amdgcn_mfma_f32_16x16x32_bf16(__builtin_bit_cast(bf16x8, a), __builtin_bit_cast(bf16x8, b), c, 0, 0, 0); }
template <bool FIRST> DEVI void mma(f32x4& c, const v4u& a, const v4u& b) {
    if constexpr (FIRST) asm volatile("v_mfma_f32_16x16x32_bf16 %0, %1, %2, 0" : "=&v"(c) : "v"(a), "v"(b));
    else asm volatile("v_mfma_f32_16x16x32_bf16 %0, %1, %2, %0" : "+v"(c) : "v"(a), "v"(b));
}
DEVI void mfma_done() { __builtin_amdgcn_sched_barrier(0); asm volatile("s_nop 7"); __builtin_amdgcn_sched_barrier(0); }
DEVI v4u lds128(unsigned a) { return *(LAS v4u*)(uintptr_t)a; }
DEVI f32x4 lds128f(unsigned a) { return *(LAS f32x4*)(uintptr_t)a; }
DEVI float lds32f(unsigned a) { return *(LAS float*)(uintptr_t)a; }
DEVI void sts32f(unsigned a, float v) { *(LAS float*)(uintptr_t)a = v; }
DEVI void sts128(unsigned a, v4u v) { *(LAS v4u*)(uintptr_t)a = v; }
DEVI void sts128f(unsigned a, f32x4 v) { *(LAS f32x4*)(uintptr_t)a = v; }
DEVI v2u ldstr(unsigned a) { return __builtin_bit_cast(v2u, __builtin_amdgcn_ds_read_tr16_b64_v4i16((LAS s16x4*)(uintptr_t)a)); }
DEVI void fence_c() { asm volatile("" ::: "memory"); }
DEVI void bar_l() { asm volatile("s_waitcnt lgkmcnt(0)\n\ts_barrier" ::: "memory"); }
template <int N> DEVI void waitv() { asm volatile("s_waitcnt vmcnt(%0)" ::"n"(N) : "memory"); }
DEVI void dma16(const void* sbase, unsigned voff, unsigned ldst) {
    asm volatile("s_mov_b32 m0, %2\n\ts_nop 0\n\tglobal_load_lds_dwordx4 %0, %1" ::"v"(voff), "s"(sbase), "s"(ldst) : "memory");
}
DEVI void post(unsigned a, unsigned v) { fence_c(); *(LAS unsigned*)(uintptr_t)a = v; fence_c(); }
DEVI unsigned peek(unsigned a) { fence_c(); return *(LAS unsigned*)(uintptr_t)a; }
DEVI bool any_lt(unsigned v, unsigned need) { return __builtin_amdgcn_ballot_w64(v < need) != 0; }
DEVI void wait_ge(unsigned a, unsigned need) { NOUNROLL for (int s = 0; s < LSPIN_MAX && any_lt(peek(a), need); s++) {} fence_c(); }
DEVI void need_full(bool lack, unsigned a, unsigned need) { if (__builtin_expect(lack, 0)) wait_ge(a, need); fence_c(); }
#define VAL(id, v)
DEVI float sfma(float a, float b, float c) { float d; asm("v_fma_f32 %0, %1, %2, %3" : "=v"(d) : "v"(a), "v"(b), "v"(c)); return d; }
DEVI float sfms(float a, float b, float c) { float d; asm("v_fma_f32 %0, %1, %2, -%3" : "=v"(d) : "v"(a), "v"(b), "v"(c)); return d; }
DEVI float sfnma(float a, float b, float c) { float d; asm("v_fma_f32 %0, -%1, %2, %3" : "=v"(d) : "v"(a), "v"(b), "v"(c)); return d; }
DEVI float smul(float a, float b) { float d; asm("v_mul_f32 %0, %1, %2" : "=v"(d) : "v"(a), "v"(b)); return d; }
DEVI float smul_t(float a, float b) { float d; asm("s_nop 0\n\tv_mul_f32 %0, %1, %2" : "=v"(d) : "v"(a), "v"(b)); return d; }
struct cpx { float x, y; };
DEVI cpx cmul(cpx a, cpx b) { return cpx{sfms(a.x, b.x, smul(a.y, b.y)), sfma(a.x, b.y, smul(a.y, b.x))}; }
DEVI float lmad(cpx a, float h, float u) {
    float d;
    asm("v_fma_f32 %0, %1, %3, %4\n\ts_nop 0\n\tv_fmac_f32_dpp %0, %3, %2 quad_perm:[1,0,3,2] row_mask:0xf bank_mask:0xf" : "=&v"(d) : "v"(a.x), "v"(a.y), "v"(h), "v"(u));
    return d;
}
DEVI f32x2 dup(float v) { f32x2 d = {v, v}; asm("" : "+v"(d)); return d; }
DEVI f32x2 pfma(f32x2 a, f32x2 b, f32x2 c) { return __builtin_elementwise_fma(a, b, c); }
DEVI f32x2 lo2(f32x4 v) { return f32x2{v.x, v.y}; }
DEVI f32x2 hi2(f32x4 v) { return f32x2{v.z, v.w}; }
#define SB() __builtin_amdgcn_sched_barrier(0)
template <int V> struct IC { static constexpr int value = V; };
template <int B, int E, class F> DEVI void sfor(F&& f) { if constexpr (B < E) { f(IC<B>{}); sfor<B + 1, E>(f); } }
#define LAMB(nn) [&](auto nn) __attribute__((always_inline))
#define STAMP(id)
#define STAMP_DUMP()
DEVI int fresh_lane() { int l; asm volatile("v_mbcnt_lo_u32_b32 %0, -1, 0\n\tv_mbcnt_hi_u32_b32 %0, -1, %0" : "=v"(l)); return l; }
DEVI v4u make_sel(int lane, int bb) {
    const int c = lane & 15, tk = 8 * (c >> 2) + 4 * bb + (c & 3) - 8 * (lane >> 4);
    v4u s;
#pragma unroll
    for (int e = 0; e < 4; e++) s[e] = (tk == 2 * e ? 0x3f80u : 0u) | (tk == 2 * e + 1 ? 0x3f800000u : 0u);
    return s;
}
#define LANE_VARS const int lane = fresh_lane(), c = lane & 15, g = lane >> 4, q8 = lane >> 3, p8 = lane & 7; (void)c; (void)g; (void)q8; (void)p8
constexpr unsigned wslot(int n) { return (unsigned)(n & 3) * SLOT; }

#define ROT1 0
#define ROT2 0
#define SLEEP0 12
constexpr unsigned CODE_LINES = 320;
template <int OFF> DEVI void ldA(v4u& d, unsigned voff, const void* sbase) {
    asm volatile("global_load_dwordx4 %0, %1, %2 offset:%3 sc1" : "=v"(d) : "v"(voff), "s"(sbase), "n"(OFF) : "memory");
}
template <int OFF> DEVI void reldA(v4u& d, unsigned voff, const void* sbase) {
    asm volatile("global_load_dwordx4 %0, %1, %2 offset:%3 sc1" : "+v"(d) : "v"(voff), "s"(sbase), "n"(OFF) : "memory");
}
DEVI void touch(unsigned& sink, const void* tp, unsigned to) { asm volatile("global_load_dword %0, %1, %2" : "+v"(sink) : "v"(to), "s"(tp) : "memory"); }

DEVI v4u make_srd(const void* base) { const unsigned long long a = (unsigned long long)base; return v4u{(unsigned)a, (unsigned)(a >> 32) & 0xffffu, 0x7fffffffu, 0x00020000u}; }
DEVI void dma4(v4u srd, unsigned ve, unsigned vo, unsigned s0, unsigned s1, unsigned s2, unsigned s3, unsigned ldst) {
    asm volatile("s_mov_b32 m0, %7\n\ts_nop 0\n\tbuffer_load_dwordx4 %0, %2, %3 offen lds\n\tbuffer_load_dwordx4 %1, %2, %4 offen offset:1024 lds\n\t"
                 "buffer_load_dwordx4 %0, %2, %5 offen offset:2048 lds\n\tbuffer_load_dwordx4 %1, %2, %6 offen offset:3072 lds"
                 ::"v"(ve), "v"(vo), "s"(srd), "s"(s0), "s"(s1), "s"(s2), "s"(s3), "s"(ldst) : "memory");
}
template <int H, int NEG> DEVI f32x2 bmul(f32x2 a, f32x2 b) {
    f32x2 d; asm("v_pk_mul_f32 %0, %1, %2 op_sel:[%3,0] op_sel_hi:[%3,1] neg_lo:[%4,0] neg_hi:[%4,0]" : "=v"(d) : "v"(a), "v"(b), "n"(H), "n"(NEG)); return d;
}
template <int H, int NEG> DEVI f32x2 bfma(f32x2 a, f32x2 b, f32x2 c) {
    f32x2 d; asm("v_pk_fma_f32 %0, %1, %2, %3 op_sel:[%4,0,0] op_sel_hi:[%4,1,1] neg_lo:[%5,0,0] neg_hi:[%5,0,0]" : "=v"(d) : "v"(a), "v"(b), "v"(c), "n"(H), "n"(NEG)); return d;
}
DEVI void mma_c(f32x4& d, const v4u& a, const v4u& b, const f32x4& c) { asm volatile("v_mfma_f32_16x16x32_bf16 %0, %1, %2, %3" : "=&v"(d) : "v"(a), "v"(b), "v"(c)); }
DEVI unsigned cvtv(float a, float b) { unsigned d; asm volatile("v_cvt_pk_bf16_f32 %0, %1, %2" : "=v"(d) : "v"(a), "v"(b)); return d; }

__global__ __launch_bounds__(512, 2) void fused(Params p) {
    const unsigned char* pc0;
    asm volatile("s_getpc_b64 %0" : "=s"(pc0));
    const int w = __builtin_amdgcn_readfirstlane(threadIdx.x >> 6);
    const int b = blockIdx.x, xcd = b & 7, idx = b >> 3;
    const int i = 8 * (xcd & 1) + (idx & 7), kq = xcd >> 1, q = idx >> 3, j = 4 * kq + q;
    const int rb = 4 * xcd + (idx >> 3), i2 = rb >> 1, qc = idx & 7;
    const bool heavy = q != 3;
    const int N1 = heavy ? 20 : 12;
    const int lw = w & 1, pr = w & 3;
    const int rot = ROT1;
    const __amdgpu_buffer_rsrc_t rws = __builtin_amdgcn_make_buffer_rsrc(p.ws, 0, WS_END, 0x00020000);
    auto unit_of = [&](int v) { const int r = (v + ROT2) & 3; return v < 4 ? 28 + 8 * (r >> 1) + 2 * pr + (r & 1) : v < 8 ? 7 * pr + r : 7 * pr + v - 4; };
    const unsigned sl_lo = L_P + (unsigned)pr * SLOT, sl_hi = L_W + (unsigned)pr * SLOT;
    const unsigned fo = WS_FLAG + (unsigned)(i * 16 + j) * 256u + pr * 8;

    if (w >= 4) {
        LANE_VARS;
        STAMP(0);
        const unsigned swz = (unsigned)((p8 ^ (q8 >> 1)) << 4);
        const unsigned voffC = q8 * 1024 + swz, voffC1 = voffC ^ 64u;
        const unsigned voffD = q8 * 1024 + (((p8 >> 1) ^ (q8 >> 1)) << 5) + ((p8 & 1) << 4);
        unsigned k8[8];
#pragma unroll
        for (int m = 0; m < 8; m++) { k8[m] = m * 8192; asm volatile("" : "+s"(k8[m])); }
        auto issue_w2h = [&](int v, int hm) __attribute__((always_inline)) {
            const int u = unit_of(v);
            const unsigned dst = ((v & 2) ? sl_hi : sl_lo) + (v & 1) * 8192;
            const v4u srd = make_srd((const unsigned char*)(u < 36 ? p.Cre : p.Cim) + (size_t)qc * 65536 + ((u - 28) & 7) * 128);
            if (hm & 1) dma4(srd, voffC, voffC1, k8[0], 7168, 14336, 21504, dst);
            if (hm & 2) dma4(srd, voffC, voffC1, k8[4], 32768 + 7168, 32768 + 14336, 32768 + 21504, dst + 4096);
        };
        auto issue_w2 = [&](int v) __attribute__((always_inline)) { issue_w2h(v, 3); };
        const unsigned fa = F_DONE + (lane & 3) * 4, fown = F_DONE + pr * 4;
        unsigned sink = 0;
        auto load_unit = [&](v4u (&r)[8], int v) __attribute__((always_inline)) {
            const v4u srd = make_srd((const unsigned char*)p.wd + (size_t)unit_of(v) * 65536 + qc * 128);
#pragma unroll
            for (int m = 0; m < 8; m++) asm volatile("buffer_load_dwordx4 %0, %1, %2, %3 offen" : "=v"(r[m]) : "v"(voffD), "s"(srd), "s"(k8[m]) : "memory");
        };
        auto load_half = [&](v4u (&r)[8], int v, int h) __attribute__((always_inline)) {
            const v4u srd = make_srd((const unsigned char*)p.wd + (size_t)unit_of(v) * 65536 + qc * 128);
#pragma unroll
            for (int m = 0; m < 8; m++) if ((m >> 2) == h) asm volatile("buffer_load_dwordx4 %0, %1, %2, %3 offen" : "=v"(r[m]) : "v"(voffD), "s"(srd), "s"(k8[m]) : "memory");
        };
        auto feeder = [&](v4u (&wr)[7][8], unsigned tg) __attribute__((always_inline)) {
#pragma unroll
            for (int u = 0; u < 7; u++) asm volatile("" : "+v"(wr[u][0]), "+v"(wr[u][1]), "+v"(wr[u][2]), "+v"(wr[u][3]), "+v"(wr[u][4]), "+v"(wr[u][5]), "+v"(wr[u][6]), "+v"(wr[u][7]));
            const unsigned ful2 = F_FUL2 + pr * 4;
            post(ful2, 2);
            STAMP(8);
            wait_ge(fa, N1 - 3 + pr);
            issue_w2(2); issue_w2(3);
            const unsigned l16 = lane * 16;
            if (heavy) {
                wait_ge(F_OUT + pr * 4, 1);
                const unsigned ao = WS_A2 + (unsigned)(i * 44 + 7 * kq + 4 + q) * 16384u + pr * 4096 + l16;
#pragma unroll
                for (int f = 0; f < 4; f++) __builtin_amdgcn_raw_buffer_store_b128(lds128(L_O + pr * 4096 + f * 1024 + l16), rws, ao + f * 1024, 0, 16);
                waitv<4>();
            } else waitv<0>();
            post(ful2, 4);
            sfor<4, 11>(LAMB(vv) {
                constexpr int v = decltype(vv)::value;
                if constexpr (v == 5) { waitv<0>(); if (heavy && lane == 0) __builtin_amdgcn_raw_buffer_store_b64(v2u{tg, ~tg}, rws, fo + 128, 0, 16); STAMP(9); }
                if constexpr (v == 7) {
                    const unsigned po = WS_FLAG + (unsigned)(i2 * 16 + 4 * pr + (lane & 3)) * 256u + 128 + (2 * (rb & 1) + ((lane >> 2) & 1)) * 8;
                    NOUNROLL for (int spin = 0; spin < SPIN_MAX; spin++) {
                        fence_c();
                        const v2u f = __builtin_amdgcn_raw_buffer_load_b64(rws, po, 0, 16);
                        if ((((unsigned)__builtin_amdgcn_ballot_w64(f.x == tg && f.y == ~tg)) & 0x77u) == 0x77u) break;
                        __builtin_amdgcn_s_sleep(1);
                    }
                    post(F_RDY + pr * 4, 2);
                    STAMP(10);
                }
                wait_ge(fown, N1 + v - 3);
#pragma unroll
                for (int m = 0; m < 8; m++) sts128(((v & 2) ? sl_hi : sl_lo) + l16 + (v & 1) * 8192 + m * 1024, wr[v - 4][m]);
                post(ful2, v + 1);
            });
            STAMP(11);
        };
        if (w < 6) {
            touch(sink, p.x, (128 * i + 8 * j + 4 * lw) * 1024 + (lane & 31) * 128);
            touch(sink, lw ? p.Bim : p.Bre, (32 * j + 2 * i) * 1024 + c * 128);
            bar_l();
            __builtin_amdgcn_s_sleep(SLEEP0);
            {
                const unsigned voffS = (8 * (q8 >> 2) + (q8 & 3)) * 1024 + swz;
                const v4u srd = make_srd((const unsigned char*)(lw ? p.Bim : p.Bre) + (size_t)(32 * j) * 1024);
                NOUNROLL for (int n = 0; n < 8; n++) {
                    const unsigned so = 128 * ((n + rot) & 7);
                    dma4(srd, voffS, voffS ^ 64u, so, so + 15360, so + 2048, so + 17408, L_W + (n >> 1) * SLOT + lw * 8192 + (n & 1) * 4096);
                }
            }
            v4u wr[7][8];
            const unsigned ffull = F_FULL + lw * 4;
            load_unit(wr[0], 4);
            waitv<32>(); post(ffull, 1);
            waitv<24>(); post(ffull, 2);
            waitv<8>(); post(ffull, 4);
            asm volatile("" ::"v"(sink));
            STAMP(1);
            const unsigned voffG = q8 * (FF * 2) + (((p8 >> 1) ^ ((q8 >> 1) & 1)) << 5) + ((p8 & 1) << 4), voffG1 = voffG ^ 64u;
            const unsigned char* gb = (const unsigned char*)(lw ? p.wu : p.wg) + (7 * kq + q) * 128;
            unsigned kg[8];
#pragma unroll
            for (int m = 0; m < 8; m++) { kg[m] = m * 8 * (FF * 2) - (m & 3) * 1024; asm volatile("" : "+s"(kg[m])); }
            auto issue_g = [&](int n) __attribute__((always_inline)) {
                const int t = n - 4;
                const v4u srd = make_srd(gb + (size_t)(((t + rot) & 7) * 64) * (FF * 2) + (t >> 3) * 512);
                const unsigned dst = L_W + (n & 3) * SLOT + lw * 8192;
                dma4(srd, voffG, voffG1, kg[0], kg[1], kg[2], kg[3], dst);
                dma4(srd, voffG, voffG1, kg[4], kg[5], kg[6], kg[7], dst + 4096);
            };
            sfor<4, 20>(LAMB(nn) {
                constexpr int n = decltype(nn)::value, e = n - 4;
                if (n < 12 || heavy) {
                    if (any_lt(peek(fa), n - 3)) { waitv<4>(); post(ffull, n); wait_ge(fa, n - 3); }
                    fence_c();
                    issue_g(n);
                    waitv<12>();
                    post(ffull, n);
                }
                if constexpr (n == 12) { if (!heavy) { waitv<4>(); post(ffull, 12); } }
                if constexpr (e < 4) issue_w2h(e >> 1, 1 << (e & 1)); else load_half(wr[(e >> 1) - 1], (e >> 1) + 3, e & 1);
            });
            waitv<4>();
            post(ffull, N1);
            waitv<0>();
            STAMP(7);
            feeder(wr, peek(L_TAG));
        } else {
            unsigned tag0, prm0, prm1, prm2;
            touch(sink, pc0, min(idx * 10 + 5 * lw + p8, (int)CODE_LINES - 1) * 128);
            const unsigned po = 2 * (32 * j + 8 * (lane >> 4) + 4 * ((lane >> 1) & 1) + ((lane >> 2) & 3));
            asm volatile("global_load_ushort %0, %4, %5\n\tglobal_load_ushort %1, %4, %6\n\tglobal_load_ushort %2, %4, %7\n\tglobal_load_dword %3, %8, %9 sc1"
                         : "=&v"(prm0), "=&v"(prm1), "=&v"(prm2), "=&v"(tag0) : "v"(po), "s"(p.lre), "s"(p.lim), "s"(p.ldt), "v"(WS_EPOCH), "s"(p.ws) : "memory");
            *(LAS unsigned*)(uintptr_t)(F_DONE + (lane & 31) * 4) = 0u;
            bar_l();
            sts128(L_SEL + lw * 1024 + lane * 16, make_sel(lane, lw));
            asm volatile("s_waitcnt vmcnt(0)" : "+v"(prm0), "+v"(prm1), "+v"(prm2), "+v"(tag0)::"memory");
            const unsigned tg = tag0 + 1u;
            const bool odd = lane & 1;
            const unsigned la = lane * 4;
            const float lr = bflo(prm0), li = bflo(prm1);
            float dt = __expf(bflo(prm2));
            dt = bflo(pk2(dt, dt));
            const float ex = __expf(dt * lr);
            const float fr = __builtin_amdgcn_fractf(dt * li * 0.15915494309189535f);
            const float ar = smul_t(ex, __builtin_amdgcn_cosf(fr)), ai = smul_t(ex, __builtin_amdgcn_sinf(fr));
            const cpx a1 = {ar, ai};
            const cpx a2 = cmul(a1, a1), a4 = cmul(a2, a2);
            cpx pw[8];
            pw[1] = cmul(a4, a4); pw[2] = cmul(pw[1], pw[1]); pw[4] = cmul(pw[2], pw[2]);
            pw[3] = cmul(pw[1], pw[2]); pw[5] = cmul(pw[1], pw[4]); pw[6] = cmul(pw[2], pw[4]); pw[7] = cmul(pw[3], pw[4]);
            const unsigned sg = odd ? 0u : 0x80000000u;
            auto sgn = [&](cpx v) { return cpx{v.x, __uint_as_float(__float_as_uint(v.y) ^ sg)}; };
            const cpx r64 = cmul(pw[4], pw[4]), r128 = cmul(r64, r64);
            cpx a64 = sgn(r64), a128 = sgn(r128), a256 = sgn(cmul(r128, r128));
#pragma unroll
            for (int s = 1; s < 8; s++) { pw[s] = sgn(pw[s]); asm volatile("" : "+v"(pw[s].x), "+v"(pw[s].y)); }
            asm volatile("" : "+v"(a64.x), "+v"(a64.y), "+v"(a128.x), "+v"(a128.y), "+v"(a256.x), "+v"(a256.y));
            float cst[7];
            {
                const float nr = ar - 1.0f, den = smul_t(__builtin_amdgcn_rcpf(sfma(lr, lr, smul(li, li))), 2.0f);
                cpx co = {smul(sfma(nr, lr, smul(ai, li)), den), smul(sfms(ai, lr, smul(nr, li)), den)};
                cst[0] = odd ? a1.y : a1.x; cst[1] = odd ? a4.y : a4.x;
#pragma unroll
                for (int t = 0; t < 5; t++) { cst[2 + t] = odd ? co.y : co.x; if (t < 4) co = cmul(co, a1); }
#pragma unroll
                for (int t = 0; t < 7; t++) asm volatile("" : "+v"(cst[t]));
            }
            touch(sink, lw ? p.wu : p.wg, (b * 56 + min(lane, 55)) * 128);
            touch(sink, p.wd, (b * 56 + 28 * lw + min(lane, 27)) * 128);
            touch(sink, lw ? p.Cim : p.Cre, (b * 16 + c) * 128);
            touch(sink, lw ? (const void*)p.ws : (const void*)p.out, lw ? min((unsigned)c * 0xc0000u + idx * 0x4000u, WS_END - 4) : (lane & 3) * 0x100000 + idx * 0x4000);
            if (lw == 0) {
#pragma unroll
                for (int t = 0; t < 7; t++) sts32f(L_CST + t * 256 + la, cst[t]);
                *(LAS unsigned*)(uintptr_t)L_TAG = tg;
            }
            post(F_COMM + lw * 4, 1);
            STAMP(1);
            waitv<0>();
            asm volatile("" ::"v"(sink));
            v4u wr[7][8];
            sfor<0, 3>(LAMB(uu) { constexpr int u = decltype(uu)::value; load_unit(wr[u], u + 4); waitv<8>(); });
            STAMP(2);
            wait_ge(fown, 1);
            issue_w2(0); issue_w2(1);
            STAMP(3);
            wait_ge(F_DONE + lw * 8 + (lane & 1) * 4, 4);
            STAMP(4);
            float P[8], h;
            {
                float e[8];
#pragma unroll
                for (int s = 0; s < 8; s++) e[s] = lds32f(L_SEG + (8 * lw + s) * 256 + la);
                h = e[0];
#pragma unroll
                for (int s = 1; s < 8; s++) { P[s] = h; h = lmad(pw[1], h, e[s]); }
            }
            float elo = 0;
            if (lw == 0) { sts32f(L_XCH + la, h); post(F_COMM, 2); }
            else {
                wait_ge(F_COMM, 2);
                elo = lds32f(L_XCH + la);
                __builtin_amdgcn_raw_buffer_store_b64(v2u{__float_as_uint(lmad(a64, elo, h)), tg}, rws, WS_GRAN + (j * 16 + i) * 512 + lane * 8, 0, 16);
            }
            STAMP(5);
            {
                const int top = i - 1 - lw;
                v2u gr[8];
                NOUNROLL for (int spin = 0; spin < SPIN_MAX; spin++) {
                    fence_c();
#pragma unroll
                    for (int n = 0; n < 8; n++) gr[n] = __builtin_amdgcn_raw_buffer_load_b64(rws, WS_GRAN + j * 8192 + lane * 8, ((top - 2 * n) & 15) * 512, 16);
                    unsigned long long bad = 0;
#pragma unroll
                    for (int n = 0; n < 8; n++) if (top - 2 * n >= 0) bad |= __builtin_amdgcn_ballot_w64(gr[n].y != tg);
                    if (bad == 0) break;
                }
                h = 0;
#pragma unroll
                for (int n = 7; n >= 0; n--) if (top - 2 * n >= 0) h = lmad(a256, h, __uint_as_float(gr[n].x));
            }
            STAMP(6);
            sts32f(L_XCH + 256 + lw * 256 + la, h);
            post(F_COMM + lw * 4, 3);
            wait_ge(F_COMM + (lw ^ 1) * 4, 3);
            const float ho = lds32f(L_XCH + 256 + (lw ^ 1) * 256 + la);
            const float C = lw ? lmad(a64, lmad(a128, h, ho), elo) : lmad(a128, ho, h);
            sts32f(L_HIN + (8 * lw) * 256 + la, C);
#pragma unroll
            for (int s = 1; s < 8; s++) sts32f(L_HIN + (8 * lw + s) * 256 + la, lmad(pw[s], C, P[s]));
            post(F_COMM + lw * 4, 4);
            sfor<3, 7>(LAMB(uu) { constexpr int u = decltype(uu)::value; wait_ge(fown, min(N1, 8 + u)); load_unit(wr[u], u + 4); });
            waitv<0>();
            STAMP(7);
            if (lw == 0) {
                const unsigned pf = WS_FLAG + (unsigned)(i2 * 16 + c) * 256u + (2 * (rb & 1) + (g & 1)) * 8;
                NOUNROLL for (int spin = 0; spin < SPIN_MAX; spin++) {
                    fence_c();
                    const v2u f = __builtin_amdgcn_raw_buffer_load_b64(rws, pf, 0, 16);
                    if (__builtin_amdgcn_ballot_w64(f.x != tg || f.y != ~tg) == 0) break;
                    __builtin_amdgcn_s_sleep(2);
                }
                post(F_RDY + (lane & 3) * 4, 1);
            }
            feeder(wr, tg);
        }
        return;
    }
    constexpr int PD = 4;
    unsigned stall = 0; (void)stall;
    v4u af[4][8];
    const unsigned char* a2b = p.ws + WS_A2 + (size_t)i2 * (44 * 16384) + (rb & 1) * 8192 + 4096;
    LANE_VARS;
    unsigned fdone = F_DONE + w * 4, l16 = lane * 16;
    asm volatile("" : "+v"(fdone), "+v"(l16));
    constexpr auto aoff = [](int f) { return ((f >> 1) & 1) * 4096 + ((f >> 2) & 1) * 2048 + (f & 1) * 1024 - 4096; };
    {
        v4u xa[16][2];
        STAMP(0);
        const unsigned ffl = F_FULL + (lane & 1) * 4;
        unsigned cb[2];
        cb[0] = (unsigned)c * 128u + (unsigned)((g ^ (c >> 1)) << 4); cb[1] = cb[0] ^ 64u;
        {
            const unsigned voffX = (8 * (q8 >> 2) + 4 * (q8 & 1) + ((q8 >> 1) & 1)) * 1024 + ((p8 ^ (q8 >> 1)) << 4), voffX1 = voffX ^ 64u;
            const v4u srd = make_srd((const unsigned char*)p.x + (size_t)(128 * i + 32 * w) * 1024 - 4096);
            auto issue_x = [&](int t) __attribute__((always_inline)) {
                const unsigned s0 = (t >> 3) * 2048 + ((t + rot) & 7) * 128, s1 = (t >> 3) * 2048 + ((t + 1 + rot) & 7) * 128 + 2048;
                dma4(srd, voffX, voffX1, s0 + 4096, s0 + 4096 + 15360, s1, s1 + 15360, sl_lo + (t & 7) * 2048);
            };
            issue_x(0); issue_x(2); issue_x(4); issue_x(6);
            bar_l();
            const v4u sel0 = make_sel(lane, 0), sel1 = make_sel(lane, 1);
            v4u ones = {0x3f803f80u, 0x3f803f80u, 0x3f803f80u, 0x3f803f80u};
            asm volatile("" : "+v"(ones));
            f32x4 sm[2], sq[2], nm4[2], ct[6];
            const unsigned xl0 = cb[0] + sl_lo, xl1 = cb[1] + sl_lo;
            auto stats = [&](int bb) __attribute__((always_inline)) {
                const float s01 = (lane & 1) ? sq[bb].y : sq[bb].x, s23 = (lane & 1) ? sq[bb].w : sq[bb].z;
                const float mean = -nm4[bb].x;
                const float rstd = __builtin_amdgcn_rsqf(fmaxf(((lane & 2) ? s23 : s01) * (1.0f / DM) - mean * mean, 0.0f) + 1e-5f);
                if (g == (c >> 2)) {
                    sts32f(L_RSTD + (32 * w + 8 * g + 4 * bb + (c & 3)) * 4, rstd);
                    if (q == 0) __builtin_amdgcn_raw_buffer_store_b64(v2u{__float_as_uint(rstd), __float_as_uint(mean)}, rws,
                                    WS_STAT + (i * 4 + kq) * 1024 + (32 * w + 8 * g + 4 * (c & 1) + 2 * bb + ((c >> 1) & 1)) * 8, 0, 16);
                }
            };
            auto centre = [&](auto kk) __attribute__((always_inline)) {
                constexpr int k = decltype(kk)::value, e = k - 4;
                if constexpr (k < 64) mma_c(ct[k % 6], (k & 1) ? sel1 : sel0, xa[(k >> 1) & 15][k >> 5], nm4[k >> 5]);
                if constexpr (e >= 0) {
                    xa[(e >> 1) & 15][e >> 5][2 * (e & 1)] = cvtv(ct[e % 6].x, ct[e % 6].y);
                    xa[(e >> 1) & 15][e >> 5][2 * (e & 1) + 1] = cvtv(ct[e % 6].z, ct[e % 6].w);
                }
                SB();
            };
            SB();
            sfor<0, 17>(LAMB(tt) {
                constexpr int t = decltype(tt)::value, u = t - 1;
                if constexpr (t < 16) {
                    constexpr int t5 = ((t + 1) & ~1) + 5, top = t5 < 7 ? 7 : t5 > 15 ? 15 : t5;
                    waitv<2 * (top - t)>();
                    xa[2 * (t & 7)][t >> 3] = lds128(xl0 + (t & 7) * 2048); xa[2 * (t & 7) + 1][t >> 3] = lds128(xl1 + (t & 7) * 2048);
                }
                SB();
                if constexpr (t == 9) { asm volatile("s_nop 3"); SB(); nm4[0] = sm[0] * (-1.0f / DM); asm volatile("s_nop 1" : "+v"(nm4[0])); SB(); }
                if constexpr (t >= 9) { centre(IC<4 * (t - 9)>{}); centre(IC<4 * (t - 9) + 1>{}); centre(IC<4 * (t - 9) + 2>{}); centre(IC<4 * (t - 9) + 3>{}); }
                if constexpr (u >= 0) {
                    mma<(u & 7) == 0>(sm[u >> 3], ones, xa[2 * (u & 7)][u >> 3]); mma<(u & 7) == 0>(sq[u >> 3], xa[2 * (u & 7)][u >> 3], xa[2 * (u & 7)][u >> 3]);
                    mma<false>(sm[u >> 3], ones, xa[2 * (u & 7) + 1][u >> 3]); mma<false>(sq[u >> 3], xa[2 * (u & 7) + 1][u >> 3], xa[2 * (u & 7) + 1][u >> 3]);
                }
                if constexpr ((t & 1) == 0 && t >= 2 && t + 6 < 16) issue_x(t + 6);
                SB();
            });
            STAMP(1);
            stats(0);
            SB();
            nm4[1] = sm[1] * (-1.0f / DM); asm volatile("s_nop 1" : "+v"(nm4[1]));
            SB();
            sfor<32, 64>(LAMB(kk) { centre(kk); });
            stats(1);
            SB();
            sfor<64, 68>(LAMB(kk) { centre(kk); });
        }
        STAMP(2);
        v4u yr[2], yi[2];
        f32x4 eX;
        unsigned tb[4];
        v4u bf[PD];
        {
            cb[0] += L_W; cb[1] += L_W;
            asm volatile("" : "+v"(cb[0]), "+v"(cb[1]));
            f32x4 acc[4][2];
            unsigned fl = 0; bool lack = false;
            {
                const int q4 = c >> 2, p4 = c & 3;
                const unsigned tb1 = L_W + (unsigned)(8 * g + q4) * 128u + (unsigned)((((q4 >> 1) & 1) | ((g & 1) << 1)) << 5) + p4 * 8;
#pragma unroll
                for (int t = 0; t < 4; t++) { tb[t] = tb1 ^ (unsigned)(t << 5); asm volatile("" : "+v"(tb[t])); }
            }
            f32x4 ca, c4, co, rs0, rs1;
            wait_ge(F_FULL + (lane & 3) * 4, 1);
            STAMP(3);
            sfor<-PD, 64>(LAMB(nn) {
                constexpr int n = decltype(nn)::value, m = n + PD;
                v4u cur;
                if constexpr (n >= 0) { cur = bf[n % PD]; mma<(n < 4)>(acc[n & 3][0], xa[n >> 2][0], cur); SB(); }
                if constexpr (m > 0 && (m & 15) == 0) need_full(lack, ffl, (m >> 4) + 1);
                if constexpr (m < 64) bf[m % PD] = lds128(cb[(m >> 2) & 1] + wslot(m >> 4) + (((m >> 1) & 1) * 4 + ((m >> 3) & 1) * 2 + (m & 1)) * 2048);
                else { const unsigned a = tb[(m >> 1) & 3] + wslot(0) + (m & 1) * 8192; const v2u lo = ldstr(a), hi = ldstr(a + 512); bf[m % PD] = v4u{lo.x, lo.y, hi.x, hi.y}; }
                SB();
                if constexpr (n >= 0) { mma<(n < 4)>(acc[n & 3][1], xa[n >> 2][1], cur); SB(); }
                if constexpr ((m & 15) == 10) fl = peek(ffl);
                if constexpr ((m & 15) == 14) lack = any_lt(fl, (m >> 4) + 2);
                if constexpr ((m & 15) == 15 && m < 63) post(fdone, (m >> 4) + 1);
                if constexpr (m == 52) ca = lds128f(L_CST + c * 16);
                if constexpr (m == 53) c4 = lds128f(L_CST + 256 + c * 16);
                if constexpr (m == 54) co = lds128f(L_CST + 512 + c * 16);
                if constexpr (m == 55) rs0 = lds128f(L_RSTD + (32 * w + 8 * g) * 4);
                if constexpr (m == 56) rs1 = lds128f(L_RSTD + (32 * w + 8 * g + 4) * 4);
                SB();
            });
            mfma_done();
            STAMP(4); VAL(24, stall); stall = 0;
            f32x4 ev;
            f32x2 sr[2][4], si[2][4];
#pragma unroll
            for (int st = 0; st < 2; st++) {
                const f32x4 r0 = acc[st][0] * rs0, r1 = acc[st][1] * rs1, i0 = acc[2 + st][0] * rs0, i1 = acc[2 + st][1] * rs1;
                sr[st][0] = lo2(r0); sr[st][1] = hi2(r0); sr[st][2] = lo2(r1); sr[st][3] = hi2(r1);
                si[st][0] = lo2(i0); si[st][1] = hi2(i0); si[st][2] = lo2(i1); si[st][3] = hi2(i1);
            }
            const f32x2 a1[2] = {lo2(ca), hi2(ca)}, cf[2] = {lo2(co), hi2(co)};
#pragma unroll
            for (int t = 1; t < 4; t++) {
                f32x2 tr[2], ti[2];
#pragma unroll
                for (int st = 0; st < 2; st++) { tr[st] = bfma<1, 1>(a1[st], si[st][t - 1], sr[st][t]); ti[st] = bfma<1, 0>(a1[st], sr[st][t - 1], si[st][t]); }
#pragma unroll
                for (int st = 0; st < 2; st++) { sr[st][t] = bfma<0, 0>(a1[st], sr[st][t - 1], tr[st]); si[st][t] = bfma<0, 0>(a1[st], si[st][t - 1], ti[st]); }
            }
#pragma unroll
            for (int st = 0; st < 2; st++) {
                ev[2 * st] = sfma(c4[2 * st], sr[st][3].x, sfnma(c4[2 * st + 1], si[st][3].x, sr[st][3].y));
                ev[2 * st + 1] = sfma(c4[2 * st], si[st][3].x, sfma(c4[2 * st + 1], sr[st][3].x, si[st][3].y));
                eX[2 * st] = sr[st][3].x; eX[2 * st + 1] = si[st][3].x;
            }
            sts128f(L_SEG + w * 1024 + l16, ev);
            post(fdone, 4);
            SB();
#pragma unroll
            for (int st = 0; st < 2; st++)
#pragma unroll
                for (int t = 0; t < 4; t++) {
                    yr[st][t] = pk2(bfma<0, 0>(cf[st], sr[st][t], bmul<1, 1>(cf[st], si[st][t])));
                    yi[st][t] = pk2(bfma<0, 1>(cf[st], si[st][t], bmul<1, 1>(cf[st], sr[st][t])));
                }
            asm volatile("" : "+v"(yr[0]), "+v"(yr[1]), "+v"(yi[0]), "+v"(yi[1]), "+v"(eX));
        }
        STAMP(5);
        auto flagA = [&]() __attribute__((always_inline)) {
            const unsigned tg = peek(L_TAG);
            if (l16 == 0) __builtin_amdgcn_raw_buffer_store_b64(v2u{tg, ~tg}, rws, fo, 0, 16);
        };
        f32x4 acc[4][2][2];
        f32x2 r12;
        unsigned fc = 0;
        auto gpass = [&](auto pp) __attribute__((always_inline)) {
            constexpr int pass = decltype(pp)::value;
            unsigned fl = 0; bool lack = false;
            sfor<0, 128>(LAMB(nn) {
                constexpr int n = decltype(nn)::value, m = n + PD;
                constexpr bool pre = pass == 0 || m < 128;
                constexpr unsigned off = wslot(m >> 4) + (m & 1) * 8192 + ((m >> 3) & 1) * 4096;
                const v4u cur = bf[n % PD];
                mma<(n < 8)>(acc[(n >> 1) & 3][n & 1][0], cur, xa[n >> 3][0]);
                SB();
                if constexpr ((m & 15) == 0 && pre) need_full(lack, ffl, 5 + 8 * pass + (m >> 4));
                v2u lo, hi;
                if constexpr (pre) lo = ldstr(tb[(m >> 1) & 3] + off);
                if constexpr ((m & 15) == 10 && (pass == 0 || m < 112)) fl = peek(ffl);
                if constexpr ((m & 15) == 15 && (pass == 0 || m < 112)) lack = any_lt(fl, m == 127 ? N1 - 7 : 6 + 8 * pass + (m >> 4));
                if constexpr (m > 16 && (m & 15) == 1) post(fdone, 4 + 8 * pass + (m >> 4));
                if constexpr (m == 68 && pass == 1) { waitv<0>(); flagA(); }
                if constexpr (m == 92 && pass == 1) fc = peek(fdone + (F_RDY - F_DONE));
                if constexpr (m == 98 && pass == 1) need_full(any_lt(fc, 1), fdone + (F_RDY - F_DONE), 1);
                if constexpr (m >= 100 && pass == 1 && (m & 1) == 0) { constexpr int f = (m - 100) >> 1; ldA<aoff(f)>(af[f >> 3][f & 7], l16, a2b + (size_t)unit_of(f >> 3) * 16384); }
                if constexpr (m == 124 && pass == 0) fc = peek(ffl + (F_COMM - F_FULL));
                if constexpr (m == 125 && pass == 0) { const unsigned ra = L_RSTD + w * 128 + (((l16 >> 1) & 0x60) | ((l16 >> 2) & 0xc)); r12 = f32x2{lds32f(ra), lds32f(ra + 16)}; }
                SB();
                mma<(n < 8)>(acc[(n >> 1) & 3][n & 1][1], cur, xa[n >> 3][1]);
                SB();
                if constexpr (pre) { hi = ldstr(tb[(m >> 1) & 3] + off + 512); bf[n % PD] = v4u{lo.x, lo.y, hi.x, hi.y}; }
                SB();
            });
        };
        auto gated = [&](auto ff) __attribute__((always_inline)) {
            constexpr int uu = decltype(ff)::value >> 1, bb = decltype(ff)::value & 1;
            const f32x4 h0 = acc[2 * uu][0][bb] * acc[2 * uu][1][bb], h1 = acc[2 * uu + 1][0][bb] * acc[2 * uu + 1][1][bb];
            return v4u{pk2(bmul<bb, 0>(r12, lo2(h0))), pk2(bmul<bb, 0>(r12, hi2(h0))), pk2(bmul<bb, 0>(r12, lo2(h1))), pk2(bmul<bb, 0>(r12, hi2(h1)))};
        };
        gpass(IC<0>{});
        {
            r12 = r12 * r12;
            const unsigned ao = WS_A2 + (unsigned)i * (44u * 16384u) + w * 4096 + l16;
            need_full(any_lt(fc, 4), ffl + (F_COMM - F_FULL), 4);
            mfma_done();
            STAMP(6); VAL(25, stall); stall = 0;
            const unsigned go = ao + (7 * kq + q) * 16384;
            sfor<0, 2>(LAMB(ff) { __builtin_amdgcn_raw_buffer_store_b128(gated(ff), rws, go + decltype(ff)::value * 1024, 0, 16); });
            SB();
            const unsigned cl = L_CST + (l16 & 0xf0);
            const v4u sel[2] = {lds128(L_SEL + l16), lds128(L_SEL + 1024 + l16)};
            const f32x4 hin = lds128f(L_HIN + w * 1024 + l16), c4 = lds128f(cl + 256);
            f32x4 cw[4];
#pragma unroll
            for (int t = 0; t < 4; t++) cw[t] = lds128f(cl + (3 + t) * 256);
            SB();
            sfor<2, 4>(LAMB(ff) { __builtin_amdgcn_raw_buffer_store_b128(gated(ff), rws, go + decltype(ff)::value * 1024, 0, 16); });
            SB();
            const f32x4 z = {0, 0, 0, 0};
            f32x4 zr[2][2], zi[2][2];
#pragma unroll
            for (int st = 0; st < 2; st++) {
#pragma unroll
                for (int bb = 0; bb < 2; bb++) { zr[st][bb] = mfma(yr[st], sel[bb], z); zi[st][bb] = mfma(yi[st], sel[bb], z); }
                const float hx = hin[2 * st], hy = hin[2 * st + 1];
                const f32x2 hr = {hx, sfma(c4[2 * st], hx, sfnma(c4[2 * st + 1], hy, eX[2 * st]))};
                const f32x2 hi = {hy, sfma(c4[2 * st], hy, sfma(c4[2 * st + 1], hx, eX[2 * st + 1]))};
                v4u fr, fi;
#pragma unroll
                for (int t = 0; t < 4; t++) {
                    const f32x2 wv = st ? hi2(cw[t]) : lo2(cw[t]);
                    fr[t] = pk2(bfma<0, 0>(wv, hr, bmul<1, 1>(wv, hi)));
                    fi[t] = pk2(bfma<0, 1>(wv, hi, bmul<1, 1>(wv, hr)));
                }
#pragma unroll
                for (int bb = 0; bb < 2; bb++) { zr[st][bb] = mfma(fr, sel[bb], zr[st][bb]); zi[st][bb] = mfma(fi, sel[bb], zi[st][bb]); }
            }
            const unsigned so = ao + (28 + (j >> 1)) * 16384 + (j & 1) * 2048;
#pragma unroll
            for (int bb = 0; bb < 2; bb++) {
                __builtin_amdgcn_raw_buffer_store_b128(pk8(zr[0][bb], zr[1][bb]), rws, so + bb * 1024, 0, 16);
                __builtin_amdgcn_raw_buffer_store_b128(pk8(zi[0][bb], zi[1][bb]), rws, so + 8 * 16384 + bb * 1024, 0, 16);
            }
            SB();
            STAMP(7);
        }
        if (heavy) {
            gpass(IC<1>{});
            mfma_done();
            STAMP(8); VAL(26, stall); stall = 0;
            sfor<0, 4>(LAMB(ff) { sts128(L_O + w * 4096 + decltype(ff)::value * 1024 + l16, gated(ff)); });
            post(fdone + (F_OUT - F_DONE), 1);
        } else {
            waitv<0>(); flagA();
            wait_ge(fdone + (F_RDY - F_DONE), 1);
            sfor<0, 16>(LAMB(ff) { constexpr int f = decltype(ff)::value; ldA<aoff(f)>(af[f >> 3][f & 7], l16, a2b + (size_t)unit_of(f >> 3) * 16384); });
#pragma unroll
            for (int u = 0; u < 2; u++) asm volatile("s_waitcnt vmcnt(0)" : "+v"(af[u][0]), "+v"(af[u][1]), "+v"(af[u][2]), "+v"(af[u][3]), "+v"(af[u][4]), "+v"(af[u][5]), "+v"(af[u][6]), "+v"(af[u][7])::"memory");
        }
    }
    STAMP(9);
    v2u xv[4], dv;
    f32x2 stv[4];
    {
        LANE_VARS;
        f32x4 acc[4][4];
        constexpr int PW = 3;
        const unsigned ful2 = fdone + (F_FUL2 - F_DONE);
        const unsigned cb0 = (unsigned)c * 128u + (unsigned)((g ^ (c >> 1)) << 4);
        const unsigned tb2 = (unsigned)(4 * g + (c >> 2)) * 128u + (unsigned)((2 * (g & 1) + (c >> 3)) << 5) + (c & 3) * 8;
        unsigned cbs[2][2], tbs[2][4], ra[2];
#pragma unroll
        for (int k = 0; k < 2; k++) { cbs[0][k] = (cb0 ^ (unsigned)(k << 6)) + sl_lo; asm volatile("" : "+v"(cbs[0][k])); }
        v4u bf[PW];
        unsigned fl = 0; bool lack = false;
        wait_ge(ful2, 1);
        constexpr auto fv = [](int n) { return n < 72 ? n >> 3 : 9 + ((n >> 1) & 1); };
        constexpr auto fk = [](int n) { return n < 72 ? (n >> 2) & 1 : n & 1; };
        constexpr auto fd = [](int n) { return n < 72 ? n & 3 : (n - 72) >> 2; };
        constexpr auto top = [](int B) { return B < 3 ? 3 + 2 * B : (B + 6 < 21 ? B + 6 : 21); };
        sfor<-PW, 92>(LAMB(nn) {
            constexpr int n = decltype(nn)::value, m = n + PW;
            constexpr bool run = n >= 0 && n < 88;
            constexpr int v = fv(n), ks = fk(n), dt = fd(n), G = 2 * v + ks;
            constexpr int mv = fv(m), mk = fk(m), md = fd(m);
            auto lda = [&](auto gg) __attribute__((always_inline)) {
                constexpr int g2 = decltype(gg)::value >> 2, f = decltype(gg)::value & 7;
                if constexpr (g2 < 8) ldA<aoff(f)>(af[(g2 >> 1) & 3][f], l16, a2b + (size_t)unit_of(g2 >> 1) * 16384);
                else if constexpr (g2 < 22) reldA<aoff(f)>(af[(g2 >> 1) & 3][f], l16, a2b + (size_t)unit_of(g2 >> 1) * 16384);
            };
            auto put = [&](auto tt) __attribute__((always_inline)) {
                constexpr int tf = decltype(tt)::value, pd = (n - 76) >> 2;
                sts128f((ra[tf >> 1] ^ (unsigned)(pd << 6)) + (tf & 1) * 512, acc[pd][tf]);
            };
            v4u cur; v2u lo, hi;
            if constexpr (run) {
                if constexpr (dt == 0) {
                    asm volatile("s_waitcnt vmcnt(%4)" : "+v"(af[v & 3][4 * ks]), "+v"(af[v & 3][4 * ks + 1]), "+v"(af[v & 3][4 * ks + 2]), "+v"(af[v & 3][4 * ks + 3]) : "n"((top(G) - G) * 4) : "memory");
                    if constexpr (n == 36) wait_ge(fdone + (F_RDY - F_DONE), 2);
                }
                cur = bf[n % PW];
                mma<(n < 4)>(acc[dt][0], cur, af[v & 3][4 * ks]);
                SB();
            }
            if constexpr (n == 88) mfma_done();
            if constexpr (m > 0 && (m & 7) == 0 && m <= 72) need_full(lack, ful2, m == 72 ? 11 : mv + 1);
            if constexpr (m < 32) bf[m % PW] = lds128(cbs[(mv >> 1) & 1][mk] + (mv & 1) * 8192 + md * 2048);
            else if constexpr (m < 88) lo = ldstr(tbs[(mv >> 1) & 1][md] + (mv & 1) * 8192 + mk * 4096);
            SB();
            if constexpr (run) { mma<(n < 4)>(acc[dt][1], cur, af[v & 3][4 * ks + 1]); SB(); }
            if constexpr (m >= 32 && m < 88) { hi = ldstr(tbs[(mv >> 1) & 1][md] + (mv & 1) * 8192 + mk * 4096 + 2048); bf[m % PW] = v4u{lo.x, lo.y, hi.x, hi.y}; }
            if constexpr (n >= 0 && n < 12) lda(IC<16 + 2 * n>{});
            SB();
            if constexpr (run) { mma<(n < 4)>(acc[dt][2], cur, af[v & 3][4 * ks + 2]); SB(); }
            if constexpr (n >= 12 && n < 72) lda(IC<n + 28>{});
            SB();
            if constexpr (run) { mma<(n < 4)>(acc[dt][3], cur, af[v & 3][4 * ks + 3]); SB(); }
            if constexpr (n >= 0 && n < 12) lda(IC<17 + 2 * n>{});
            if constexpr (n >= 76) put(IC<(n & 3)>{});
            if constexpr (m == 9 || m == 10) { cbs[1][m - 9] = (cb0 ^ (unsigned)((m - 9) << 6)) + sl_hi; asm volatile("" : "+v"(cbs[1][m - 9])); }
            if constexpr (m >= 24 && m < 28) { tbs[0][md] = (tb2 ^ (unsigned)(md << 5)) + sl_lo; asm volatile("" : "+v"(tbs[0][md])); }
            if constexpr (m >= 40 && m < 44) { tbs[1][md] = (tb2 ^ (unsigned)(md << 5)) + sl_hi; asm volatile("" : "+v"(tbs[1][md])); }
            if constexpr ((m & 7) == 4 && m < 72) fl = peek(ful2);
            if constexpr ((m & 7) == 7 && m < 72) lack = any_lt(fl, m == 71 ? 11 : mv + 2);
            if constexpr ((m & 7) == 7 && m < 56) post(fdone, N1 + mv + 1);
            if constexpr (n == 62) {
                const int tok = 64 * rb + 16 * w + g, d = 64 * qc + 4 * c;
#pragma unroll
                for (int e = 0; e < 4; e++) {
                    xv[e] = *(const GAS v2u*)(p.x + (size_t)(tok + 4 * e) * DM + d);
                    stv[e] = __builtin_bit_cast(f32x2, __builtin_amdgcn_raw_buffer_load_b64(rws, WS_STAT + (i2 * 4 + w) * 1024 + ((tok & 127) + 4 * e) * 8, 0, 16));
                }
                dv = *(const GAS v2u*)(p.Dsk + d);
            }
            if constexpr (n == 70) {
                const unsigned r0 = (8 * (c >> 2) + 4 * (c & 1) + ((c >> 1) & 1)) * 256 + ((g ^ c) << 4);
                ra[0] = r0 + sl_lo; ra[1] = r0 + sl_hi + 8192; asm volatile("" : "+v"(ra[0]), "+v"(ra[1]));
            }
            if constexpr (n == 31) { STAMP(10); VAL(28, stall); stall = 0; }
            if constexpr (n == 87) { STAMP(11); VAL(29, stall); }
            SB();
        });
    }
    {
        LANE_VARS;
        f32x4 o[4];
        const f32x4 df = {bflo(dv.x), bfhi(dv.x), bflo(dv.y), bfhi(dv.y)};
#pragma unroll
        for (int e = 0; e < 4; e++) {
            const f32x2 rs = dup(stv[e].x), mn = dup(stv[e].y);
            const f32x4 xf = {bflo(xv[e].x), bfhi(xv[e].x), bflo(xv[e].y), bfhi(xv[e].y)};
            o[e] = xf + (xf - f32x4{mn.x, mn.y, mn.x, mn.y}) * f32x4{rs.x, rs.y, rs.x, rs.y} * df;
            asm volatile("" : "+v"(o[e]));
        }
        unsigned rd[4];
#pragma unroll
        for (int e = 0; e < 4; e++) {
            const int row = 16 * (w & 1) + 4 * e + g;
            rd[e] = ((w & 2) ? L_W + 8192 : L_P) + row * 256 + ((c ^ (4 * (row >> 3) + 2 * (row & 1) + ((row >> 2) & 1))) << 4);
            asm volatile("" : "+v"(rd[e]));
        }
        GAS float* op = (GAS float*)p.out + (size_t)(64 * rb + 16 * w + g) * DM + 64 * qc + 4 * c;
        asm volatile("" : "+v"(op));
        STAMP(12);
        bar_l();
        STAMP(13);
        f32x4 t[4][4];
#pragma unroll
        for (int e = 0; e < 4; e++)
#pragma unroll
            for (int s = 0; s < 4; s++) t[e][s] = lds128f(rd[e] + s * SLOT);
        SB();
#pragma unroll
        for (int e = 0; e < 4; e++) {
            const f32x4 r = (o[e] + t[e][0]) + (t[e][1] + t[e][2]) + t[e][3];
            asm volatile("global_store_dwordx4 %0, %1, off " OUT_POL "\n\ts_nop 1" ::"v"(op + e * 4 * DM), "v"(r) : "memory");
        }
        if (b == 255 && w == 0 && lane == 0) __builtin_amdgcn_raw_buffer_store_b32(peek(L_TAG), rws, WS_EPOCH, 0, 16);
    }
    STAMP(14);
}

extern "C" void kernel_launch(void* const* d_in, const int* in_sizes, int n_in, void* d_out, int out_size, void* d_ws, size_t ws_size,
                              hipStream_t stream) {
    Params p;
    p.x = (const u16*)d_in[0]; p.lre = (const u16*)d_in[1]; p.lim = (const u16*)d_in[2];
    p.Bre = (const u16*)d_in[3]; p.Bim = (const u16*)d_in[4]; p.Cre = (const u16*)d_in[5]; p.Cim = (const u16*)d_in[6];
    p.ldt = (const u16*)d_in[7]; p.Dsk = (const u16*)d_in[8];
    p.wg = (const u16*)d_in[10]; p.wu = (const u16*)d_in[11]; p.wd = (const u16*)d_in[12];
    p.out = (float*)d_out; p.ws = (unsigned char*)d_ws;
    (void)hipFuncSetAttribute((const void*)fused, hipFuncAttributeMaxDynamicSharedMemorySize, L_TOTAL);
    fused<<<256, 512, L_TOTAL, stream>>>(p);
}
```

```cpp
#include <hip/hip_runtime.h>
#include <stdint.h>

typedef unsigned short u16;
typedef unsigned v4u __attribute__((ext_vector_type(4)));
typedef unsigned v2u __attribute__((ext_vector_type(2)));
typedef short s16x4 __attribute__((ext_vector_type(4)));
typedef __bf16 bf16x2 __attribute__((ext_vector_type(2)));
typedef __bf16 bf16x8 __attribute__((ext_vector_type(8)));
typedef float f32x2 __attribute__((ext_vector_type(2)));
typedef float f32x4 __attribute__((ext_vector_type(4)));
#define GAS __attribute__((address_space(1)))
#define LAS __attribute__((address_space(3)))
#define DEVI __device__ __forceinline__
#define NOUNROLL _Pragma("nounroll")

#define SPIN_SLEEP 0
constexpr int DM = 512, FF = 1792;
constexpr unsigned WS_A2 = 0;
constexpr unsigned WS_XB = 16u * 44u * 16384u;
constexpr unsigned WS_GRAN = WS_XB + 256u * 36864u;
constexpr unsigned WS_FLAG = WS_GRAN + 16u * 16u * 512u;
constexpr unsigned WS_EPOCH = WS_FLAG + 1024u * 128u;
constexpr unsigned WS_END = WS_EPOCH + 256u;
constexpr unsigned SLOT = 16384;
constexpr unsigned L_RSTD = 0, L_MEAN = 512;
constexpr unsigned F_DONE = 1024, F_SENT = F_DONE + 16, F_FULL = F_DONE + 32, F_COMM = F_DONE + 40, F_RDY = F_DONE + 48, F_OUT = F_DONE + 64, F_XI = F_DONE + 80, F_OFREE = F_DONE + 96, F_RDB = F_DONE + 112, L_TAG = F_DONE + 128;
constexpr unsigned L_CST = 1536;
constexpr unsigned L_XCH = L_CST + 7 * 256;
constexpr unsigned L_SEG = 4096, L_HIN = 8192, L_SEL = 12288;
constexpr unsigned L_P = SLOT, L_O = 5 * SLOT, L_W = 6 * SLOT, L_TOTAL = 10 * SLOT;
constexpr int SPIN_MAX = 1 << 18;
constexpr int LSPIN_MAX = 1 << 22;

struct Params {
    const u16 *x, *lre, *lim, *Bre, *Bim, *Cre, *Cim, *ldt, *Dsk, *wg, *wu, *wd;
    float* out;
    unsigned char* ws;
};

DEVI float bf2f(u16 v) { return __uint_as_float(((unsigned)v) << 16); }
DEVI float bflo(unsigned v) { return __uint_as_float(v << 16); }
DEVI float bfhi(unsigned v) { return __uint_as_float(v & 0xffff0000u); }
DEVI unsigned pk2(float a, float b) { f32x2 p = {a, b}; return __builtin_bit_cast(unsigned, __builtin_convertvector(p, bf16x2)); }
DEVI unsigned pk2(f32x2 p) { return __builtin_bit_cast(unsigned, __builtin_convertvector(p, bf16x2)); }
DEVI v4u pk8(f32x4 a, f32x4 b) { return v4u{pk2(a.x, a.y), pk2(a.z, a.w), pk2(b.x, b.y), pk2(b.z, b.w)}; }
DEVI float dot2(unsigned a, unsigned b, float c) { return __builtin_amdgcn_fdot2_f32_bf16(__builtin_bit_cast(bf16x2, a), __builtin_bit_cast(bf16x2, b), c, false); }
template <int CTRL> DEVI float dppf(float v) { return __builtin_bit_cast(float, __builtin_amdgcn_update_dpp(0, __builtin_bit_cast(int, v), CTRL, 0xf, 0xf, true)); }
DEVI f32x4 mfma(v4u a, v4u b, f32x4 c) { return __builtin_amdgcn_mfma_f32_16x16x32_bf16(__builtin_bit_cast(bf16x8, a), __builtin_bit_cast(bf16x8, b), c, 0, 0, 0); }
template <bool FIRST> DEVI void mma(f32x4& c, const v4u& a, const v4u& b) {
    if constexpr (FIRST) asm volatile("v_mfma_f32_16x16x32_bf16 %0, %1, %2, 0" : "=&v"(c) : "v"(a), "v"(b));
    else asm volatile("v_mfma_f32_16x16x32_bf16 %0, %1, %2, %0" : "+v"(c) : "v"(a), "v"(b));
}
DEVI void mfma_done() { __builtin_amdgcn_sched_barrier(0); asm volatile("s_nop 7"); __builtin_amdgcn_sched_barrier(0); }
DEVI v4u lds128(unsigned a) { return *(LAS v4u*)(uintptr_t)a; }
DEVI f32x4 lds128f(unsigned a) { return *(LAS f32x4*)(uintptr_t)a; }
DEVI float lds32f(unsigned a) { return *(LAS float*)(uintptr_t)a; }
DEVI void sts32f(unsigned a, float v) { *(LAS float*)(uintptr_t)a = v; }
DEVI void sts128(unsigned a, v4u v) { *(LAS v4u*)(uintptr_t)a = v; }
DEVI void sts128f(unsigned a, f32x4 v) { *(LAS f32x4*)(uintptr_t)a = v; }
DEVI v2u ldstr(unsigned a) { return __builtin_bit_cast(v2u, __builtin_amdgcn_ds_read_tr16_b64_v4i16((LAS s16x4*)(uintptr_t)a)); }
DEVI void fence_c() { asm volatile("" ::: "memory"); }
DEVI void bar_l() { asm volatile("s_waitcnt lgkmcnt(0)\n\ts_barrier" ::: "memory"); }
template <int N> DEVI void waitv() { asm volatile("s_waitcnt vmcnt(%0)" ::"n"(N) : "memory"); }
DEVI void dma16(const void* sbase, unsigned voff, unsigned ldst) {
    asm volatile("s_mov_b32 m0, %2\n\ts_nop 0\n\tglobal_load_lds_dwordx4 %0, %1" ::"v"(voff), "s"(sbase), "s"(ldst) : "memory");
}
DEVI void post(unsigned a, unsigned v) { fence_c(); *(LAS unsigned*)(uintptr_t)a = v; fence_c(); }
DEVI unsigned peek(unsigned a) { fence_c(); return *(LAS unsigned*)(uintptr_t)a; }
DEVI bool any_lt(unsigned v, unsigned need) { return __builtin_amdgcn_ballot_w64(v < need) != 0; }
DEVI void wait_ge(unsigned a, unsigned need) {
    int s = LSPIN_MAX;
    NOUNROLL while (any_lt(peek(a), need)) { asm volatile("s_sub_i32 %0, %0, 1" : "+s"(s) :: "scc"); if (s < 0) break; if (SPIN_SLEEP) __builtin_amdgcn_s_sleep(SPIN_SLEEP); }
    fence_c();
}
template <int V> DEVI void postm(unsigned a, int lane) {
    asm volatile("s_mov_b64 exec, %2\n\tds_max_u32 %0, %1\n\ts_mov_b64 exec, -1" ::"v"(a), "v"(lane), "n"(1 << V) : "memory");
}
DEVI unsigned vm_outstanding() {
    unsigned v; asm volatile("s_getreg_b32 %0, hwreg(HW_REG_IB_STS)" : "=s"(v)::"memory");
    return (v & 15u) | ((v >> 18) & 0x30u);
}
DEVI void postv(unsigned a, int lane, unsigned v) {
    asm volatile("s_lshl_b64 exec, 1, %2\n\tds_max_u32 %0, %1\n\ts_mov_b64 exec, -1" ::"v"(a), "v"(lane), "s"(v) : "memory", "scc");
}
template <int V> DEVI void postc(unsigned a, int lane) {
    asm volatile("s_mov_b64 exec, %2\n\tds_write_b32 %0, %1\n\ts_mov_b64 exec, -1" ::"v"(a), "v"(lane), "n"(1 << V) : "memory");
}
DEVI void need_full(bool lack, unsigned a, unsigned need) { if (__builtin_expect(lack, 0)) wait_ge(a, need); fence_c(); }
#define VAL(id, v)
DEVI float sfma(float a, float b, float c) { float d; asm("v_fma_f32 %0, %1, %2, %3" : "=v"(d) : "v"(a), "v"(b), "v"(c)); return d; }
DEVI float sfms(float a, float b, float c) { float d; asm("v_fma_f32 %0, %1, %2, -%3" : "=v"(d) : "v"(a), "v"(b), "v"(c)); return d; }
DEVI float sfnma(float a, float b, float c) { float d; asm("v_fma_f32 %0, -%1, %2, %3" : "=v"(d) : "v"(a), "v"(b), "v"(c)); return d; }
DEVI float sadd(float a, float b) { float d; asm("v_add_f32 %0, %1, %2" : "=v"(d) : "v"(a), "v"(b)); return d; }
DEVI float smul(float a, float b) { float d; asm("v_mul_f32 %0, %1, %2" : "=v"(d) : "v"(a), "v"(b)); return d; }
DEVI float smul_t(float a, float b) { float d; asm("s_nop 0\n\tv_mul_f32 %0, %1, %2" : "=v"(d) : "v"(a), "v"(b)); return d; }
struct cpx { float x, y; };
DEVI cpx cmul(cpx a, cpx b) { return cpx{sfms(a.x, b.x, smul(a.y, b.y)), sfma(a.x, b.y, smul(a.y, b.x))}; }
DEVI float lmad(cpx a, float h, float u) {
    float d;
    asm("v_fma_f32 %0, %1, %3, %4\n\ts_nop 0\n\tv_fmac_f32_dpp %0, %3, %2 quad_perm:[1,0,3,2] row_mask:0xf bank_mask:0xf" : "=&v"(d) : "v"(a.x), "v"(a.y), "v"(h), "v"(u));
    return d;
}
DEVI f32x2 dup(float v) { f32x2 d = {v, v}; asm("" : "+v"(d)); return d; }
DEVI f32x2 pfma(f32x2 a, f32x2 b, f32x2 c) { return __builtin_elementwise_fma(a, b, c); }
DEVI f32x2 lo2(f32x4 v) { return f32x2{v.x, v.y}; }
DEVI f32x2 hi2(f32x4 v) { return f32x2{v.z, v.w}; }
#define SB() __builtin_amdgcn_sched_barrier(0)
template <int V> struct IC { static constexpr int value = V; };
template <int B, int E, class F> DEVI void sfor(F&& f) { if constexpr (B < E) { f(IC<B>{}); sfor<B + 1, E>(f); } }
#define LAMB(nn) [&](auto nn) __attribute__((always_inline))
#define STAMP(id)
#define STAMP_INIT()
#define STAMP_DUMP()
DEVI int fresh_lane() { int l; asm volatile("v_mbcnt_lo_u32_b32 %0, -1, 0\n\tv_mbcnt_hi_u32_b32 %0, -1, %0" : "=v"(l)); return l; }
DEVI v4u make_sel(int lane, int bb) {
    const int c = lane & 15, tk = 8 * (c >> 2) + 4 * bb + (c & 3) - 8 * (lane >> 4);
    v4u s;
#pragma unroll
    for (int e = 0; e < 4; e++) s[e] = (tk == 2 * e ? 0x3f80u : 0u) | (tk == 2 * e + 1 ? 0x3f800000u : 0u);
    return s;
}
#define LANE_VARS const int lane = fresh_lane(), c = lane & 15, g = lane >> 4, q8 = lane >> 3, p8 = lane & 7; (void)c; (void)g; (void)q8; (void)p8
constexpr unsigned wslot(int n) { return (unsigned)(n & 3) * SLOT; }

#define ROT1 0
#define ROT2 0
#define XI_STEP 8
#define TOUCH_GATE 2
#define LB_SLEEP 8
#define LB_FIRST 10
#define FL_FIRST 20
#define FL_SLEEP 6
#define MIDV 64
#define X_FIRST 1
#define X_SLEEP 1
#define PDV 6
#define SLEEP0 12
constexpr unsigned CODE_LINES = 288;
template <int OFF> DEVI void ldA(v4u& d, unsigned voff, const void* sbase) {
    asm volatile("global_load_dwordx4 %0, %1, %2 offset:%3 sc1" : "=v"(d) : "v"(voff), "s"(sbase), "n"(OFF) : "memory");
}
template <int OFF> DEVI void reldA(v4u& d, unsigned voff, const void* sbase) {
    asm volatile("global_load_dwordx4 %0, %1, %2 offset:%3 sc1" : "+v"(d) : "v"(voff), "s"(sbase), "n"(OFF) : "memory");
}
DEVI void touch(unsigned& sink, const void* tp, unsigned to) { asm volatile("global_load_dword %0, %1, %2" : "+v"(sink) : "v"(to), "s"(tp) : "memory"); }

DEVI v4u make_srd(const void* base) { const unsigned long long a = (unsigned long long)base; return v4u{(unsigned)a, (unsigned)(a >> 32) & 0xffffu, 0x7fffffffu, 0x00020000u}; }
DEVI void dma4(v4u srd, unsigned ve, unsigned vo, unsigned s0, unsigned s1, unsigned s2, unsigned s3, unsigned ldst) {
    asm volatile("s_mov_b32 m0, %7\n\ts_nop 0\n\tbuffer_load_dwordx4 %0, %2, %3 offen lds\n\tbuffer_load_dwordx4 %1, %2, %4 offen offset:1024 lds\n\t"
                 "buffer_load_dwordx4 %0, %2, %5 offen offset:2048 lds\n\tbuffer_load_dwordx4 %1, %2, %6 offen offset:3072 lds"
                 ::"v"(ve), "v"(vo), "s"(srd), "s"(s0), "s"(s1), "s"(s2), "s"(s3), "s"(ldst) : "memory");
}
template <int H, int NEG> DEVI f32x2 bmul(f32x2 a, f32x2 b) {
    f32x2 d; asm("v_pk_mul_f32 %0, %1, %2 op_sel:[%3,0] op_sel_hi:[%3,1] neg_lo:[%4,0] neg_hi:[%4,0]" : "=v"(d) : "v"(a), "v"(b), "n"(H), "n"(NEG)); return d;
}
template <int H, int NEG> DEVI f32x2 bfma(f32x2 a, f32x2 b, f32x2 c) {
    f32x2 d; asm("v_pk_fma_f32 %0, %1, %2, %3 op_sel:[%4,0,0] op_sel_hi:[%4,1,1] neg_lo:[%5,0,0] neg_hi:[%5,0,0]" : "=v"(d) : "v"(a), "v"(b), "v"(c), "n"(H), "n"(NEG)); return d;
}
DEVI void mma_c(f32x4& d, const v4u& a, const v4u& b, const f32x4& c) { asm volatile("v_mfma_f32_16x16x32_bf16 %0, %1, %2, %3" : "=&v"(d) : "v"(a), "v"(b), "v"(c)); }
DEVI unsigned cvtv(float a, float b) { unsigned d; asm volatile("v_cvt_pk_bf16_f32 %0, %1, %2" : "=v"(d) : "v"(a), "v"(b)); return d; }

__global__ __launch_bounds__(512, 2) void fused(Params p) {
    const unsigned char* pc0;
    asm volatile("s_getpc_b64 %0" : "=s"(pc0));
    const int w = __builtin_amdgcn_readfirstlane(threadIdx.x >> 6);
    const int b = blockIdx.x, xcd = b & 7, idx = b >> 3;
    const int i = 8 * (xcd & 1) + (idx & 7), kq = xcd >> 1, q = idx >> 3, j = 4 * kq + q;
    const bool heavy = q != 3;
    const int N1 = heavy ? 20 : 12;
    const int lw = w & 1, pr = w & 3;
    const int rot = ROT1;
    const __amdgpu_buffer_rsrc_t rws = __builtin_amdgcn_make_buffer_rsrc(p.ws, 0, WS_END, 0x00020000);
    auto unit_of = [&](int v) { return v < 4 ? 7 * kq + v : v < 8 ? 28 + 8 * ((v >> 1) & 1) + 2 * kq + (v & 1) : 7 * kq + v - 4; };
    const unsigned sl_lo = L_P + (unsigned)pr * SLOT;
    const unsigned xo = WS_XB + (unsigned)((i * 4 + q) * 4) * 36864u + pr * 3072;
    const unsigned fo = WS_FLAG + (unsigned)(i * 16 + j) * 512u;

    if (w >= 4) {
        LANE_VARS;
        STAMP_INIT();
        STAMP(0);
        const unsigned swz = (unsigned)((p8 ^ (q8 >> 1)) << 4);
        const unsigned fa = F_DONE + (lane & 3) * 4;
        unsigned sink = 0, tg;
        auto poll16 = [&](int which) __attribute__((always_inline)) {
            wait_ge(F_OUT + (lane & 3) * 4, which + 1);
            if (which == 2) STAMP(18); else STAMP(16);
            const unsigned pf = WS_FLAG + (unsigned)(i * 16 + 4 * kq + (lane & 3)) * 512u + which * 128 + ((lane >> 2) & 3) * 8;
            NOUNROLL for (int spin = 0; spin < SPIN_MAX; spin++) {
                fence_c();
                const v2u f = __builtin_amdgcn_raw_buffer_load_b64(rws, pf, 0, 16);
                if (__builtin_amdgcn_ballot_w64(f.x != tg || f.y != ~tg) == 0) break;
                __builtin_amdgcn_s_sleep(FL_SLEEP);
            }
            post(F_RDY + which * 4, 1);
            if (which == 2) STAMP(19); else STAMP(17);
        };
        if (w < 6) {
            touch(sink, p.x, (128 * i + 8 * j + 4 * lw) * 1024 + (lane & 31) * 128);
            touch(sink, lw ? p.Bim : p.Bre, (32 * j + 2 * i) * 1024 + c * 128);
            bar_l();
            wait_ge(F_XI + (lane & 3) * 4, 1);
            {
                const unsigned voffS = (8 * (q8 >> 2) + (q8 & 3)) * 1024 + swz;
                const v4u srd = make_srd((const unsigned char*)(lw ? p.Bim : p.Bre) + (size_t)(32 * j) * 1024);
                NOUNROLL for (int n = 0; n < 8; n++) {
                    const unsigned so = 128 * ((n + rot) & 7);
                    dma4(srd, voffS, voffS ^ 64u, so, so + 15360, so + 2048, so + 17408, L_W + (n >> 1) * SLOT + lw * 8192 + (n & 1) * 4096);
                }
            }
            unsigned ffull = F_FULL + lw * 4;
            asm volatile("" : "+v"(ffull));
            waitv<24>(); postc<1>(ffull, lane);
            waitv<16>(); postc<2>(ffull, lane);
            asm volatile("" ::"v"(sink));
            STAMP(1);
            const unsigned voffG = q8 * (FF * 2) + (((p8 >> 1) ^ ((q8 >> 1) & 1)) << 5) + ((p8 & 1) << 4), voffG1 = voffG ^ 64u;
            const unsigned voffC = q8 * 1024 + swz, voffC1 = voffC ^ 64u;
            const unsigned voffD = q8 * 1024 + (((p8 >> 1) ^ (q8 >> 1) ^ (2 * (kq & 1))) << 5) + ((p8 & 1) << 4);
            const unsigned char* gb = (const unsigned char*)(lw ? p.wu : p.wg) + (7 * kq + q) * 128;
            wait_ge(F_XI + (lane & 3) * 4, 3);
            NOUNROLL for (int n = 4; n < N1 + 11; n++) {
                waitv<16>(); postv(ffull, lane, n - 2);
                if (n >= 8 && any_lt(peek(fa), n - 7)) { waitv<8>(); postv(ffull, lane, n - 1); waitv<0>(); postv(ffull, lane, n); wait_ge(fa, n - 7); }
                fence_c();
                const unsigned dst = ((n & 4) ? L_P : L_W) + (n & 3) * SLOT + lw * 8192;
                const int v = n - N1;
                if (v < 0) {
                    const int t = n - 4;
                    const v4u srd = make_srd(gb + (size_t)(((t + rot) & 7) * 64) * (FF * 2) + (t >> 3) * 512);
                    constexpr unsigned R = 8 * FF * 2;
                    dma4(srd, voffG, voffG1, 0, R - 1024, 2 * R - 2048, 3 * R - 3072, dst);
                    dma4(srd, voffG, voffG1, 4 * R, 5 * R - 1024, 6 * R - 2048, 7 * R - 3072, dst + 4096);
                } else if ((v & ~3) == 4) {
                    const unsigned char* cbp = (const unsigned char*)((v & 2) ? p.Cim : p.Cre) + (size_t)q * 131072 + (2 * kq + (v & 1)) * 128;
                    dma4(make_srd(cbp + ((2 * lw) ^ kq) * 32768), voffC, voffC1, 0, 7168, 14336, 21504, dst);
                    dma4(make_srd(cbp + ((2 * lw + 1) ^ kq) * 32768), voffC, voffC1, 0, 7168, 14336, 21504, dst + 4096);
                } else {
                    const v4u srd = make_srd((const unsigned char*)p.wd + (size_t)unit_of(v) * 65536 + q * 256 + (lw ^ (kq >> 1)) * 128);
                    dma4(srd, voffD, voffD, 0, 7168, 14336, 21504, dst);
                    dma4(srd, voffD, voffD, 32768, 32768 + 7168, 32768 + 14336, 32768 + 21504, dst + 4096);
                }
            }
            waitv<16>(); postv(ffull, lane, N1 + 9);
            waitv<8>(); postv(ffull, lane, N1 + 10);
            waitv<0>(); postv(ffull, lane, N1 + 11);
            tg = peek(L_TAG);
            STAMP(7);
        } else {
            unsigned tag0, prm0, prm1, prm2;
            touch(sink, pc0, min(idx * 9 + 4 * lw + p8, (int)CODE_LINES - 1) * 128);
            const unsigned po = 2 * (32 * j + 8 * (lane >> 4) + 4 * ((lane >> 1) & 1) + ((lane >> 2) & 3));
            asm volatile("global_load_ushort %0, %4, %5\n\tglobal_load_ushort %1, %4, %6\n\tglobal_load_ushort %2, %4, %7\n\tglobal_load_dword %3, %8, %9 sc1"
                         : "=&v"(prm0), "=&v"(prm1), "=&v"(prm2), "=&v"(tag0) : "v"(po), "s"(p.lre), "s"(p.lim), "s"(p.ldt), "v"(WS_EPOCH), "s"(p.ws) : "memory");
            *(LAS unsigned*)(uintptr_t)(F_DONE + (lane & 31) * 4) = 0u;
            bar_l();
            sts128(L_SEL + lw * 1024 + lane * 16, make_sel(lane, lw));
            asm volatile("s_waitcnt vmcnt(0)" : "+v"(prm0), "+v"(prm1), "+v"(prm2), "+v"(tag0)::"memory");
            tg = tag0 + 1u;
            const bool odd = lane & 1;
            const unsigned la = lane * 4;
            const float lr = bflo(prm0), li = bflo(prm1);
            float dt = __expf(bflo(prm2));
            dt = bflo(pk2(dt, dt));
            const float ex = __expf(dt * lr);
            const float fr = __builtin_amdgcn_fractf(dt * li * 0.15915494309189535f);
            const float ar = smul_t(ex, __builtin_amdgcn_cosf(fr)), ai = smul_t(ex, __builtin_amdgcn_sinf(fr));
            const cpx a1 = {ar, ai};
            const cpx a2 = cmul(a1, a1), a4 = cmul(a2, a2);
            cpx pw[8];
            pw[1] = cmul(a4, a4); pw[2] = cmul(pw[1], pw[1]); pw[4] = cmul(pw[2], pw[2]);
            pw[3] = cmul(pw[1], pw[2]); pw[5] = cmul(pw[1], pw[4]); pw[6] = cmul(pw[2], pw[4]); pw[7] = cmul(pw[3], pw[4]);
            const unsigned sg = odd ? 0u : 0x80000000u;
            auto sgn = [&](cpx v) { return cpx{v.x, __uint_as_float(__float_as_uint(v.y) ^ sg)}; };
            const cpx r64 = cmul(pw[4], pw[4]), r128 = cmul(r64, r64);
            cpx a64 = sgn(r64), a128 = sgn(r128), a256 = sgn(cmul(r128, r128));
#pragma unroll
            for (int s = 1; s < 8; s++) { pw[s] = sgn(pw[s]); asm volatile("" : "+v"(pw[s].x), "+v"(pw[s].y)); }
            asm volatile("" : "+v"(a64.x), "+v"(a64.y), "+v"(a128.x), "+v"(a128.y), "+v"(a256.x), "+v"(a256.y));
            float cst[7];
            {
                const float nr = ar - 1.0f, den = smul_t(__builtin_amdgcn_rcpf(sfma(lr, lr, smul(li, li))), 2.0f);
                cpx co = {smul(sfma(nr, lr, smul(ai, li)), den), smul(sfms(ai, lr, smul(nr, li)), den)};
                cst[0] = odd ? a1.y : a1.x; cst[1] = odd ? a4.y : a4.x;
#pragma unroll
                for (int t = 0; t < 5; t++) { cst[2 + t] = odd ? co.y : co.x; if (t < 4) co = cmul(co, a1); }
#pragma unroll
                for (int t = 0; t < 7; t++) asm volatile("" : "+v"(cst[t]));
            }
            if (lw == 0) {
#pragma unroll
                for (int t = 0; t < 7; t++) sts32f(L_CST + t * 256 + la, cst[t]);
                *(LAS unsigned*)(uintptr_t)L_TAG = tg;
            }
            post(F_COMM + lw * 4, 1);
            STAMP(1);
            wait_ge(F_XI + (lane & 3) * 4, TOUCH_GATE);
            touch(sink, lw ? p.wu : p.wg, (32 * i + (lane & 31)) * (FF * 2) + (7 * kq + q + ((heavy && lane >= 32) ? 4 : 0)) * 128);
            touch(sink, p.wd, (448 * kq + 28 * i + min(lane, 27)) * 1024 + (2 * q + lw) * 128);
            touch(sink, lw ? p.Cim : p.Cre, (128 * q + 8 * i + p8) * 1024 + (2 * kq + (q8 & 1)) * 128);
            touch(sink, lw ? (const void*)p.ws : (const void*)p.out, lw ? min((unsigned)(lane & 31) * 0xb0000u + idx * 0x4000u, WS_END - 4) : (lane & 3) * 0x100000 + idx * 0x4000);
            wait_ge(F_DONE + lw * 8 + (lane & 1) * 4, 4);
            STAMP(4);
            float P[8], h;
            {
                float e[8];
#pragma unroll
                for (int s = 0; s < 8; s++) e[s] = lds32f(L_SEG + (8 * lw + s) * 256 + la);
                h = e[0];
#pragma unroll
                for (int s = 1; s < 8; s++) { P[s] = h; h = lmad(pw[1], h, e[s]); }
            }
            float elo = 0;
            if (lw == 0) { sts32f(L_XCH + la, h); post(F_COMM, 2); }
            else {
                wait_ge(F_COMM, 2);
                elo = lds32f(L_XCH + la);
                __builtin_amdgcn_raw_buffer_store_b64(v2u{__float_as_uint(lmad(a64, elo, h)), tg}, rws, WS_GRAN + (j * 16 + i) * 512 + lane * 8, 0, 16);
            }
            STAMP(5);
            {
                const int top = i - 1 - lw;
                v2u gr[8] = {};
                __builtin_amdgcn_s_sleep(LB_FIRST);
                NOUNROLL for (int spin = 0; spin < SPIN_MAX; spin++) {
                    __builtin_amdgcn_s_sleep(LB_SLEEP);
                    fence_c();
#pragma unroll
                    for (int n = 0; n < 8; n++) gr[n] = __builtin_amdgcn_raw_buffer_load_b64(rws, WS_GRAN + j * 8192 + lane * 8, ((top - 2 * n) & 15) * 512, 16);
                    unsigned long long bad = 0;
#pragma unroll
                    for (int n = 0; n < 8; n++) if (top - 2 * n >= 0) bad |= __builtin_amdgcn_ballot_w64(gr[n].y != tg);
                    if (bad == 0) break;
                }
                h = 0;
#pragma unroll
                for (int n = 7; n >= 0; n--) if (top - 2 * n >= 0) h = lmad(a256, h, __uint_as_float(gr[n].x));
            }
            STAMP(6);
            sts32f(L_XCH + 256 + lw * 256 + la, h);
            post(F_COMM + lw * 4, 3);
            wait_ge(F_COMM + (lw ^ 1) * 4, 3);
            const float ho = lds32f(L_XCH + 256 + (lw ^ 1) * 256 + la);
            const float C = lw ? lmad(a64, lmad(a128, h, ho), elo) : lmad(a128, ho, h);
            sts32f(L_HIN + (8 * lw) * 256 + la, C);
#pragma unroll
            for (int s = 1; s < 8; s++) sts32f(L_HIN + (8 * lw + s) * 256 + la, lmad(pw[s], C, P[s]));
            post(F_COMM + lw * 4, 4);
            waitv<0>();
            asm volatile("" ::"v"(sink));
            STAMP(7);
            poll16(lw);
            if (lw == 0) poll16(2);
        }
        f32x4 base[4];
        {
            const int d = 128 * q + 32 * kq + 4 * p8;
            const v2u dv = *(const GAS v2u*)(p.Dsk + d);
            const float df[4] = {bflo(dv.x), bfhi(dv.x), bflo(dv.y), bfhi(dv.y)};
            v2u xv[4];
#pragma unroll
            for (int e = 0; e < 4; e++) xv[e] = *(const GAS v2u*)(p.x + (size_t)(128 * i + 32 * pr + 8 * e + q8) * DM + d);
#pragma unroll
            for (int e = 0; e < 4; e++) {
                const unsigned tp = (32 * pr + 8 * e + 4 * ((q8 >> 1) & 1) + 2 * (q8 & 1) + (q8 >> 2)) * 4;
                const float rs = lds32f(L_RSTD + tp), nm = smul(lds32f(L_MEAN + tp), rs);
                const float xf[4] = {bflo(xv[e].x), bfhi(xv[e].x), bflo(xv[e].y), bfhi(xv[e].y)};
#pragma unroll
                for (int k = 0; k < 4; k++) base[e][k] = sfma(sfma(xf[k], rs, nm), df[k], xf[k]);
            }
        }
        STAMP(8);
        const unsigned t16 = ((tg & 0x3fffu) + 0x2000u) << 16;
        float sum[16];
        const unsigned xi = xo + kq * 36864u + lane * 16;
        auto bad3 = [&](const v4u* in) { return ((in[0].y ^ t16) | (in[0].w ^ t16) | (in[1].y ^ t16)) | ((in[1].w ^ t16) | (in[2].y ^ t16) | (in[2].w ^ t16)); };
        auto val = [&](const v4u* in, int k) { const unsigned dw = in[k / 6][2 * ((k / 3) & 1) + (k % 3 == 2)]; return (k % 3 == 1) ? bfhi(dw) : bflo(dw); };
        wait_ge(fa, N1 + 4);
        wait_ge(F_SENT + pr * 4, 3);
        __builtin_amdgcn_s_sleep(X_FIRST);
        v4u in[9];
        NOUNROLL for (int spin = 0; spin < SPIN_MAX; spin++) {
            fence_c();
#pragma unroll
            for (int n = 0; n < 6; n++) in[n] = __builtin_amdgcn_raw_buffer_load_b128(rws, xi + (n / 3) * 12288 + (n % 3) * 1024, 0, 16);
            if (__builtin_amdgcn_ballot_w64((bad3(in) | bad3(in + 3)) > 0xffffu) == 0) break;
        }
        STAMP(9);
#pragma unroll
        for (int n = 6; n < 9; n++) in[n] = __builtin_amdgcn_raw_buffer_load_b128(rws, xi + 2 * 12288 + (n % 3) * 1024, 0, 16);
        SB();
#pragma unroll
        for (int k = 0; k < 16; k++) { sum[k] = sadd(val(in, k), val(in + 3, k)); asm volatile("" : "+v"(sum[k])); }
        SB();
        STAMP(10);
        NOUNROLL for (int spin = 0; spin < SPIN_MAX; spin++) {
            if (__builtin_amdgcn_ballot_w64(bad3(in + 6) > 0xffffu) == 0) break;
            fence_c();
#pragma unroll
            for (int n = 6; n < 9; n++) in[n] = __builtin_amdgcn_raw_buffer_load_b128(rws, xi + 2 * 12288 + (n % 3) * 1024, 0, 16);
        }
        STAMP(11);
#pragma unroll
        for (int k = 0; k < 16; k++) sum[k] = sadd(sum[k], val(in + 6, k));
        const unsigned sc = L_P + 3 * SLOT + pr * 4096;
#pragma unroll
        for (int e = 0; e < 4; e++)
            sts128f(sc + (8 * (c >> 2) + 4 * (c & 1) + 2 * (e & 1) + ((c >> 1) & 1)) * 128 + (((4 * (e >> 1) + g) ^ (c & 7)) << 4), f32x4{sum[4 * e], sum[4 * e + 1], sum[4 * e + 2], sum[4 * e + 3]});
        wait_ge(F_SENT + pr * 4, 4);
        f32x4 ta[4], tb[4];
#pragma unroll
        for (int e = 0; e < 4; e++) {
            const unsigned ro = (8 * e + q8) * 128 + ((p8 ^ (4 * (e & 1) + 2 * (q8 & 1) + (q8 >> 2))) << 4);
            ta[e] = lds128f(sc + ro); tb[e] = lds128f(L_O + pr * 4096 + ro);
        }
        SB();
#pragma unroll
        for (int e = 0; e < 4; e++) *(GAS f32x4*)(p.out + (size_t)(128 * i + 32 * pr + 8 * e + q8) * DM + 128 * q + 32 * kq + 4 * p8) = (base[e] + tb[e]) + ta[e];
        if (b == 255 && w == 4 && lane == 0) __builtin_amdgcn_raw_buffer_store_b32(tg, rws, WS_EPOCH, 0, 16);
        STAMP(12); STAMP_DUMP();
        return;
    }
    constexpr int PD = PDV, MID = MIDV;
    unsigned stall = 0; (void)stall;
    v4u af[8][4];
    const unsigned char* a2b = p.ws + WS_A2 + (size_t)i * (44 * 16384) + w * 4096;
    LANE_VARS;
    unsigned fdone = F_DONE + w * 4, l16 = lane * 16;
    asm volatile("" : "+v"(fdone), "+v"(l16));
    v2u flagv; unsigned flago; int raised = 0;
    auto raise = [&](int level) __attribute__((always_inline)) {
        asm volatile("s_mov_b64 exec, 1\n\tglobal_store_dwordx2 %0, %1, %2 sc1\n\ts_mov_b64 exec, -1" ::"v"(flago), "v"(flagv), "s"(p.ws + (level - 1) * 128) : "memory");
        post(fdone + (F_OUT - F_DONE), level);
        raised = level;
        if (level == 1) STAMP(17); else if (level == 2) STAMP(18); else STAMP(19);
    };
    auto try_raise = [&](int level, unsigned younger) __attribute__((always_inline)) {
        if (__builtin_expect(raised < level && vm_outstanding() <= younger, 0)) raise(level);
    };
    {
        v4u xa[16][2];
        STAMP_INIT();
        STAMP(0);
        const unsigned ffl = F_FULL + (lane & 1) * 4;
        unsigned cb[2];
        cb[0] = (unsigned)c * 128u + (unsigned)((g ^ (c >> 1)) << 4); cb[1] = cb[0] ^ 64u;
        {
            const unsigned voffX = (8 * (q8 >> 2) + 4 * (q8 & 1) + ((q8 >> 1) & 1)) * 1024 + ((p8 ^ (q8 >> 1)) << 4), voffX1 = voffX ^ 64u;
            const v4u srd = make_srd((const unsigned char*)p.x + (size_t)(128 * i + 32 * w) * 1024 - 4096);
            auto issue_x = [&](int t) __attribute__((always_inline)) {
                const unsigned s0 = (t >> 3) * 2048 + ((t + rot) & 7) * 128, s1 = (t >> 3) * 2048 + ((t + 1 + rot) & 7) * 128 + 2048;
                dma4(srd, voffX, voffX1, s0 + 4096, s0 + 4096 + 15360, s1, s1 + 15360, sl_lo + (t & 7) * 2048);
            };
            issue_x(0); issue_x(2); issue_x(4); issue_x(6);
            bar_l();
            const v4u sel0 = make_sel(lane, 0), sel1 = make_sel(lane, 1);
            v4u ones = {0x3f803f80u, 0x3f803f80u, 0x3f803f80u, 0x3f803f80u};
            asm volatile("" : "+v"(ones));
            f32x4 sm[2], sq[2], nm4[2], ct[6];
            const unsigned xl0 = cb[0] + sl_lo, xl1 = cb[1] + sl_lo;
            auto stats = [&](int bb) __attribute__((always_inline)) {
                const float s01 = (lane & 1) ? sq[bb].y : sq[bb].x, s23 = (lane & 1) ? sq[bb].w : sq[bb].z;
                const float mean = -nm4[bb].x;
                const float rstd = __builtin_amdgcn_rsqf(fmaxf(((lane & 2) ? s23 : s01) * (1.0f / DM) - mean * mean, 0.0f) + 1e-5f);
                if (g == (c >> 2)) {
                    sts32f(L_RSTD + (32 * w + 8 * g + 4 * bb + (c & 3)) * 4, rstd);
                    sts32f(L_MEAN + (32 * w + 8 * g + 4 * bb + (c & 3)) * 4, nm4[bb].x);
                }
            };
            auto centre = [&](auto kk) __attribute__((always_inline)) {
                constexpr int k = decltype(kk)::value, e = k - 4;
                if constexpr (k < 64) mma_c(ct[k % 6], (k & 1) ? sel1 : sel0, xa[(k >> 1) & 15][k >> 5], nm4[k >> 5]);
                if constexpr (e >= 0) {
                    xa[(e >> 1) & 15][e >> 5][2 * (e & 1)] = cvtv(ct[e % 6].x, ct[e % 6].y);
                    xa[(e >> 1) & 15][e >> 5][2 * (e & 1) + 1] = cvtv(ct[e % 6].z, ct[e % 6].w);
                }
                SB();
            };
            SB();
            sfor<0, 17>(LAMB(tt) {
                constexpr int t = decltype(tt)::value, u = t - 1;
                if constexpr (t < 16) {
                    constexpr int t5 = ((t + 1) & ~1) + 5, top = t5 < 7 ? 7 : t5 > 15 ? 15 : t5;
                    waitv<2 * (top - t)>();
                    xa[2 * (t & 7)][t >> 3] = lds128(xl0 + (t & 7) * 2048); xa[2 * (t & 7) + 1][t >> 3] = lds128(xl1 + (t & 7) * 2048);
                }
                SB();
                if constexpr (t == 9) { asm volatile("s_nop 3"); SB(); nm4[0] = sm[0] * (-1.0f / DM); asm volatile("s_nop 1" : "+v"(nm4[0])); SB(); }
                if constexpr (t >= 9) { centre(IC<4 * (t - 9)>{}); centre(IC<4 * (t - 9) + 1>{}); centre(IC<4 * (t - 9) + 2>{}); centre(IC<4 * (t - 9) + 3>{}); }
                if constexpr (u >= 0) {
                    mma<(u & 7) == 0>(sm[u >> 3], ones, xa[2 * (u & 7)][u >> 3]); mma<(u & 7) == 0>(sq[u >> 3], xa[2 * (u & 7)][u >> 3], xa[2 * (u & 7)][u >> 3]);
                    mma<false>(sm[u >> 3], ones, xa[2 * (u & 7) + 1][u >> 3]); mma<false>(sq[u >> 3], xa[2 * (u & 7) + 1][u >> 3], xa[2 * (u & 7) + 1][u >> 3]);
                }
                if constexpr ((t & 1) == 0 && t >= 2 && t + 6 < 16) issue_x(t + 6);
                if constexpr (t == XI_STEP) post(fdone + (F_XI - F_DONE), 1);
                if constexpr (t == 13) post(fdone + (F_XI - F_DONE), 2);
                SB();
            });
            post(fdone + (F_XI - F_DONE), 3);
            STAMP(1);
            stats(0);
            SB();
            nm4[1] = sm[1] * (-1.0f / DM); asm volatile("s_nop 1" : "+v"(nm4[1]));
            SB();
            sfor<32, 64>(LAMB(kk) { centre(kk); });
            stats(1);
            SB();
            sfor<64, 68>(LAMB(kk) { centre(kk); });
        }
        STAMP(2);
        f32x4 eX;
        unsigned tb[4];
        v4u bf[PD];
        {
            cb[0] += L_W; cb[1] += L_W;
            asm volatile("" : "+v"(cb[0]), "+v"(cb[1]));
            f32x4 acc[4][2];
            unsigned fl = 0; bool lack = false;
            {
                const int q4 = c >> 2, p4 = c & 3;
                const unsigned tb1 = L_P + (unsigned)(8 * g + q4) * 128u + (unsigned)((((q4 >> 1) & 1) | ((g & 1) << 1)) << 5) + p4 * 8;
#pragma unroll
                for (int t = 0; t < 4; t++) { tb[t] = tb1 ^ (unsigned)(t << 5); asm volatile("" : "+v"(tb[t])); }
            }
            f32x4 ca, c4, co, rs0, rs1;
            wait_ge(F_FULL + (lane & 3) * 4, 1);
            STAMP(3);
            sfor<-PD, 64>(LAMB(nn) {
                constexpr int n = decltype(nn)::value, m = n + PD;
                v4u cur;
                if constexpr (n >= 0) { cur = bf[n % PD]; mma<(n < 4)>(acc[n & 3][0], xa[n >> 2][0], cur); SB(); }
                if constexpr (m > 0 && (m & 15) == 0) need_full(lack, ffl, (m >> 4) + 1);
                if constexpr (m < 64) bf[m % PD] = lds128(cb[(m >> 2) & 1] + wslot(m >> 4) + (((m >> 1) & 1) * 4 + ((m >> 3) & 1) * 2 + (m & 1)) * 2048);
                else { const unsigned a = tb[(m >> 1) & 3] + wslot(0) + (m & 1) * 8192; const v2u lo = ldstr(a), hi = ldstr(a + 512); bf[m % PD] = v4u{lo.x, lo.y, hi.x, hi.y}; }
                SB();
                if constexpr (n >= 0) { mma<(n < 4)>(acc[n & 3][1], xa[n >> 2][1], cur); SB(); }
                if constexpr ((m & 15) == 10) fl = peek(ffl);
                if constexpr ((m & 15) == 14) lack = any_lt(fl, (m >> 4) + 2);
                if constexpr ((m & 15) == 15 && m < 63) post(fdone, (m >> 4) + 1);
                if constexpr (m == 52) ca = lds128f(L_CST + c * 16);
                if constexpr (m == 53) c4 = lds128f(L_CST + 256 + c * 16);
                if constexpr (m == 54) co = lds128f(L_CST + 512 + c * 16);
                if constexpr (m == 55) rs0 = lds128f(L_RSTD + (32 * w + 8 * g) * 4);
                if constexpr (m == 56) rs1 = lds128f(L_RSTD + (32 * w + 8 * g + 4) * 4);
                SB();
            });
            mfma_done();
            STAMP(4); VAL(24, stall); stall = 0;
            f32x4 ev;
            f32x2 sr[2][4], si[2][4];
#pragma unroll
            for (int st = 0; st < 2; st++) {
                const f32x4 r0 = acc[st][0] * rs0, r1 = acc[st][1] * rs1, i0 = acc[2 + st][0] * rs0, i1 = acc[2 + st][1] * rs1;
                sr[st][0] = lo2(r0); sr[st][1] = hi2(r0); sr[st][2] = lo2(r1); sr[st][3] = hi2(r1);
                si[st][0] = lo2(i0); si[st][1] = hi2(i0); si[st][2] = lo2(i1); si[st][3] = hi2(i1);
            }
            const f32x2 a1[2] = {lo2(ca), hi2(ca)}, cf[2] = {lo2(co), hi2(co)};
#pragma unroll
            for (int t = 1; t < 4; t++) {
                f32x2 tr[2], ti[2];
#pragma unroll
                for (int st = 0; st < 2; st++) { tr[st] = bfma<1, 1>(a1[st], si[st][t - 1], sr[st][t]); ti[st] = bfma<1, 0>(a1[st], sr[st][t - 1], si[st][t]); }
                SB();
#pragma unroll
                for (int st = 0; st < 2; st++) { sr[st][t] = bfma<0, 0>(a1[st], sr[st][t - 1], tr[st]); si[st][t] = bfma<0, 0>(a1[st], si[st][t - 1], ti[st]); }
                SB();
            }
#pragma unroll
            for (int st = 0; st < 2; st++) {
                ev[2 * st] = sfma(c4[2 * st], sr[st][3].x, sfnma(c4[2 * st + 1], si[st][3].x, sr[st][3].y));
                ev[2 * st + 1] = sfma(c4[2 * st], si[st][3].x, sfma(c4[2 * st + 1], sr[st][3].x, si[st][3].y));
                eX[2 * st] = sr[st][3].x; eX[2 * st + 1] = si[st][3].x;
            }
            sts128f(L_SEG + w * 1024 + l16, ev);
            post(fdone, 4);
            SB();
            unsigned ya = L_O + w * 4096 + l16;
            asm volatile("" : "+v"(ya), "+v"(eX));
#pragma unroll
            for (int st = 0; st < 2; st++) {
                f32x2 tr[4], ti[4];
#pragma unroll
                for (int t = 0; t < 4; t++) { tr[t] = bmul<1, 1>(cf[st], si[st][t]); ti[t] = bmul<1, 1>(cf[st], sr[st][t]); }
                SB();
#pragma unroll
                for (int t = 0; t < 4; t++) { tr[t] = bfma<0, 0>(cf[st], sr[st][t], tr[t]); ti[t] = bfma<0, 1>(cf[st], si[st][t], ti[t]); }
                SB();
                sts128(ya + st * 1024, v4u{pk2(tr[0]), pk2(tr[1]), pk2(tr[2]), pk2(tr[3])});
                sts128(ya + 2048 + st * 1024, v4u{pk2(ti[0]), pk2(ti[1]), pk2(ti[2]), pk2(ti[3])});
                SB();
            }
        }
        STAMP(5);
        f32x4 acc[4][2][2];
        f32x2 r12;
        unsigned fc = 0;
        auto gpass = [&](auto pp, auto n0, auto n1) __attribute__((always_inline)) {
            constexpr int pass = decltype(pp)::value;
            unsigned fl = 0, dn = 0; bool lack = false;
            sfor<decltype(n0)::value, decltype(n1)::value>(LAMB(nn) {
                constexpr int n = decltype(nn)::value, m = n + PD;
                constexpr bool pre = pass == 0 || m < 128;
                constexpr unsigned off = wslot(m >> 4) + (m & 1) * 8192 + ((m >> 3) & 1) * 4096;
                const v4u cur = bf[(n + 64 + 128 * pass) % PD];
                mma<(n < 8)>(acc[(n >> 1) & 3][n & 1][0], cur, xa[n >> 3][0]);
                SB();
                if constexpr ((m & 15) == 0) post(fdone, dn);
                if constexpr ((m & 15) == 0 && pre) need_full(lack, ffl, 5 + 8 * pass + (m >> 4));
                v2u lo, hi;
                if constexpr (pre) lo = ldstr(tb[(m >> 1) & 3] + off);
                if constexpr ((m & 15) == 10 && (pass == 0 || m < 112)) fl = peek(ffl);
                if constexpr ((m & 15) == 15 && (pass == 0 || m < 112)) lack = any_lt(fl, m == 127 ? N1 - 7 : 6 + 8 * pass + (m >> 4));
                if constexpr ((m & 15) == 13) { dn = 5 + 8 * pass + (m >> 4); asm volatile("" : "+v"(dn)); }
                if constexpr (n == 88 && pass == 1) fc = peek(F_RDY);
                if constexpr (n == 94 && pass == 1) { if (__builtin_expect(any_lt(fc, 1), 0)) { if (raised < 2) { waitv<0>(); raise(2); } wait_ge(F_RDY, 1); } fence_c(); }
                if constexpr (n >= 96 && pass == 1 && (n & 1) == 0) { constexpr int f = (n - 96) >> 1; ldA<(f & 3) * 1024>(af[f >> 2][f & 3], l16, a2b + (size_t)unit_of(f >> 2) * 16384); }
                if constexpr (pass == 1 && (n & 7) == 7 && n >= 24 && n < MID) try_raise(1, 0);
                if constexpr (pass == 1 && (n & 7) == 7 && n >= MID + 24) try_raise(2, n < 96 ? 0 : (n - 94) >> 1);
                if constexpr (n == 121 && pass == 0) { const unsigned ra = L_RSTD + w * 128 + (((l16 >> 1) & 0x60) | ((l16 >> 2) & 0xc)); r12 = f32x2{lds32f(ra), lds32f(ra + 16)}; }
                SB();
                mma<(n < 8)>(acc[(n >> 1) & 3][n & 1][1], cur, xa[n >> 3][1]);
                SB();
                if constexpr (pre) { hi = ldstr(tb[(m >> 1) & 3] + off + 512); bf[(n + 64 + 128 * pass) % PD] = v4u{lo.x, lo.y, hi.x, hi.y}; }
                if constexpr ((m & 57) == 57 && (pass == 0 || m < 64)) {
                    tb[(m >> 1) & 3] += (m & 64) ? L_P - L_W : L_W - L_P; asm volatile("" : "+v"(tb[(m >> 1) & 3]));
                }
                SB();
            });
        };
        auto gated = [&](auto ff) __attribute__((always_inline)) {
            constexpr int uu = decltype(ff)::value >> 1, bb = decltype(ff)::value & 1;
            const f32x4 h0 = acc[2 * uu][0][bb] * acc[2 * uu][1][bb], h1 = acc[2 * uu + 1][0][bb] * acc[2 * uu + 1][1][bb];
            return v4u{pk2(bmul<bb, 0>(r12, lo2(h0))), pk2(bmul<bb, 0>(r12, hi2(h0))), pk2(bmul<bb, 0>(r12, lo2(h1))), pk2(bmul<bb, 0>(r12, hi2(h1)))};
        };
        gpass(IC<0>{}, IC<0>{}, IC<128>{});
        unsigned ao = WS_A2 + (unsigned)i * (44u * 16384u) + w * 4096 + l16;
        {
            r12 = r12 * r12;
            const unsigned go = ao + (7 * kq + q) * 16384;
            mfma_done();
            STAMP(6); VAL(25, stall); stall = 0;
            sfor<0, 4>(LAMB(ff) { __builtin_amdgcn_raw_buffer_store_b128(gated(ff), rws, go + decltype(ff)::value * 1024, 0, 16); });
            const unsigned tg = peek(L_TAG);
            flagv = v2u{tg, ~tg}; flago = fo + w * 8;
            asm volatile("" : "+v"(flagv), "+v"(flago), "+v"(ao));
            SB();
        }
        if (heavy) gpass(IC<1>{}, IC<0>{}, IC<MID>{});
        else {
#pragma unroll
            for (int t = 0; t < 16; t++) asm volatile("" : "=v"(acc[t >> 2][(t >> 1) & 1][t & 1]));
        }
        {
            STAMP(16);
            const unsigned cl = L_CST + (l16 & 0xf0);
            unsigned ya = L_O + w * 4096 + l16;
            asm volatile("" : "+v"(ya));
            const unsigned fcm = peek(ffl + (F_COMM - F_FULL));
            const v4u sel[2] = {lds128(L_SEL + l16), lds128(L_SEL + 1024 + l16)};
            const v4u yr[2] = {lds128(ya), lds128(ya + 1024)}, yi[2] = {lds128(ya + 2048), lds128(ya + 3072)};
            const f32x4 c4 = lds128f(cl + 256);
            f32x4 cw[4];
#pragma unroll
            for (int t = 0; t < 4; t++) cw[t] = lds128f(cl + (3 + t) * 256);
            SB();
            need_full(any_lt(fcm, 4), ffl + (F_COMM - F_FULL), 4);
            const f32x4 hin = lds128f(L_HIN + w * 1024 + l16);
            f32x4 zr[2][2], zi[2][2];
#pragma unroll
            for (int st = 0; st < 2; st++)
#pragma unroll
                for (int bb = 0; bb < 2; bb++) { mma<true>(zr[st][bb], yr[st], sel[bb]); mma<true>(zi[st][bb], yi[st], sel[bb]); }
            SB();
            f32x2 hr[2], hi[2];
            {
                float t0[2], t1[2];
#pragma unroll
                for (int st = 0; st < 2; st++) { t0[st] = sfnma(c4[2 * st + 1], hin[2 * st + 1], eX[2 * st]); t1[st] = sfma(c4[2 * st + 1], hin[2 * st], eX[2 * st + 1]); }
                SB();
#pragma unroll
                for (int st = 0; st < 2; st++) { hr[st] = f32x2{hin[2 * st], sfma(c4[2 * st], hin[2 * st], t0[st])}; hi[st] = f32x2{hin[2 * st + 1], sfma(c4[2 * st], hin[2 * st + 1], t1[st])}; }
                SB();
            }
#pragma unroll
            for (int st = 0; st < 2; st++) {
                f32x2 tr[4], ti[4];
#pragma unroll
                for (int t = 0; t < 4; t++) { const f32x2 wv = st ? hi2(cw[t]) : lo2(cw[t]); tr[t] = bmul<1, 1>(wv, hi[st]); ti[t] = bmul<1, 1>(wv, hr[st]); }
                SB();
#pragma unroll
                for (int t = 0; t < 4; t++) { const f32x2 wv = st ? hi2(cw[t]) : lo2(cw[t]); tr[t] = bfma<0, 0>(wv, hr[st], tr[t]); ti[t] = bfma<0, 1>(wv, hi[st], ti[t]); }
                SB();
                v4u fr = {pk2(tr[0]), pk2(tr[1]), pk2(tr[2]), pk2(tr[3])}, fi = {pk2(ti[0]), pk2(ti[1]), pk2(ti[2]), pk2(ti[3])};
                asm volatile("s_nop 1" : "+v"(fr), "+v"(fi));
#pragma unroll
                for (int bb = 0; bb < 2; bb++) { mma<false>(zr[st][bb], fr, sel[bb]); mma<false>(zi[st][bb], fi, sel[bb]); }
                SB();
            }
            mfma_done();
            SB();
            if (raised < 1) { waitv<0>(); raise(1); }
            const unsigned so = ao + (28 + (j >> 1)) * 16384 + (j & 1) * 2048;
#pragma unroll
            for (int bb = 0; bb < 2; bb++) {
                __builtin_amdgcn_raw_buffer_store_b128(pk8(zr[0][bb], zr[1][bb]), rws, so + bb * 1024, 0, 16);
                __builtin_amdgcn_raw_buffer_store_b128(pk8(zi[0][bb], zi[1][bb]), rws, so + 8 * 16384 + bb * 1024, 0, 16);
            }
            SB();
            STAMP(7);
        }
        if (heavy) {
            gpass(IC<1>{}, IC<MID>{}, IC<128>{});
            if (raised < 2) { waitv<16>(); raise(2); }
            mfma_done();
            STAMP(8); VAL(26, stall); stall = 0;
            const unsigned go = ao + (7 * kq + 4 + q) * 16384;
            sfor<0, 4>(LAMB(ff) { __builtin_amdgcn_raw_buffer_store_b128(gated(ff), rws, go + decltype(ff)::value * 1024, 0, 16); });
            SB();
        } else {
            waitv<0>(); raise(2);
            wait_ge(F_RDY, 1);
            sfor<0, 16>(LAMB(ff) { constexpr int f = decltype(ff)::value; ldA<(f & 3) * 1024>(af[f >> 2][f & 3], l16, a2b + (size_t)unit_of(f >> 2) * 16384); });
#pragma unroll
            for (int u = 0; u < 4; u++) asm volatile("s_waitcnt vmcnt(0)" : "+v"(af[u][0]), "+v"(af[u][1]), "+v"(af[u][2]), "+v"(af[u][3])::"memory");
        }
    }
    STAMP(9);
    {
        LANE_VARS;
        f32x4 acc[8][2];
        const unsigned ffl = F_FULL + (lane & 1) * 4;
        unsigned cbs[2], tbs[4], oa, xa0 = xo + l16;
        {
            const unsigned tb2 = L_P + (unsigned)(4 * g + (c >> 2)) * 128u + (unsigned)((2 * (g & 1) + (c >> 3)) << 5) + (c & 3) * 8;
#pragma unroll
            for (int k = 0; k < 4; k++) { tbs[k] = tb2 ^ (unsigned)(k << 5); asm volatile("" : "+v"(tbs[k])); }
        }
        v4u bf[PD], pk[3];
        unsigned fl = 0, fr = 0, dn = 0; bool lack = false;
        float tagf = 0;
        wait_ge(ffl, N1 + 1);
        constexpr auto fv = [](int n) { return n < 80 ? n >> 4 : 5 + ((n - 80) % 24) / 4; };
        constexpr auto fk = [](int n) { return n < 80 ? (n >> 3) & 1 : (n >> 1) & 1; };
        constexpr auto fd = [](int n) { return n < 80 ? n & 7 : 2 * (((n - 80) / 24 + 1) & 3) + (n & 1); };
        constexpr auto first = [](int n) { return n >= 0 && (n < 80 ? (n & 15) == 0 : n < 104 && (n & 3) == 0); };
        sfor<-PD, 176>(LAMB(nn) {
            constexpr int n = decltype(nn)::value, m = n + PD;
            constexpr int v = fv(n), ks = fk(n), dt = fd(n);
            constexpr int mv = fv(m), mk = fk(m), md = fd(m);
            constexpr bool mc = (mv & ~3) == 4;
            constexpr unsigned moff = (mv & 3) * SLOT + (mc ? md * 2048 : (md >> 2) * 8192 + mk * 4096);
            v4u cur; v2u lo, hi;
            if constexpr (n >= 0) {
                if constexpr (first(n)) {
                    constexpr int NW[11] = {16, 12, 8, 12, 12, 20, 16, 12, 8, 4, 0};
                    asm volatile("s_waitcnt vmcnt(%4)" : "+v"(af[v & 7][0]), "+v"(af[v & 7][1]), "+v"(af[v & 7][2]), "+v"(af[v & 7][3]) : "n"(NW[v]) : "memory");
                }
                cur = bf[n % PD];
                mma<(n < 8)>(acc[dt][0], cur, af[v & 7][2 * ks]);
                SB();
            }
            if constexpr (m > 0 && (m & 15) == 0 && m <= 64) post(fdone, dn);
            if constexpr (m > 0 && (m & 15) == 0 && m <= 80) need_full(lack, ffl, N1 + (m == 80 ? 8 : mv + 1));
            if constexpr (m == 92) need_full(lack, ffl, N1 + 11);
            if constexpr (m < 176) { if constexpr (mc) bf[m % PD] = lds128(cbs[mk] + moff); else lo = ldstr(tbs[md & 3] + moff); }
            if constexpr (((m & 15) == 10 && m < 80) || m == 86) fl = peek(ffl);
            if constexpr ((m & 15) == 15 && m < 80) lack = any_lt(fl, N1 + (m == 79 ? 8 : mv + 2));
            if constexpr (m == 91) lack = any_lt(fl, N1 + 11);
            if constexpr ((m & 15) == 13 && m < 64) { dn = N1 + mv + 1; asm volatile("" : "+v"(dn)); }
            if constexpr (n > 8 && n < 64 && (n & 7) == 1) try_raise(3, n < 32 ? 0 : (n - 31) >> 1);
            if constexpr (n == 26) fr = peek(F_RDY + 4);
            if constexpr (n == 31) { if (__builtin_expect(any_lt(fr, 1), 0)) { if (raised < 3) { waitv<0>(); raise(3); } wait_ge(F_RDY + 4, 1); } fence_c(); }
            if constexpr (n >= 32 && n < 64 && (n & 1) == 0) { constexpr int f = (n - 32) >> 1; ldA<(f & 3) * 1024>(af[4 + (f >> 2)][f & 3], l16, a2b + (size_t)unit_of(4 + (f >> 2)) * 16384); }
            if constexpr (n == 61) fr = peek(F_RDY + 8);
            if constexpr (n == 65) { if (raised < 3) raise(3); }
            if constexpr (n == 66) need_full(any_lt(fr, 1), F_RDY + 8, 1);
            if constexpr (n >= 67 && n < 79) { constexpr int f = n - 67; reldA<(f & 3) * 1024>(af[f >> 2][f & 3], l16, a2b + (size_t)unit_of(8 + (f >> 2)) * 16384); }
            if constexpr (n == 20 || n == 22) { constexpr int k = (n - 20) >> 1; cbs[k] = (L_W + (unsigned)c * 128u + (unsigned)((g ^ (c >> 1)) << 4)) ^ (unsigned)(k << 6); asm volatile("" : "+v"(cbs[k])); }
            if constexpr (n == 84) { tagf = __uint_as_float(((flagv.x & 0x3fffu) + 0x2000u) << 16); asm volatile("" : "+v"(tagf)); }
            if constexpr (n == 160) { oa = L_O + w * 4096 + (8 * (c >> 2) + 4 * (c & 1) + ((c >> 1) & 1)) * 128 + ((g ^ (c & 7)) << 4); asm volatile("" : "+v"(oa)); }
            SB();
            if constexpr (n >= 0) { mma<(n < 8)>(acc[dt][1], cur, af[v & 7][2 * ks + 1]); SB(); }
            if constexpr (m < 176 && !mc) { hi = ldstr(tbs[md & 3] + moff + 2048); bf[m % PD] = v4u{lo.x, lo.y, hi.x, hi.y}; }
            if constexpr (n >= 104) {
                constexpr int sl = (n - 80) / 24, r = (n - 80) % 24 - 2;
                auto val = [&](int k) { return acc[2 * sl + (k >> 3)][(k >> 2) & 1][k & 3]; };
                if constexpr (r >= 0 && r < 6) {
                    pk[r >> 1][2 * (r & 1)] = cvtv(val(3 * r), val(r < 5 ? 3 * r + 1 : 15));
                    pk[r >> 1][2 * (r & 1) + 1] = cvtv(val(r < 5 ? 3 * r + 2 : 15), tagf);
                }
                if constexpr (r == 2 || r == 4 || r == 6)
                    asm volatile("global_store_dwordx4 %0, %1, %2 offset:%3 sc1" ::"v"(xa0), "v"(pk[(r >> 1) - 1]), "s"(p.ws + (kq ^ sl) * 36864u + (sl - 1) * 12288), "n"(((r >> 1) - 1) * 1024) : "memory");
                if constexpr (r == 7) postc<sl>(fdone + (F_SENT - F_DONE), lane);
            }
            if constexpr (n == 79) { STAMP(10); VAL(28, stall); stall = 0; }
            SB();
        });
        mfma_done();
        STAMP(11); VAL(29, stall);
#pragma unroll
        for (int e = 0; e < 4; e++) sts128f((oa ^ (unsigned)((e >> 1) << 6)) + (e & 1) * 256, acc[e >> 1][e & 1]);
        postc<4>(fdone + (F_SENT - F_DONE), lane);
    }
    STAMP(14); STAMP_DUMP();
}

extern "C" void kernel_launch(void* const* d_in, const int* in_sizes, int n_in, void* d_out, int out_size, void* d_ws, size_t ws_size,
                              hipStream_t stream) {
    Params p;
    p.x = (const u16*)d_in[0]; p.lre = (const u16*)d_in[1]; p.lim = (const u16*)d_in[2];
    p.Bre = (const u16*)d_in[3]; p.Bim = (const u16*)d_in[4]; p.Cre = (const u16*)d_in[5]; p.Cim = (const u16*)d_in[6];
    p.ldt = (const u16*)d_in[7]; p.Dsk = (const u16*)d_in[8];
    p.wg = (const u16*)d_in[10]; p.wu = (const u16*)d_in[11]; p.wd = (const u16*)d_in[12];
    p.out = (float*)d_out; p.ws = (unsigned char*)d_ws;
    (void)hipFuncSetAttribute((const void*)fused, hipFuncAttributeMaxDynamicSharedMemorySize, L_TOTAL);
    fused<<<256, 512, L_TOTAL, stream>>>(p);
}
```

```cpp
#include <hip/hip_runtime.h>
#include <stdint.h>

typedef unsigned short u16;
typedef unsigned v4u __attribute__((ext_vector_type(4)));
typedef unsigned v2u __attribute__((ext_vector_type(2)));
typedef short s16x4 __attribute__((ext_vector_type(4)));
typedef __bf16 bf16x2 __attribute__((ext_vector_type(2)));
typedef __bf16 bf16x8 __attribute__((ext_vector_type(8)));
typedef float f32x2 __attribute__((ext_vector_type(2)));
typedef float f32x4 __attribute__((ext_vector_type(4)));
#define GAS __attribute__((address_space(1)))
#define LAS __attribute__((address_space(3)))
#define DEVI __device__ __forceinline__
#define NOUNROLL _Pragma("nounroll")

#define SPIN_SLEEP 0
constexpr int DM = 512, FF = 1792;
constexpr unsigned WS_A2 = 0;
constexpr unsigned WS_XB = 16u * 44u * 16384u;
constexpr unsigned WS_GRAN = WS_XB + 256u * 36864u;
constexpr unsigned WS_FLAG = WS_GRAN + 16u * 16u * 512u;
constexpr unsigned WS_EPOCH = WS_FLAG + 1024u * 128u;
constexpr unsigned WS_END = WS_EPOCH + 256u;
constexpr unsigned SLOT = 16384;
constexpr unsigned L_RSTD = 0, L_MEAN = 512;
constexpr unsigned F_DONE = 1024, F_SENT = F_DONE + 16, F_FULL = F_DONE + 32, F_COMM = F_DONE + 40, F_RDY = F_DONE + 48, F_OUT = F_DONE + 64, F_XI = F_DONE + 80, F_BS = F_DONE + 96, L_TAG = F_DONE + 128;
constexpr unsigned L_CST = 1280;
constexpr unsigned L_XCH = L_CST + 8 * 256;
constexpr unsigned L_SEG = 4096, L_HIN = 8192, L_SEL = 12288;
constexpr unsigned L_P = SLOT, L_O = 5 * SLOT, L_W = 6 * SLOT, L_TOTAL = 10 * SLOT;
constexpr int SPIN_MAX = 1 << 18;
constexpr int LSPIN_MAX = 1 << 22;

struct Params {
    const u16 *x, *lre, *lim, *Bre, *Bim, *Cre, *Cim, *ldt, *Dsk, *wg, *wu, *wd;
    float* out;
    unsigned char* ws;
};

DEVI float bf2f(u16 v) { return __uint_as_float(((unsigned)v) << 16); }
DEVI float bflo(unsigned v) { return __uint_as_float(v << 16); }
DEVI float bfhi(unsigned v) { return __uint_as_float(v & 0xffff0000u); }
DEVI unsigned pk2(float a, float b) { f32x2 p = {a, b}; return __builtin_bit_cast(unsigned, __builtin_convertvector(p, bf16x2)); }
DEVI unsigned pk2(f32x2 p) { return __builtin_bit_cast(unsigned, __builtin_convertvector(p, bf16x2)); }
DEVI v4u pk8(f32x4 a, f32x4 b) { return v4u{pk2(a.x, a.y), pk2(a.z, a.w), pk2(b.x, b.y), pk2(b.z, b.w)}; }
DEVI float dot2(unsigned a, unsigned b, float c) { return __builtin_amdgcn_fdot2_f32_bf16(__builtin_bit_cast(bf16x2, a), __builtin_bit_cast(bf16x2, b), c, false); }
template <int CTRL> DEVI float dppf(float v) { return __builtin_bit_cast(float, __builtin_amdgcn_update_dpp(0, __builtin_bit_cast(int, v), CTRL, 0xf, 0xf, true)); }
DEVI f32x4 mfma(v4u a, v4u b, f32x4 c) { return __builtin_amdgcn_mfma_f32_16x16x32_bf16(__builtin_bit_cast(bf16x8, a), __builtin_bit_cast(bf16x8, b), c, 0, 0, 0); }
template <bool FIRST> DEVI void mma(f32x4& c, const v4u& a, const v4u& b) {
    if constexpr (FIRST) asm volatile("v_mfma_f32_16x16x32_bf16 %0, %1, %2, 0" : "=&v"(c) : "v"(a), "v"(b));
    else asm volatile("v_mfma_f32_16x16x32_bf16 %0, %1, %2, %0" : "+v"(c) : "v"(a), "v"(b));
}
DEVI void mfma_done() { __builtin_amdgcn_sched_barrier(0); asm volatile("s_nop 7"); __builtin_amdgcn_sched_barrier(0); }
DEVI v4u lds128(unsigned a) { return *(LAS v4u*)(uintptr_t)a; }
DEVI f32x4 lds128f(unsigned a) { return *(LAS f32x4*)(uintptr_t)a; }
DEVI float lds32f(unsigned a) { return *(LAS float*)(uintptr_t)a; }
DEVI void sts32f(unsigned a, float v) { *(LAS float*)(uintptr_t)a = v; }
DEVI void sts128(unsigned a, v4u v) { *(LAS v4u*)(uintptr_t)a = v; }
DEVI void sts128f(unsigned a, f32x4 v) { *(LAS f32x4*)(uintptr_t)a = v; }
DEVI v2u ldstr(unsigned a) { return __builtin_bit_cast(v2u, __builtin_amdgcn_ds_read_tr16_b64_v4i16((LAS s16x4*)(uintptr_t)a)); }
DEVI void fence_c() { asm volatile("" ::: "memory"); }
DEVI void bar_l() { asm volatile("s_waitcnt lgkmcnt(0)\n\ts_barrier" ::: "memory"); }
template <int N> DEVI void waitv() { asm volatile("s_waitcnt vmcnt(%0)" ::"n"(N) : "memory"); }
DEVI void dma16(const void* sbase, unsigned voff, unsigned ldst) {
    asm volatile("s_mov_b32 m0, %2\n\ts_nop 0\n\tglobal_load_lds_dwordx4 %0, %1" ::"v"(voff), "s"(sbase), "s"(ldst) : "memory");
}
DEVI void post(unsigned a, unsigned v) { fence_c(); *(LAS unsigned*)(uintptr_t)a = v; fence_c(); }
DEVI unsigned peek(unsigned a) { fence_c(); return *(LAS unsigned*)(uintptr_t)a; }
DEVI bool any_lt(unsigned v, unsigned need) { return __builtin_amdgcn_ballot_w64(v < need) != 0; }
DEVI void wait_ge(unsigned a, unsigned need) {
    int s = LSPIN_MAX;
    NOUNROLL while (any_lt(peek(a), need)) { asm volatile("s_sub_i32 %0, %0, 1" : "+s"(s) :: "scc"); if (s < 0) break; if (SPIN_SLEEP) __builtin_amdgcn_s_sleep(SPIN_SLEEP); }
    fence_c();
}
template <int V> DEVI void postm(unsigned a, int lane) {
    asm volatile("s_mov_b64 exec, %2\n\tds_max_u32 %0, %1\n\ts_mov_b64 exec, -1" ::"v"(a), "v"(lane), "n"(1 << V) : "memory");
}
DEVI unsigned vm_outstanding() {
    unsigned v; asm volatile("s_getreg_b32 %0, hwreg(HW_REG_IB_STS)" : "=s"(v)::"memory");
    return (v & 15u) | ((v >> 18) & 0x30u);
}
DEVI void postv(unsigned a, int lane, unsigned v) {
    asm volatile("s_lshl_b64 exec, 1, %2\n\tds_max_u32 %0, %1\n\ts_mov_b64 exec, -1" ::"v"(a), "v"(lane), "s"(v) : "memory", "scc");
}
template <int V> DEVI void postc(unsigned a, int lane) {
    asm volatile("s_mov_b64 exec, %2\n\tds_write_b32 %0, %1\n\ts_mov_b64 exec, -1" ::"v"(a), "v"(lane), "n"(1 << V) : "memory");
}
DEVI void need_full(bool lack, unsigned a, unsigned need) { if (__builtin_expect(lack, 0)) wait_ge(a, need); fence_c(); }
#define VAL(id, v)
DEVI float sfma(float a, float b, float c) { float d; asm("v_fma_f32 %0, %1, %2, %3" : "=v"(d) : "v"(a), "v"(b), "v"(c)); return d; }
DEVI float sfms(float a, float b, float c) { float d; asm("v_fma_f32 %0, %1, %2, -%3" : "=v"(d) : "v"(a), "v"(b), "v"(c)); return d; }
DEVI float sfnma(float a, float b, float c) { float d; asm("v_fma_f32 %0, -%1, %2, %3" : "=v"(d) : "v"(a), "v"(b), "v"(c)); return d; }
DEVI float sadd(float a, float b) { float d; asm("v_add_f32 %0, %1, %2" : "=v"(d) : "v"(a), "v"(b)); return d; }
DEVI float smul(float a, float b) { float d; asm("v_mul_f32 %0, %1, %2" : "=v"(d) : "v"(a), "v"(b)); return d; }
DEVI float smul_t(float a, float b) { float d; asm("s_nop 0\n\tv_mul_f32 %0, %1, %2" : "=v"(d) : "v"(a), "v"(b)); return d; }
struct cpx { float x, y; };
DEVI cpx cmul(cpx a, cpx b) { return cpx{sfms(a.x, b.x, smul(a.y, b.y)), sfma(a.x, b.y, smul(a.y, b.x))}; }
DEVI float lmad(cpx a, float h, float u) {
    float d;
    asm("v_fma_f32 %0, %1, %3, %4\n\ts_nop 0\n\tv_fmac_f32_dpp %0, %3, %2 quad_perm:[1,0,3,2] row_mask:0xf bank_mask:0xf" : "=&v"(d) : "v"(a.x), "v"(a.y), "v"(h), "v"(u));
    return d;
}
DEVI f32x2 dup(float v) { f32x2 d = {v, v}; asm("" : "+v"(d)); return d; }
DEVI f32x2 pfma(f32x2 a, f32x2 b, f32x2 c) { return __builtin_elementwise_fma(a, b, c); }
DEVI f32x2 lo2(f32x4 v) { return f32x2{v.x, v.y}; }
DEVI f32x2 hi2(f32x4 v) { return f32x2{v.z, v.w}; }
#define SB() __builtin_amdgcn_sched_barrier(0)
template <int V> struct IC { static constexpr int value = V; };
template <int B, int E, class F> DEVI void sfor(F&& f) { if constexpr (B < E) { f(IC<B>{}); sfor<B + 1, E>(f); } }
#define LAMB(nn) [&](auto nn) __attribute__((always_inline))
#define STAMP(id)
#define STAMP_INIT()
#define STAMP_DUMP()
DEVI int fresh_lane() { int l; asm volatile("v_mbcnt_lo_u32_b32 %0, -1, 0\n\tv_mbcnt_hi_u32_b32 %0, -1, %0" : "=v"(l)); return l; }
DEVI v4u make_sel(int lane, int bb) {
    const int c = lane & 15, tk = 8 * (c >> 2) + 4 * bb + (c & 3) - 8 * (lane >> 4);
    v4u s;
#pragma unroll
    for (int e = 0; e < 4; e++) s[e] = (tk == 2 * e ? 0x3f80u : 0u) | (tk == 2 * e + 1 ? 0x3f800000u : 0u);
    return s;
}
#define LANE_VARS const int lane = fresh_lane(), c = lane & 15, g = lane >> 4, q8 = lane >> 3, p8 = lane & 7; (void)c; (void)g; (void)q8; (void)p8
constexpr unsigned wslot(int n) { return (unsigned)(n & 3) * SLOT; }

#define ROT1 0
#define ROT2 0
#define XI_STEP 8
#define TOUCH_GATE 1
#define LB_SLEEP 8
#define LB_FIRST 10
#define FL_FIRST 20
#define FL_SLEEP 6
#define MIDV 64
#define X_FIRST 10
#define X_SLEEP 1
#define PDV 4
#define INFL 8
#define TW waitv<0>();
#define PACE 14
#define PACE2 8
#define XLV 3
#define LGATE 84
#define SLEEP0 12
constexpr unsigned CODE_LINES = 288;
template <int OFF> DEVI void reldA(v4u& d, unsigned voff, v4u srd, unsigned so) {
    asm volatile("buffer_load_dwordx4 %0, %1, %2, %3 offen offset:%4 sc1" : "+v"(d) : "v"(voff), "s"(srd), "s"(so), "n"(OFF) : "memory");
}
template <int OFF> DEVI void stA(const v4u& d, unsigned voff, v4u srd, unsigned so) {
    asm volatile("buffer_store_dwordx4 %0, %1, %2, %3 offen offset:%4 sc1\n\ts_nop 1" ::"v"(d), "v"(voff), "s"(srd), "s"(so), "n"(OFF) : "memory");
}
template <int OFF> DEVI void stX(const v4u& d, unsigned voff, v4u srd, unsigned so) {
    asm volatile("buffer_store_dwordx4 %0, %1, %2, %3 offen offset:%4 sc1" ::"v"(d), "v"(voff), "s"(srd), "s"(so), "n"(OFF) : "memory");
}
DEVI void touch(unsigned& sink, const void* tp, unsigned to) { asm volatile("global_load_dword %0, %1, %2" : "+v"(sink) : "v"(to), "s"(tp) : "memory"); }

DEVI v4u make_srd(const void* base) { const unsigned long long a = (unsigned long long)base; return v4u{(unsigned)a, (unsigned)(a >> 32) & 0xffffu, 0x7fffffffu, 0x00020000u}; }
DEVI void dma4(v4u srd, unsigned ve, unsigned vo, unsigned s0, unsigned s1, unsigned s2, unsigned s3, unsigned ldst) {
    asm volatile("s_mov_b32 m0, %7\n\ts_nop 0\n\tbuffer_load_dwordx4 %0, %2, %3 offen lds\n\tbuffer_load_dwordx4 %1, %2, %4 offen offset:1024 lds\n\t"
                 "buffer_load_dwordx4 %0, %2, %5 offen offset:2048 lds\n\tbuffer_load_dwordx4 %1, %2, %6 offen offset:3072 lds"
                 ::"v"(ve), "v"(vo), "s"(srd), "s"(s0), "s"(s1), "s"(s2), "s"(s3), "s"(ldst) : "memory");
}
DEVI void dma1(v4u srd, unsigned v, unsigned so, unsigned ldst) {
    asm volatile("s_mov_b32 m0, %3\n\ts_nop 0\n\tbuffer_load_dwordx4 %0, %1, %2 offen lds" ::"v"(v), "s"(srd), "s"(so), "s"(ldst) : "memory");
}
template <int H, int NEG> DEVI f32x2 bmul(f32x2 a, f32x2 b) {
    f32x2 d; asm("v_pk_mul_f32 %0, %1, %2 op_sel:[%3,0] op_sel_hi:[%3,1] neg_lo:[%4,0] neg_hi:[%4,0]" : "=v"(d) : "v"(a), "v"(b), "n"(H), "n"(NEG)); return d;
}
template <int H, int NEG> DEVI f32x2 bfma(f32x2 a, f32x2 b, f32x2 c) {
    f32x2 d; asm("v_pk_fma_f32 %0, %1, %2, %3 op_sel:[%4,0,0] op_sel_hi:[%4,1,1] neg_lo:[%5,0,0] neg_hi:[%5,0,0]" : "=v"(d) : "v"(a), "v"(b), "v"(c), "n"(H), "n"(NEG)); return d;
}
DEVI void mma_c(f32x4& d, const v4u& a, const v4u& b, const f32x4& c) { asm volatile("v_mfma_f32_16x16x32_bf16 %0, %1, %2, %3" : "=&v"(d) : "v"(a), "v"(b), "v"(c)); }
DEVI unsigned cvtv(float a, float b) { unsigned d; asm volatile("v_cvt_pk_bf16_f32 %0, %1, %2" : "=v"(d) : "v"(a), "v"(b)); return d; }

__global__ __launch_bounds__(512, 2) void fused(Params p) {
    const unsigned char* pc0;
    asm volatile("s_getpc_b64 %0" : "=s"(pc0));
    const int w = __builtin_amdgcn_readfirstlane(threadIdx.x >> 6);
    const int b = blockIdx.x, xcd = b & 7, idx = b >> 3;
    const int i = 8 * (xcd & 1) + (idx & 7), kq = xcd >> 1, q = idx >> 3, j = 4 * kq + q;
    const bool heavy = q != 3;
    const int N1 = heavy ? 20 : 12;
    const int lw = w & 1, pr = w & 3;
    const int rot = ROT1;
    const __amdgpu_buffer_rsrc_t rws = __builtin_amdgcn_make_buffer_rsrc(p.ws, 0, WS_END, 0x00020000);
    auto unit_of = [&](int v) { return v < 4 ? 7 * kq + v : v < 8 ? 28 + 8 * ((v >> 1) & 1) + 2 * kq + (v & 1) : 7 * kq + v - 4; };
    const unsigned sl_lo = L_P + (unsigned)pr * SLOT;
    const unsigned xo = WS_XB + (unsigned)((i * 4 + q) * 4) * 36864u + pr * 3072;
    const unsigned fo = WS_FLAG + (unsigned)(i * 16 + j) * 512u;

    if (w >= 4) {
        LANE_VARS;
        STAMP_INIT();
        STAMP(0);
        const unsigned swz = (unsigned)((p8 ^ (q8 >> 1)) << 4);
        const unsigned fa = F_DONE + (lane & 3) * 4;
        unsigned sink = 0, tg;
        auto poll16 = [&](int which) __attribute__((always_inline)) {
            wait_ge(F_OUT + (lane & 3) * 4, which + 1);
            STAMP(16);
            const unsigned pf = WS_FLAG + (unsigned)(i * 16 + 4 * kq + (lane & 3)) * 512u + which * 128 + ((lane >> 2) & 3) * 8;
            NOUNROLL for (int spin = 0; spin < SPIN_MAX; spin++) {
                fence_c();
                const v2u f = __builtin_amdgcn_raw_buffer_load_b64(rws, pf, 0, 16);
                if (__builtin_amdgcn_ballot_w64(f.x != tg || f.y != ~tg) == 0) break;
                __builtin_amdgcn_s_sleep(FL_SLEEP);
            }
            post(F_RDY + which * 4, 1);
            STAMP(17);
        };
        if (w < 6) {
            unsigned ffull = F_FULL + lw * 4;
            asm volatile("" : "+v"(ffull));
            {
                const unsigned voffS = (8 * (q8 >> 2) + (q8 & 3)) * 1024 + swz;
                const v4u srd = make_srd((const unsigned char*)(lw ? p.Bim : p.Bre) + (size_t)(32 * j) * 1024);
                NOUNROLL for (int n = 2; n < 8; n++) {
                    const unsigned so = 128 * ((n + rot) & 7);
                    dma4(srd, voffS, voffS ^ 64u, so, so + 15360, so + 2048, so + 17408, L_W + (n >> 1) * SLOT + lw * 8192 + (n & 1) * 4096);
                    if (n == 3) {
                        touch(sink, p.x, (128 * i + 8 * j + 4 * lw) * 1024 + (lane & 31) * 128);
                        touch(sink, lw ? p.Bim : p.Bre, (32 * j + 2 * i) * 1024 + c * 128);
                        bar_l();
                    }
                    if (n == 4) { waitv<6>(); postc<2>(ffull, lane); }
                    if (n >= 5) { waitv<4>(); postv(ffull, lane, n >> 1); }
                }
                waitv<0>(); postc<4>(ffull, lane);
                asm volatile("" ::"v"(sink));
            }
            STAMP(1);
            const unsigned voffG = q8 * (FF * 2) + (((p8 >> 1) ^ ((q8 >> 1) & 1)) << 5) + ((p8 & 1) << 4), voffG1 = voffG ^ 64u;
            const unsigned voffC = q8 * 1024 + swz, voffC1 = voffC ^ 64u;
            const unsigned voffD = q8 * 1024 + (((p8 >> 1) ^ (q8 >> 1) ^ (2 * (kq & 1))) << 5) + ((p8 & 1) << 4);
            const unsigned char* gb = (const unsigned char*)(lw ? p.wu : p.wg) + (7 * kq + q) * 128;
            unsigned thr = (lane >> 2) + 1;
            int credit = 0, cbase = 0;
            auto item = [&](int n, const unsigned char* b0, const unsigned char* b1, unsigned ve, unsigned vo, unsigned st) __attribute__((always_inline)) {
                v4u s0 = make_srd(b0), s1 = make_srd(b1);
                unsigned dst = ((n & 4) ? L_P : L_W) + (n & 3) * SLOT + lw * 8192, dst1 = dst + 4096;
                asm volatile("" : "+s"(s0), "+s"(s1), "+s"(dst), "+s"(dst1));
                int sp = LSPIN_MAX;
                NOUNROLL while (n - 7 > credit) {
                    const unsigned dn = peek(fa);
                    postv(ffull, lane, n - ((vm_outstanding() + 7) >> 3));
                    unsigned long long mk = __builtin_amdgcn_ballot_w64(dn >= thr);
                    mk &= mk >> 1; mk &= mk >> 2;
                    credit = __builtin_popcountll(mk & 0x1111111111111111ull) + cbase;
                    asm volatile("s_sub_i32 %0, %0, 1" : "+s"(sp)::"scc");
                    if (sp < 0) break;
                }
                waitv<INFL>();
                dma4(s0, ve, vo, 0, st - 1024, 2 * st - 2048, 3 * st - 3072, dst);
                postv(ffull, lane, n - (INFL + 7) / 8);
                waitv<INFL>();
                dma4(s1, ve, vo, 0, st - 1024, 2 * st - 2048, 3 * st - 3072, dst1);
            };
            wait_ge(F_XI + (lane & 3) * 4, 3);
            NOUNROLL for (int n = 4; n < N1; n++) {
                const int t = n - 4;
                const unsigned char* bp = gb + (size_t)(((t + rot) & 7) * 64) * (FF * 2) + (t >> 3) * 512;
                item(n, bp, bp + 32 * FF * 2, voffG, voffG1, 8 * FF * 2);
            }
            cbase = N1 - 8; thr += cbase;
            const unsigned char* dp = (const unsigned char*)p.wd + (size_t)(7 * kq) * 65536 + q * 256 + (lw ^ (kq >> 1)) * 128;
            NOUNROLL for (int v = 0; v < 11; v++) {
                if ((v & ~3) == 4) {
                    const unsigned char* cbp = (const unsigned char*)((v & 2) ? p.Cim : p.Cre) + (size_t)q * 131072 + (2 * kq + (v & 1)) * 128;
                    item(N1 + v, cbp + ((2 * lw) ^ kq) * 32768, cbp + ((2 * lw + 1) ^ kq) * 32768, voffC, voffC1, 8192);
                } else {
                    item(N1 + v, dp, dp + 32768, voffD, voffD, 8192);
                    dp += 65536;
                }
            }
            waitv<16>(); postv(ffull, lane, N1 + 9);
            waitv<8>(); postv(ffull, lane, N1 + 10);
            waitv<0>(); postv(ffull, lane, N1 + 11);
            tg = peek(L_TAG);
            STAMP(7);
        } else {
            unsigned tag0, prm0, prm1, prm2;
            const unsigned cto = min(idx * 9 + 4 * lw + p8, (int)CODE_LINES - 1) * 128;
            touch(sink, pc0, cto);
            const unsigned po = 2 * (32 * j + 8 * (lane >> 4) + 4 * ((lane >> 1) & 1) + ((lane >> 2) & 3));
            asm volatile("global_load_ushort %0, %4, %5\n\tglobal_load_ushort %1, %4, %6\n\tglobal_load_ushort %2, %4, %7\n\tglobal_load_dword %3, %8, %9 sc1"
                         : "=&v"(prm0), "=&v"(prm1), "=&v"(prm2), "=&v"(tag0) : "v"(po), "s"(p.lre), "s"(p.lim), "s"(p.ldt), "v"(WS_EPOCH), "s"(p.ws) : "memory");
            *(LAS unsigned*)(uintptr_t)(F_DONE + (lane & 31) * 4) = 0u;
            bar_l();
            sts128(L_SEL + lw * 1024 + lane * 16, make_sel(lane, lw));
            asm volatile("s_waitcnt vmcnt(0)" : "+v"(prm0), "+v"(prm1), "+v"(prm2), "+v"(tag0)::"memory");
            tg = tag0 + 1u;
            const bool odd = lane & 1;
            const unsigned la = lane * 4;
            const float lr = bflo(prm0), li = bflo(prm1);
            float dt = __expf(bflo(prm2));
            dt = bflo(pk2(dt, dt));
            const float ex = __expf(dt * lr);
            const float fr = __builtin_amdgcn_fractf(dt * li * 0.15915494309189535f);
            const float ar = smul_t(ex, __builtin_amdgcn_cosf(fr)), ai = smul_t(ex, __builtin_amdgcn_sinf(fr));
            const cpx a1 = {ar, ai};
            const cpx a2 = cmul(a1, a1), a4 = cmul(a2, a2);
            const unsigned sg = odd ? 0u : 0x80000000u;
            auto sgn = [&](cpx v) { return cpx{v.x, __uint_as_float(__float_as_uint(v.y) ^ sg)}; };
            const cpx r8 = cmul(a4, a4), r16 = cmul(r8, r8), r32 = cmul(r16, r16), r64 = cmul(r32, r32);
            const cpx a8 = sgn(r8);
            float pw[8];
            pw[0] = lw ? (odd ? r64.y : r64.x) : (odd ? 0.0f : 1.0f);
#pragma unroll
            for (int s = 1; s < 8; s++) pw[s] = lmad(a8, pw[s - 1], 0.0f);
#pragma unroll
            for (int s = 0; s < 8; s++) sts32f(L_HIN + (15 - 8 * lw - s) * 256 + la, pw[s]);
            if (lw == 0) {
                const float nr = ar - 1.0f, den = smul_t(__builtin_amdgcn_rcpf(sfma(lr, lr, smul(li, li))), 2.0f);
                float co = smul(odd ? sfms(ai, lr, smul(nr, li)) : sfma(nr, lr, smul(ai, li)), den);
                sts32f(L_CST + la, odd ? a1.y : a1.x); sts32f(L_CST + 256 + la, odd ? a4.y : a4.x);
                const cpx a1s = sgn(a1);
#pragma unroll
                for (int t = 0; t < 5; t++) { sts32f(L_CST + (2 + t) * 256 + la, co); if (t < 4) co = lmad(a1s, co, 0.0f); }
                *(LAS unsigned*)(uintptr_t)L_TAG = tg;
            }
            post(F_COMM + lw * 4, 1);
            STAMP(1);
            cpx wn[8];
            {
                const cpx r128 = cmul(r64, r64), r256 = cmul(r128, r128);
                wn[0] = lw ? r128 : cpx{1.0f, 0.0f};
#pragma unroll
                for (int n = 1; n < 8; n++) wn[n] = cmul(wn[n - 1], r256);
#pragma unroll
                for (int n = 0; n < 8; n++) { wn[n] = sgn(wn[n]); asm volatile("" : "+v"(wn[n].x), "+v"(wn[n].y)); }
            }
            wait_ge(F_XI + (lane & 3) * 4, TOUCH_GATE);
            touch(sink, lw ? p.wu : p.wg, (32 * i + (lane & 31)) * (FF * 2) + (7 * kq + q + ((heavy && lane >= 32) ? 4 : 0)) * 128);
            TW touch(sink, p.wd, (448 * kq + 28 * i + min(lane, 27)) * 1024 + (2 * q + lw) * 128);
            TW touch(sink, lw ? p.Cim : p.Cre, (128 * q + 8 * i + p8) * 1024 + (2 * kq + (q8 & 1)) * 128);
            TW touch(sink, lw ? (const void*)p.ws : (const void*)p.out, lw ? min((unsigned)(lane & 31) * 0xb0000u + idx * 0x4000u, WS_END - 4) : (lane & 3) * 0x100000 + idx * 0x4000);
            unsigned gro = WS_GRAN + j * 8192 + lane * 8;
            asm volatile("" : "+v"(gro));
            wait_ge(fa, 4);
            STAMP(4);
            if (lw) {
                float e[16];
#pragma unroll
                for (int s = 0; s < 16; s++) e[s] = lds32f(L_HIN + s * 256 + la);
                postc<2>(F_COMM + 4, lane);
#pragma unroll
                for (int d = 8; d > 0; d >>= 1)
#pragma unroll
                    for (int s = 0; s < d; s++) e[s] = sadd(e[s], e[s + d]);
                __builtin_amdgcn_raw_buffer_store_b64(v2u{__float_as_uint(e[0]), tg}, rws, gro + i * 512, 0, 16);
            }
            STAMP(5);
            {
                float e[8], h = 0;
#pragma unroll
                for (int s = 0; s < 8; s++) e[s] = lds32f(L_SEG + (8 * lw + s) * 256 + la);
#pragma unroll
                for (int s = 0; s < 8; s++) sts32f(L_SEG + (8 * lw + s) * 256 + la, pw[s]);
                wait_ge(F_COMM + (lw ^ 1) * 4, 2);
                if (lw) h = lds32f(L_XCH + la);
#pragma unroll
                for (int s = 0; s < 8; s++) { sts32f(L_HIN + (8 * lw + s) * 256 + la, h); if (s < 7 || !lw) h = lmad(a8, h, e[s]); }
                if (!lw) { sts32f(L_XCH + la, h); postc<2>(F_COMM, lane); }
            }
            STAMP(6);
            {
                const int top = i - 1 - lw;
                v2u gr[8] = {};
                NOUNROLL for (int spin = 0; spin < SPIN_MAX; spin++) {
                    fence_c();
#pragma unroll
                    for (int n = 0; n < 8; n++) gr[n] = __builtin_amdgcn_raw_buffer_load_b64(rws, gro, ((top - 2 * n) & 15) * 512, 16);
                    unsigned long long bad = 0;
#pragma unroll
                    for (int n = 0; n < 8; n++) if (top - 2 * n >= 0) bad |= __builtin_amdgcn_ballot_w64(gr[n].y != tg);
                    if (bad == 0) break;
                    __builtin_amdgcn_s_sleep(LB_SLEEP);
                }
                float h = 0;
#pragma unroll
                for (int n = 7; n >= 0; n--) if (top - 2 * n >= 0) h = lmad(wn[n], __uint_as_float(gr[n].x), h);
                sts32f(L_XCH + 256 + lw * 256 + la, h);
            }
            postc<4>(F_COMM + lw * 4, lane);
            waitv<0>();
            asm volatile("" ::"v"(sink));
            STAMP(7);
            poll16(lw);
        }
        f32x4 base[4];
        {
            const int d = 128 * q + 32 * kq + 4 * p8;
            const v2u dv = *(const GAS v2u*)(p.Dsk + d);
            const float df[4] = {bflo(dv.x), bfhi(dv.x), bflo(dv.y), bfhi(dv.y)};
            v2u xv[4];
#pragma unroll
            for (int e = 0; e < 4; e++) xv[e] = *(const GAS v2u*)(p.x + (size_t)(128 * i + 32 * pr + 8 * e + q8) * DM + d);
#pragma unroll
            for (int e = 0; e < 4; e++) {
                const unsigned tp = (32 * pr + 8 * e + 4 * ((q8 >> 1) & 1) + 2 * (q8 & 1) + (q8 >> 2)) * 4;
                const float rs = lds32f(L_RSTD + tp), nm = smul(lds32f(L_MEAN + tp), rs);
                const float xf[4] = {bflo(xv[e].x), bfhi(xv[e].x), bflo(xv[e].y), bfhi(xv[e].y)};
#pragma unroll
                for (int k = 0; k < 4; k++) base[e][k] = sfma(sfma(xf[k], rs, nm), df[k], xf[k]);
            }
        }
        STAMP(8);
        const unsigned t16 = ((tg & 0x3fffu) + 0x2000u) << 16;
        const unsigned xi = xo + kq * 36864u + lane * 16;
        auto bad3 = [&](const v4u* in) { return ((in[0].y ^ t16) | (in[0].w ^ t16) | (in[1].y ^ t16)) | ((in[1].w ^ t16) | (in[2].y ^ t16) | (in[2].w ^ t16)); };
        auto val = [&](const v4u* in, int k) { const unsigned dw = in[k / 6][2 * ((k / 3) & 1) + (k % 3 == 2)]; return (k % 3 == 1) ? bfhi(dw) : bflo(dw); };
        const unsigned sc = L_O + pr * 4096;
        unsigned wa = sc + (8 * (c >> 2) + 4 * (c & 1) + ((c >> 1) & 1)) * 128 + ((g ^ (c & 7)) << 4), ra = sc + q8 * 128 + ((p8 ^ (2 * (q8 & 1) + (q8 >> 2))) << 4);
        GAS float* op = (GAS float*)p.out + (size_t)(128 * i + 32 * pr + q8) * DM + 128 * q + 32 * kq + 4 * p8;
        asm volatile("" : "+v"(wa), "+v"(ra), "+v"(op));
        wait_ge(F_SENT + pr * 4, 3);
        __builtin_amdgcn_s_sleep(X_FIRST);
        v4u in[9];
        f32x4 own[4];
        NOUNROLL for (int spin = 0; spin < SPIN_MAX; spin++) {
            fence_c();
#pragma unroll
            for (int n = 0; n < 9; n++) in[n] = __builtin_amdgcn_raw_buffer_load_b128(rws, xi + (n / 3) * 12288 + (n % 3) * 1024, 0, 16);
            SB();
            wait_ge(F_SENT + pr * 4, 4);
#pragma unroll
            for (int e = 0; e < 4; e++) own[e] = lds128f(sc + e * 1024 + lane * 16);
            if (__builtin_amdgcn_ballot_w64((bad3(in) | bad3(in + 3) | bad3(in + 6)) > 0xffffu) == 0) break;
        }
        STAMP(11);
#pragma unroll
        for (int e = 0; e < 4; e++) {
            f32x4 t = own[e];
#pragma unroll
            for (int sr = 0; sr < 9; sr += 3) t += f32x4{val(in + sr, 4 * e), val(in + sr, 4 * e + 1), val(in + sr, 4 * e + 2), val(in + sr, 4 * e + 3)};
            own[e] = t;
        }
        asm volatile("" : "+v"(own[0]), "+v"(own[1]), "+v"(own[2]), "+v"(own[3]));
#pragma unroll
        for (int e = 0; e < 4; e++) sts128f((wa ^ (unsigned)((e >> 1) << 6)) + (e & 1) * 256, own[e]);
        fence_c();
        f32x4 ta[4];
#pragma unroll
        for (int e = 0; e < 4; e++) ta[e] = lds128f((ra ^ (unsigned)((e & 1) << 6)) + e * 1024);
        SB();
#pragma unroll
        for (int e = 0; e < 4; e++) *(GAS f32x4*)(op + e * 8 * DM) = base[e] + ta[e];
        if (b == 255 && w == 4 && lane == 0) __builtin_amdgcn_raw_buffer_store_b32(tg, rws, WS_EPOCH, 0, 16);
        STAMP(12); STAMP_DUMP();
        return;
    }
    constexpr int PD = PDV, XL = XLV;
    unsigned stall = 0; (void)stall;
    v4u xa[16][2];
#define AF(u, k) xa[2 * (u) + ((k) >> 1)][(k) & 1]
    v4u sws = make_srd(p.ws);
    unsigned oG = WS_A2 + (unsigned)(i * 44 + 7 * kq) * 16384u + w * 4096, oC = WS_A2 + (unsigned)(i * 44 + 28 + 2 * kq) * 16384u + w * 4096, oF = fo + w * 8;
    asm volatile("" : "+s"(sws), "+s"(oG), "+s"(oC), "+s"(oF));
    auto uoff = [&](int v) __attribute__((always_inline)) { return v < 4 ? oG + v * 16384 : v < 8 ? oC + (8 * ((v >> 1) & 1) + (v & 1)) * 16384 : oG + (v - 4) * 16384; };
    LANE_VARS;
    unsigned fdone = F_DONE + w * 4, l16 = lane * 16;
    asm volatile("" : "+v"(fdone), "+v"(l16));
    unsigned tgs = 0; int raised = 0;
    auto raise = [&](int level) __attribute__((always_inline)) {
        asm volatile("s_mov_b64 exec, 1\n\tbuffer_store_dwordx2 %0, off, %1, %2 offset:%3 sc1\n\ts_mov_b64 exec, -1" ::"v"(v2u{tgs, ~tgs}), "s"(sws), "s"(oF), "n"((level - 1) * 128) : "memory");
        post(fdone + (F_OUT - F_DONE), level);
        raised = level;

        if (level == 1) STAMP(17); else STAMP(18);
    };
    auto try_raise = [&](int level) __attribute__((always_inline)) {
        unsigned v; asm volatile("s_getreg_b32 %0, hwreg(HW_REG_IB_STS, 0, 4)" : "=s"(v)::"memory");
        if (__builtin_expect((int)(v + raised) == level - 1, 0)) raise(level);
    };
    unsigned oX[3];
#pragma unroll
    for (int sl = 1; sl < 4; sl++) { oX[sl - 1] = xo + (kq ^ sl) * 36864u + (sl - 1) * 12288; asm volatile("" : "+s"(oX[sl - 1])); }
    unsigned cbs[2], tbs[4];
    v4u bf[PD];
    const unsigned ffl = F_FULL + (lane & 1) * 4;
    auto set2 = [&](auto kk) __attribute__((always_inline)) {
        constexpr int k = decltype(kk)::value;
        if constexpr (k == 0) tbs[0] = L_P + ((l16 << 1) & 0x780) + ((l16 >> 1) & 0x18) + (((l16 >> 2) & 0x40) | ((l16 >> 2) & 0x20));
        else if constexpr (k < 4) tbs[k] = tbs[0] ^ (unsigned)(k << 5);
        else if constexpr (k == 4) cbs[0] = L_W + ((l16 << 3) & 0x780) + (((l16 >> 4) ^ (l16 >> 1)) & 0x70);
        else cbs[1] = cbs[0] ^ 64u;
        if constexpr (k < 4) asm volatile("" : "+v"(tbs[k])); else asm volatile("" : "+v"(cbs[k - 4]));
    };
    auto prime2 = [&](auto mm) __attribute__((always_inline)) { constexpr int m = decltype(mm)::value; const v2u lo = ldstr(tbs[m]), hi = ldstr(tbs[m] + 2048); bf[m] = v4u{lo.x, lo.y, hi.x, hi.y}; };
    {
        STAMP_INIT();
        STAMP(0);
        unsigned cb[2];
        cb[0] = (unsigned)c * 128u + (unsigned)((g ^ (c >> 1)) << 4); cb[1] = cb[0] ^ 64u;
        f32x4 eX;
        unsigned tb[4];
        f32x4 nm4[2];
        {
            const unsigned voffX = (8 * (q8 >> 2) + 4 * (q8 & 1) + ((q8 >> 1) & 1)) * 1024 + ((p8 ^ (q8 >> 1)) << 4), voffX1 = voffX ^ 64u;
            const v4u srd = make_srd((const unsigned char*)p.x + (size_t)(128 * i + 32 * w) * 1024);
            auto issue_x = [&](int ln) __attribute__((always_inline)) {
                const unsigned s0 = ((ln + rot) & 7) * 128;
                dma4(srd, voffX, voffX1, s0, s0 + 15360, s0, s0 + 15360, sl_lo + (ln & 3) * 4096);
            };
            {
                const unsigned voffS = (8 * (q8 >> 2) + (q8 & 3)) * 1024 + ((p8 ^ (q8 >> 1)) << 4), so = 128 * (((w >> 1) + rot) & 7);
                dma4(make_srd((const unsigned char*)(lw ? p.Bim : p.Bre) + (size_t)(32 * j) * 1024), voffS, voffS ^ 64u, so, so + 15360, so + 2048, so + 17408, L_W + lw * 8192 + (w >> 1) * 4096);
            }
#pragma unroll
            for (int ln = 0; ln < XL; ln++) issue_x(ln);
            v4u ones = {0x3f803f80u, 0x3f803f80u, 0x3f803f80u, 0x3f803f80u};
            unsigned xl[2] = {cb[0] + sl_lo, cb[1] + sl_lo};
            cb[0] += L_W; cb[1] += L_W;
            asm volatile("" : "+v"(ones), "+v"(cb[0]), "+v"(cb[1]), "+v"(xl[0]), "+v"(xl[1]));
            f32x4 sm[2], sq[2], bsum;
            v4u bx[4];
            unsigned cbw[2] = {cb[0] + (w >> 1) * 8192 + (w & 1) * 2048, cb[1] + (w >> 1) * 8192 + (w & 1) * 2048};
            asm volatile("" : "+v"(cbw[0]), "+v"(cbw[1]));
            f32x4 acc[4][2];
            unsigned fl = 0; bool lack = false;
            const unsigned fcs = (lane & 4) ? F_COMM + (lane & 1) * 4 : F_BS + (lane & 3) * 4;
            static_assert(PD == 4 && XL <= 3, "prologue schedule");
            waitv<4 * (XL - 1)>();
#pragma unroll
            for (int f = 0; f < 4; f++) xa[f >> 1][f & 1] = lds128(xl[f >> 1] + (f & 1) * 2048);
            asm volatile("s_barrier" ::: "memory");
            STAMP(1);
            sfor<-PD, 64>(LAMB(nn) {
                constexpr int n = decltype(nn)::value, m = n + PD, ks = n >> 2, ln = n >> 3;
                v4u cur;
                if constexpr (n >= 0) { cur = bf[n % PD]; mma<(n < 4)>(acc[n & 3][0], xa[ks][0], cur); SB(); }
                if constexpr (m > 0 && (m & 15) == 0 && m < 64) need_full(lack, ffl, (m >> 4) + 1);
                if constexpr (m < 64) bf[m % PD] = lds128(cb[(m >> 2) & 1] + wslot(m >> 4) + (((m >> 1) & 1) * 4 + ((m >> 3) & 1) * 2 + (m & 1)) * 2048);
                SB();
                if constexpr (n >= 0) { mma<(n < 4)>(acc[n & 3][1], xa[ks][1], cur); SB(); }
                if constexpr (n >= 0 && (n & 7) < 4 && ln + XL < 8)
                    dma1(srd, (n & 1) ? voffX1 : voffX, ((ln + XL + rot) & 7) * 128 + (n & 1) * 16384 + (n & 2) * 1024, sl_lo + ((ln + XL) & 3) * 4096 + (n & 3) * 1024);
                if constexpr (n >= 0 && (n & 7) == 4 && ln + XL == 7) post(fdone + (F_XI - F_DONE), 1);
                if constexpr (n >= 0 && (n & 7) == 4 && ln < 7) waitv<4 * ((ln + XL < 7 ? ln + XL : 7) - ln - 1)>();
                if constexpr (n >= 0 && (n & 7) >= 4 && ln < 7) xa[2 * ln + 2 + ((n >> 1) & 1)][n & 1] = lds128(xl[(n >> 1) & 1] + ((ln + 1) & 3) * 4096 + (n & 1) * 2048);
                if constexpr (n == 56) post(fdone + (F_XI - F_DONE), 3);
                if constexpr ((m & 15) == 10 && m < 48) fl = peek(ffl);
                if constexpr ((m & 15) == 14 && m < 48) lack = any_lt(fl, (m >> 4) + 2);
                if constexpr ((m & 15) == 15 && m < 63) post(fdone, (m >> 4) + 1);
                if constexpr (m < 64 && (m & 15) < 4) bx[m & 3] = lds128(cbw[m & 1] + wslot(m >> 4) + ((m >> 1) & 1) * 4096);
                if constexpr (n == 55) { sts32f(L_CST + 7 * 256 + c * 16 + (w & 1) * 8 + (w >> 1) * 4, bsum.x); post(fdone + (F_BS - F_DONE), 1); }
                if constexpr (n == 60) fl = peek(fcs);
                SB();
                if constexpr (n >= 0 && (n & 15) < 4) mma<(n == 0)>(bsum, ones, bx[n & 3]);
                if constexpr (n >= 0) { if constexpr (n & 1) mma<(n < 4)>(sq[(n >> 1) & 1], xa[ks][(n >> 1) & 1], xa[ks][(n >> 1) & 1]); else mma<(n < 4)>(sm[(n >> 1) & 1], ones, xa[ks][(n >> 1) & 1]); }
                SB();
            });
            need_full(any_lt(fl, 1), fcs, 1);
            const unsigned cl = L_CST + (l16 & 0xf0);
            const f32x4 ca = lds128f(cl), c4 = lds128f(cl + 256), co = lds128f(cl + 512), bs = lds128f(cl + 7 * 256), w15 = lds128f(L_HIN + w * 1024 + l16);
            mfma_done();
            STAMP(2); VAL(24, stall); stall = 0;
            {
                float dg[2];
#pragma unroll
                for (int bb = 0; bb < 2; bb++) {
                    nm4[bb] = sm[bb] * (-1.0f / DM);
                    const float s01 = (lane & 1) ? sq[bb].y : sq[bb].x, s23 = (lane & 1) ? sq[bb].w : sq[bb].z;
                    dg[bb] = __builtin_amdgcn_rsqf(fmaxf(((lane & 2) ? s23 : s01) * (1.0f / DM) - nm4[bb].x * nm4[bb].x, 0.0f) + 1e-5f);
                }
                if (g == (c >> 2)) {
                    const unsigned sa = L_RSTD + (32 * w + 8 * g + (c & 3)) * 4;
                    sts32f(sa, dg[0]); sts32f(sa + 16, dg[1]); sts32f(sa + L_MEAN, nm4[0].x); sts32f(sa + L_MEAN + 16, nm4[1].x);
                }
            }
            fence_c();
            const unsigned ra = L_RSTD + (32 * w + 8 * g) * 4;
            const f32x4 nt0 = lds128f(ra + L_MEAN), nt1 = lds128f(ra + L_MEAN + 16), rs0 = lds128f(ra), rs1 = lds128f(ra + 16);
            {
                const int q4 = c >> 2, p4 = c & 3;
                const unsigned tb1 = L_P + (unsigned)(8 * g + q4) * 128u + (unsigned)((((q4 >> 1) & 1) | ((g & 1) << 1)) << 5) + p4 * 8;
#pragma unroll
                for (int t = 0; t < 4; t++) { tb[t] = tb1 ^ (unsigned)(t << 5); asm volatile("" : "+v"(tb[t])); }
            }
            SB();
            STAMP(3);
            f32x4 ev;
            f32x2 sr[2][4], si[2][4];
#pragma unroll
            for (int st = 0; st < 2; st++) {
                const f32x2 b2 = st ? hi2(bs) : lo2(bs);
#pragma unroll
                for (int t = 0; t < 4; t++) {
                    const f32x4 nt = (t & 2) ? nt1 : nt0;
                    const f32x2 n2 = (t & 1) ? hi2(nt) : lo2(nt);
                    sr[st][t] = bfma<0, 0>(b2, n2, (t & 1) ? hi2(acc[st][t >> 1]) : lo2(acc[st][t >> 1]));
                    si[st][t] = bfma<1, 0>(b2, n2, (t & 1) ? hi2(acc[2 + st][t >> 1]) : lo2(acc[2 + st][t >> 1]));
                }
            }
            SB();
#pragma unroll
            for (int st = 0; st < 2; st++)
#pragma unroll
                for (int t = 0; t < 4; t++) {
                    const f32x4 rs = (t & 2) ? rs1 : rs0;
                    const f32x2 r2 = (t & 1) ? hi2(rs) : lo2(rs);
                    sr[st][t] *= r2; si[st][t] *= r2;
                }
            SB();
            const f32x2 a1[2] = {lo2(ca), hi2(ca)}, cf[2] = {lo2(co), hi2(co)};
#pragma unroll
            for (int t = 1; t < 4; t++) {
                f32x2 tr[2], ti[2];
#pragma unroll
                for (int st = 0; st < 2; st++) { tr[st] = bfma<1, 1>(a1[st], si[st][t - 1], sr[st][t]); ti[st] = bfma<1, 0>(a1[st], sr[st][t - 1], si[st][t]); }
                SB();
#pragma unroll
                for (int st = 0; st < 2; st++) { sr[st][t] = bfma<0, 0>(a1[st], sr[st][t - 1], tr[st]); si[st][t] = bfma<0, 0>(a1[st], si[st][t - 1], ti[st]); }
                SB();
            }
#pragma unroll
            for (int st = 0; st < 2; st++) {
                ev[2 * st] = sfma(c4[2 * st], sr[st][3].x, sfnma(c4[2 * st + 1], si[st][3].x, sr[st][3].y));
                ev[2 * st + 1] = sfma(c4[2 * st], si[st][3].x, sfma(c4[2 * st + 1], sr[st][3].x, si[st][3].y));
                eX[2 * st] = sr[st][3].x; eX[2 * st + 1] = si[st][3].x;
            }
            sts128f(L_SEG + w * 1024 + l16, ev);
            {
                f32x4 ew;
#pragma unroll
                for (int st = 0; st < 2; st++) {
                    ew[2 * st] = sfms(w15[2 * st], ev[2 * st], smul(w15[2 * st + 1], ev[2 * st + 1]));
                    ew[2 * st + 1] = sfma(w15[2 * st], ev[2 * st + 1], smul(w15[2 * st + 1], ev[2 * st]));
                }
                sts128f(L_HIN + w * 1024 + l16, ew);
            }
            post(fdone, 4);
            SB();
            unsigned ya = L_O + w * 4096 + l16;
            asm volatile("" : "+v"(ya), "+v"(eX));
#pragma unroll
            for (int st = 0; st < 2; st++) {
                f32x2 tr[4], ti[4];
#pragma unroll
                for (int t = 0; t < 4; t++) { tr[t] = bmul<1, 1>(cf[st], si[st][t]); ti[t] = bmul<1, 1>(cf[st], sr[st][t]); }
                SB();
#pragma unroll
                for (int t = 0; t < 4; t++) { tr[t] = bfma<0, 0>(cf[st], sr[st][t], tr[t]); ti[t] = bfma<0, 1>(cf[st], si[st][t], ti[t]); }
                SB();
                sts128(ya + st * 1024, v4u{pk2(tr[0]), pk2(tr[1]), pk2(tr[2]), pk2(tr[3])});
                sts128(ya + 2048 + st * 1024, v4u{pk2(ti[0]), pk2(ti[1]), pk2(ti[2]), pk2(ti[3])});
                SB();
            }
        }
        STAMP(4);
        {
            const v4u sel0 = lds128(L_SEL + l16), sel1 = lds128(L_SEL + 1024 + l16);
            f32x4 ct[6];
            unsigned fl = 0;
            sfor<0, 68>(LAMB(kk) {
                constexpr int k = decltype(kk)::value, e = k - 4;
                if constexpr (k < 64) mma_c(ct[k % 6], (k & 1) ? sel1 : sel0, xa[k >> 2][(k >> 1) & 1], nm4[(k >> 1) & 1]);
                if constexpr (k >= 64 || k < 4) asm volatile("s_nop 1");
                if constexpr (e >= 0) {
                    xa[e >> 2][(e >> 1) & 1][2 * (e & 1)] = cvtv(ct[e % 6].x, ct[e % 6].y);
                    xa[e >> 2][(e >> 1) & 1][2 * (e & 1) + 1] = cvtv(ct[e % 6].z, ct[e % 6].w);
                }
                if constexpr (k == 40) fl = peek(ffl);
                if constexpr (k == 52) need_full(any_lt(fl, 5), ffl, 5);
                if constexpr (k >= 53 && k < 53 + PD) { constexpr int m = k - 53; const unsigned a = tb[(m >> 1) & 3] + (m & 1) * 8192; const v2u lo = ldstr(a), hi = ldstr(a + 512); bf[(64 + m) % PD] = v4u{lo.x, lo.y, hi.x, hi.y}; }
                SB();
            });
        }
        STAMP(5);
        unsigned go = oG + q * 16384, so = oC + (q >> 1) * 16384 + (q & 1) * 2048;
        asm volatile("" : "+s"(go), "+s"(so));
        f32x4 acc[4][2][2];
        f32x2 r12;
        unsigned fc = 0;
        auto gpass = [&](auto pp, auto n0, auto n1) __attribute__((always_inline)) {
            constexpr int pass = decltype(pp)::value;
            unsigned fl = 0, dn = 0; bool lack = false;
            sfor<decltype(n0)::value, decltype(n1)::value>(LAMB(nn) {
                constexpr int n = decltype(nn)::value, m = n + PD;
                constexpr bool pre = pass == 0 || m < 128;
                constexpr unsigned off = wslot(m >> 4) + (m & 1) * 8192 + ((m >> 3) & 1) * 4096;
                const v4u cur = bf[(n + 64 + 128 * pass) % PD];
                mma<(n < 8)>(acc[(n >> 1) & 3][n & 1][0], cur, xa[n >> 3][0]);
                SB();
                if constexpr ((m & 15) == 0) post(fdone, dn);
                if constexpr ((m & 15) == 0) need_full(lack, ffl, 5 + 8 * pass + (m >> 4));
                v2u lo, hi;
                if constexpr (pre) lo = ldstr(tb[(m >> 1) & 3] + off); else lo = ldstr(tbs[m & 3]);
                if constexpr (pass == 1 && n >= 69 && n < 81 && (n & 1)) set2(IC<(n - 69) / 2>{});
                if constexpr ((m & 15) == 10) fl = peek(ffl);
                if constexpr ((m & 15) == 15) lack = any_lt(fl, m == 127 && pass == 0 ? N1 - 7 : 6 + 8 * pass + (m >> 4));
                if constexpr ((m & 15) == 13) { dn = 5 + 8 * pass + (m >> 4); asm volatile("" : "+v"(dn)); }
                if constexpr (n == 88 && pass == 1) fc = peek(F_RDY);
                if constexpr (n == 94 && pass == 1) { if (__builtin_expect(any_lt(fc, 1), 0)) { wait_ge(F_RDY, 1); } fence_c(); }
                if constexpr (n >= 96 && pass == 1 && (n & 1) == 0) { constexpr int f = (n - 96) >> 1; reldA<(f & 3) * 1024>(AF(f >> 2, f & 3), l16, sws, uoff(f >> 2)); }
                if constexpr (pass == 1 && (n == 33 || n == 41 || n == 49 || n == 57)) try_raise(1);
                if constexpr (pass == 1 && n == 67) { if (raised < 1) { waitv<0>(); raise(1); } }
                if constexpr (n == 120 && pass == 0) fc = peek(ffl + (F_COMM - F_FULL));
                if constexpr (n == 121 && pass == 0) { const unsigned ra = L_RSTD + w * 128 + (((l16 >> 1) & 0x60) | ((l16 >> 2) & 0xc)); r12 = f32x2{lds32f(ra), lds32f(ra + 16)}; }
                SB();
                mma<(n < 8)>(acc[(n >> 1) & 3][n & 1][1], cur, xa[n >> 3][1]);
                SB();
                if constexpr (pre) hi = ldstr(tb[(m >> 1) & 3] + off + 512); else hi = ldstr(tbs[m & 3] + 2048);
                bf[n % PD] = v4u{lo.x, lo.y, hi.x, hi.y};
                if constexpr ((m & 57) == 57 && (pass == 0 || m < 64)) {
                    tb[(m >> 1) & 3] += (m & 64) ? L_P - L_W : L_W - L_P; asm volatile("" : "+v"(tb[(m >> 1) & 3]));
                }
                SB();
            });
        };
        auto gated = [&](auto ff) __attribute__((always_inline)) {
            constexpr int uu = decltype(ff)::value >> 1, bb = decltype(ff)::value & 1;
            const f32x4 h0 = acc[2 * uu][0][bb] * acc[2 * uu][1][bb], h1 = acc[2 * uu + 1][0][bb] * acc[2 * uu + 1][1][bb];
            return v4u{pk2(bmul<bb, 0>(r12, lo2(h0))), pk2(bmul<bb, 0>(r12, hi2(h0))), pk2(bmul<bb, 0>(r12, lo2(h1))), pk2(bmul<bb, 0>(r12, hi2(h1)))};
        };
        gpass(IC<0>{}, IC<0>{}, IC<128>{});
        {
            r12 = r12 * r12;
            mfma_done();
            STAMP(6); VAL(25, stall); stall = 0;
            sfor<0, 2>(LAMB(ff) { stA<decltype(ff)::value * 1024>(gated(ff), l16, sws, go); });
            SB();
            need_full(any_lt(fc, 4), ffl + (F_COMM - F_FULL), 4);
            const unsigned tg = peek(L_TAG);
            const unsigned cl = L_CST + (l16 & 0xf0);
            unsigned ya = L_O + w * 4096 + l16;
            asm volatile("" : "+v"(ya));
            const f32x4 h0 = lds128f(L_XCH + 256 + (l16 & 0xf0)), h1 = lds128f(L_XCH + 512 + (l16 & 0xf0));
            const f32x4 ap = lds128f(L_SEG + w * 1024 + l16), pf = lds128f(L_HIN + w * 1024 + l16);
            const v4u sel[2] = {lds128(L_SEL + l16), lds128(L_SEL + 1024 + l16)};
            const v4u yr[2] = {lds128(ya), lds128(ya + 1024)}, yi[2] = {lds128(ya + 2048), lds128(ya + 3072)};
            const f32x4 c4 = lds128f(cl + 256);
            f32x4 cw[4];
            SB();
            sfor<2, 4>(LAMB(ff) { stA<decltype(ff)::value * 1024>(gated(ff), l16, sws, go); });
            tgs = __builtin_amdgcn_readfirstlane(tg);
#pragma unroll
            for (int t = 0; t < 4; t++) cw[t] = lds128f(cl + (3 + t) * 256);
            SB();
            f32x4 zr[2][2], zi[2][2];
#pragma unroll
            for (int st = 0; st < 2; st++)
#pragma unroll
                for (int bb = 0; bb < 2; bb++) { mma<true>(zr[st][bb], yr[st], sel[bb]); mma<true>(zi[st][bb], yi[st], sel[bb]); }
            SB();
            f32x2 hr[2], hi[2];
            {
                const f32x4 cy = h0 + h1;
                f32x4 hin;
                float t0[2], t1[2];
#pragma unroll
                for (int st = 0; st < 2; st++) { t0[st] = sfnma(ap[2 * st + 1], cy[2 * st + 1], pf[2 * st]); t1[st] = sfma(ap[2 * st + 1], cy[2 * st], pf[2 * st + 1]); }
                SB();
#pragma unroll
                for (int st = 0; st < 2; st++) { hin[2 * st] = sfma(ap[2 * st], cy[2 * st], t0[st]); hin[2 * st + 1] = sfma(ap[2 * st], cy[2 * st + 1], t1[st]); }
                SB();
#pragma unroll
                for (int st = 0; st < 2; st++) { t0[st] = sfnma(c4[2 * st + 1], hin[2 * st + 1], eX[2 * st]); t1[st] = sfma(c4[2 * st + 1], hin[2 * st], eX[2 * st + 1]); }
                SB();
#pragma unroll
                for (int st = 0; st < 2; st++) { hr[st] = f32x2{hin[2 * st], sfma(c4[2 * st], hin[2 * st], t0[st])}; hi[st] = f32x2{hin[2 * st + 1], sfma(c4[2 * st], hin[2 * st + 1], t1[st])}; }
                SB();
            }
#pragma unroll
            for (int st = 0; st < 2; st++) {
                f32x2 tr[4], ti[4];
#pragma unroll
                for (int t = 0; t < 4; t++) { const f32x2 wv = st ? hi2(cw[t]) : lo2(cw[t]); tr[t] = bmul<1, 1>(wv, hi[st]); ti[t] = bmul<1, 1>(wv, hr[st]); }
                SB();
#pragma unroll
                for (int t = 0; t < 4; t++) { const f32x2 wv = st ? hi2(cw[t]) : lo2(cw[t]); tr[t] = bfma<0, 0>(wv, hr[st], tr[t]); ti[t] = bfma<0, 1>(wv, hi[st], ti[t]); }
                SB();
                v4u fr = {pk2(tr[0]), pk2(tr[1]), pk2(tr[2]), pk2(tr[3])}, fi = {pk2(ti[0]), pk2(ti[1]), pk2(ti[2]), pk2(ti[3])};
                asm volatile("s_nop 1" : "+v"(fr), "+v"(fi));
#pragma unroll
                for (int bb = 0; bb < 2; bb++) { mma<false>(zr[st][bb], fr, sel[bb]); mma<false>(zi[st][bb], fi, sel[bb]); }
                SB();
            }
            mfma_done();
            stA<0>(pk8(zr[0][0], zr[1][0]), l16, sws, so); stA<0>(pk8(zi[0][0], zi[1][0]), l16, sws, so + 8 * 16384);
            stA<1024>(pk8(zr[0][1], zr[1][1]), l16, sws, so); stA<1024>(pk8(zi[0][1], zi[1][1]), l16, sws, so + 8 * 16384);
            SB();
            STAMP(7);
        }
        if (heavy) {
            gpass(IC<1>{}, IC<0>{}, IC<128>{});
            mfma_done();
            STAMP(8); VAL(26, stall); stall = 0;
            sfor<0, 4>(LAMB(ff) { stA<decltype(ff)::value * 1024>(gated(ff), l16, sws, go + 4 * 16384); });
            SB();
        }
        int q2 = q;
        asm volatile("" : "+s"(q2));
        if (q2 == 3) {
            waitv<0>(); raise(1); raise(2);
            sfor<0, 6>(set2);
            wait_ge(ffl, N1 + 1);
            sfor<0, PD>(prime2);
            wait_ge(F_RDY, 1);
            sfor<0, 16>(LAMB(ff) { constexpr int f = decltype(ff)::value; reldA<(f & 3) * 1024>(AF(f >> 2, f & 3), l16, sws, uoff(f >> 2)); });
#pragma unroll
            for (int u = 0; u < 4; u++) asm volatile("s_waitcnt vmcnt(0)" : "+v"(AF(u, 0)), "+v"(AF(u, 1)), "+v"(AF(u, 2)), "+v"(AF(u, 3))::"memory");
        }
    }
    STAMP(9);
    {
        f32x4 acc[8][2];
        v4u pk[3];
        unsigned fl = 0, fr = 0, dn = 0; bool lack = false;
        float tagf = 0;
        constexpr auto fv = [](int n) { return n < 48 ? n >> 4 : n < 108 ? 3 + (n - 48) / 12 : n < 144 ? 8 + ((n - 108) % 12) / 4 : 3 + (n - 144) / 4; };
        constexpr auto fk = [](int n) { return n < 48 ? (n >> 3) & 1 : n < 108 ? ((n - 48) % 12) / 6 : (n >> 1) & 1; };
        constexpr auto fd = [](int n) { return n < 48 ? n & 7 : n < 108 ? 2 + (n - 48) % 6 : n < 144 ? 2 * ((n - 108) / 12 + 1) + (n & 1) : n & 1; };
        constexpr auto first = [](int n) { return n >= 0 && (n < 48 ? (n & 15) == 0 : n < 108 ? (n - 48) % 12 == 0 : n < 120 && (n & 3) == 0); };
        constexpr int LG = LGATE;
        sfor<0, 176>(LAMB(nn) {
            constexpr int n = decltype(nn)::value, m = n + PD;
            constexpr int v = fv(n), ks = fk(n), dt = fd(n);
            constexpr int mv = fv(m), mk = fk(m), md = fd(m);
            constexpr bool mc = (mv & ~3) == 4;
            constexpr unsigned moff = (mv & 3) * SLOT + (mc ? md * 2048 : (md >> 2) * 8192 + mk * 4096);
            constexpr auto chk = [](int q) { return q == 16 ? 2 : q == 32 ? 4 : q == 60 ? 8 : q == 108 ? 11 : 0; };
            v4u cur; v2u lo, hi;
            if constexpr (n >= 0) {
                if constexpr (first(n)) {
                    constexpr int mid = n <= 32 ? 0 : (n - 31) / 2 < 16 ? (n - 31) / 2 : 16, late = n <= LG ? 0 : n - LG < 12 ? n - LG : 12;
                    constexpr int NW = v == 0 ? 16 : v == 1 ? 12 : v < 4 ? 63 : v < 8 ? mid - 4 * (v - 3) + late : late - 4 * (v - 7);
                    static_assert(NW >= 0, "load schedule");
                    asm volatile("s_waitcnt vmcnt(%4)" : "+v"(AF(v & 7, 0)), "+v"(AF(v & 7, 1)), "+v"(AF(v & 7, 2)), "+v"(AF(v & 7, 3)) : "n"(NW) : "memory");
                }
                cur = bf[n % PD];
                mma<(n < 8)>(acc[dt][0], cur, AF(v & 7, 2 * ks));
                SB();
            }
            if constexpr (m > 0 && (m & 15) == 0 && m <= 48) post(fdone, dn);
            if constexpr (chk(m) > 0) need_full(lack, ffl, N1 + chk(m));
            if constexpr (m < 176) { if constexpr (mc) bf[m % PD] = lds128(cbs[mk] + moff); else lo = ldstr(tbs[md & 3] + moff); }
            if constexpr (chk(m + 6) > 0) fl = peek(ffl);
            if constexpr (chk(m + 1) > 0) lack = any_lt(fl, N1 + chk(m + 1));
            if constexpr ((m & 15) == 13 && m < 48) { dn = N1 + mv + 1; asm volatile("" : "+v"(dn)); }
            if constexpr (n == 19 || n == 25) try_raise(2);
            if constexpr (n == 31) { if (raised < 2) { waitv<0>(); raise(2); } }
            if constexpr (n >= 32 && n < 64 && (n & 1) == 0) { constexpr int f = (n - 32) >> 1; reldA<(f & 3) * 1024>(AF(4 + (f >> 2), f & 3), l16, sws, uoff(4 + (f >> 2))); }
            if constexpr (n == LG - 6) fr = peek(F_RDY + 4);
            if constexpr (n == LG - 1) need_full(any_lt(fr, 1), F_RDY + 4, 1);
            if constexpr (n >= LG && n < LG + 12) { constexpr int f = n - LG; reldA<(f & 3) * 1024>(AF(f >> 2, f & 3), l16, sws, uoff(8 + (f >> 2))); }
            if constexpr (n == 70) { tagf = __uint_as_float(((tgs & 0x3fffu) + 0x2000u) << 16); asm volatile("" : "+v"(tagf)); }
            SB();
            if constexpr (n >= 0) { mma<(n < 8)>(acc[dt][1], cur, AF(v & 7, 2 * ks + 1)); SB(); }
            if constexpr (m < 176 && !mc) { hi = ldstr(tbs[md & 3] + moff + 2048); bf[m % PD] = v4u{lo.x, lo.y, hi.x, hi.y}; }
            if constexpr (n >= 120 && n < 156) {
                constexpr int sl = (n - 108) / 12, r = (n - 108) % 12 - 2;
                auto val = [&](int k) { return acc[2 * sl + (k >> 3)][(k >> 2) & 1][k & 3]; };
                if constexpr (r >= 0 && r < 9 && r % 3 < 2) {
                    constexpr int gi = 2 * (r / 3) + r % 3;
                    pk[gi >> 1][2 * (gi & 1)] = cvtv(val(3 * gi), val(gi < 5 ? 3 * gi + 1 : 15));
                    pk[gi >> 1][2 * (gi & 1) + 1] = cvtv(val(gi < 5 ? 3 * gi + 2 : 15), tagf);
                }
                if constexpr (r >= 0 && r % 3 == 2)
                    stX<(r / 3) * 1024>(pk[r / 3], l16, sws, oX[sl - 1]);
                if constexpr (r == 9 && sl == 3) post(fdone + (F_SENT - F_DONE), 3);
            }
            if constexpr (n == 107) { STAMP(10); VAL(28, stall); stall = 0; }
            SB();
        });
        mfma_done();
        STAMP(11); VAL(29, stall);
#pragma unroll
        for (int e = 0; e < 4; e++) sts128f(L_O + w * 4096 + e * 1024 + l16, acc[e >> 1][e & 1]);
        post(fdone + (F_SENT - F_DONE), 4);
    }
    STAMP(14); STAMP_DUMP();
}

extern "C" void kernel_launch(void* const* d_in, const int* in_sizes, int n_in, void* d_out, int out_size, void* d_ws, size_t ws_size,
                              hipStream_t stream) {
    Params p;
    p.x = (const u16*)d_in[0]; p.lre = (const u16*)d_in[1]; p.lim = (const u16*)d_in[2];
    p.Bre = (const u16*)d_in[3]; p.Bim = (const u16*)d_in[4]; p.Cre = (const u16*)d_in[5]; p.Cim = (const u16*)d_in[6];
    p.ldt = (const u16*)d_in[7]; p.Dsk = (const u16*)d_in[8];
    p.wg = (const u16*)d_in[10]; p.wu = (const u16*)d_in[11]; p.wd = (const u16*)d_in[12];
    p.out = (float*)d_out; p.ws = (unsigned char*)d_ws;
    (void)hipFuncSetAttribute((const void*)fused, hipFuncAttributeMaxDynamicSharedMemorySize, L_TOTAL);
    fused<<<256, 512, L_TOTAL, stream>>>(p);
}
```

```cpp
#define PROBE 0
#define PROBEW 0
#include <hip/hip_runtime.h>
#include <stdint.h>

typedef unsigned short u16;
typedef unsigned v4u __attribute__((ext_vector_type(4)));
typedef unsigned v2u __attribute__((ext_vector_type(2)));
typedef short s16x4 __attribute__((ext_vector_type(4)));
typedef __bf16 bf16x2 __attribute__((ext_vector_type(2)));
typedef __bf16 bf16x8 __attribute__((ext_vector_type(8)));
typedef float f32x2 __attribute__((ext_vector_type(2)));
typedef float f32x4 __attribute__((ext_vector_type(4)));
#define GAS __attribute__((address_space(1)))
#define LAS __attribute__((address_space(3)))
#define DEVI __device__ __forceinline__
#define NOUNROLL _Pragma("nounroll")

#define SPIN_SLEEP 0
constexpr int DM = 512, FF = 1792;
constexpr unsigned WS_A2 = 0;
constexpr unsigned WS_XB = 16u * 44u * 16384u;
constexpr unsigned WS_GRAN = WS_XB + 256u * 36864u;
constexpr unsigned WS_FLAG = WS_GRAN + 16u * 16u * 512u;
constexpr unsigned WS_EPOCH = WS_FLAG + 1024u * 128u;
constexpr unsigned WS_END = WS_EPOCH + 256u;
constexpr unsigned SLOT = 16384;
constexpr unsigned L_RSTD = 0, L_MEAN = 512;
constexpr unsigned F_DONE = 1024, F_SENT = F_DONE + 16, F_FULL = F_DONE + 32, F_COMM = F_DONE + 40, F_RDY = F_DONE + 48, F_OUT = F_DONE + 64, F_XI = F_DONE + 80, F_BS = F_DONE + 96, L_TAG = F_DONE + 128;
constexpr unsigned L_CST = 1280;
constexpr unsigned L_XCH = L_CST + 8 * 256;
constexpr unsigned L_SEG = 4096, L_HIN = 8192, L_SEL = 12288;
constexpr unsigned L_P = SLOT, L_O = 5 * SLOT, L_W = 6 * SLOT, L_TOTAL = 10 * SLOT;
constexpr int SPIN_MAX = 1 << 18;
constexpr int LSPIN_MAX = 1 << 22;

struct Params {
    const u16 *x, *lre, *lim, *Bre, *Bim, *Cre, *Cim, *ldt, *Dsk, *wg, *wu, *wd;
    float* out;
    unsigned char* ws;
};

DEVI float bf2f(u16 v) { return __uint_as_float(((unsigned)v) << 16); }
DEVI float bflo(unsigned v) { return __uint_as_float(v << 16); }
DEVI float bfhi(unsigned v) { return __uint_as_float(v & 0xffff0000u); }
DEVI unsigned pk2(float a, float b) { f32x2 p = {a, b}; return __builtin_bit_cast(unsigned, __builtin_convertvector(p, bf16x2)); }
DEVI unsigned pk2(f32x2 p) { return __builtin_bit_cast(unsigned, __builtin_convertvector(p, bf16x2)); }
DEVI v4u pk8(f32x4 a, f32x4 b) { return v4u{pk2(a.x, a.y), pk2(a.z, a.w), pk2(b.x, b.y), pk2(b.z, b.w)}; }
DEVI float dot2(unsigned a, unsigned b, float c) { return __builtin_amdgcn_fdot2_f32_bf16(__builtin_bit_cast(bf16x2, a), __builtin_bit_cast(bf16x2, b), c, false); }
template <int CTRL> DEVI float dppf(float v) { return __builtin_bit_cast(float, __builtin_amdgcn_update_dpp(0, __builtin_bit_cast(int, v), CTRL, 0xf, 0xf, true)); }
DEVI f32x4 mfma(v4u a, v4u b, f32x4 c) { return __builtin_amdgcn_mfma_f32_16x16x32_bf16(__builtin_bit_cast(bf16x8, a), __builtin_bit_cast(bf16x8, b), c, 0, 0, 0); }
template <bool FIRST> DEVI void mma(f32x4& c, const v4u& a, const v4u& b) {
    if constexpr (FIRST) asm volatile("v_mfma_f32_16x16x32_bf16 %0, %1, %2, 0" : "=&v"(c) : "v"(a), "v"(b));
    else asm volatile("v_mfma_f32_16x16x32_bf16 %0, %1, %2, %0" : "+v"(c) : "v"(a), "v"(b));
}
DEVI void mfma_done() { __builtin_amdgcn_sched_barrier(0); asm volatile("s_nop 7"); __builtin_amdgcn_sched_barrier(0); }
DEVI v4u lds128(unsigned a) { return *(LAS v4u*)(uintptr_t)a; }
DEVI f32x4 lds128f(unsigned a) { return *(LAS f32x4*)(uintptr_t)a; }
DEVI float lds32f(unsigned a) { return *(LAS float*)(uintptr_t)a; }
DEVI void sts32f(unsigned a, float v) { *(LAS float*)(uintptr_t)a = v; }
DEVI void sts128(unsigned a, v4u v) { *(LAS v4u*)(uintptr_t)a = v; }
DEVI void sts128f(unsigned a, f32x4 v) { *(LAS f32x4*)(uintptr_t)a = v; }
DEVI v2u ldstr(unsigned a) { return __builtin_bit_cast(v2u, __builtin_amdgcn_ds_read_tr16_b64_v4i16((LAS s16x4*)(uintptr_t)a)); }
DEVI void fence_c() { asm volatile("" ::: "memory"); }
DEVI void bar_l() { asm volatile("s_waitcnt lgkmcnt(0)\n\ts_barrier" ::: "memory"); }
template <int N> DEVI void waitv() { asm volatile("s_waitcnt vmcnt(%0)" ::"n"(N) : "memory"); }
DEVI void dma16(const void* sbase, unsigned voff, unsigned ldst) {
    asm volatile("s_mov_b32 m0, %2\n\ts_nop 0\n\tglobal_load_lds_dwordx4 %0, %1" ::"v"(voff), "s"(sbase), "s"(ldst) : "memory");
}
DEVI void post(unsigned a, unsigned v) { fence_c(); *(LAS unsigned*)(uintptr_t)a = v; fence_c(); }
DEVI unsigned peek(unsigned a) { fence_c(); return *(LAS unsigned*)(uintptr_t)a; }
DEVI bool any_lt(unsigned v, unsigned need) { return __builtin_amdgcn_ballot_w64(v < need) != 0; }
DEVI void wait_ge(unsigned a, unsigned need) {
    int s = LSPIN_MAX;
    NOUNROLL while (any_lt(peek(a), need)) { asm volatile("s_sub_i32 %0, %0, 1" : "+s"(s) :: "scc"); if (s < 0) break; if (SPIN_SLEEP) __builtin_amdgcn_s_sleep(SPIN_SLEEP); }
    fence_c();
}
template <int V> DEVI void postm(unsigned a, int lane) {
    asm volatile("s_mov_b64 exec, %2\n\tds_max_u32 %0, %1\n\ts_mov_b64 exec, -1" ::"v"(a), "v"(lane), "n"(1 << V) : "memory");
}
DEVI unsigned vm_outstanding() {
    unsigned v; asm volatile("s_getreg_b32 %0, hwreg(HW_REG_IB_STS)" : "=s"(v)::"memory");
    return (v & 15u) | ((v >> 18) & 0x30u);
}
DEVI void postv(unsigned a, int lane, unsigned v) {
    asm volatile("s_lshl_b64 exec, 1, %2\n\tds_max_u32 %0, %1\n\ts_mov_b64 exec, -1" ::"v"(a), "v"(lane), "s"(v) : "memory", "scc");
}
template <int V> DEVI void postc(unsigned a, int lane) {
    asm volatile("s_mov_b64 exec, %2\n\tds_write_b32 %0, %1\n\ts_mov_b64 exec, -1" ::"v"(a), "v"(lane), "n"(1 << V) : "memory");
}
DEVI void need_full(bool lack, unsigned a, unsigned need) { if (__builtin_expect(lack, 0)) wait_ge(a, need); fence_c(); }
#define VAL(id, v)
DEVI float sfma(float a, float b, float c) { float d; asm("v_fma_f32 %0, %1, %2, %3" : "=v"(d) : "v"(a), "v"(b), "v"(c)); return d; }
DEVI float sfms(float a, float b, float c) { float d; asm("v_fma_f32 %0, %1, %2, -%3" : "=v"(d) : "v"(a), "v"(b), "v"(c)); return d; }
DEVI float sfnma(float a, float b, float c) { float d; asm("v_fma_f32 %0, -%1, %2, %3" : "=v"(d) : "v"(a), "v"(b), "v"(c)); return d; }
DEVI float sadd(float a, float b) { float d; asm("v_add_f32 %0, %1, %2" : "=v"(d) : "v"(a), "v"(b)); return d; }
DEVI float smul(float a, float b) { float d; asm("v_mul_f32 %0, %1, %2" : "=v"(d) : "v"(a), "v"(b)); return d; }
DEVI float smul_t(float a, float b) { float d; asm("s_nop 0\n\tv_mul_f32 %0, %1, %2" : "=v"(d) : "v"(a), "v"(b)); return d; }
struct cpx { float x, y; };
DEVI cpx cmul(cpx a, cpx b) { return cpx{sfms(a.x, b.x, smul(a.y, b.y)), sfma(a.x, b.y, smul(a.y, b.x))}; }
DEVI float lmad(cpx a, float h, float u) {
    float d;
    asm("v_fma_f32 %0, %1, %3, %4\n\ts_nop 0\n\tv_fmac_f32_dpp %0, %3, %2 quad_perm:[1,0,3,2] row_mask:0xf bank_mask:0xf" : "=&v"(d) : "v"(a.x), "v"(a.y), "v"(h), "v"(u));
    return d;
}
DEVI f32x2 dup(float v) { f32x2 d = {v, v}; asm("" : "+v"(d)); return d; }
DEVI f32x2 pfma(f32x2 a, f32x2 b, f32x2 c) { return __builtin_elementwise_fma(a, b, c); }
DEVI f32x2 lo2(f32x4 v) { return f32x2{v.x, v.y}; }
DEVI f32x2 hi2(f32x4 v) { return f32x2{v.z, v.w}; }
#define SB() __builtin_amdgcn_sched_barrier(0)
template <int V> struct IC { static constexpr int value = V; };
template <int B, int E, class F> DEVI void sfor(F&& f) { if constexpr (B < E) { f(IC<B>{}); sfor<B + 1, E>(f); } }
#define LAMB(nn) [&](auto nn) __attribute__((always_inline))
#define STAMP(id) do { if ((id) == PROBE && w == PROBEW) { SB(); unsigned long long t_; asm volatile("s_memrealtime %0\n\ts_waitcnt lgkmcnt(0)" : "=s"(t_) :: "memory"); *(LAS unsigned*)(uintptr_t)14336 = (unsigned)t_; SB(); } } while (0)
#define STAMP_INIT()
#define STAMP_DUMP() do { if (w == 4) { const unsigned d_ = 4000u + __builtin_amdgcn_readfirstlane(peek(14336)) - (unsigned)tstart; \
    while ((unsigned)__builtin_amdgcn_s_memrealtime() - (unsigned)tstart < min(d_, 9000u)) __builtin_amdgcn_s_sleep(4); } } while (0)
DEVI int fresh_lane() { int l; asm volatile("v_mbcnt_lo_u32_b32 %0, -1, 0\n\tv_mbcnt_hi_u32_b32 %0, -1, %0" : "=v"(l)); return l; }
DEVI v4u make_sel(int lane, int bb) {
    const int c = lane & 15, tk = 8 * (c >> 2) + 4 * bb + (c & 3) - 8 * (lane >> 4);
    v4u s;
#pragma unroll
    for (int e = 0; e < 4; e++) s[e] = (tk == 2 * e ? 0x3f80u : 0u) | (tk == 2 * e + 1 ? 0x3f800000u : 0u);
    return s;
}
#define LANE_VARS const int lane = fresh_lane(), c = lane & 15, g = lane >> 4, q8 = lane >> 3, p8 = lane & 7; (void)c; (void)g; (void)q8; (void)p8
constexpr unsigned wslot(int n) { return (unsigned)(n & 3) * SLOT; }

#define ROT1 0
#define ROT2 0
#define XI_STEP 8
#define TOUCH_GATE 1
#define LB_SLEEP 8
#define LB_FIRST 10
#define FL_FIRST 20
#define FL_SLEEP 6
#define MIDV 64
#define X_FIRST 10
#define X_SLEEP 1
#define PDV 4
#define INFL 8
#define TW waitv<0>();
#define PACE 14
#define PACE2 8
#define XLV 3
#define LGATE 84
#define SLEEP0 12
constexpr unsigned CODE_LINES = 288;
template <int OFF> DEVI void reldA(v4u& d, unsigned voff, v4u srd, unsigned so) {
    asm volatile("buffer_load_dwordx4 %0, %1, %2, %3 offen offset:%4 sc1" : "+v"(d) : "v"(voff), "s"(srd), "s"(so), "n"(OFF) : "memory");
}
template <int OFF> DEVI void stA(const v4u& d, unsigned voff, v4u srd, unsigned so) {
    asm volatile("buffer_store_dwordx4 %0, %1, %2, %3 offen offset:%4 sc1\n\ts_nop 1" ::"v"(d), "v"(voff), "s"(srd), "s"(so), "n"(OFF) : "memory");
}
template <int OFF> DEVI void stX(const v4u& d, unsigned voff, v4u srd, unsigned so) {
    asm volatile("buffer_store_dwordx4 %0, %1, %2, %3 offen offset:%4 sc1" ::"v"(d), "v"(voff), "s"(srd), "s"(so), "n"(OFF) : "memory");
}
DEVI void touch(unsigned& sink, const void* tp, unsigned to) { asm volatile("global_load_dword %0, %1, %2" : "+v"(sink) : "v"(to), "s"(tp) : "memory"); }

DEVI v4u make_srd(const void* base) { const unsigned long long a = (unsigned long long)base; return v4u{(unsigned)a, (unsigned)(a >> 32) & 0xffffu, 0x7fffffffu, 0x00020000u}; }
DEVI void dma4(v4u srd, unsigned ve, unsigned vo, unsigned s0, unsigned s1, unsigned s2, unsigned s3, unsigned ldst) {
    asm volatile("s_mov_b32 m0, %7\n\ts_nop 0\n\tbuffer_load_dwordx4 %0, %2, %3 offen lds\n\tbuffer_load_dwordx4 %1, %2, %4 offen offset:1024 lds\n\t"
                 "buffer_load_dwordx4 %0, %2, %5 offen offset:2048 lds\n\tbuffer_load_dwordx4 %1, %2, %6 offen offset:3072 lds"
                 ::"v"(ve), "v"(vo), "s"(srd), "s"(s0), "s"(s1), "s"(s2), "s"(s3), "s"(ldst) : "memory");
}
DEVI void dma1(v4u srd, unsigned v, unsigned so, unsigned ldst) {
    asm volatile("s_mov_b32 m0, %3\n\ts_nop 0\n\tbuffer_load_dwordx4 %0, %1, %2 offen lds" ::"v"(v), "s"(srd), "s"(so), "s"(ldst) : "memory");
}
template <int H, int NEG> DEVI f32x2 bmul(f32x2 a, f32x2 b) {
    f32x2 d; asm("v_pk_mul_f32 %0, %1, %2 op_sel:[%3,0] op_sel_hi:[%3,1] neg_lo:[%4,0] neg_hi:[%4,0]" : "=v"(d) : "v"(a), "v"(b), "n"(H), "n"(NEG)); return d;
}
template <int H, int NEG> DEVI f32x2 bfma(f32x2 a, f32x2 b, f32x2 c) {
    f32x2 d; asm("v_pk_fma_f32 %0, %1, %2, %3 op_sel:[%4,0,0] op_sel_hi:[%4,1,1] neg_lo:[%5,0,0] neg_hi:[%5,0,0]" : "=v"(d) : "v"(a), "v"(b), "v"(c), "n"(H), "n"(NEG)); return d;
}
DEVI void mma_c(f32x4& d, const v4u& a, const v4u& b, const f32x4& c) { asm volatile("v_mfma_f32_16x16x32_bf16 %0, %1, %2, %3" : "=&v"(d) : "v"(a), "v"(b), "v"(c)); }
DEVI unsigned cvtv(float a, float b) { unsigned d; asm volatile("v_cvt_pk_bf16_f32 %0, %1, %2" : "=v"(d) : "v"(a), "v"(b)); return d; }

__global__ __launch_bounds__(512, 2) void fused(Params p) {
    const unsigned char* pc0;
    asm volatile("s_getpc_b64 %0" : "=s"(pc0));
    const unsigned long long tstart = __builtin_amdgcn_s_memrealtime();
    const int w = __builtin_amdgcn_readfirstlane(threadIdx.x >> 6);
    const int b = blockIdx.x, xcd = b & 7, idx = b >> 3;
    const int i = 8 * (xcd & 1) + (idx & 7), kq = xcd >> 1, q = idx >> 3, j = 4 * kq + q;
    const bool heavy = q != 3;
    const int N1 = heavy ? 20 : 12;
    const int lw = w & 1, pr = w & 3;
    const int rot = ROT1;
    const __amdgpu_buffer_rsrc_t rws = __builtin_amdgcn_make_buffer_rsrc(p.ws, 0, WS_END, 0x00020000);
    auto unit_of = [&](int v) { return v < 4 ? 7 * kq + v : v < 8 ? 28 + 8 * ((v >> 1) & 1) + 2 * kq + (v & 1) : 7 * kq + v - 4; };
    const unsigned sl_lo = L_P + (unsigned)pr * SLOT;
    const unsigned xo = WS_XB + (unsigned)((i * 4 + q) * 4) * 36864u + pr * 3072;
    const unsigned fo = WS_FLAG + (unsigned)(i * 16 + j) * 512u;

    if (w >= 4) {
        LANE_VARS;
        STAMP_INIT();
        STAMP(0);
        const unsigned swz = (unsigned)((p8 ^ (q8 >> 1)) << 4);
        const unsigned fa = F_DONE + (lane & 3) * 4;
        unsigned sink = 0, tg;
        auto poll16 = [&](int which) __attribute__((always_inline)) {
            wait_ge(F_OUT + (lane & 3) * 4, which + 1);
            STAMP(16);
            const unsigned pf = WS_FLAG + (unsigned)(i * 16 + 4 * kq + (lane & 3)) * 512u + which * 128 + ((lane >> 2) & 3) * 8;
            NOUNROLL for (int spin = 0; spin < SPIN_MAX; spin++) {
                fence_c();
                const v2u f = __builtin_amdgcn_raw_buffer_load_b64(rws, pf, 0, 16);
                if (__builtin_amdgcn_ballot_w64(f.x != tg || f.y != ~tg) == 0) break;
                __builtin_amdgcn_s_sleep(FL_SLEEP);
            }
            post(F_RDY + which * 4, 1);
            STAMP(17);
        };
        if (w < 6) {
            unsigned ffull = F_FULL + lw * 4;
            asm volatile("" : "+v"(ffull));
            {
                const unsigned voffS = (8 * (q8 >> 2) + (q8 & 3)) * 1024 + swz;
                const v4u srd = make_srd((const unsigned char*)(lw ? p.Bim : p.Bre) + (size_t)(32 * j) * 1024);
                NOUNROLL for (int n = 2; n < 8; n++) {
                    const unsigned so = 128 * ((n + rot) & 7);
                    dma4(srd, voffS, voffS ^ 64u, so, so + 15360, so + 2048, so + 17408, L_W + (n >> 1) * SLOT + lw * 8192 + (n & 1) * 4096);
                    if (n == 3) {
                        touch(sink, p.x, (128 * i + 8 * j + 4 * lw) * 1024 + (lane & 31) * 128);
                        touch(sink, lw ? p.Bim : p.Bre, (32 * j + 2 * i) * 1024 + c * 128);
                        bar_l();
                    }
                    if (n == 4) { waitv<6>(); postc<2>(ffull, lane); }
                    if (n >= 5) { waitv<4>(); postv(ffull, lane, n >> 1); }
                }
                waitv<0>(); postc<4>(ffull, lane);
                asm volatile("" ::"v"(sink));
            }
            STAMP(1);
            const unsigned voffG = q8 * (FF * 2) + (((p8 >> 1) ^ ((q8 >> 1) & 1)) << 5) + ((p8 & 1) << 4), voffG1 = voffG ^ 64u;
            const unsigned voffC = q8 * 1024 + swz, voffC1 = voffC ^ 64u;
            const unsigned voffD = q8 * 1024 + (((p8 >> 1) ^ (q8 >> 1) ^ (2 * (kq & 1))) << 5) + ((p8 & 1) << 4);
            const unsigned char* gb = (const unsigned char*)(lw ? p.wu : p.wg) + (7 * kq + q) * 128;
            unsigned thr = (lane >> 2) + 1;
            int credit = 0, cbase = 0;
            auto item = [&](int n, const unsigned char* b0, const unsigned char* b1, unsigned ve, unsigned vo, unsigned st) __attribute__((always_inline)) {
                v4u s0 = make_srd(b0), s1 = make_srd(b1);
                unsigned dst = ((n & 4) ? L_P : L_W) + (n & 3) * SLOT + lw * 8192, dst1 = dst + 4096;
                asm volatile("" : "+s"(s0), "+s"(s1), "+s"(dst), "+s"(dst1));
                int sp = LSPIN_MAX;
                NOUNROLL while (n - 7 > credit) {
                    const unsigned dn = peek(fa);
                    postv(ffull, lane, n - ((vm_outstanding() + 7) >> 3));
                    unsigned long long mk = __builtin_amdgcn_ballot_w64(dn >= thr);
                    mk &= mk >> 1; mk &= mk >> 2;
                    credit = __builtin_popcountll(mk & 0x1111111111111111ull) + cbase;
                    asm volatile("s_sub_i32 %0, %0, 1" : "+s"(sp)::"scc");
                    if (sp < 0) break;
                }
                waitv<INFL>();
                dma4(s0, ve, vo, 0, st - 1024, 2 * st - 2048, 3 * st - 3072, dst);
                postv(ffull, lane, n - (INFL + 7) / 8);
                waitv<INFL>();
                dma4(s1, ve, vo, 0, st - 1024, 2 * st - 2048, 3 * st - 3072, dst1);
            };
            wait_ge(F_XI + (lane & 3) * 4, 3);
            NOUNROLL for (int n = 4; n < N1; n++) {
                const int t = n - 4;
                const unsigned char* bp = gb + (size_t)(((t + rot) & 7) * 64) * (FF * 2) + (t >> 3) * 512;
                item(n, bp, bp + 32 * FF * 2, voffG, voffG1, 8 * FF * 2);
            }
            cbase = N1 - 8; thr += cbase;
            const unsigned char* dp = (const unsigned char*)p.wd + (size_t)(7 * kq) * 65536 + q * 256 + (lw ^ (kq >> 1)) * 128;
            NOUNROLL for (int v = 0; v < 11; v++) {
                if ((v & ~3) == 4) {
                    const unsigned char* cbp = (const unsigned char*)((v & 2) ? p.Cim : p.Cre) + (size_t)q * 131072 + (2 * kq + (v & 1)) * 128;
                    item(N1 + v, cbp + ((2 * lw) ^ kq) * 32768, cbp + ((2 * lw + 1) ^ kq) * 32768, voffC, voffC1, 8192);
                } else {
                    item(N1 + v, dp, dp + 32768, voffD, voffD, 8192);
                    dp += 65536;
                }
            }
            waitv<16>(); postv(ffull, lane, N1 + 9);
            waitv<8>(); postv(ffull, lane, N1 + 10);
            waitv<0>(); postv(ffull, lane, N1 + 11);
            tg = peek(L_TAG);
            STAMP(7);
        } else {
            unsigned tag0, prm0, prm1, prm2;
            const unsigned cto = min(idx * 9 + 4 * lw + p8, (int)CODE_LINES - 1) * 128;
            touch(sink, pc0, cto);
            const unsigned po = 2 * (32 * j + 8 * (lane >> 4) + 4 * ((lane >> 1) & 1) + ((lane >> 2) & 3));
            asm volatile("global_load_ushort %0, %4, %5\n\tglobal_load_ushort %1, %4, %6\n\tglobal_load_ushort %2, %4, %7\n\tglobal_load_dword %3, %8, %9 sc1"
                         : "=&v"(prm0), "=&v"(prm1), "=&v"(prm2), "=&v"(tag0) : "v"(po), "s"(p.lre), "s"(p.lim), "s"(p.ldt), "v"(WS_EPOCH), "s"(p.ws) : "memory");
            *(LAS unsigned*)(uintptr_t)(F_DONE + (lane & 31) * 4) = 0u;
            bar_l();
            sts128(L_SEL + lw * 1024 + lane * 16, make_sel(lane, lw));
            asm volatile("s_waitcnt vmcnt(0)" : "+v"(prm0), "+v"(prm1), "+v"(prm2), "+v"(tag0)::"memory");
            tg = tag0 + 1u;
            const bool odd = lane & 1;
            const unsigned la = lane * 4;
            const float lr = bflo(prm0), li = bflo(prm1);
            float dt = __expf(bflo(prm2));
            dt = bflo(pk2(dt, dt));
            const float ex = __expf(dt * lr);
            const float fr = __builtin_amdgcn_fractf(dt * li * 0.15915494309189535f);
            const float ar = smul_t(ex, __builtin_amdgcn_cosf(fr)), ai = smul_t(ex, __builtin_amdgcn_sinf(fr));
            const cpx a1 = {ar, ai};
            const cpx a2 = cmul(a1, a1), a4 = cmul(a2, a2);
            const unsigned sg = odd ? 0u : 0x80000000u;
            auto sgn = [&](cpx v) { return cpx{v.x, __uint_as_float(__float_as_uint(v.y) ^ sg)}; };
            const cpx r8 = cmul(a4, a4), r16 = cmul(r8, r8), r32 = cmul(r16, r16), r64 = cmul(r32, r32);
            const cpx a8 = sgn(r8);
            float pw[8];
            pw[0] = lw ? (odd ? r64.y : r64.x) : (odd ? 0.0f : 1.0f);
#pragma unroll
            for (int s = 1; s < 8; s++) pw[s] = lmad(a8, pw[s - 1], 0.0f);
#pragma unroll
            for (int s = 0; s < 8; s++) sts32f(L_HIN + (15 - 8 * lw - s) * 256 + la, pw[s]);
            if (lw == 0) {
                const float nr = ar - 1.0f, den = smul_t(__builtin_amdgcn_rcpf(sfma(lr, lr, smul(li, li))), 2.0f);
                float co = smul(odd ? sfms(ai, lr, smul(nr, li)) : sfma(nr, lr, smul(ai, li)), den);
                sts32f(L_CST + la, odd ? a1.y : a1.x); sts32f(L_CST + 256 + la, odd ? a4.y : a4.x);
                const cpx a1s = sgn(a1);
#pragma unroll
                for (int t = 0; t < 5; t++) { sts32f(L_CST + (2 + t) * 256 + la, co); if (t < 4) co = lmad(a1s, co, 0.0f); }
                *(LAS unsigned*)(uintptr_t)L_TAG = tg;
            }
            post(F_COMM + lw * 4, 1);
            STAMP(1);
            cpx wn[8];
            {
                const cpx r128 = cmul(r64, r64), r256 = cmul(r128, r128);
                wn[0] = lw ? r128 : cpx{1.0f, 0.0f};
#pragma unroll
                for (int n = 1; n < 8; n++) wn[n] = cmul(wn[n - 1], r256);
#pragma unroll
                for (int n = 0; n < 8; n++) { wn[n] = sgn(wn[n]); asm volatile("" : "+v"(wn[n].x), "+v"(wn[n].y)); }
            }
            wait_ge(F_XI + (lane & 3) * 4, TOUCH_GATE);
            touch(sink, lw ? p.wu : p.wg, (32 * i + (lane & 31)) * (FF * 2) + (7 * kq + q + ((heavy && lane >= 32) ? 4 : 0)) * 128);
            TW touch(sink, p.wd, (448 * kq + 28 * i + min(lane, 27)) * 1024 + (2 * q + lw) * 128);
            TW touch(sink, lw ? p.Cim : p.Cre, (128 * q + 8 * i + p8) * 1024 + (2 * kq + (q8 & 1)) * 128);
            TW touch(sink, lw ? (const void*)p.ws : (const void*)p.out, lw ? min((unsigned)(lane & 31) * 0xb0000u + idx * 0x4000u, WS_END - 4) : (lane & 3) * 0x100000 + idx * 0x4000);
            unsigned gro = WS_GRAN + j * 8192 + lane * 8;
            asm volatile("" : "+v"(gro));
            wait_ge(fa, 4);
            STAMP(4);
            if (lw) {
                float e[16];
#pragma unroll
                for (int s = 0; s < 16; s++) e[s] = lds32f(L_HIN + s * 256 + la);
                postc<2>(F_COMM + 4, lane);
#pragma unroll
                for (int d = 8; d > 0; d >>= 1)
#pragma unroll
                    for (int s = 0; s < d; s++) e[s] = sadd(e[s], e[s + d]);
                __builtin_amdgcn_raw_buffer_store_b64(v2u{__float_as_uint(e[0]), tg}, rws, gro + i * 512, 0, 16);
            }
            STAMP(5);
            {
                float e[8], h = 0;
#pragma unroll
                for (int s = 0; s < 8; s++) e[s] = lds32f(L_SEG + (8 * lw + s) * 256 + la);
#pragma unroll
                for (int s = 0; s < 8; s++) sts32f(L_SEG + (8 * lw + s) * 256 + la, pw[s]);
                wait_ge(F_COMM + (lw ^ 1) * 4, 2);
                if (lw) h = lds32f(L_XCH + la);
#pragma unroll
                for (int s = 0; s < 8; s++) { sts32f(L_HIN + (8 * lw + s) * 256 + la, h); if (s < 7 || !lw) h = lmad(a8, h, e[s]); }
                if (!lw) { sts32f(L_XCH + la, h); postc<2>(F_COMM, lane); }
            }
            STAMP(6);
            {
                const int top = i - 1 - lw;
                v2u gr[8] = {};
                NOUNROLL for (int spin = 0; spin < SPIN_MAX; spin++) {
                    fence_c();
#pragma unroll
                    for (int n = 0; n < 8; n++) gr[n] = __builtin_amdgcn_raw_buffer_load_b64(rws, gro, ((top - 2 * n) & 15) * 512, 16);
                    unsigned long long bad = 0;
#pragma unroll
                    for (int n = 0; n < 8; n++) if (top - 2 * n >= 0) bad |= __builtin_amdgcn_ballot_w64(gr[n].y != tg);
                    if (bad == 0) break;
                    __builtin_amdgcn_s_sleep(LB_SLEEP);
                }
                float h = 0;
#pragma unroll
                for (int n = 7; n >= 0; n--) if (top - 2 * n >= 0) h = lmad(wn[n], __uint_as_float(gr[n].x), h);
                sts32f(L_XCH + 256 + lw * 256 + la, h);
            }
            postc<4>(F_COMM + lw * 4, lane);
            waitv<0>();
            asm volatile("" ::"v"(sink));
            STAMP(7);
            poll16(lw);
        }
        f32x4 base[4];
        {
            const int d = 128 * q + 32 * kq + 4 * p8;
            const v2u dv = *(const GAS v2u*)(p.Dsk + d);
            const float df[4] = {bflo(dv.x), bfhi(dv.x), bflo(dv.y), bfhi(dv.y)};
            v2u xv[4];
#pragma unroll
            for (int e = 0; e < 4; e++) xv[e] = *(const GAS v2u*)(p.x + (size_t)(128 * i + 32 * pr + 8 * e + q8) * DM + d);
#pragma unroll
            for (int e = 0; e < 4; e++) {
                const unsigned tp = (32 * pr + 8 * e + 4 * ((q8 >> 1) & 1) + 2 * (q8 & 1) + (q8 >> 2)) * 4;
                const float rs = lds32f(L_RSTD + tp), nm = smul(lds32f(L_MEAN + tp), rs);
                const float xf[4] = {bflo(xv[e].x), bfhi(xv[e].x), bflo(xv[e].y), bfhi(xv[e].y)};
#pragma unroll
                for (int k = 0; k < 4; k++) base[e][k] = sfma(sfma(xf[k], rs, nm), df[k], xf[k]);
            }
        }
        STAMP(8);
        const unsigned t16 = ((tg & 0x3fffu) + 0x2000u) << 16;
        const unsigned xi = xo + kq * 36864u + lane * 16;
        auto bad3 = [&](const v4u* in) { return ((in[0].y ^ t16) | (in[0].w ^ t16) | (in[1].y ^ t16)) | ((in[1].w ^ t16) | (in[2].y ^ t16) | (in[2].w ^ t16)); };
        auto val = [&](const v4u* in, int k) { const unsigned dw = in[k / 6][2 * ((k / 3) & 1) + (k % 3 == 2)]; return (k % 3 == 1) ? bfhi(dw) : bflo(dw); };
        const unsigned sc = L_O + pr * 4096;
        unsigned wa = sc + (8 * (c >> 2) + 4 * (c & 1) + ((c >> 1) & 1)) * 128 + ((g ^ (c & 7)) << 4), ra = sc + q8 * 128 + ((p8 ^ (2 * (q8 & 1) + (q8 >> 2))) << 4);
        GAS float* op = (GAS float*)p.out + (size_t)(128 * i + 32 * pr + q8) * DM + 128 * q + 32 * kq + 4 * p8;
        asm volatile("" : "+v"(wa), "+v"(ra), "+v"(op));
        wait_ge(F_SENT + pr * 4, 3);
        __builtin_amdgcn_s_sleep(X_FIRST);
        v4u in[9];
        f32x4 own[4];
        NOUNROLL for (int spin = 0; spin < SPIN_MAX; spin++) {
            fence_c();
#pragma unroll
            for (int n = 0; n < 9; n++) in[n] = __builtin_amdgcn_raw_buffer_load_b128(rws, xi + (n / 3) * 12288 + (n % 3) * 1024, 0, 16);
            SB();
            wait_ge(F_SENT + pr * 4, 4);
#pragma unroll
            for (int e = 0; e < 4; e++) own[e] = lds128f(sc + e * 1024 + lane * 16);
            if (__builtin_amdgcn_ballot_w64((bad3(in) | bad3(in + 3) | bad3(in + 6)) > 0xffffu) == 0) break;
        }
        STAMP(11);
#pragma unroll
        for (int e = 0; e < 4; e++) {
            f32x4 t = own[e];
#pragma unroll
            for (int sr = 0; sr < 9; sr += 3) t += f32x4{val(in + sr, 4 * e), val(in + sr, 4 * e + 1), val(in + sr, 4 * e + 2), val(in + sr, 4 * e + 3)};
            own[e] = t;
        }
        asm volatile("" : "+v"(own[0]), "+v"(own[1]), "+v"(own[2]), "+v"(own[3]));
#pragma unroll
        for (int e = 0; e < 4; e++) sts128f((wa ^ (unsigned)((e >> 1) << 6)) + (e & 1) * 256, own[e]);
        fence_c();
        f32x4 ta[4];
#pragma unroll
        for (int e = 0; e < 4; e++) ta[e] = lds128f((ra ^ (unsigned)((e & 1) << 6)) + e * 1024);
        SB();
#pragma unroll
        for (int e = 0; e < 4; e++) *(GAS f32x4*)(op + e * 8 * DM) = base[e] + ta[e];
        if (b == 255 && w == 4 && lane == 0) __builtin_amdgcn_raw_buffer_store_b32(tg, rws, WS_EPOCH, 0, 16);
        STAMP(12); STAMP_DUMP();
        return;
    }
    constexpr int PD = PDV, XL = XLV;
    unsigned stall = 0; (void)stall;
    v4u xa[16][2];
#define AF(u, k) xa[2 * (u) + ((k) >> 1)][(k) & 1]
    v4u sws = make_srd(p.ws);
    unsigned oG = WS_A2 + (unsigned)(i * 44 + 7 * kq) * 16384u + w * 4096, oC = WS_A2 + (unsigned)(i * 44 + 28 + 2 * kq) * 16384u + w * 4096, oF = fo + w * 8;
    asm volatile("" : "+s"(sws), "+s"(oG), "+s"(oC), "+s"(oF));
    auto uoff = [&](int v) __attribute__((always_inline)) { return v < 4 ? oG + v * 16384 : v < 8 ? oC + (8 * ((v >> 1) & 1) + (v & 1)) * 16384 : oG + (v - 4) * 16384; };
    LANE_VARS;
    unsigned fdone = F_DONE + w * 4, l16 = lane * 16;
    asm volatile("" : "+v"(fdone), "+v"(l16));
    unsigned tgs = 0; int raised = 0;
    auto raise = [&](int level) __attribute__((always_inline)) {
        asm volatile("s_mov_b64 exec, 1\n\tbuffer_store_dwordx2 %0, off, %1, %2 offset:%3 sc1\n\ts_mov_b64 exec, -1" ::"v"(v2u{tgs, ~tgs}), "s"(sws), "s"(oF), "n"((level - 1) * 128) : "memory");
        post(fdone + (F_OUT - F_DONE), level);
        raised = level;

        if (level == 1) STAMP(17); else STAMP(18);
    };
    auto try_raise = [&](int level) __attribute__((always_inline)) {
        unsigned v; asm volatile("s_getreg_b32 %0, hwreg(HW_REG_IB_STS, 0, 4)" : "=s"(v)::"memory");
        if (__builtin_expect((int)(v + raised) == level - 1, 0)) raise(level);
    };
    unsigned oX[3];
#pragma unroll
    for (int sl = 1; sl < 4; sl++) { oX[sl - 1] = xo + (kq ^ sl) * 36864u + (sl - 1) * 12288; asm volatile("" : "+s"(oX[sl - 1])); }
    unsigned cbs[2], tbs[4];
    v4u bf[PD];
    const unsigned ffl = F_FULL + (lane & 1) * 4;
    auto set2 = [&](auto kk) __attribute__((always_inline)) {
        constexpr int k = decltype(kk)::value;
        if constexpr (k == 0) tbs[0] = L_P + ((l16 << 1) & 0x780) + ((l16 >> 1) & 0x18) + (((l16 >> 2) & 0x40) | ((l16 >> 2) & 0x20));
        else if constexpr (k < 4) tbs[k] = tbs[0] ^ (unsigned)(k << 5);
        else if constexpr (k == 4) cbs[0] = L_W + ((l16 << 3) & 0x780) + (((l16 >> 4) ^ (l16 >> 1)) & 0x70);
        else cbs[1] = cbs[0] ^ 64u;
        if constexpr (k < 4) asm volatile("" : "+v"(tbs[k])); else asm volatile("" : "+v"(cbs[k - 4]));
    };
    auto prime2 = [&](auto mm) __attribute__((always_inline)) { constexpr int m = decltype(mm)::value; const v2u lo = ldstr(tbs[m]), hi = ldstr(tbs[m] + 2048); bf[m] = v4u{lo.x, lo.y, hi.x, hi.y}; };
    {
        STAMP_INIT();
        STAMP(0);
        unsigned cb[2];
        cb[0] = (unsigned)c * 128u + (unsigned)((g ^ (c >> 1)) << 4); cb[1] = cb[0] ^ 64u;
        f32x4 eX;
        unsigned tb[4];
        f32x4 nm4[2];
        {
            const unsigned voffX = (8 * (q8 >> 2) + 4 * (q8 & 1) + ((q8 >> 1) & 1)) * 1024 + ((p8 ^ (q8 >> 1)) << 4), voffX1 = voffX ^ 64u;
            const v4u srd = make_srd((const unsigned char*)p.x + (size_t)(128 * i + 32 * w) * 1024);
            auto issue_x = [&](int ln) __attribute__((always_inline)) {
                const unsigned s0 = ((ln + rot) & 7) * 128;
                dma4(srd, voffX, voffX1, s0, s0 + 15360, s0, s0 + 15360, sl_lo + (ln & 3) * 4096);
            };
            {
                const unsigned voffS = (8 * (q8 >> 2) + (q8 & 3)) * 1024 + ((p8 ^ (q8 >> 1)) << 4), so = 128 * (((w >> 1) + rot) & 7);
                dma4(make_srd((const unsigned char*)(lw ? p.Bim : p.Bre) + (size_t)(32 * j) * 1024), voffS, voffS ^ 64u, so, so + 15360, so + 2048, so + 17408, L_W + lw * 8192 + (w >> 1) * 4096);
            }
#pragma unroll
            for (int ln = 0; ln < XL; ln++) issue_x(ln);
            v4u ones = {0x3f803f80u, 0x3f803f80u, 0x3f803f80u, 0x3f803f80u};
            unsigned xl[2] = {cb[0] + sl_lo, cb[1] + sl_lo};
            cb[0] += L_W; cb[1] += L_W;
            asm volatile("" : "+v"(ones), "+v"(cb[0]), "+v"(cb[1]), "+v"(xl[0]), "+v"(xl[1]));
            f32x4 sm[2], sq[2], bsum;
            v4u bx[4];
            unsigned cbw[2] = {cb[0] + (w >> 1) * 8192 + (w & 1) * 2048, cb[1] + (w >> 1) * 8192 + (w & 1) * 2048};
            asm volatile("" : "+v"(cbw[0]), "+v"(cbw[1]));
            f32x4 acc[4][2];
            unsigned fl = 0; bool lack = false;
            const unsigned fcs = (lane & 4) ? F_COMM + (lane & 1) * 4 : F_BS + (lane & 3) * 4;
            static_assert(PD == 4 && XL <= 3, "prologue schedule");
            waitv<4 * (XL - 1)>();
#pragma unroll
            for (int f = 0; f < 4; f++) xa[f >> 1][f & 1] = lds128(xl[f >> 1] + (f & 1) * 2048);
            asm volatile("s_barrier" ::: "memory");
            STAMP(1);
            sfor<-PD, 64>(LAMB(nn) {
                constexpr int n = decltype(nn)::value, m = n + PD, ks = n >> 2, ln = n >> 3;
                v4u cur;
                if constexpr (n >= 0) { cur = bf[n % PD]; mma<(n < 4)>(acc[n & 3][0], xa[ks][0], cur); SB(); }
                if constexpr (m > 0 && (m & 15) == 0 && m < 64) need_full(lack, ffl, (m >> 4) + 1);
                if constexpr (m < 64) bf[m % PD] = lds128(cb[(m >> 2) & 1] + wslot(m >> 4) + (((m >> 1) & 1) * 4 + ((m >> 3) & 1) * 2 + (m & 1)) * 2048);
                SB();
                if constexpr (n >= 0) { mma<(n < 4)>(acc[n & 3][1], xa[ks][1], cur); SB(); }
                if constexpr (n >= 0 && (n & 7) < 4 && ln + XL < 8)
                    dma1(srd, (n & 1) ? voffX1 : voffX, ((ln + XL + rot) & 7) * 128 + (n & 1) * 16384 + (n & 2) * 1024, sl_lo + ((ln + XL) & 3) * 4096 + (n & 3) * 1024);
                if constexpr (n >= 0 && (n & 7) == 4 && ln + XL == 7) post(fdone + (F_XI - F_DONE), 1);
                if constexpr (n >= 0 && (n & 7) == 4 && ln < 7) waitv<4 * ((ln + XL < 7 ? ln + XL : 7) - ln - 1)>();
                if constexpr (n >= 0 && (n & 7) >= 4 && ln < 7) xa[2 * ln + 2 + ((n >> 1) & 1)][n & 1] = lds128(xl[(n >> 1) & 1] + ((ln + 1) & 3) * 4096 + (n & 1) * 2048);
                if constexpr (n == 56) post(fdone + (F_XI - F_DONE), 3);
                if constexpr ((m & 15) == 10 && m < 48) fl = peek(ffl);
                if constexpr ((m & 15) == 14 && m < 48) lack = any_lt(fl, (m >> 4) + 2);
                if constexpr ((m & 15) == 15 && m < 63) post(fdone, (m >> 4) + 1);
                if constexpr (m < 64 && (m & 15) < 4) bx[m & 3] = lds128(cbw[m & 1] + wslot(m >> 4) + ((m >> 1) & 1) * 4096);
                if constexpr (n == 55) { sts32f(L_CST + 7 * 256 + c * 16 + (w & 1) * 8 + (w >> 1) * 4, bsum.x); post(fdone + (F_BS - F_DONE), 1); }
                if constexpr (n == 60) fl = peek(fcs);
                SB();
                if constexpr (n >= 0 && (n & 15) < 4) mma<(n == 0)>(bsum, ones, bx[n & 3]);
                if constexpr (n >= 0) { if constexpr (n & 1) mma<(n < 4)>(sq[(n >> 1) & 1], xa[ks][(n >> 1) & 1], xa[ks][(n >> 1) & 1]); else mma<(n < 4)>(sm[(n >> 1) & 1], ones, xa[ks][(n >> 1) & 1]); }
                SB();
            });
            need_full(any_lt(fl, 1), fcs, 1);
            const unsigned cl = L_CST + (l16 & 0xf0);
            const f32x4 ca = lds128f(cl), c4 = lds128f(cl + 256), co = lds128f(cl + 512), bs = lds128f(cl + 7 * 256), w15 = lds128f(L_HIN + w * 1024 + l16);
            mfma_done();
            STAMP(2); VAL(24, stall); stall = 0;
            {
                float dg[2];
#pragma unroll
                for (int bb = 0; bb < 2; bb++) {
                    nm4[bb] = sm[bb] * (-1.0f / DM);
                    const float s01 = (lane & 1) ? sq[bb].y : sq[bb].x, s23 = (lane & 1) ? sq[bb].w : sq[bb].z;
                    dg[bb] = __builtin_amdgcn_rsqf(fmaxf(((lane & 2) ? s23 : s01) * (1.0f / DM) - nm4[bb].x * nm4[bb].x, 0.0f) + 1e-5f);
                }
                if (g == (c >> 2)) {
                    const unsigned sa = L_RSTD + (32 * w + 8 * g + (c & 3)) * 4;
                    sts32f(sa, dg[0]); sts32f(sa + 16, dg[1]); sts32f(sa + L_MEAN, nm4[0].x); sts32f(sa + L_MEAN + 16, nm4[1].x);
                }
            }
            fence_c();
            const unsigned ra = L_RSTD + (32 * w + 8 * g) * 4;
            const f32x4 nt0 = lds128f(ra + L_MEAN), nt1 = lds128f(ra + L_MEAN + 16), rs0 = lds128f(ra), rs1 = lds128f(ra + 16);
            {
                const int q4 = c >> 2, p4 = c & 3;
                const unsigned tb1 = L_P + (unsigned)(8 * g + q4) * 128u + (unsigned)((((q4 >> 1) & 1) | ((g & 1) << 1)) << 5) + p4 * 8;
#pragma unroll
                for (int t = 0; t < 4; t++) { tb[t] = tb1 ^ (unsigned)(t << 5); asm volatile("" : "+v"(tb[t])); }
            }
            SB();
            STAMP(3);
            f32x4 ev;
            f32x2 sr[2][4], si[2][4];
#pragma unroll
            for (int st = 0; st < 2; st++) {
                const f32x2 b2 = st ? hi2(bs) : lo2(bs);
#pragma unroll
                for (int t = 0; t < 4; t++) {
                    const f32x4 nt = (t & 2) ? nt1 : nt0;
                    const f32x2 n2 = (t & 1) ? hi2(nt) : lo2(nt);
                    sr[st][t] = bfma<0, 0>(b2, n2, (t & 1) ? hi2(acc[st][t >> 1]) : lo2(acc[st][t >> 1]));
                    si[st][t] = bfma<1, 0>(b2, n2, (t & 1) ? hi2(acc[2 + st][t >> 1]) : lo2(acc[2 + st][t >> 1]));
                }
            }
            SB();
#pragma unroll
            for (int st = 0; st < 2; st++)
#pragma unroll
                for (int t = 0; t < 4; t++) {
                    const f32x4 rs = (t & 2) ? rs1 : rs0;
                    const f32x2 r2 = (t & 1) ? hi2(rs) : lo2(rs);
                    sr[st][t] *= r2; si[st][t] *= r2;
                }
            SB();
            const f32x2 a1[2] = {lo2(ca), hi2(ca)}, cf[2] = {lo2(co), hi2(co)};
#pragma unroll
            for (int t = 1; t < 4; t++) {
                f32x2 tr[2], ti[2];
#pragma unroll
                for (int st = 0; st < 2; st++) { tr[st] = bfma<1, 1>(a1[st], si[st][t - 1], sr[st][t]); ti[st] = bfma<1, 0>(a1[st], sr[st][t - 1], si[st][t]); }
                SB();
#pragma unroll
                for (int st = 0; st < 2; st++) { sr[st][t] = bfma<0, 0>(a1[st], sr[st][t - 1], tr[st]); si[st][t] = bfma<0, 0>(a1[st], si[st][t - 1], ti[st]); }
                SB();
            }
#pragma unroll
            for (int st = 0; st < 2; st++) {
                ev[2 * st] = sfma(c4[2 * st], sr[st][3].x, sfnma(c4[2 * st + 1], si[st][3].x, sr[st][3].y));
                ev[2 * st + 1] = sfma(c4[2 * st], si[st][3].x, sfma(c4[2 * st + 1], sr[st][3].x, si[st][3].y));
                eX[2 * st] = sr[st][3].x; eX[2 * st + 1] = si[st][3].x;
            }
            sts128f(L_SEG + w * 1024 + l16, ev);
            {
                f32x4 ew;
#pragma unroll
                for (int st = 0; st < 2; st++) {
                    ew[2 * st] = sfms(w15[2 * st], ev[2 * st], smul(w15[2 * st + 1], ev[2 * st + 1]));
                    ew[2 * st + 1] = sfma(w15[2 * st], ev[2 * st + 1], smul(w15[2 * st + 1], ev[2 * st]));
                }
                sts128f(L_HIN + w * 1024 + l16, ew);
            }
            post(fdone, 4);
            SB();
            unsigned ya = L_O + w * 4096 + l16;
            asm volatile("" : "+v"(ya), "+v"(eX));
#pragma unroll
            for (int st = 0; st < 2; st++) {
                f32x2 tr[4], ti[4];
#pragma unroll
                for (int t = 0; t < 4; t++) { tr[t] = bmul<1, 1>(cf[st], si[st][t]); ti[t] = bmul<1, 1>(cf[st], sr[st][t]); }
                SB();
#pragma unroll
                for (int t = 0; t < 4; t++) { tr[t] = bfma<0, 0>(cf[st], sr[st][t], tr[t]); ti[t] = bfma<0, 1>(cf[st], si[st][t], ti[t]); }
                SB();
                sts128(ya + st * 1024, v4u{pk2(tr[0]), pk2(tr[1]), pk2(tr[2]), pk2(tr[3])});
                sts128(ya + 2048 + st * 1024, v4u{pk2(ti[0]), pk2(ti[1]), pk2(ti[2]), pk2(ti[3])});
                SB();
            }
        }
        STAMP(4);
        {
            const v4u sel0 = lds128(L_SEL + l16), sel1 = lds128(L_SEL + 1024 + l16);
            f32x4 ct[6];
            unsigned fl = 0;
            sfor<0, 68>(LAMB(kk) {
                constexpr int k = decltype(kk)::value, e = k - 4;
                if constexpr (k < 64) mma_c(ct[k % 6], (k & 1) ? sel1 : sel0, xa[k >> 2][(k >> 1) & 1], nm4[(k >> 1) & 1]);
                if constexpr (k >= 64 || k < 4) asm volatile("s_nop 1");
                if constexpr (e >= 0) {
                    xa[e >> 2][(e >> 1) & 1][2 * (e & 1)] = cvtv(ct[e % 6].x, ct[e % 6].y);
                    xa[e >> 2][(e >> 1) & 1][2 * (e & 1) + 1] = cvtv(ct[e % 6].z, ct[e % 6].w);
                }
                if constexpr (k == 40) fl = peek(ffl);
                if constexpr (k == 52) need_full(any_lt(fl, 5), ffl, 5);
                if constexpr (k >= 53 && k < 53 + PD) { constexpr int m = k - 53; const unsigned a = tb[(m >> 1) & 3] + (m & 1) * 8192; const v2u lo = ldstr(a), hi = ldstr(a + 512); bf[(64 + m) % PD] = v4u{lo.x, lo.y, hi.x, hi.y}; }
                SB();
            });
        }
        STAMP(5);
        unsigned go = oG + q * 16384, so = oC + (q >> 1) * 16384 + (q & 1) * 2048;
        asm volatile("" : "+s"(go), "+s"(so));
        f32x4 acc[4][2][2];
        f32x2 r12;
        unsigned fc = 0;
        auto gpass = [&](auto pp, auto n0, auto n1) __attribute__((always_inline)) {
            constexpr int pass = decltype(pp)::value;
            unsigned fl = 0, dn = 0; bool lack = false;
            sfor<decltype(n0)::value, decltype(n1)::value>(LAMB(nn) {
                constexpr int n = decltype(nn)::value, m = n + PD;
                constexpr bool pre = pass == 0 || m < 128;
                constexpr unsigned off = wslot(m >> 4) + (m & 1) * 8192 + ((m >> 3) & 1) * 4096;
                const v4u cur = bf[(n + 64 + 128 * pass) % PD];
                mma<(n < 8)>(acc[(n >> 1) & 3][n & 1][0], cur, xa[n >> 3][0]);
                SB();
                if constexpr ((m & 15) == 0) post(fdone, dn);
                if constexpr ((m & 15) == 0) need_full(lack, ffl, 5 + 8 * pass + (m >> 4));
                v2u lo, hi;
                if constexpr (pre) lo = ldstr(tb[(m >> 1) & 3] + off); else lo = ldstr(tbs[m & 3]);
                if constexpr (pass == 1 && n >= 69 && n < 81 && (n & 1)) set2(IC<(n - 69) / 2>{});
                if constexpr ((m & 15) == 10) fl = peek(ffl);
                if constexpr ((m & 15) == 15) lack = any_lt(fl, m == 127 && pass == 0 ? N1 - 7 : 6 + 8 * pass + (m >> 4));
                if constexpr ((m & 15) == 13) { dn = 5 + 8 * pass + (m >> 4); asm volatile("" : "+v"(dn)); }
                if constexpr (n == 88 && pass == 1) fc = peek(F_RDY);
                if constexpr (n == 94 && pass == 1) { if (__builtin_expect(any_lt(fc, 1), 0)) { wait_ge(F_RDY, 1); } fence_c(); }
                if constexpr (n >= 96 && pass == 1 && (n & 1) == 0) { constexpr int f = (n - 96) >> 1; reldA<(f & 3) * 1024>(AF(f >> 2, f & 3), l16, sws, uoff(f >> 2)); }
                if constexpr (pass == 1 && (n == 33 || n == 41 || n == 49 || n == 57)) try_raise(1);
                if constexpr (pass == 1 && n == 67) { if (raised < 1) { waitv<0>(); raise(1); } }
                if constexpr (n == 120 && pass == 0) fc = peek(ffl + (F_COMM - F_FULL));
                if constexpr (n == 121 && pass == 0) { const unsigned ra = L_RSTD + w * 128 + (((l16 >> 1) & 0x60) | ((l16 >> 2) & 0xc)); r12 = f32x2{lds32f(ra), lds32f(ra + 16)}; }
                SB();
                mma<(n < 8)>(acc[(n >> 1) & 3][n & 1][1], cur, xa[n >> 3][1]);
                SB();
                if constexpr (pre) hi = ldstr(tb[(m >> 1) & 3] + off + 512); else hi = ldstr(tbs[m & 3] + 2048);
                bf[n % PD] = v4u{lo.x, lo.y, hi.x, hi.y};
                if constexpr ((m & 57) == 57 && (pass == 0 || m < 64)) {
                    tb[(m >> 1) & 3] += (m & 64) ? L_P - L_W : L_W - L_P; asm volatile("" : "+v"(tb[(m >> 1) & 3]));
                }
                SB();
            });
        };
        auto gated = [&](auto ff) __attribute__((always_inline)) {
            constexpr int uu = decltype(ff)::value >> 1, bb = decltype(ff)::value & 1;
            const f32x4 h0 = acc[2 * uu][0][bb] * acc[2 * uu][1][bb], h1 = acc[2 * uu + 1][0][bb] * acc[2 * uu + 1][1][bb];
            return v4u{pk2(bmul<bb, 0>(r12, lo2(h0))), pk2(bmul<bb, 0>(r12, hi2(h0))), pk2(bmul<bb, 0>(r12, lo2(h1))), pk2(bmul<bb, 0>(r12, hi2(h1)))};
        };
        gpass(IC<0>{}, IC<0>{}, IC<128>{});
        {
            r12 = r12 * r12;
            mfma_done();
            STAMP(6); VAL(25, stall); stall = 0;
            sfor<0, 2>(LAMB(ff) { stA<decltype(ff)::value * 1024>(gated(ff), l16, sws, go); });
            SB();
            need_full(any_lt(fc, 4), ffl + (F_COMM - F_FULL), 4);
            const unsigned tg = peek(L_TAG);
            const unsigned cl = L_CST + (l16 & 0xf0);
            unsigned ya = L_O + w * 4096 + l16;
            asm volatile("" : "+v"(ya));
            const f32x4 h0 = lds128f(L_XCH + 256 + (l16 & 0xf0)), h1 = lds128f(L_XCH + 512 + (l16 & 0xf0));
            const f32x4 ap = lds128f(L_SEG + w * 1024 + l16), pf = lds128f(L_HIN + w * 1024 + l16);
            const v4u sel[2] = {lds128(L_SEL + l16), lds128(L_SEL + 1024 + l16)};
            const v4u yr[2] = {lds128(ya), lds128(ya + 1024)}, yi[2] = {lds128(ya + 2048), lds128(ya + 3072)};
            const f32x4 c4 = lds128f(cl + 256);
            f32x4 cw[4];
            SB();
            sfor<2, 4>(LAMB(ff) { stA<decltype(ff)::value * 1024>(gated(ff), l16, sws, go); });
            tgs = __builtin_amdgcn_readfirstlane(tg);
#pragma unroll
            for (int t = 0; t < 4; t++) cw[t] = lds128f(cl + (3 + t) * 256);
            SB();
            f32x4 zr[2][2], zi[2][2];
#pragma unroll
            for (int st = 0; st < 2; st++)
#pragma unroll
                for (int bb = 0; bb < 2; bb++) { mma<true>(zr[st][bb], yr[st], sel[bb]); mma<true>(zi[st][bb], yi[st], sel[bb]); }
            SB();
            f32x2 hr[2], hi[2];
            {
                const f32x4 cy = h0 + h1;
                f32x4 hin;
                float t0[2], t1[2];
#pragma unroll
                for (int st = 0; st < 2; st++) { t0[st] = sfnma(ap[2 * st + 1], cy[2 * st + 1], pf[2 * st]); t1[st] = sfma(ap[2 * st + 1], cy[2 * st], pf[2 * st + 1]); }
                SB();
#pragma unroll
                for (int st = 0; st < 2; st++) { hin[2 * st] = sfma(ap[2 * st], cy[2 * st], t0[st]); hin[2 * st + 1] = sfma(ap[2 * st], cy[2 * st + 1], t1[st]); }
                SB();
#pragma unroll
                for (int st = 0; st < 2; st++) { t0[st] = sfnma(c4[2 * st + 1], hin[2 * st + 1], eX[2 * st]); t1[st] = sfma(c4[2 * st + 1], hin[2 * st], eX[2 * st + 1]); }
                SB();
#pragma unroll
                for (int st = 0; st < 2; st++) { hr[st] = f32x2{hin[2 * st], sfma(c4[2 * st], hin[2 * st], t0[st])}; hi[st] = f32x2{hin[2 * st + 1], sfma(c4[2 * st], hin[2 * st + 1], t1[st])}; }
                SB();
            }
#pragma unroll
            for (int st = 0; st < 2; st++) {
                f32x2 tr[4], ti[4];
#pragma unroll
                for (int t = 0; t < 4; t++) { const f32x2 wv = st ? hi2(cw[t]) : lo2(cw[t]); tr[t] = bmul<1, 1>(wv, hi[st]); ti[t] = bmul<1, 1>(wv, hr[st]); }
                SB();
#pragma unroll
                for (int t = 0; t < 4; t++) { const f32x2 wv = st ? hi2(cw[t]) : lo2(cw[t]); tr[t] = bfma<0, 0>(wv, hr[st], tr[t]); ti[t] = bfma<0, 1>(wv, hi[st], ti[t]); }
                SB();
                v4u fr = {pk2(tr[0]), pk2(tr[1]), pk2(tr[2]), pk2(tr[3])}, fi = {pk2(ti[0]), pk2(ti[1]), pk2(ti[2]), pk2(ti[3])};
                asm volatile("s_nop 1" : "+v"(fr), "+v"(fi));
#pragma unroll
                for (int bb = 0; bb < 2; bb++) { mma<false>(zr[st][bb], fr, sel[bb]); mma<false>(zi[st][bb], fi, sel[bb]); }
                SB();
            }
            mfma_done();
            stA<0>(pk8(zr[0][0], zr[1][0]), l16, sws, so); stA<0>(pk8(zi[0][0], zi[1][0]), l16, sws, so + 8 * 16384);
            stA<1024>(pk8(zr[0][1], zr[1][1]), l16, sws, so); stA<1024>(pk8(zi[0][1], zi[1][1]), l16, sws, so + 8 * 16384);
            SB();
            STAMP(7);
        }
        if (heavy) {
            gpass(IC<1>{}, IC<0>{}, IC<128>{});
            mfma_done();
            STAMP(8); VAL(26, stall); stall = 0;
            sfor<0, 4>(LAMB(ff) { stA<decltype(ff)::value * 1024>(gated(ff), l16, sws, go + 4 * 16384); });
            SB();
        }
        int q2 = q;
        asm volatile("" : "+s"(q2));
        if (q2 == 3) {
            waitv<0>(); raise(1); raise(2);
            sfor<0, 6>(set2);
            wait_ge(ffl, N1 + 1);
            sfor<0, PD>(prime2);
            wait_ge(F_RDY, 1);
            sfor<0, 16>(LAMB(ff) { constexpr int f = decltype(ff)::value; reldA<(f & 3) * 1024>(AF(f >> 2, f & 3), l16, sws, uoff(f >> 2)); });
#pragma unroll
            for (int u = 0; u < 4; u++) asm volatile("s_waitcnt vmcnt(0)" : "+v"(AF(u, 0)), "+v"(AF(u, 1)), "+v"(AF(u, 2)), "+v"(AF(u, 3))::"memory");
        }
    }
    STAMP(9);
    {
        f32x4 acc[8][2];
        v4u pk[3];
        unsigned fl = 0, fr = 0, dn = 0; bool lack = false;
        float tagf = 0;
        constexpr auto fv = [](int n) { return n < 48 ? n >> 4 : n < 108 ? 3 + (n - 48) / 12 : n < 144 ? 8 + ((n - 108) % 12) / 4 : 3 + (n - 144) / 4; };
        constexpr auto fk = [](int n) { return n < 48 ? (n >> 3) & 1 : n < 108 ? ((n - 48) % 12) / 6 : (n >> 1) & 1; };
        constexpr auto fd = [](int n) { return n < 48 ? n & 7 : n < 108 ? 2 + (n - 48) % 6 : n < 144 ? 2 * ((n - 108) / 12 + 1) + (n & 1) : n & 1; };
        constexpr auto first = [](int n) { return n >= 0 && (n < 48 ? (n & 15) == 0 : n < 108 ? (n - 48) % 12 == 0 : n < 120 && (n & 3) == 0); };
        constexpr int LG = LGATE;
        sfor<0, 176>(LAMB(nn) {
            constexpr int n = decltype(nn)::value, m = n + PD;
            constexpr int v = fv(n), ks = fk(n), dt = fd(n);
            constexpr int mv = fv(m), mk = fk(m), md = fd(m);
            constexpr bool mc = (mv & ~3) == 4;
            constexpr unsigned moff = (mv & 3) * SLOT + (mc ? md * 2048 : (md >> 2) * 8192 + mk * 4096);
            constexpr auto chk = [](int q) { return q == 16 ? 2 : q == 32 ? 4 : q == 60 ? 8 : q == 108 ? 11 : 0; };
            v4u cur; v2u lo, hi;
            if constexpr (n >= 0) {
                if constexpr (first(n)) {
                    constexpr int mid = n <= 32 ? 0 : (n - 31) / 2 < 16 ? (n - 31) / 2 : 16, late = n <= LG ? 0 : n - LG < 12 ? n - LG : 12;
                    constexpr int NW = v == 0 ? 16 : v == 1 ? 12 : v < 4 ? 63 : v < 8 ? mid - 4 * (v - 3) + late : late - 4 * (v - 7);
                    static_assert(NW >= 0, "load schedule");
                    asm volatile("s_waitcnt vmcnt(%4)" : "+v"(AF(v & 7, 0)), "+v"(AF(v & 7, 1)), "+v"(AF(v & 7, 2)), "+v"(AF(v & 7, 3)) : "n"(NW) : "memory");
                }
                cur = bf[n % PD];
                mma<(n < 8)>(acc[dt][0], cur, AF(v & 7, 2 * ks));
                SB();
            }
            if constexpr (m > 0 && (m & 15) == 0 && m <= 48) post(fdone, dn);
            if constexpr (chk(m) > 0) need_full(lack, ffl, N1 + chk(m));
            if constexpr (m < 176) { if constexpr (mc) bf[m % PD] = lds128(cbs[mk] + moff); else lo = ldstr(tbs[md & 3] + moff); }
            if constexpr (chk(m + 6) > 0) fl = peek(ffl);
            if constexpr (chk(m + 1) > 0) lack = any_lt(fl, N1 + chk(m + 1));
            if constexpr ((m & 15) == 13 && m < 48) { dn = N1 + mv + 1; asm volatile("" : "+v"(dn)); }
            if constexpr (n == 19 || n == 25) try_raise(2);
            if constexpr (n == 31) { if (raised < 2) { waitv<0>(); raise(2); } }
            if constexpr (n >= 32 && n < 64 && (n & 1) == 0) { constexpr int f = (n - 32) >> 1; reldA<(f & 3) * 1024>(AF(4 + (f >> 2), f & 3), l16, sws, uoff(4 + (f >> 2))); }
            if constexpr (n == LG - 6) fr = peek(F_RDY + 4);
            if constexpr (n == LG - 1) need_full(any_lt(fr, 1), F_RDY + 4, 1);
            if constexpr (n >= LG && n < LG + 12) { constexpr int f = n - LG; reldA<(f & 3) * 1024>(AF(f >> 2, f & 3), l16, sws, uoff(8 + (f >> 2))); }
            if constexpr (n == 70) { tagf = __uint_as_float(((tgs & 0x3fffu) + 0x2000u) << 16); asm volatile("" : "+v"(tagf)); }
            SB();
            if constexpr (n >= 0) { mma<(n < 8)>(acc[dt][1], cur, AF(v & 7, 2 * ks + 1)); SB(); }
            if constexpr (m < 176 && !mc) { hi = ldstr(tbs[md & 3] + moff + 2048); bf[m % PD] = v4u{lo.x, lo.y, hi.x, hi.y}; }
            if constexpr (n >= 120 && n < 156) {
                constexpr int sl = (n - 108) / 12, r = (n - 108) % 12 - 2;
                auto val = [&](int k) { return acc[2 * sl + (k >> 3)][(k >> 2) & 1][k & 3]; };
                if constexpr (r >= 0 && r < 9 && r % 3 < 2) {
                    constexpr int gi = 2 * (r / 3) + r % 3;
                    pk[gi >> 1][2 * (gi & 1)] = cvtv(val(3 * gi), val(gi < 5 ? 3 * gi + 1 : 15));
                    pk[gi >> 1][2 * (gi & 1) + 1] = cvtv(val(gi < 5 ? 3 * gi + 2 : 15), tagf);
                }
                if constexpr (r >= 0 && r % 3 == 2)
                    stX<(r / 3) * 1024>(pk[r / 3], l16, sws, oX[sl - 1]);
                if constexpr (r == 9 && sl == 3) post(fdone + (F_SENT - F_DONE), 3);
            }
            if constexpr (n == 107) { STAMP(10); VAL(28, stall); stall = 0; }
            SB();
        });
        mfma_done();
        STAMP(11); VAL(29, stall);
#pragma unroll
        for (int e = 0; e < 4; e++) sts128f(L_O + w * 4096 + e * 1024 + l16, acc[e >> 1][e & 1]);
        post(fdone + (F_SENT - F_DONE), 4);
    }
    STAMP(14); STAMP_DUMP();
}

extern "C" void kernel_launch(void* const* d_in, const int* in_sizes, int n_in, void* d_out, int out_size, void* d_ws, size_t ws_size,
                              hipStream_t stream) {
    Params p;
    p.x = (const u16*)d_in[0]; p.lre = (const u16*)d_in[1]; p.lim = (const u16*)d_in[2];
    p.Bre = (const u16*)d_in[3]; p.Bim = (const u16*)d_in[4]; p.Cre = (const u16*)d_in[5]; p.Cim = (const u16*)d_in[6];
    p.ldt = (const u16*)d_in[7]; p.Dsk = (const u16*)d_in[8];
    p.wg = (const u16*)d_in[10]; p.wu = (const u16*)d_in[11]; p.wd = (const u16*)d_in[12];
    p.out = (float*)d_out; p.ws = (unsigned char*)d_ws;
    (void)hipFuncSetAttribute((const void*)fused, hipFuncAttributeMaxDynamicSharedMemorySize, L_TOTAL);
    fused<<<256, 512, L_TOTAL, stream>>>(p);
}
```

```cpp
#define ROT1 (idx & 7)
#include <hip/hip_runtime.h>
#include <stdint.h>

typedef unsigned short u16;
typedef unsigned v4u __attribute__((ext_vector_type(4)));
typedef unsigned v2u __attribute__((ext_vector_type(2)));
typedef short s16x4 __attribute__((ext_vector_type(4)));
typedef __bf16 bf16x2 __attribute__((ext_vector_type(2)));
typedef __bf16 bf16x8 __attribute__((ext_vector_type(8)));
typedef float f32x2 __attribute__((ext_vector_type(2)));
typedef float f32x4 __attribute__((ext_vector_type(4)));
#define GAS __attribute__((address_space(1)))
#define LAS __attribute__((address_space(3)))
#define DEVI __device__ __forceinline__
#define NOUNROLL _Pragma("nounroll")

#define SPIN_SLEEP 0
constexpr int DM = 512, FF = 1792;
constexpr unsigned WS_A2 = 0;
constexpr unsigned WS_XB = 16u * 44u * 16384u;
constexpr unsigned WS_GRAN = WS_XB + 256u * 36864u;
constexpr unsigned WS_FLAG = WS_GRAN + 16u * 16u * 512u;
constexpr unsigned WS_EPOCH = WS_FLAG + 1024u * 128u;
constexpr unsigned WS_END = WS_EPOCH + 256u;
constexpr unsigned SLOT = 16384;
constexpr unsigned L_RSTD = 0, L_MEAN = 512;
constexpr unsigned F_DONE = 1024, F_SENT = F_DONE + 16, F_FULL = F_DONE + 32, F_COMM = F_DONE + 40, F_RDY = F_DONE + 48, F_OUT = F_DONE + 64, F_XI = F_DONE + 80, F_BS = F_DONE + 96, L_TAG = F_DONE + 128;
constexpr unsigned L_CST = 1280;
constexpr unsigned L_XCH = L_CST + 8 * 256;
constexpr unsigned L_SEG = 4096, L_HIN = 8192, L_SEL = 12288;
constexpr unsigned L_P = SLOT, L_O = 5 * SLOT, L_W = 6 * SLOT, L_TOTAL = 10 * SLOT;
constexpr int SPIN_MAX = 1 << 18;
constexpr int LSPIN_MAX = 1 << 22;

struct Params {
    const u16 *x, *lre, *lim, *Bre, *Bim, *Cre, *Cim, *ldt, *Dsk, *wg, *wu, *wd;
    float* out;
    unsigned char* ws;
};

DEVI float bf2f(u16 v) { return __uint_as_float(((unsigned)v) << 16); }
DEVI float bflo(unsigned v) { return __uint_as_float(v << 16); }
DEVI float bfhi(unsigned v) { return __uint_as_float(v & 0xffff0000u); }
DEVI unsigned pk2(float a, float b) { f32x2 p = {a, b}; return __builtin_bit_cast(unsigned, __builtin_convertvector(p, bf16x2)); }
DEVI unsigned pk2(f32x2 p) { return __builtin_bit_cast(unsigned, __builtin_convertvector(p, bf16x2)); }
DEVI v4u pk8(f32x4 a, f32x4 b) { return v4u{pk2(a.x, a.y), pk2(a.z, a.w), pk2(b.x, b.y), pk2(b.z, b.w)}; }
DEVI float dot2(unsigned a, unsigned b, float c) { return __builtin_amdgcn_fdot2_f32_bf16(__builtin_bit_cast(bf16x2, a), __builtin_bit_cast(bf16x2, b), c, false); }
template <int CTRL> DEVI float dppf(float v) { return __builtin_bit_cast(float, __builtin_amdgcn_update_dpp(0, __builtin_bit_cast(int, v), CTRL, 0xf, 0xf, true)); }
DEVI f32x4 mfma(v4u a, v4u b, f32x4 c) { return __builtin_amdgcn_mfma_f32_16x16x32_bf16(__builtin_bit_cast(bf16x8, a), __builtin_bit_cast(bf16x8, b), c, 0, 0, 0); }
template <bool FIRST> DEVI void mma(f32x4& c, const v4u& a, const v4u& b) {
    if constexpr (FIRST) asm volatile("v_mfma_f32_16x16x32_bf16 %0, %1, %2, 0" : "=&v"(c) : "v"(a), "v"(b));
    else asm volatile("v_mfma_f32_16x16x32_bf16 %0, %1, %2, %0" : "+v"(c) : "v"(a), "v"(b));
}
DEVI void mfma_done() { __builtin_amdgcn_sched_barrier(0); asm volatile("s_nop 7"); __builtin_amdgcn_sched_barrier(0); }
DEVI v4u lds128(unsigned a) { return *(LAS v4u*)(uintptr_t)a; }
DEVI f32x4 lds128f(unsigned a) { return *(LAS f32x4*)(uintptr_t)a; }
DEVI float lds32f(unsigned a) { return *(LAS float*)(uintptr_t)a; }
DEVI void sts32f(unsigned a, float v) { *(LAS float*)(uintptr_t)a = v; }
DEVI void sts128(unsigned a, v4u v) { *(LAS v4u*)(uintptr_t)a = v; }
DEVI void sts128f(unsigned a, f32x4 v) { *(LAS f32x4*)(uintptr_t)a = v; }
DEVI v2u ldstr(unsigned a) { return __builtin_bit_cast(v2u, __builtin_amdgcn_ds_read_tr16_b64_v4i16((LAS s16x4*)(uintptr_t)a)); }
DEVI void fence_c() { asm volatile("" ::: "memory"); }
DEVI void bar_l() { asm volatile("s_waitcnt lgkmcnt(0)\n\ts_barrier" ::: "memory"); }
template <int N> DEVI void waitv() { asm volatile("s_waitcnt vmcnt(%0)" ::"n"(N) : "memory"); }
DEVI void dma16(const void* sbase, unsigned voff, unsigned ldst) {
    asm volatile("s_mov_b32 m0, %2\n\ts_nop 0\n\tglobal_load_lds_dwordx4 %0, %1" ::"v"(voff), "s"(sbase), "s"(ldst) : "memory");
}
DEVI void post(unsigned a, unsigned v) { fence_c(); *(LAS unsigned*)(uintptr_t)a = v; fence_c(); }
DEVI unsigned peek(unsigned a) { fence_c(); return *(LAS unsigned*)(uintptr_t)a; }
DEVI bool any_lt(unsigned v, unsigned need) { return __builtin_amdgcn_ballot_w64(v < need) != 0; }
DEVI void wait_ge(unsigned a, unsigned need) {
    int s = LSPIN_MAX;
    NOUNROLL while (any_lt(peek(a), need)) { asm volatile("s_sub_i32 %0, %0, 1" : "+s"(s) :: "scc"); if (s < 0) break; if (SPIN_SLEEP) __builtin_amdgcn_s_sleep(SPIN_SLEEP); }
    fence_c();
}
template <int V> DEVI void postm(unsigned a, int lane) {
    asm volatile("s_mov_b64 exec, %2\n\tds_max_u32 %0, %1\n\ts_mov_b64 exec, -1" ::"v"(a), "v"(lane), "n"(1 << V) : "memory");
}
DEVI unsigned vm_outstanding() {
    unsigned v; asm volatile("s_getreg_b32 %0, hwreg(HW_REG_IB_STS)" : "=s"(v)::"memory");
    return (v & 15u) | ((v >> 18) & 0x30u);
}
DEVI void postv(unsigned a, int lane, unsigned v) {
    asm volatile("s_lshl_b64 exec, 1, %2\n\tds_max_u32 %0, %1\n\ts_mov_b64 exec, -1" ::"v"(a), "v"(lane), "s"(v) : "memory", "scc");
}
template <int V> DEVI void postc(unsigned a, int lane) {
    asm volatile("s_mov_b64 exec, %2\n\tds_write_b32 %0, %1\n\ts_mov_b64 exec, -1" ::"v"(a), "v"(lane), "n"(1 << V) : "memory");
}
DEVI void need_full(bool lack, unsigned a, unsigned need) { if (__builtin_expect(lack, 0)) wait_ge(a, need); fence_c(); }
#define VAL(id, v)
DEVI float sfma(float a, float b, float c) { float d; asm("v_fma_f32 %0, %1, %2, %3" : "=v"(d) : "v"(a), "v"(b), "v"(c)); return d; }
DEVI float sfms(float a, float b, float c) { float d; asm("v_fma_f32 %0, %1, %2, -%3" : "=v"(d) : "v"(a), "v"(b), "v"(c)); return d; }
DEVI float sfnma(float a, float b, float c) { float d; asm("v_fma_f32 %0, -%1, %2, %3" : "=v"(d) : "v"(a), "v"(b), "v"(c)); return d; }
DEVI float sadd(float a, float b) { float d; asm("v_add_f32 %0, %1, %2" : "=v"(d) : "v"(a), "v"(b)); return d; }
DEVI float smul(float a, float b) { float d; asm("v_mul_f32 %0, %1, %2" : "=v"(d) : "v"(a), "v"(b)); return d; }
DEVI float smul_t(float a, float b) { float d; asm("s_nop 0\n\tv_mul_f32 %0, %1, %2" : "=v"(d) : "v"(a), "v"(b)); return d; }
struct cpx { float x, y; };
DEVI cpx cmul(cpx a, cpx b) { return cpx{sfms(a.x, b.x, smul(a.y, b.y)), sfma(a.x, b.y, smul(a.y, b.x))}; }
DEVI float lmad(cpx a, float h, float u) {
    float d;
    asm("v_fma_f32 %0, %1, %3, %4\n\ts_nop 0\n\tv_fmac_f32_dpp %0, %3, %2 quad_perm:[1,0,3,2] row_mask:0xf bank_mask:0xf" : "=&v"(d) : "v"(a.x), "v"(a.y), "v"(h), "v"(u));
    return d;
}
DEVI f32x2 dup(float v) { f32x2 d = {v, v}; asm("" : "+v"(d)); return d; }
DEVI f32x2 pfma(f32x2 a, f32x2 b, f32x2 c) { return __builtin_elementwise_fma(a, b, c); }
DEVI f32x2 lo2(f32x4 v) { return f32x2{v.x, v.y}; }
DEVI f32x2 hi2(f32x4 v) { return f32x2{v.z, v.w}; }
#define SB() __builtin_amdgcn_sched_barrier(0)
template <int V> struct IC { static constexpr int value = V; };
template <int B, int E, class F> DEVI void sfor(F&& f) { if constexpr (B < E) { f(IC<B>{}); sfor<B + 1, E>(f); } }
#define LAMB(nn) [&](auto nn) __attribute__((always_inline))
#define STAMP(id)
#define STAMP_INIT()
#define STAMP_DUMP()
DEVI int fresh_lane() { int l; asm volatile("v_mbcnt_lo_u32_b32 %0, -1, 0\n\tv_mbcnt_hi_u32_b32 %0, -1, %0" : "=v"(l)); return l; }
DEVI v4u make_sel(int lane, int bb) {
    const int c = lane & 15, tk = 8 * (c >> 2) + 4 * bb + (c & 3) - 8 * (lane >> 4);
    v4u s;
#pragma unroll
    for (int e = 0; e < 4; e++) s[e] = (tk == 2 * e ? 0x3f80u : 0u) | (tk == 2 * e + 1 ? 0x3f800000u : 0u);
    return s;
}
#define LANE_VARS const int lane = fresh_lane(), c = lane & 15, g = lane >> 4, q8 = lane >> 3, p8 = lane & 7; (void)c; (void)g; (void)q8; (void)p8
constexpr unsigned wslot(int n) { return (unsigned)(n & 3) * SLOT; }

#define ROT2 0
#define XI_STEP 8
#define TOUCH_GATE 1
#define LB_SLEEP 8
#define LB_FIRST 10
#define FL_FIRST 20
#define FL_SLEEP 6
#define MIDV 64
#define X_FIRST 10
#define X_SLEEP 1
#define PDV 4
#define INFL 8
#define TW waitv<0>();
#define PACE 14
#define PACE2 8
#define XLV 3
#define LGATE 84
#define SLEEP0 12
constexpr unsigned CODE_LINES = 288;
template <int OFF> DEVI void reldA(v4u& d, unsigned voff, v4u srd, unsigned so) {
    asm volatile("buffer_load_dwordx4 %0, %1, %2, %3 offen offset:%4 sc1" : "+v"(d) : "v"(voff), "s"(srd), "s"(so), "n"(OFF) : "memory");
}
template <int OFF> DEVI void stA(const v4u& d, unsigned voff, v4u srd, unsigned so) {
    asm volatile("buffer_store_dwordx4 %0, %1, %2, %3 offen offset:%4 sc1\n\ts_nop 1" ::"v"(d), "v"(voff), "s"(srd), "s"(so), "n"(OFF) : "memory");
}
template <int OFF> DEVI void stX(const v4u& d, unsigned voff, v4u srd, unsigned so) {
    asm volatile("buffer_store_dwordx4 %0, %1, %2, %3 offen offset:%4 sc1" ::"v"(d), "v"(voff), "s"(srd), "s"(so), "n"(OFF) : "memory");
}
DEVI void touch(unsigned& sink, const void* tp, unsigned to) { asm volatile("global_load_dword %0, %1, %2" : "+v"(sink) : "v"(to), "s"(tp) : "memory"); }

DEVI v4u make_srd(const void* base) { const unsigned long long a = (unsigned long long)base; return v4u{(unsigned)a, (unsigned)(a >> 32) & 0xffffu, 0x7fffffffu, 0x00020000u}; }
DEVI void dma4(v4u srd, unsigned ve, unsigned vo, unsigned s0, unsigned s1, unsigned s2, unsigned s3, unsigned ldst) {
    asm volatile("s_mov_b32 m0, %7\n\ts_nop 0\n\tbuffer_load_dwordx4 %0, %2, %3 offen lds\n\tbuffer_load_dwordx4 %1, %2, %4 offen offset:1024 lds\n\t"
                 "buffer_load_dwordx4 %0, %2, %5 offen offset:2048 lds\n\tbuffer_load_dwordx4 %1, %2, %6 offen offset:3072 lds"
                 ::"v"(ve), "v"(vo), "s"(srd), "s"(s0), "s"(s1), "s"(s2), "s"(s3), "s"(ldst) : "memory");
}
DEVI void dma1(v4u srd, unsigned v, unsigned so, unsigned ldst) {
    asm volatile("s_mov_b32 m0, %3\n\ts_nop 0\n\tbuffer_load_dwordx4 %0, %1, %2 offen lds" ::"v"(v), "s"(srd), "s"(so), "s"(ldst) : "memory");
}
template <int H, int NEG> DEVI f32x2 bmul(f32x2 a, f32x2 b) {
    f32x2 d; asm("v_pk_mul_f32 %0, %1, %2 op_sel:[%3,0] op_sel_hi:[%3,1] neg_lo:[%4,0] neg_hi:[%4,0]" : "=v"(d) : "v"(a), "v"(b), "n"(H), "n"(NEG)); return d;
}
template <int H, int NEG> DEVI f32x2 bfma(f32x2 a, f32x2 b, f32x2 c) {
    f32x2 d; asm("v_pk_fma_f32 %0, %1, %2, %3 op_sel:[%4,0,0] op_sel_hi:[%4,1,1] neg_lo:[%5,0,0] neg_hi:[%5,0,0]" : "=v"(d) : "v"(a), "v"(b), "v"(c), "n"(H), "n"(NEG)); return d;
}
DEVI void mma_c(f32x4& d, const v4u& a, const v4u& b, const f32x4& c) { asm volatile("v_mfma_f32_16x16x32_bf16 %0, %1, %2, %3" : "=&v"(d) : "v"(a), "v"(b), "v"(c)); }
DEVI unsigned cvtv(float a, float b) { unsigned d; asm volatile("v_cvt_pk_bf16_f32 %0, %1, %2" : "=v"(d) : "v"(a), "v"(b)); return d; }

__global__ __launch_bounds__(512, 2) void fused(Params p) {
    const unsigned char* pc0;
    asm volatile("s_getpc_b64 %0" : "=s"(pc0));
    const int w = __builtin_amdgcn_readfirstlane(threadIdx.x >> 6);
    const int b = blockIdx.x, xcd = b & 7, idx = b >> 3;
    const int i = 8 * (xcd & 1) + (idx & 7), kq = xcd >> 1, q = idx >> 3, j = 4 * kq + q;
    const bool heavy = q != 3;
    const int N1 = heavy ? 20 : 12;
    const int lw = w & 1, pr = w & 3;
    const int rot = ROT1;
    const __amdgpu_buffer_rsrc_t rws = __builtin_amdgcn_make_buffer_rsrc(p.ws, 0, WS_END, 0x00020000);
    auto unit_of = [&](int v) { return v < 4 ? 7 * kq + v : v < 8 ? 28 + 8 * ((v >> 1) & 1) + 2 * kq + (v & 1) : 7 * kq + v - 4; };
    const unsigned sl_lo = L_P + (unsigned)pr * SLOT;
    const unsigned xo = WS_XB + (unsigned)((i * 4 + q) * 4) * 36864u + pr * 3072;
    const unsigned fo = WS_FLAG + (unsigned)(i * 16 + j) * 512u;

    if (w >= 4) {
        LANE_VARS;
        STAMP_INIT();
        STAMP(0);
        const unsigned swz = (unsigned)((p8 ^ (q8 >> 1)) << 4);
        const unsigned fa = F_DONE + (lane & 3) * 4;
        unsigned sink = 0, tg;
        auto poll16 = [&](int which) __attribute__((always_inline)) {
            wait_ge(F_OUT + (lane & 3) * 4, which + 1);
            STAMP(16);
            const unsigned pf = WS_FLAG + (unsigned)(i * 16 + 4 * kq + (lane & 3)) * 512u + which * 128 + ((lane >> 2) & 3) * 8;
            NOUNROLL for (int spin = 0; spin < SPIN_MAX; spin++) {
                fence_c();
                const v2u f = __builtin_amdgcn_raw_buffer_load_b64(rws, pf, 0, 16);
                if (__builtin_amdgcn_ballot_w64(f.x != tg || f.y != ~tg) == 0) break;
                __builtin_amdgcn_s_sleep(FL_SLEEP);
            }
            post(F_RDY + which * 4, 1);
            STAMP(17);
        };
        if (w < 6) {
            unsigned ffull = F_FULL + lw * 4;
            asm volatile("" : "+v"(ffull));
            {
                const unsigned voffS = (8 * (q8 >> 2) + (q8 & 3)) * 1024 + swz;
                const v4u srd = make_srd((const unsigned char*)(lw ? p.Bim : p.Bre) + (size_t)(32 * j) * 1024);
                NOUNROLL for (int n = 2; n < 8; n++) {
                    const unsigned so = 128 * ((n + rot) & 7);
                    dma4(srd, voffS, voffS ^ 64u, so, so + 15360, so + 2048, so + 17408, L_W + (n >> 1) * SLOT + lw * 8192 + (n & 1) * 4096);
                    if (n == 3) {
                        touch(sink, p.x, (128 * i + 8 * j + 4 * lw) * 1024 + (lane & 31) * 128);
                        touch(sink, lw ? p.Bim : p.Bre, (32 * j + 2 * i) * 1024 + c * 128);
                        bar_l();
                    }
                    if (n == 4) { waitv<6>(); postc<2>(ffull, lane); }
                    if (n >= 5) { waitv<4>(); postv(ffull, lane, n >> 1); }
                }
                waitv<0>(); postc<4>(ffull, lane);
                asm volatile("" ::"v"(sink));
            }
            STAMP(1);
            const unsigned voffG = q8 * (FF * 2) + (((p8 >> 1) ^ ((q8 >> 1) & 1)) << 5) + ((p8 & 1) << 4), voffG1 = voffG ^ 64u;
            const unsigned voffC = q8 * 1024 + swz, voffC1 = voffC ^ 64u;
            const unsigned voffD = q8 * 1024 + (((p8 >> 1) ^ (q8 >> 1) ^ (2 * (kq & 1))) << 5) + ((p8 & 1) << 4);
            const unsigned char* gb = (const unsigned char*)(lw ? p.wu : p.wg) + (7 * kq + q) * 128;
            unsigned thr = (lane >> 2) + 1;
            int credit = 0, cbase = 0;
            auto item = [&](int n, const unsigned char* b0, const unsigned char* b1, unsigned ve, unsigned vo, unsigned st) __attribute__((always_inline)) {
                v4u s0 = make_srd(b0), s1 = make_srd(b1);
                unsigned dst = ((n & 4) ? L_P : L_W) + (n & 3) * SLOT + lw * 8192, dst1 = dst + 4096;
                asm volatile("" : "+s"(s0), "+s"(s1), "+s"(dst), "+s"(dst1));
                int sp = LSPIN_MAX;
                NOUNROLL while (n - 7 > credit) {
                    const unsigned dn = peek(fa);
                    postv(ffull, lane, n - ((vm_outstanding() + 7) >> 3));
                    unsigned long long mk = __builtin_amdgcn_ballot_w64(dn >= thr);
                    mk &= mk >> 1; mk &= mk >> 2;
                    credit = __builtin_popcountll(mk & 0x1111111111111111ull) + cbase;
                    asm volatile("s_sub_i32 %0, %0, 1" : "+s"(sp)::"scc");
                    if (sp < 0) break;
                }
                waitv<INFL>();
                dma4(s0, ve, vo, 0, st - 1024, 2 * st - 2048, 3 * st - 3072, dst);
                postv(ffull, lane, n - (INFL + 7) / 8);
                waitv<INFL>();
                dma4(s1, ve, vo, 0, st - 1024, 2 * st - 2048, 3 * st - 3072, dst1);
            };
            wait_ge(F_XI + (lane & 3) * 4, 3);
            NOUNROLL for (int n = 4; n < N1; n++) {
                const int t = n - 4;
                const unsigned char* bp = gb + (size_t)(((t + rot) & 7) * 64) * (FF * 2) + (t >> 3) * 512;
                item(n, bp, bp + 32 * FF * 2, voffG, voffG1, 8 * FF * 2);
            }
            cbase = N1 - 8; thr += cbase;
            const unsigned char* dp = (const unsigned char*)p.wd + (size_t)(7 * kq) * 65536 + q * 256 + (lw ^ (kq >> 1)) * 128;
            NOUNROLL for (int v = 0; v < 11; v++) {
                if ((v & ~3) == 4) {
                    const unsigned char* cbp = (const unsigned char*)((v & 2) ? p.Cim : p.Cre) + (size_t)q * 131072 + (2 * kq + (v & 1)) * 128;
                    item(N1 + v, cbp + ((2 * lw) ^ kq) * 32768, cbp + ((2 * lw + 1) ^ kq) * 32768, voffC, voffC1, 8192);
                } else {
                    item(N1 + v, dp, dp + 32768, voffD, voffD, 8192);
                    dp += 65536;
                }
            }
            waitv<16>(); postv(ffull, lane, N1 + 9);
            waitv<8>(); postv(ffull, lane, N1 + 10);
            waitv<0>(); postv(ffull, lane, N1 + 11);
            tg = peek(L_TAG);
            STAMP(7);
        } else {
            unsigned tag0, prm0, prm1, prm2;
            const unsigned cto = min(idx * 9 + 4 * lw + p8, (int)CODE_LINES - 1) * 128;
            touch(sink, pc0, cto);
            const unsigned po = 2 * (32 * j + 8 * (lane >> 4) + 4 * ((lane >> 1) & 1) + ((lane >> 2) & 3));
            asm volatile("global_load_ushort %0, %4, %5\n\tglobal_load_ushort %1, %4, %6\n\tglobal_load_ushort %2, %4, %7\n\tglobal_load_dword %3, %8, %9 sc1"
                         : "=&v"(prm0), "=&v"(prm1), "=&v"(prm2), "=&v"(tag0) : "v"(po), "s"(p.lre), "s"(p.lim), "s"(p.ldt), "v"(WS_EPOCH), "s"(p.ws) : "memory");
            *(LAS unsigned*)(uintptr_t)(F_DONE + (lane & 31) * 4) = 0u;
            bar_l();
            sts128(L_SEL + lw * 1024 + lane * 16, make_sel(lane, lw));
            asm volatile("s_waitcnt vmcnt(0)" : "+v"(prm0), "+v"(prm1), "+v"(prm2), "+v"(tag0)::"memory");
            tg = tag0 + 1u;
            const bool odd = lane & 1;
            const unsigned la = lane * 4;
            const float lr = bflo(prm0), li = bflo(prm1);
            float dt = __expf(bflo(prm2));
            dt = bflo(pk2(dt, dt));
            const float ex = __expf(dt * lr);
            const float fr = __builtin_amdgcn_fractf(dt * li * 0.15915494309189535f);
            const float ar = smul_t(ex, __builtin_amdgcn_cosf(fr)), ai = smul_t(ex, __builtin_amdgcn_sinf(fr));
            const cpx a1 = {ar, ai};
            const cpx a2 = cmul(a1, a1), a4 = cmul(a2, a2);
            const unsigned sg = odd ? 0u : 0x80000000u;
            auto sgn = [&](cpx v) { return cpx{v.x, __uint_as_float(__float_as_uint(v.y) ^ sg)}; };
            const cpx r8 = cmul(a4, a4), r16 = cmul(r8, r8), r32 = cmul(r16, r16), r64 = cmul(r32, r32);
            const cpx a8 = sgn(r8);
            float pw[8];
            pw[0] = lw ? (odd ? r64.y : r64.x) : (odd ? 0.0f : 1.0f);
#pragma unroll
            for (int s = 1; s < 8; s++) pw[s] = lmad(a8, pw[s - 1], 0.0f);
#pragma unroll
            for (int s = 0; s < 8; s++) sts32f(L_HIN + (15 - 8 * lw - s) * 256 + la, pw[s]);
            if (lw == 0) {
                const float nr = ar - 1.0f, den = smul_t(__builtin_amdgcn_rcpf(sfma(lr, lr, smul(li, li))), 2.0f);
                float co = smul(odd ? sfms(ai, lr, smul(nr, li)) : sfma(nr, lr, smul(ai, li)), den);
                sts32f(L_CST + la, odd ? a1.y : a1.x); sts32f(L_CST + 256 + la, odd ? a4.y : a4.x);
                const cpx a1s = sgn(a1);
#pragma unroll
                for (int t = 0; t < 5; t++) { sts32f(L_CST + (2 + t) * 256 + la, co); if (t < 4) co = lmad(a1s, co, 0.0f); }
                *(LAS unsigned*)(uintptr_t)L_TAG = tg;
            }
            post(F_COMM + lw * 4, 1);
            STAMP(1);
            cpx wn[8];
            {
                const cpx r128 = cmul(r64, r64), r256 = cmul(r128, r128);
                wn[0] = lw ? r128 : cpx{1.0f, 0.0f};
#pragma unroll
                for (int n = 1; n < 8; n++) wn[n] = cmul(wn[n - 1], r256);
#pragma unroll
                for (int n = 0; n < 8; n++) { wn[n] = sgn(wn[n]); asm volatile("" : "+v"(wn[n].x), "+v"(wn[n].y)); }
            }
            wait_ge(F_XI + (lane & 3) * 4, TOUCH_GATE);
            touch(sink, lw ? p.wu : p.wg, (32 * i + (lane & 31)) * (FF * 2) + (7 * kq + q + ((heavy && lane >= 32) ? 4 : 0)) * 128);
            TW touch(sink, p.wd, (448 * kq + 28 * i + min(lane, 27)) * 1024 + (2 * q + lw) * 128);
            TW touch(sink, lw ? p.Cim : p.Cre, (128 * q + 8 * i + p8) * 1024 + (2 * kq + (q8 & 1)) * 128);
            TW touch(sink, lw ? (const void*)p.ws : (const void*)p.out, lw ? min((unsigned)(lane & 31) * 0xb0000u + idx * 0x4000u, WS_END - 4) : (lane & 3) * 0x100000 + idx * 0x4000);
            unsigned gro = WS_GRAN + j * 8192 + lane * 8;
            asm volatile("" : "+v"(gro));
            wait_ge(fa, 4);
            STAMP(4);
            if (lw) {
                float e[16];
#pragma unroll
                for (int s = 0; s < 16; s++) e[s] = lds32f(L_HIN + s * 256 + la);
                postc<2>(F_COMM + 4, lane);
#pragma unroll
                for (int d = 8; d > 0; d >>= 1)
#pragma unroll
                    for (int s = 0; s < d; s++) e[s] = sadd(e[s], e[s + d]);
                __builtin_amdgcn_raw_buffer_store_b64(v2u{__float_as_uint(e[0]), tg}, rws, gro + i * 512, 0, 16);
            }
            STAMP(5);
            {
                float e[8], h = 0;
#pragma unroll
                for (int s = 0; s < 8; s++) e[s] = lds32f(L_SEG + (8 * lw + s) * 256 + la);
#pragma unroll
                for (int s = 0; s < 8; s++) sts32f(L_SEG + (8 * lw + s) * 256 + la, pw[s]);
                wait_ge(F_COMM + (lw ^ 1) * 4, 2);
                if (lw) h = lds32f(L_XCH + la);
#pragma unroll
                for (int s = 0; s < 8; s++) { sts32f(L_HIN + (8 * lw + s) * 256 + la, h); if (s < 7 || !lw) h = lmad(a8, h, e[s]); }
                if (!lw) { sts32f(L_XCH + la, h); postc<2>(F_COMM, lane); }
            }
            STAMP(6);
            {
                const int top = i - 1 - lw;
                v2u gr[8] = {};
                NOUNROLL for (int spin = 0; spin < SPIN_MAX; spin++) {
                    fence_c();
#pragma unroll
                    for (int n = 0; n < 8; n++) gr[n] = __builtin_amdgcn_raw_buffer_load_b64(rws, gro, ((top - 2 * n) & 15) * 512, 16);
                    unsigned long long bad = 0;
#pragma unroll
                    for (int n = 0; n < 8; n++) if (top - 2 * n >= 0) bad |= __builtin_amdgcn_ballot_w64(gr[n].y != tg);
                    if (bad == 0) break;
                    __builtin_amdgcn_s_sleep(LB_SLEEP);
                }
                float h = 0;
#pragma unroll
                for (int n = 7; n >= 0; n--) if (top - 2 * n >= 0) h = lmad(wn[n], __uint_as_float(gr[n].x), h);
                sts32f(L_XCH + 256 + lw * 256 + la, h);
            }
            postc<4>(F_COMM + lw * 4, lane);
            waitv<0>();
            asm volatile("" ::"v"(sink));
            STAMP(7);
            poll16(lw);
        }
        f32x4 base[4];
        {
            const int d = 128 * q + 32 * kq + 4 * p8;
            const v2u dv = *(const GAS v2u*)(p.Dsk + d);
            const float df[4] = {bflo(dv.x), bfhi(dv.x), bflo(dv.y), bfhi(dv.y)};
            v2u xv[4];
#pragma unroll
            for (int e = 0; e < 4; e++) xv[e] = *(const GAS v2u*)(p.x + (size_t)(128 * i + 32 * pr + 8 * e + q8) * DM + d);
#pragma unroll
            for (int e = 0; e < 4; e++) {
                const unsigned tp = (32 * pr + 8 * e + 4 * ((q8 >> 1) & 1) + 2 * (q8 & 1) + (q8 >> 2)) * 4;
                const float rs = lds32f(L_RSTD + tp), nm = smul(lds32f(L_MEAN + tp), rs);
                const float xf[4] = {bflo(xv[e].x), bfhi(xv[e].x), bflo(xv[e].y), bfhi(xv[e].y)};
#pragma unroll
                for (int k = 0; k < 4; k++) base[e][k] = sfma(sfma(xf[k], rs, nm), df[k], xf[k]);
            }
        }
        STAMP(8);
        const unsigned t16 = ((tg & 0x3fffu) + 0x2000u) << 16;
        const unsigned xi = xo + kq * 36864u + lane * 16;
        auto bad3 = [&](const v4u* in) { return ((in[0].y ^ t16) | (in[0].w ^ t16) | (in[1].y ^ t16)) | ((in[1].w ^ t16) | (in[2].y ^ t16) | (in[2].w ^ t16)); };
        auto val = [&](const v4u* in, int k) { const unsigned dw = in[k / 6][2 * ((k / 3) & 1) + (k % 3 == 2)]; return (k % 3 == 1) ? bfhi(dw) : bflo(dw); };
        const unsigned sc = L_O + pr * 4096;
        unsigned wa = sc + (8 * (c >> 2) + 4 * (c & 1) + ((c >> 1) & 1)) * 128 + ((g ^ (c & 7)) << 4), ra = sc + q8 * 128 + ((p8 ^ (2 * (q8 & 1) + (q8 >> 2))) << 4);
        GAS float* op = (GAS float*)p.out + (size_t)(128 * i + 32 * pr + q8) * DM + 128 * q + 32 * kq + 4 * p8;
        asm volatile("" : "+v"(wa), "+v"(ra), "+v"(op));
        wait_ge(F_SENT + pr * 4, 3);
        __builtin_amdgcn_s_sleep(X_FIRST);
        v4u in[9];
        f32x4 own[4];
        NOUNROLL for (int spin = 0; spin < SPIN_MAX; spin++) {
            fence_c();
#pragma unroll
            for (int n = 0; n < 9; n++) in[n] = __builtin_amdgcn_raw_buffer_load_b128(rws, xi + (n / 3) * 12288 + (n % 3) * 1024, 0, 16);
            SB();
            wait_ge(F_SENT + pr * 4, 4);
#pragma unroll
            for (int e = 0; e < 4; e++) own[e] = lds128f(sc + e * 1024 + lane * 16);
            if (__builtin_amdgcn_ballot_w64((bad3(in) | bad3(in + 3) | bad3(in + 6)) > 0xffffu) == 0) break;
        }
        STAMP(11);
#pragma unroll
        for (int e = 0; e < 4; e++) {
            f32x4 t = own[e];
#pragma unroll
            for (int sr = 0; sr < 9; sr += 3) t += f32x4{val(in + sr, 4 * e), val(in + sr, 4 * e + 1), val(in + sr, 4 * e + 2), val(in + sr, 4 * e + 3)};
            own[e] = t;
        }
        asm volatile("" : "+v"(own[0]), "+v"(own[1]), "+v"(own[2]), "+v"(own[3]));
#pragma unroll
        for (int e = 0; e < 4; e++) sts128f((wa ^ (unsigned)((e >> 1) << 6)) + (e & 1) * 256, own[e]);
        fence_c();
        f32x4 ta[4];
#pragma unroll
        for (int e = 0; e < 4; e++) ta[e] = lds128f((ra ^ (unsigned)((e & 1) << 6)) + e * 1024);
        SB();
#pragma unroll
        for (int e = 0; e < 4; e++) *(GAS f32x4*)(op + e * 8 * DM) = base[e] + ta[e];
        if (b == 255 && w == 4 && lane == 0) __builtin_amdgcn_raw_buffer_store_b32(tg, rws, WS_EPOCH, 0, 16);
        STAMP(12); STAMP_DUMP();
        return;
    }
    constexpr int PD = PDV, XL = XLV;
    unsigned stall = 0; (void)stall;
    v4u xa[16][2];
#define AF(u, k) xa[2 * (u) + ((k) >> 1)][(k) & 1]
    v4u sws = make_srd(p.ws);
    unsigned oG = WS_A2 + (unsigned)(i * 44 + 7 * kq) * 16384u + w * 4096, oC = WS_A2 + (unsigned)(i * 44 + 28 + 2 * kq) * 16384u + w * 4096, oF = fo + w * 8;
    asm volatile("" : "+s"(sws), "+s"(oG), "+s"(oC), "+s"(oF));
    auto uoff = [&](int v) __attribute__((always_inline)) { return v < 4 ? oG + v * 16384 : v < 8 ? oC + (8 * ((v >> 1) & 1) + (v & 1)) * 16384 : oG + (v - 4) * 16384; };
    LANE_VARS;
    unsigned fdone = F_DONE + w * 4, l16 = lane * 16;
    asm volatile("" : "+v"(fdone), "+v"(l16));
    unsigned tgs = 0; int raised = 0;
    auto raise = [&](int level) __attribute__((always_inline)) {
        asm volatile("s_mov_b64 exec, 1\n\tbuffer_store_dwordx2 %0, off, %1, %2 offset:%3 sc1\n\ts_mov_b64 exec, -1" ::"v"(v2u{tgs, ~tgs}), "s"(sws), "s"(oF), "n"((level - 1) * 128) : "memory");
        post(fdone + (F_OUT - F_DONE), level);
        raised = level;

        if (level == 1) STAMP(17); else STAMP(18);
    };
    auto try_raise = [&](int level) __attribute__((always_inline)) {
        unsigned v; asm volatile("s_getreg_b32 %0, hwreg(HW_REG_IB_STS, 0, 4)" : "=s"(v)::"memory");
        if (__builtin_expect((int)(v + raised) == level - 1, 0)) raise(level);
    };
    unsigned oX[3];
#pragma unroll
    for (int sl = 1; sl < 4; sl++) { oX[sl - 1] = xo + (kq ^ sl) * 36864u + (sl - 1) * 12288; asm volatile("" : "+s"(oX[sl - 1])); }
    unsigned cbs[2], tbs[4];
    v4u bf[PD];
    const unsigned ffl = F_FULL + (lane & 1) * 4;
    auto set2 = [&](auto kk) __attribute__((always_inline)) {
        constexpr int k = decltype(kk)::value;
        if constexpr (k == 0) tbs[0] = L_P + ((l16 << 1) & 0x780) + ((l16 >> 1) & 0x18) + (((l16 >> 2) & 0x40) | ((l16 >> 2) & 0x20));
        else if constexpr (k < 4) tbs[k] = tbs[0] ^ (unsigned)(k << 5);
        else if constexpr (k == 4) cbs[0] = L_W + ((l16 << 3) & 0x780) + (((l16 >> 4) ^ (l16 >> 1)) & 0x70);
        else cbs[1] = cbs[0] ^ 64u;
        if constexpr (k < 4) asm volatile("" : "+v"(tbs[k])); else asm volatile("" : "+v"(cbs[k - 4]));
    };
    auto prime2 = [&](auto mm) __attribute__((always_inline)) { constexpr int m = decltype(mm)::value; const v2u lo = ldstr(tbs[m]), hi = ldstr(tbs[m] + 2048); bf[m] = v4u{lo.x, lo.y, hi.x, hi.y}; };
    {
        STAMP_INIT();
        STAMP(0);
        unsigned cb[2];
        cb[0] = (unsigned)c * 128u + (unsigned)((g ^ (c >> 1)) << 4); cb[1] = cb[0] ^ 64u;
        f32x4 eX;
        unsigned tb[4];
        f32x4 nm4[2];
        {
            const unsigned voffX = (8 * (q8 >> 2) + 4 * (q8 & 1) + ((q8 >> 1) & 1)) * 1024 + ((p8 ^ (q8 >> 1)) << 4), voffX1 = voffX ^ 64u;
            const v4u srd = make_srd((const unsigned char*)p.x + (size_t)(128 * i + 32 * w) * 1024);
            auto issue_x = [&](int ln) __attribute__((always_inline)) {
                const unsigned s0 = ((ln + rot) & 7) * 128;
                dma4(srd, voffX, voffX1, s0, s0 + 15360, s0, s0 + 15360, sl_lo + (ln & 3) * 4096);
            };
            {
                const unsigned voffS = (8 * (q8 >> 2) + (q8 & 3)) * 1024 + ((p8 ^ (q8 >> 1)) << 4), so = 128 * (((w >> 1) + rot) & 7);
                dma4(make_srd((const unsigned char*)(lw ? p.Bim : p.Bre) + (size_t)(32 * j) * 1024), voffS, voffS ^ 64u, so, so + 15360, so + 2048, so + 17408, L_W + lw * 8192 + (w >> 1) * 4096);
            }
#pragma unroll
            for (int ln = 0; ln < XL; ln++) issue_x(ln);
            v4u ones = {0x3f803f80u, 0x3f803f80u, 0x3f803f80u, 0x3f803f80u};
            unsigned xl[2] = {cb[0] + sl_lo, cb[1] + sl_lo};
            cb[0] += L_W; cb[1] += L_W;
            asm volatile("" : "+v"(ones), "+v"(cb[0]), "+v"(cb[1]), "+v"(xl[0]), "+v"(xl[1]));
            f32x4 sm[2], sq[2], bsum;
            v4u bx[4];
            unsigned cbw[2] = {cb[0] + (w >> 1) * 8192 + (w & 1) * 2048, cb[1] + (w >> 1) * 8192 + (w & 1) * 2048};
            asm volatile("" : "+v"(cbw[0]), "+v"(cbw[1]));
            f32x4 acc[4][2];
            unsigned fl = 0; bool lack = false;
            const unsigned fcs = (lane & 4) ? F_COMM + (lane & 1) * 4 : F_BS + (lane & 3) * 4;
            static_assert(PD == 4 && XL <= 3, "prologue schedule");
            waitv<4 * (XL - 1)>();
#pragma unroll
            for (int f = 0; f < 4; f++) xa[f >> 1][f & 1] = lds128(xl[f >> 1] + (f & 1) * 2048);
            asm volatile("s_barrier" ::: "memory");
            STAMP(1);
            sfor<-PD, 64>(LAMB(nn) {
                constexpr int n = decltype(nn)::value, m = n + PD, ks = n >> 2, ln = n >> 3;
                v4u cur;
                if constexpr (n >= 0) { cur = bf[n % PD]; mma<(n < 4)>(acc[n & 3][0], xa[ks][0], cur); SB(); }
                if constexpr (m > 0 && (m & 15) == 0 && m < 64) need_full(lack, ffl, (m >> 4) + 1);
                if constexpr (m < 64) bf[m % PD] = lds128(cb[(m >> 2) & 1] + wslot(m >> 4) + (((m >> 1) & 1) * 4 + ((m >> 3) & 1) * 2 + (m & 1)) * 2048);
                SB();
                if constexpr (n >= 0) { mma<(n < 4)>(acc[n & 3][1], xa[ks][1], cur); SB(); }
                if constexpr (n >= 0 && (n & 7) < 4 && ln + XL < 8)
                    dma1(srd, (n & 1) ? voffX1 : voffX, ((ln + XL + rot) & 7) * 128 + (n & 1) * 16384 + (n & 2) * 1024, sl_lo + ((ln + XL) & 3) * 4096 + (n & 3) * 1024);
                if constexpr (n >= 0 && (n & 7) == 4 && ln + XL == 7) post(fdone + (F_XI - F_DONE), 1);
                if constexpr (n >= 0 && (n & 7) == 4 && ln < 7) waitv<4 * ((ln + XL < 7 ? ln + XL : 7) - ln - 1)>();
                if constexpr (n >= 0 && (n & 7) >= 4 && ln < 7) xa[2 * ln + 2 + ((n >> 1) & 1)][n & 1] = lds128(xl[(n >> 1) & 1] + ((ln + 1) & 3) * 4096 + (n & 1) * 2048);
                if constexpr (n == 56) post(fdone + (F_XI - F_DONE), 3);
                if constexpr ((m & 15) == 10 && m < 48) fl = peek(ffl);
                if constexpr ((m & 15) == 14 && m < 48) lack = any_lt(fl, (m >> 4) + 2);
                if constexpr ((m & 15) == 15 && m < 63) post(fdone, (m >> 4) + 1);
                if constexpr (m < 64 && (m & 15) < 4) bx[m & 3] = lds128(cbw[m & 1] + wslot(m >> 4) + ((m >> 1) & 1) * 4096);
                if constexpr (n == 55) { sts32f(L_CST + 7 * 256 + c * 16 + (w & 1) * 8 + (w >> 1) * 4, bsum.x); post(fdone + (F_BS - F_DONE), 1); }
                if constexpr (n == 60) fl = peek(fcs);
                SB();
                if constexpr (n >= 0 && (n & 15) < 4) mma<(n == 0)>(bsum, ones, bx[n & 3]);
                if constexpr (n >= 0) { if constexpr (n & 1) mma<(n < 4)>(sq[(n >> 1) & 1], xa[ks][(n >> 1) & 1], xa[ks][(n >> 1) & 1]); else mma<(n < 4)>(sm[(n >> 1) & 1], ones, xa[ks][(n >> 1) & 1]); }
                SB();
            });
            need_full(any_lt(fl, 1), fcs, 1);
            const unsigned cl = L_CST + (l16 & 0xf0);
            const f32x4 ca = lds128f(cl), c4 = lds128f(cl + 256), co = lds128f(cl + 512), bs = lds128f(cl + 7 * 256), w15 = lds128f(L_HIN + w * 1024 + l16);
            mfma_done();
            STAMP(2); VAL(24, stall); stall = 0;
            {
                float dg[2];
#pragma unroll
                for (int bb = 0; bb < 2; bb++) {
                    nm4[bb] = sm[bb] * (-1.0f / DM);
                    const float s01 = (lane & 1) ? sq[bb].y : sq[bb].x, s23 = (lane & 1) ? sq[bb].w : sq[bb].z;
                    dg[bb] = __builtin_amdgcn_rsqf(fmaxf(((lane & 2) ? s23 : s01) * (1.0f / DM) - nm4[bb].x * nm4[bb].x, 0.0f) + 1e-5f);
                }
                if (g == (c >> 2)) {
                    const unsigned sa = L_RSTD + (32 * w + 8 * g + (c & 3)) * 4;
                    sts32f(sa, dg[0]); sts32f(sa + 16, dg[1]); sts32f(sa + L_MEAN, nm4[0].x); sts32f(sa + L_MEAN + 16, nm4[1].x);
                }
            }
            fence_c();
            const unsigned ra = L_RSTD + (32 * w + 8 * g) * 4;
            const f32x4 nt0 = lds128f(ra + L_MEAN), nt1 = lds128f(ra + L_MEAN + 16), rs0 = lds128f(ra), rs1 = lds128f(ra + 16);
            {
                const int q4 = c >> 2, p4 = c & 3;
                const unsigned tb1 = L_P + (unsigned)(8 * g + q4) * 128u + (unsigned)((((q4 >> 1) & 1) | ((g & 1) << 1)) << 5) + p4 * 8;
#pragma unroll
                for (int t = 0; t < 4; t++) { tb[t] = tb1 ^ (unsigned)(t << 5); asm volatile("" : "+v"(tb[t])); }
            }
            SB();
            STAMP(3);
            f32x4 ev;
            f32x2 sr[2][4], si[2][4];
#pragma unroll
            for (int st = 0; st < 2; st++) {
                const f32x2 b2 = st ? hi2(bs) : lo2(bs);
#pragma unroll
                for (int t = 0; t < 4; t++) {
                    const f32x4 nt = (t & 2) ? nt1 : nt0;
                    const f32x2 n2 = (t & 1) ? hi2(nt) : lo2(nt);
                    sr[st][t] = bfma<0, 0>(b2, n2, (t & 1) ? hi2(acc[st][t >> 1]) : lo2(acc[st][t >> 1]));
                    si[st][t] = bfma<1, 0>(b2, n2, (t & 1) ? hi2(acc[2 + st][t >> 1]) : lo2(acc[2 + st][t >> 1]));
                }
            }
            SB();
#pragma unroll
            for (int st = 0; st < 2; st++)
#pragma unroll
                for (int t = 0; t < 4; t++) {
                    const f32x4 rs = (t & 2) ? rs1 : rs0;
                    const f32x2 r2 = (t & 1) ? hi2(rs) : lo2(rs);
                    sr[st][t] *= r2; si[st][t] *= r2;
                }
            SB();
            const f32x2 a1[2] = {lo2(ca), hi2(ca)}, cf[2] = {lo2(co), hi2(co)};
#pragma unroll
            for (int t = 1; t < 4; t++) {
                f32x2 tr[2], ti[2];
#pragma unroll
                for (int st = 0; st < 2; st++) { tr[st] = bfma<1, 1>(a1[st], si[st][t - 1], sr[st][t]); ti[st] = bfma<1, 0>(a1[st], sr[st][t - 1], si[st][t]); }
                SB();
#pragma unroll
                for (int st = 0; st < 2; st++) { sr[st][t] = bfma<0, 0>(a1[st], sr[st][t - 1], tr[st]); si[st][t] = bfma<0, 0>(a1[st], si[st][t - 1], ti[st]); }
                SB();
            }
#pragma unroll
            for (int st = 0; st < 2; st++) {
                ev[2 * st] = sfma(c4[2 * st], sr[st][3].x, sfnma(c4[2 * st + 1], si[st][3].x, sr[st][3].y));
                ev[2 * st + 1] = sfma(c4[2 * st], si[st][3].x, sfma(c4[2 * st + 1], sr[st][3].x, si[st][3].y));
                eX[2 * st] = sr[st][3].x; eX[2 * st + 1] = si[st][3].x;
            }
            sts128f(L_SEG + w * 1024 + l16, ev);
            {
                f32x4 ew;
#pragma unroll
                for (int st = 0; st < 2; st++) {
                    ew[2 * st] = sfms(w15[2 * st], ev[2 * st], smul(w15[2 * st + 1], ev[2 * st + 1]));
                    ew[2 * st + 1] = sfma(w15[2 * st], ev[2 * st + 1], smul(w15[2 * st + 1], ev[2 * st]));
                }
                sts128f(L_HIN + w * 1024 + l16, ew);
            }
            post(fdone, 4);
            SB();
            unsigned ya = L_O + w * 4096 + l16;
            asm volatile("" : "+v"(ya), "+v"(eX));
#pragma unroll
            for (int st = 0; st < 2; st++) {
                f32x2 tr[4], ti[4];
#pragma unroll
                for (int t = 0; t < 4; t++) { tr[t] = bmul<1, 1>(cf[st], si[st][t]); ti[t] = bmul<1, 1>(cf[st], sr[st][t]); }
                SB();
#pragma unroll
                for (int t = 0; t < 4; t++) { tr[t] = bfma<0, 0>(cf[st], sr[st][t], tr[t]); ti[t] = bfma<0, 1>(cf[st], si[st][t], ti[t]); }
                SB();
                sts128(ya + st * 1024, v4u{pk2(tr[0]), pk2(tr[1]), pk2(tr[2]), pk2(tr[3])});
                sts128(ya + 2048 + st * 1024, v4u{pk2(ti[0]), pk2(ti[1]), pk2(ti[2]), pk2(ti[3])});
                SB();
            }
        }
        STAMP(4);
        {
            const v4u sel0 = lds128(L_SEL + l16), sel1 = lds128(L_SEL + 1024 + l16);
            f32x4 ct[6];
            unsigned fl = 0;
            sfor<0, 68>(LAMB(kk) {
                constexpr int k = decltype(kk)::value, e = k - 4;
                if constexpr (k < 64) mma_c(ct[k % 6], (k & 1) ? sel1 : sel0, xa[k >> 2][(k >> 1) & 1], nm4[(k >> 1) & 1]);
                if constexpr (k >= 64 || k < 4) asm volatile("s_nop 1");
                if constexpr (e >= 0) {
                    xa[e >> 2][(e >> 1) & 1][2 * (e & 1)] = cvtv(ct[e % 6].x, ct[e % 6].y);
                    xa[e >> 2][(e >> 1) & 1][2 * (e & 1) + 1] = cvtv(ct[e % 6].z, ct[e % 6].w);
                }
                if constexpr (k == 40) fl = peek(ffl);
                if constexpr (k == 52) need_full(any_lt(fl, 5), ffl, 5);
                if constexpr (k >= 53 && k < 53 + PD) { constexpr int m = k - 53; const unsigned a = tb[(m >> 1) & 3] + (m & 1) * 8192; const v2u lo = ldstr(a), hi = ldstr(a + 512); bf[(64 + m) % PD] = v4u{lo.x, lo.y, hi.x, hi.y}; }
                SB();
            });
        }
        STAMP(5);
        unsigned go = oG + q * 16384, so = oC + (q >> 1) * 16384 + (q & 1) * 2048;
        asm volatile("" : "+s"(go), "+s"(so));
        f32x4 acc[4][2][2];
        f32x2 r12;
        unsigned fc = 0;
        auto gpass = [&](auto pp, auto n0, auto n1) __attribute__((always_inline)) {
            constexpr int pass = decltype(pp)::value;
            unsigned fl = 0, dn = 0; bool lack = false;
            sfor<decltype(n0)::value, decltype(n1)::value>(LAMB(nn) {
                constexpr int n = decltype(nn)::value, m = n + PD;
                constexpr bool pre = pass == 0 || m < 128;
                constexpr unsigned off = wslot(m >> 4) + (m & 1) * 8192 + ((m >> 3) & 1) * 4096;
                const v4u cur = bf[(n + 64 + 128 * pass) % PD];
                mma<(n < 8)>(acc[(n >> 1) & 3][n & 1][0], cur, xa[n >> 3][0]);
                SB();
                if constexpr ((m & 15) == 0) post(fdone, dn);
                if constexpr ((m & 15) == 0) need_full(lack, ffl, 5 + 8 * pass + (m >> 4));
                v2u lo, hi;
                if constexpr (pre) lo = ldstr(tb[(m >> 1) & 3] + off); else lo = ldstr(tbs[m & 3]);
                if constexpr (pass == 1 && n >= 69 && n < 81 && (n & 1)) set2(IC<(n - 69) / 2>{});
                if constexpr ((m & 15) == 10) fl = peek(ffl);
                if constexpr ((m & 15) == 15) lack = any_lt(fl, m == 127 && pass == 0 ? N1 - 7 : 6 + 8 * pass + (m >> 4));
                if constexpr ((m & 15) == 13) { dn = 5 + 8 * pass + (m >> 4); asm volatile("" : "+v"(dn)); }
                if constexpr (n == 88 && pass == 1) fc = peek(F_RDY);
                if constexpr (n == 94 && pass == 1) { if (__builtin_expect(any_lt(fc, 1), 0)) { wait_ge(F_RDY, 1); } fence_c(); }
                if constexpr (n >= 96 && pass == 1 && (n & 1) == 0) { constexpr int f = (n - 96) >> 1; reldA<(f & 3) * 1024>(AF(f >> 2, f & 3), l16, sws, uoff(f >> 2)); }
                if constexpr (pass == 1 && (n == 33 || n == 41 || n == 49 || n == 57)) try_raise(1);
                if constexpr (pass == 1 && n == 67) { if (raised < 1) { waitv<0>(); raise(1); } }
                if constexpr (n == 120 && pass == 0) fc = peek(ffl + (F_COMM - F_FULL));
                if constexpr (n == 121 && pass == 0) { const unsigned ra = L_RSTD + w * 128 + (((l16 >> 1) & 0x60) | ((l16 >> 2) & 0xc)); r12 = f32x2{lds32f(ra), lds32f(ra + 16)}; }
                SB();
                mma<(n < 8)>(acc[(n >> 1) & 3][n & 1][1], cur, xa[n >> 3][1]);
                SB();
                if constexpr (pre) hi = ldstr(tb[(m >> 1) & 3] + off + 512); else hi = ldstr(tbs[m & 3] + 2048);
                bf[n % PD] = v4u{lo.x, lo.y, hi.x, hi.y};
                if constexpr ((m & 57) == 57 && (pass == 0 || m < 64)) {
                    tb[(m >> 1) & 3] += (m & 64) ? L_P - L_W : L_W - L_P; asm volatile("" : "+v"(tb[(m >> 1) & 3]));
                }
                SB();
            });
        };
        auto gated = [&](auto ff) __attribute__((always_inline)) {
            constexpr int uu = decltype(ff)::value >> 1, bb = decltype(ff)::value & 1;
            const f32x4 h0 = acc[2 * uu][0][bb] * acc[2 * uu][1][bb], h1 = acc[2 * uu + 1][0][bb] * acc[2 * uu + 1][1][bb];
            return v4u{pk2(bmul<bb, 0>(r12, lo2(h0))), pk2(bmul<bb, 0>(r12, hi2(h0))), pk2(bmul<bb, 0>(r12, lo2(h1))), pk2(bmul<bb, 0>(r12, hi2(h1)))};
        };
        gpass(IC<0>{}, IC<0>{}, IC<128>{});
        {
            r12 = r12 * r12;
            mfma_done();
            STAMP(6); VAL(25, stall); stall = 0;
            sfor<0, 2>(LAMB(ff) { stA<decltype(ff)::value * 1024>(gated(ff), l16, sws, go); });
            SB();
            need_full(any_lt(fc, 4), ffl + (F_COMM - F_FULL), 4);
            const unsigned tg = peek(L_TAG);
            const unsigned cl = L_CST + (l16 & 0xf0);
            unsigned ya = L_O + w * 4096 + l16;
            asm volatile("" : "+v"(ya));
            const f32x4 h0 = lds128f(L_XCH + 256 + (l16 & 0xf0)), h1 = lds128f(L_XCH + 512 + (l16 & 0xf0));
            const f32x4 ap = lds128f(L_SEG + w * 1024 + l16), pf = lds128f(L_HIN + w * 1024 + l16);
            const v4u sel[2] = {lds128(L_SEL + l16), lds128(L_SEL + 1024 + l16)};
            const v4u yr[2] = {lds128(ya), lds128(ya + 1024)}, yi[2] = {lds128(ya + 2048), lds128(ya + 3072)};
            const f32x4 c4 = lds128f(cl + 256);
            f32x4 cw[4];
            SB();
            sfor<2, 4>(LAMB(ff) { stA<decltype(ff)::value * 1024>(gated(ff), l16, sws, go); });
            tgs = __builtin_amdgcn_readfirstlane(tg);
#pragma unroll
            for (int t = 0; t < 4; t++) cw[t] = lds128f(cl + (3 + t) * 256);
            SB();
            f32x4 zr[2][2], zi[2][2];
#pragma unroll
            for (int st = 0; st < 2; st++)
#pragma unroll
                for (int bb = 0; bb < 2; bb++) { mma<true>(zr[st][bb], yr[st], sel[bb]); mma<true>(zi[st][bb], yi[st], sel[bb]); }
            SB();
            f32x2 hr[2], hi[2];
            {
                const f32x4 cy = h0 + h1;
                f32x4 hin;
                float t0[2], t1[2];
#pragma unroll
                for (int st = 0; st < 2; st++) { t0[st] = sfnma(ap[2 * st + 1], cy[2 * st + 1], pf[2 * st]); t1[st] = sfma(ap[2 * st + 1], cy[2 * st], pf[2 * st + 1]); }
                SB();
#pragma unroll
                for (int st = 0; st < 2; st++) { hin[2 * st] = sfma(ap[2 * st], cy[2 * st], t0[st]); hin[2 * st + 1] = sfma(ap[2 * st], cy[2 * st + 1], t1[st]); }
                SB();
#pragma unroll
                for (int st = 0; st < 2; st++) { t0[st] = sfnma(c4[2 * st + 1], hin[2 * st + 1], eX[2 * st]); t1[st] = sfma(c4[2 * st + 1], hin[2 * st], eX[2 * st + 1]); }
                SB();
#pragma unroll
                for (int st = 0; st < 2; st++) { hr[st] = f32x2{hin[2 * st], sfma(c4[2 * st], hin[2 * st], t0[st])}; hi[st] = f32x2{hin[2 * st + 1], sfma(c4[2 * st], hin[2 * st + 1], t1[st])}; }
                SB();
            }
#pragma unroll
            for (int st = 0; st < 2; st++) {
                f32x2 tr[4], ti[4];
#pragma unroll
                for (int t = 0; t < 4; t++) { const f32x2 wv = st ? hi2(cw[t]) : lo2(cw[t]); tr[t] = bmul<1, 1>(wv, hi[st]); ti[t] = bmul<1, 1>(wv, hr[st]); }
                SB();
#pragma unroll
                for (int t = 0; t < 4; t++) { const f32x2 wv = st ? hi2(cw[t]) : lo2(cw[t]); tr[t] = bfma<0, 0>(wv, hr[st], tr[t]); ti[t] = bfma<0, 1>(wv, hi[st], ti[t]); }
                SB();
                v4u fr = {pk2(tr[0]), pk2(tr[1]), pk2(tr[2]), pk2(tr[3])}, fi = {pk2(ti[0]), pk2(ti[1]), pk2(ti[2]), pk2(ti[3])};
                asm volatile("s_nop 1" : "+v"(fr), "+v"(fi));
#pragma unroll
                for (int bb = 0; bb < 2; bb++) { mma<false>(zr[st][bb], fr, sel[bb]); mma<false>(zi[st][bb], fi, sel[bb]); }
                SB();
            }
            mfma_done();
            stA<0>(pk8(zr[0][0], zr[1][0]), l16, sws, so); stA<0>(pk8(zi[0][0], zi[1][0]), l16, sws, so + 8 * 16384);
            stA<1024>(pk8(zr[0][1], zr[1][1]), l16, sws, so); stA<1024>(pk8(zi[0][1], zi[1][1]), l16, sws, so + 8 * 16384);
            SB();
            STAMP(7);
        }
        if (heavy) {
            gpass(IC<1>{}, IC<0>{}, IC<128>{});
            mfma_done();
            STAMP(8); VAL(26, stall); stall = 0;
            sfor<0, 4>(LAMB(ff) { stA<decltype(ff)::value * 1024>(gated(ff), l16, sws, go + 4 * 16384); });
            SB();
        }
        int q2 = q;
        asm volatile("" : "+s"(q2));
        if (q2 == 3) {
            waitv<0>(); raise(1); raise(2);
            sfor<0, 6>(set2);
            wait_ge(ffl, N1 + 1);
            sfor<0, PD>(prime2);
            wait_ge(F_RDY, 1);
            sfor<0, 16>(LAMB(ff) { constexpr int f = decltype(ff)::value; reldA<(f & 3) * 1024>(AF(f >> 2, f & 3), l16, sws, uoff(f >> 2)); });
#pragma unroll
            for (int u = 0; u < 4; u++) asm volatile("s_waitcnt vmcnt(0)" : "+v"(AF(u, 0)), "+v"(AF(u, 1)), "+v"(AF(u, 2)), "+v"(AF(u, 3))::"memory");
        }
    }
    STAMP(9);
    {
        f32x4 acc[8][2];
        v4u pk[3];
        unsigned fl = 0, fr = 0, dn = 0; bool lack = false;
        float tagf = 0;
        constexpr auto fv = [](int n) { return n < 48 ? n >> 4 : n < 108 ? 3 + (n - 48) / 12 : n < 144 ? 8 + ((n - 108) % 12) / 4 : 3 + (n - 144) / 4; };
        constexpr auto fk = [](int n) { return n < 48 ? (n >> 3) & 1 : n < 108 ? ((n - 48) % 12) / 6 : (n >> 1) & 1; };
        constexpr auto fd = [](int n) { return n < 48 ? n & 7 : n < 108 ? 2 + (n - 48) % 6 : n < 144 ? 2 * ((n - 108) / 12 + 1) + (n & 1) : n & 1; };
        constexpr auto first = [](int n) { return n >= 0 && (n < 48 ? (n & 15) == 0 : n < 108 ? (n - 48) % 12 == 0 : n < 120 && (n & 3) == 0); };
        constexpr int LG = LGATE;
        sfor<0, 176>(LAMB(nn) {
            constexpr int n = decltype(nn)::value, m = n + PD;
            constexpr int v = fv(n), ks = fk(n), dt = fd(n);
            constexpr int mv = fv(m), mk = fk(m), md = fd(m);
            constexpr bool mc = (mv & ~3) == 4;
            constexpr unsigned moff = (mv & 3) * SLOT + (mc ? md * 2048 : (md >> 2) * 8192 + mk * 4096);
            constexpr auto chk = [](int q) { return q == 16 ? 2 : q == 32 ? 4 : q == 60 ? 8 : q == 108 ? 11 : 0; };
            v4u cur; v2u lo, hi;
            if constexpr (n >= 0) {
                if constexpr (first(n)) {
                    constexpr int mid = n <= 32 ? 0 : (n - 31) / 2 < 16 ? (n - 31) / 2 : 16, late = n <= LG ? 0 : n - LG < 12 ? n - LG : 12;
                    constexpr int NW = v == 0 ? 16 : v == 1 ? 12 : v < 4 ? 63 : v < 8 ? mid - 4 * (v - 3) + late : late - 4 * (v - 7);
                    static_assert(NW >= 0, "load schedule");
                    asm volatile("s_waitcnt vmcnt(%4)" : "+v"(AF(v & 7, 0)), "+v"(AF(v & 7, 1)), "+v"(AF(v & 7, 2)), "+v"(AF(v & 7, 3)) : "n"(NW) : "memory");
                }
                cur = bf[n % PD];
                mma<(n < 8)>(acc[dt][0], cur, AF(v & 7, 2 * ks));
                SB();
            }
            if constexpr (m > 0 && (m & 15) == 0 && m <= 48) post(fdone, dn);
            if constexpr (chk(m) > 0) need_full(lack, ffl, N1 + chk(m));
            if constexpr (m < 176) { if constexpr (mc) bf[m % PD] = lds128(cbs[mk] + moff); else lo = ldstr(tbs[md & 3] + moff); }
            if constexpr (chk(m + 6) > 0) fl = peek(ffl);
            if constexpr (chk(m + 1) > 0) lack = any_lt(fl, N1 + chk(m + 1));
            if constexpr ((m & 15) == 13 && m < 48) { dn = N1 + mv + 1; asm volatile("" : "+v"(dn)); }
            if constexpr (n == 19 || n == 25) try_raise(2);
            if constexpr (n == 31) { if (raised < 2) { waitv<0>(); raise(2); } }
            if constexpr (n >= 32 && n < 64 && (n & 1) == 0) { constexpr int f = (n - 32) >> 1; reldA<(f & 3) * 1024>(AF(4 + (f >> 2), f & 3), l16, sws, uoff(4 + (f >> 2))); }
            if constexpr (n == LG - 6) fr = peek(F_RDY + 4);
            if constexpr (n == LG - 1) need_full(any_lt(fr, 1), F_RDY + 4, 1);
            if constexpr (n >= LG && n < LG + 12) { constexpr int f = n - LG; reldA<(f & 3) * 1024>(AF(f >> 2, f & 3), l16, sws, uoff(8 + (f >> 2))); }
            if constexpr (n == 70) { tagf = __uint_as_float(((tgs & 0x3fffu) + 0x2000u) << 16); asm volatile("" : "+v"(tagf)); }
            SB();
            if constexpr (n >= 0) { mma<(n < 8)>(acc[dt][1], cur, AF(v & 7, 2 * ks + 1)); SB(); }
            if constexpr (m < 176 && !mc) { hi = ldstr(tbs[md & 3] + moff + 2048); bf[m % PD] = v4u{lo.x, lo.y, hi.x, hi.y}; }
            if constexpr (n >= 120 && n < 156) {
                constexpr int sl = (n - 108) / 12, r = (n - 108) % 12 - 2;
                auto val = [&](int k) { return acc[2 * sl + (k >> 3)][(k >> 2) & 1][k & 3]; };
                if constexpr (r >= 0 && r < 9 && r % 3 < 2) {
                    constexpr int gi = 2 * (r / 3) + r % 3;
                    pk[gi >> 1][2 * (gi & 1)] = cvtv(val(3 * gi), val(gi < 5 ? 3 * gi + 1 : 15));
                    pk[gi >> 1][2 * (gi & 1) + 1] = cvtv(val(gi < 5 ? 3 * gi + 2 : 15), tagf);
                }
                if constexpr (r >= 0 && r % 3 == 2)
                    stX<(r / 3) * 1024>(pk[r / 3], l16, sws, oX[sl - 1]);
                if constexpr (r == 9 && sl == 3) post(fdone + (F_SENT - F_DONE), 3);
            }
            if constexpr (n == 107) { STAMP(10); VAL(28, stall); stall = 0; }
            SB();
        });
        mfma_done();
        STAMP(11); VAL(29, stall);
#pragma unroll
        for (int e = 0; e < 4; e++) sts128f(L_O + w * 4096 + e * 1024 + l16, acc[e >> 1][e & 1]);
        post(fdone + (F_SENT - F_DONE), 4);
    }
    STAMP(14); STAMP_DUMP();
}

extern "C" void kernel_launch(void* const* d_in, const int* in_sizes, int n_in, void* d_out, int out_size, void* d_ws, size_t ws_size,
                              hipStream_t stream) {
    Params p;
    p.x = (const u16*)d_in[0]; p.lre = (const u16*)d_in[1]; p.lim = (const u16*)d_in[2];
    p.Bre = (const u16*)d_in[3]; p.Bim = (const u16*)d_in[4]; p.Cre = (const u16*)d_in[5]; p.Cim = (const u16*)d_in[6];
    p.ldt = (const u16*)d_in[7]; p.Dsk = (const u16*)d_in[8];
    p.wg = (const u16*)d_in[10]; p.wu = (const u16*)d_in[11]; p.wd = (const u16*)d_in[12];
    p.out = (float*)d_out; p.ws = (unsigned char*)d_ws;
    (void)hipFuncSetAttribute((const void*)fused, hipFuncAttributeMaxDynamicSharedMemorySize, L_TOTAL);
    fused<<<256, 512, L_TOTAL, stream>>>(p);
}
```

```cpp
#define WS_SHIFT ((ws_size - WS_END) & ~(size_t)0x1fffff)
#include <hip/hip_runtime.h>
#include <stdint.h>

typedef unsigned short u16;
typedef unsigned v4u __attribute__((ext_vector_type(4)));
typedef unsigned v2u __attribute__((ext_vector_type(2)));
typedef short s16x4 __attribute__((ext_vector_type(4)));
typedef __bf16 bf16x2 __attribute__((ext_vector_type(2)));
typedef __bf16 bf16x8 __attribute__((ext_vector_type(8)));
typedef float f32x2 __attribute__((ext_vector_type(2)));
typedef float f32x4 __attribute__((ext_vector_type(4)));
#define GAS __attribute__((address_space(1)))
#define LAS __attribute__((address_space(3)))
#define DEVI __device__ __forceinline__
#define NOUNROLL _Pragma("nounroll")

#define SPIN_SLEEP 0
constexpr int DM = 512, FF = 1792;
constexpr unsigned WS_A2 = 0;
constexpr unsigned WS_XB = 16u * 44u * 16384u;
constexpr unsigned WS_GRAN = WS_XB + 256u * 36864u;
constexpr unsigned WS_FLAG = WS_GRAN + 16u * 16u * 512u;
constexpr unsigned WS_EPOCH = WS_FLAG + 1024u * 128u;
constexpr unsigned WS_END = WS_EPOCH + 256u;
constexpr unsigned SLOT = 16384;
constexpr unsigned L_RSTD = 0, L_MEAN = 512;
constexpr unsigned F_DONE = 1024, F_SENT = F_DONE + 16, F_FULL = F_DONE + 32, F_COMM = F_DONE + 40, F_RDY = F_DONE + 48, F_OUT = F_DONE + 64, F_XI = F_DONE + 80, F_BS = F_DONE + 96, L_TAG = F_DONE + 128;
constexpr unsigned L_CST = 1280;
constexpr unsigned L_XCH = L_CST + 8 * 256;
constexpr unsigned L_SEG = 4096, L_PW = 8192, L_TOT = L_PW + 2048, L_PRE = L_TOT + 1024, L_SEL = 12288;
constexpr unsigned L_P = SLOT, L_O = 5 * SLOT, L_W = 6 * SLOT, L_TOTAL = 10 * SLOT;
constexpr int SPIN_MAX = 1 << 18;
constexpr int LSPIN_MAX = 1 << 22;

struct Params {
    const u16 *x, *lre, *lim, *Bre, *Bim, *Cre, *Cim, *ldt, *Dsk, *wg, *wu, *wd;
    float* out;
    unsigned char* ws;
};

DEVI float bf2f(u16 v) { return __uint_as_float(((unsigned)v) << 16); }
DEVI float bflo(unsigned v) { return __uint_as_float(v << 16); }
DEVI float bfhi(unsigned v) { return __uint_as_float(v & 0xffff0000u); }
DEVI unsigned pk2(float a, float b) { f32x2 p = {a, b}; return __builtin_bit_cast(unsigned, __builtin_convertvector(p, bf16x2)); }
DEVI unsigned pk2(f32x2 p) { return __builtin_bit_cast(unsigned, __builtin_convertvector(p, bf16x2)); }
DEVI v4u pk8(f32x4 a, f32x4 b) { return v4u{pk2(a.x, a.y), pk2(a.z, a.w), pk2(b.x, b.y), pk2(b.z, b.w)}; }
DEVI float dot2(unsigned a, unsigned b, float c) { return __builtin_amdgcn_fdot2_f32_bf16(__builtin_bit_cast(bf16x2, a), __builtin_bit_cast(bf16x2, b), c, false); }
template <int CTRL> DEVI float dppf(float v) { return __builtin_bit_cast(float, __builtin_amdgcn_update_dpp(0, __builtin_bit_cast(int, v), CTRL, 0xf, 0xf, true)); }
DEVI f32x4 mfma(v4u a, v4u b, f32x4 c) { return __builtin_amdgcn_mfma_f32_16x16x32_bf16(__builtin_bit_cast(bf16x8, a), __builtin_bit_cast(bf16x8, b), c, 0, 0, 0); }
template <bool FIRST> DEVI void mma(f32x4& c, const v4u& a, const v4u& b) {
    if constexpr (FIRST) asm volatile("v_mfma_f32_16x16x32_bf16 %0, %1, %2, 0" : "=&v"(c) : "v"(a), "v"(b));
    else asm volatile("v_mfma_f32_16x16x32_bf16 %0, %1, %2, %0" : "+v"(c) : "v"(a), "v"(b));
}
DEVI void mfma_done() { __builtin_amdgcn_sched_barrier(0); asm volatile("s_nop 7"); __builtin_amdgcn_sched_barrier(0); }
DEVI v4u lds128(unsigned a) { return *(LAS v4u*)(uintptr_t)a; }
DEVI f32x4 lds128f(unsigned a) { return *(LAS f32x4*)(uintptr_t)a; }
DEVI float lds32f(unsigned a) { return *(LAS float*)(uintptr_t)a; }
DEVI void sts32f(unsigned a, float v) { *(LAS float*)(uintptr_t)a = v; }
DEVI void sts128(unsigned a, v4u v) { *(LAS v4u*)(uintptr_t)a = v; }
DEVI void sts128f(unsigned a, f32x4 v) { *(LAS f32x4*)(uintptr_t)a = v; }
DEVI v2u ldstr(unsigned a) { return __builtin_bit_cast(v2u, __builtin_amdgcn_ds_read_tr16_b64_v4i16((LAS s16x4*)(uintptr_t)a)); }
DEVI void fence_c() { asm volatile("" ::: "memory"); }
DEVI void bar_l() { asm volatile("s_waitcnt lgkmcnt(0)\n\ts_barrier" ::: "memory"); }
template <int N> DEVI void waitv() { asm volatile("s_waitcnt vmcnt(%0)" ::"n"(N) : "memory"); }
DEVI void dma16(const void* sbase, unsigned voff, unsigned ldst) {
    asm volatile("s_mov_b32 m0, %2\n\ts_nop 0\n\tglobal_load_lds_dwordx4 %0, %1" ::"v"(voff), "s"(sbase), "s"(ldst) : "memory");
}
DEVI void post(unsigned a, unsigned v) { fence_c(); *(LAS unsigned*)(uintptr_t)a = v; fence_c(); }
DEVI unsigned peek(unsigned a) { fence_c(); return *(LAS unsigned*)(uintptr_t)a; }
DEVI bool any_lt(unsigned v, unsigned need) { return __builtin_amdgcn_ballot_w64(v < need) != 0; }
DEVI void wait_ge(unsigned a, unsigned need) {
    int s = LSPIN_MAX;
    NOUNROLL while (any_lt(peek(a), need)) { asm volatile("s_sub_i32 %0, %0, 1" : "+s"(s) :: "scc"); if (s < 0) break; if (SPIN_SLEEP) __builtin_amdgcn_s_sleep(SPIN_SLEEP); }
    fence_c();
}
template <int V> DEVI void postm(unsigned a, int lane) {
    asm volatile("s_mov_b64 exec, %2\n\tds_max_u32 %0, %1\n\ts_mov_b64 exec, -1" ::"v"(a), "v"(lane), "n"(1 << V) : "memory");
}
DEVI unsigned vm_outstanding() {
    unsigned v; asm volatile("s_getreg_b32 %0, hwreg(HW_REG_IB_STS)" : "=s"(v)::"memory");
    return (v & 15u) | ((v >> 18) & 0x30u);
}
DEVI void postv(unsigned a, int lane, unsigned v) {
    asm volatile("s_lshl_b64 exec, 1, %2\n\tds_max_u32 %0, %1\n\ts_mov_b64 exec, -1" ::"v"(a), "v"(lane), "s"(v) : "memory", "scc");
}
template <int V> DEVI void postc(unsigned a, int lane) {
    asm volatile("s_mov_b64 exec, %2\n\tds_write_b32 %0, %1\n\ts_mov_b64 exec, -1" ::"v"(a), "v"(lane), "n"(1 << V) : "memory");
}
DEVI void need_full(bool lack, unsigned a, unsigned need) { if (__builtin_expect(lack, 0)) wait_ge(a, need); fence_c(); }
#define VAL(id, v)
DEVI float sfma(float a, float b, float c) { float d; asm("v_fma_f32 %0, %1, %2, %3" : "=v"(d) : "v"(a), "v"(b), "v"(c)); return d; }
DEVI float sfms(float a, float b, float c) { float d; asm("v_fma_f32 %0, %1, %2, -%3" : "=v"(d) : "v"(a), "v"(b), "v"(c)); return d; }
DEVI float sfnma(float a, float b, float c) { float d; asm("v_fma_f32 %0, -%1, %2, %3" : "=v"(d) : "v"(a), "v"(b), "v"(c)); return d; }
DEVI float sadd(float a, float b) { float d; asm("v_add_f32 %0, %1, %2" : "=v"(d) : "v"(a), "v"(b)); return d; }
DEVI float smul(float a, float b) { float d; asm("v_mul_f32 %0, %1, %2" : "=v"(d) : "v"(a), "v"(b)); return d; }
DEVI float smul_t(float a, float b) { float d; asm("s_nop 0\n\tv_mul_f32 %0, %1, %2" : "=v"(d) : "v"(a), "v"(b)); return d; }
struct cpx { float x, y; };
DEVI cpx cmul(cpx a, cpx b) { return cpx{sfms(a.x, b.x, smul(a.y, b.y)), sfma(a.x, b.y, smul(a.y, b.x))}; }
DEVI float lmad(cpx a, float h, float u) {
    float d;
    asm("v_fma_f32 %0, %1, %3, %4\n\ts_nop 0\n\tv_fmac_f32_dpp %0, %3, %2 quad_perm:[1,0,3,2] row_mask:0xf bank_mask:0xf" : "=&v"(d) : "v"(a.x), "v"(a.y), "v"(h), "v"(u));
    return d;
}
DEVI f32x2 dup(float v) { f32x2 d = {v, v}; asm("" : "+v"(d)); return d; }
DEVI f32x2 pfma(f32x2 a, f32x2 b, f32x2 c) { return __builtin_elementwise_fma(a, b, c); }
DEVI f32x2 lo2(f32x4 v) { return f32x2{v.x, v.y}; }
DEVI f32x2 hi2(f32x4 v) { return f32x2{v.z, v.w}; }
#define SB() __builtin_amdgcn_sched_barrier(0)
template <int V> struct IC { static constexpr int value = V; };
template <int B, int E, class F> DEVI void sfor(F&& f) { if constexpr (B < E) { f(IC<B>{}); sfor<B + 1, E>(f); } }
#define LAMB(nn) [&](auto nn) __attribute__((always_inline))
#define STAMP(id)
#define STAMP_INIT()
#define STAMP_DUMP()
DEVI int fresh_lane() { int l; asm volatile("v_mbcnt_lo_u32_b32 %0, -1, 0\n\tv_mbcnt_hi_u32_b32 %0, -1, %0" : "=v"(l)); return l; }
DEVI v4u make_sel(int lane, int bb) {
    const int c = lane & 15, tk = 8 * (c >> 2) + 4 * bb + (c & 3) - 8 * (lane >> 4);
    v4u s;
#pragma unroll
    for (int e = 0; e < 4; e++) s[e] = (tk == 2 * e ? 0x3f80u : 0u) | (tk == 2 * e + 1 ? 0x3f800000u : 0u);
    return s;
}
#define LANE_VARS const int lane = fresh_lane(), c = lane & 15, g = lane >> 4, q8 = lane >> 3, p8 = lane & 7; (void)c; (void)g; (void)q8; (void)p8
constexpr unsigned wslot(int n) { return (unsigned)(n & 3) * SLOT; }

#define ROT1 (idx & 7)
#define ROT2 0
#define XI_STEP 8
#define TOUCH_GATE 1
#define LB_SLEEP 8
#define LB_FIRST 10
#define FL_FIRST 20
#define FL_SLEEP 6
#define MIDV 64
#define X_FIRST 10
#define X_SLEEP 1
#define PDV 4
#define INFL 8
#define TSL 6
#define TW __builtin_amdgcn_s_sleep(TSL);
#define PACE 14
#define PACE2 8
#define XLV 3
#define LGATE 84
#define TGN 36
#define EGATE 100
#define SLEEP0 12
constexpr unsigned CODE_LINES = 288;
template <int OFF> DEVI void reldA(v4u& d, unsigned voff, v4u srd, unsigned so) {
    asm volatile("buffer_load_dwordx4 %0, %1, %2, %3 offen offset:%4 sc1" : "+v"(d) : "v"(voff), "s"(srd), "s"(so), "n"(OFF) : "memory");
}
template <int OFF> DEVI void stA(const v4u& d, unsigned voff, v4u srd, unsigned so) {
    asm volatile("buffer_store_dwordx4 %0, %1, %2, %3 offen offset:%4 sc1\n\ts_nop 1" ::"v"(d), "v"(voff), "s"(srd), "s"(so), "n"(OFF) : "memory");
}
template <int OFF> DEVI void stX(const v4u& d, unsigned voff, v4u srd, unsigned so) {
    asm volatile("buffer_store_dwordx4 %0, %1, %2, %3 offen offset:%4 sc1" ::"v"(d), "v"(voff), "s"(srd), "s"(so), "n"(OFF) : "memory");
}
DEVI void touch(unsigned& sink, const void* tp, unsigned to) { asm volatile("global_load_dword %0, %1, %2" : "+v"(sink) : "v"(to), "s"(tp) : "memory"); }

DEVI v4u make_srd(const void* base) { const unsigned long long a = (unsigned long long)base; return v4u{(unsigned)a, (unsigned)(a >> 32) & 0xffffu, 0x7fffffffu, 0x00020000u}; }
DEVI void dma4(v4u srd, unsigned ve, unsigned vo, unsigned s0, unsigned s1, unsigned s2, unsigned s3, unsigned ldst) {
    asm volatile("s_mov_b32 m0, %7\n\ts_nop 0\n\tbuffer_load_dwordx4 %0, %2, %3 offen lds\n\tbuffer_load_dwordx4 %1, %2, %4 offen offset:1024 lds\n\t"
                 "buffer_load_dwordx4 %0, %2, %5 offen offset:2048 lds\n\tbuffer_load_dwordx4 %1, %2, %6 offen offset:3072 lds"
                 ::"v"(ve), "v"(vo), "s"(srd), "s"(s0), "s"(s1), "s"(s2), "s"(s3), "s"(ldst) : "memory");
}
DEVI void dma1(v4u srd, unsigned v, unsigned so, unsigned ldst) {
    asm volatile("s_mov_b32 m0, %3\n\ts_nop 0\n\tbuffer_load_dwordx4 %0, %1, %2 offen lds" ::"v"(v), "s"(srd), "s"(so), "s"(ldst) : "memory");
}
template <int H, int NEG> DEVI f32x2 bmul(f32x2 a, f32x2 b) {
    f32x2 d; asm("v_pk_mul_f32 %0, %1, %2 op_sel:[%3,0] op_sel_hi:[%3,1] neg_lo:[%4,0] neg_hi:[%4,0]" : "=v"(d) : "v"(a), "v"(b), "n"(H), "n"(NEG)); return d;
}
template <int H, int NEG> DEVI f32x2 bfma(f32x2 a, f32x2 b, f32x2 c) {
    f32x2 d; asm("v_pk_fma_f32 %0, %1, %2, %3 op_sel:[%4,0,0] op_sel_hi:[%4,1,1] neg_lo:[%5,0,0] neg_hi:[%5,0,0]" : "=v"(d) : "v"(a), "v"(b), "v"(c), "n"(H), "n"(NEG)); return d;
}
DEVI f32x2 cmadp(f32x2 a, f32x2 h, f32x2 u) {
    f32x2 t, d;
    asm("v_pk_fma_f32 %0, %1, %2, %3 op_sel:[0,0,0] op_sel_hi:[0,1,1]" : "=v"(t) : "v"(h), "v"(a), "v"(u));
    asm("v_pk_fma_f32 %0, %1, %2, %3 op_sel:[1,0,0] op_sel_hi:[0,0,1] neg_lo:[1,0,0]" : "=v"(d) : "v"(a), "v"(f32x2{h.y, h.y}), "v"(t));
    return d;
}
DEVI void mma_c(f32x4& d, const v4u& a, const v4u& b, const f32x4& c) { asm volatile("v_mfma_f32_16x16x32_bf16 %0, %1, %2, %3" : "=&v"(d) : "v"(a), "v"(b), "v"(c)); }
DEVI unsigned cvtv(float a, float b) { unsigned d; asm volatile("v_cvt_pk_bf16_f32 %0, %1, %2" : "=v"(d) : "v"(a), "v"(b)); return d; }

__global__ __launch_bounds__(512, 2) void fused(Params p) {
    const unsigned char* pc0;
    asm volatile("s_getpc_b64 %0" : "=s"(pc0));
    const int w = __builtin_amdgcn_readfirstlane(threadIdx.x >> 6);
    const int b = blockIdx.x, xcd = b & 7, idx = b >> 3;
    const int i = 8 * (xcd & 1) + (idx & 7), kq = xcd >> 1, q = idx >> 3, j = 4 * kq + q;
    const bool heavy = q != 3;
    const int N1 = heavy ? 20 : 12;
    const int lw = w & 1, pr = w & 3;
    const int rot = ROT1;
    const __amdgpu_buffer_rsrc_t rws = __builtin_amdgcn_make_buffer_rsrc(p.ws, 0, WS_END, 0x00020000);
    auto unit_of = [&](int v) { return v < 4 ? 7 * kq + v : v < 8 ? 28 + 8 * ((v >> 1) & 1) + 2 * kq + (v & 1) : 7 * kq + v - 4; };
    const unsigned sl_lo = L_P + (unsigned)pr * SLOT;
    const unsigned xo = WS_XB + (unsigned)((i * 4 + q) * 4) * 36864u + pr * 3072;
    const unsigned fo = WS_FLAG + (unsigned)(i * 16 + j) * 512u;

    if (w >= 4) {
        LANE_VARS;
        STAMP_INIT();
        STAMP(0);
        const unsigned swz = (unsigned)((p8 ^ (q8 >> 1)) << 4);
        const unsigned fa = F_DONE + (lane & 3) * 4;
        unsigned sink = 0, tg;
        auto poll16 = [&](int which) __attribute__((always_inline)) {
            wait_ge(F_OUT + (lane & 3) * 4, which + 1);
            STAMP(16);
            const unsigned pf = WS_FLAG + (unsigned)(i * 16 + 4 * kq + (lane & 3)) * 512u + which * 128 + ((lane >> 2) & 3) * 8;
            NOUNROLL for (int spin = 0; spin < SPIN_MAX; spin++) {
                fence_c();
                const v2u f = __builtin_amdgcn_raw_buffer_load_b64(rws, pf, 0, 16);
                const unsigned bad = (unsigned)__builtin_amdgcn_ballot_w64(f.x != tg || f.y != ~tg);
                if (bad == 0) break;
                if ((bad & 0x7777u) == 0) { if (which) break; postc<1>(F_RDY, lane); }
                __builtin_amdgcn_s_sleep(FL_SLEEP);
            }
            postc<2>(F_RDY + which * 4, lane);
            STAMP(17);
        };
        if (w < 6) {
            unsigned ffull = F_FULL + lw * 4;
            asm volatile("" : "+v"(ffull));
            {
                const unsigned voffS = (8 * (q8 >> 2) + (q8 & 3)) * 1024 + swz;
                const v4u srd = make_srd((const unsigned char*)(lw ? p.Bim : p.Bre) + (size_t)(32 * j) * 1024);
                NOUNROLL for (int n = 2; n < 8; n++) {
                    const unsigned so = 128 * ((n + rot) & 7);
                    dma4(srd, voffS, voffS ^ 64u, so, so + 15360, so + 2048, so + 17408, L_W + (n >> 1) * SLOT + lw * 8192 + (n & 1) * 4096);
                    if (n == 3) {
                        touch(sink, p.x, (128 * i + 8 * j + 4 * lw) * 1024 + (lane & 31) * 128);
                        touch(sink, lw ? p.Bim : p.Bre, (32 * j + 2 * i) * 1024 + c * 128);
                        bar_l();
                    }
                    if (n == 4) { waitv<6>(); postc<2>(ffull, lane); }
                    if (n >= 5) { waitv<4>(); postv(ffull, lane, n >> 1); }
                }
                waitv<0>(); postc<4>(ffull, lane);
                asm volatile("" ::"v"(sink));
            }
            STAMP(1);
            const unsigned voffG = q8 * (FF * 2) + (((p8 >> 1) ^ ((q8 >> 1) & 1)) << 5) + ((p8 & 1) << 4), voffG1 = voffG ^ 64u;
            const unsigned voffC = q8 * 1024 + swz, voffC1 = voffC ^ 64u;
            const unsigned voffD = q8 * 1024 + (((p8 >> 1) ^ (q8 >> 1) ^ (2 * (kq & 1))) << 5) + ((p8 & 1) << 4);
            const unsigned char* gb = (const unsigned char*)(lw ? p.wu : p.wg) + (7 * kq + q) * 128;
            unsigned thr = (lane >> 2) + 1;
            int credit = 0, cbase = 0;
            auto item = [&](int n, const unsigned char* b0, const unsigned char* b1, unsigned ve, unsigned vo, unsigned st) __attribute__((always_inline)) {
                v4u s0 = make_srd(b0), s1 = make_srd(b1);
                unsigned dst = ((n & 4) ? L_P : L_W) + (n & 3) * SLOT + lw * 8192, dst1 = dst + 4096;
                asm volatile("" : "+s"(s0), "+s"(s1), "+s"(dst), "+s"(dst1));
                int sp = LSPIN_MAX;
                NOUNROLL while (n - 7 > credit) {
                    const unsigned dn = peek(fa);
                    postv(ffull, lane, n - ((vm_outstanding() + 7) >> 3));
                    unsigned long long mk = __builtin_amdgcn_ballot_w64(dn >= thr);
                    mk &= mk >> 1; mk &= mk >> 2;
                    credit = __builtin_popcountll(mk & 0x1111111111111111ull) + cbase;
                    asm volatile("s_sub_i32 %0, %0, 1" : "+s"(sp)::"scc");
                    if (sp < 0) break;
                }
                waitv<INFL>();
                dma4(s0, ve, vo, 0, st - 1024, 2 * st - 2048, 3 * st - 3072, dst);
                postv(ffull, lane, n - (INFL + 7) / 8);
                waitv<INFL>();
                dma4(s1, ve, vo, 0, st - 1024, 2 * st - 2048, 3 * st - 3072, dst1);
            };
            wait_ge(F_XI + (lane & 3) * 4, 3);
            NOUNROLL for (int n = 4; n < N1; n++) {
                const int t = n - 4;
                const unsigned char* bp = gb + (size_t)(((t + rot) & 7) * 64) * (FF * 2) + (t >> 3) * 512;
                item(n, bp, bp + 32 * FF * 2, voffG, voffG1, 8 * FF * 2);
            }
            cbase = N1 - 8; thr += cbase;
            const unsigned char* dp = (const unsigned char*)p.wd + (size_t)(7 * kq) * 65536 + q * 256 + (lw ^ (kq >> 1)) * 128;
            NOUNROLL for (int v = 0; v < 11; v++) {
                if ((v & ~3) == 4) {
                    const unsigned char* cbp = (const unsigned char*)((v & 2) ? p.Cim : p.Cre) + (size_t)q * 131072 + (2 * kq + (v & 1)) * 128;
                    item(N1 + v, cbp + ((2 * lw) ^ kq) * 32768, cbp + ((2 * lw + 1) ^ kq) * 32768, voffC, voffC1, 8192);
                } else {
                    item(N1 + v, dp, dp + 32768, voffD, voffD, 8192);
                    dp += 65536;
                }
            }
            waitv<16>(); postv(ffull, lane, N1 + 9);
            waitv<8>(); postv(ffull, lane, N1 + 10);
            waitv<0>(); postv(ffull, lane, N1 + 11);
            tg = peek(L_TAG);
            STAMP(7);
        } else {
            unsigned tag0, prm0, prm1, prm2;
            const unsigned cto = min(idx * 9 + 4 * lw + p8, (int)CODE_LINES - 1) * 128;
            touch(sink, pc0, cto);
            NOUNROLL for (int t = heavy ? 4 : 0; t < 4; t++)
                touch(sink, lw ? p.wu : p.wg, (32 * i + (lane & 31)) * (FF * 2) + (7 * kq + min(2 * t + (lane >> 5), 6)) * 128);
            const unsigned po = 2 * (32 * j + 8 * (lane >> 4) + 4 * ((lane >> 1) & 1) + ((lane >> 2) & 3));
            asm volatile("global_load_ushort %0, %4, %5\n\tglobal_load_ushort %1, %4, %6\n\tglobal_load_ushort %2, %4, %7\n\tglobal_load_dword %3, %8, %9 sc1"
                         : "=&v"(prm0), "=&v"(prm1), "=&v"(prm2), "=&v"(tag0) : "v"(po), "s"(p.lre), "s"(p.lim), "s"(p.ldt), "v"(WS_EPOCH), "s"(p.ws) : "memory");
            *(LAS unsigned*)(uintptr_t)(F_DONE + (lane & 31) * 4) = 0u;
            bar_l();
            sts128(L_SEL + lw * 1024 + lane * 16, make_sel(lane, lw));
            asm volatile("s_waitcnt vmcnt(0)" : "+v"(prm0), "+v"(prm1), "+v"(prm2), "+v"(tag0)::"memory");
            tg = tag0 + 1u;
            const bool odd = lane & 1;
            const unsigned la = lane * 4;
            const float lr = bflo(prm0), li = bflo(prm1);
            float dt = __expf(bflo(prm2));
            dt = bflo(pk2(dt, dt));
            const float ex = __expf(dt * lr);
            const float fr = __builtin_amdgcn_fractf(dt * li * 0.15915494309189535f);
            const float ar = smul_t(ex, __builtin_amdgcn_cosf(fr)), ai = smul_t(ex, __builtin_amdgcn_sinf(fr));
            const cpx a1 = {ar, ai};
            const cpx a2 = cmul(a1, a1), a4 = cmul(a2, a2);
            const unsigned sg = odd ? 0u : 0x80000000u;
            auto sgn = [&](cpx v) { return cpx{v.x, __uint_as_float(__float_as_uint(v.y) ^ sg)}; };
            const cpx r8 = cmul(a4, a4), r16 = cmul(r8, r8), r32 = cmul(r16, r16), r64 = cmul(r32, r32);
            const cpx a32 = sgn(r32);
            {
                const cpx r = lw ? r32 : r8, rs = sgn(r);
                float v = odd ? r.y : r.x;
                sts32f(L_PW + lw * 1024 + la, odd ? 0.0f : 1.0f);
#pragma unroll
                for (int s = 1; s < 4; s++) { sts32f(L_PW + lw * 1024 + s * 256 + la, v); if (s < 3) v = lmad(rs, v, 0.0f); }
            }
            if (lw == 0) {
                const float nr = ar - 1.0f, den = smul_t(__builtin_amdgcn_rcpf(sfma(lr, lr, smul(li, li))), 2.0f);
                float co = smul(odd ? sfms(ai, lr, smul(nr, li)) : sfma(nr, lr, smul(ai, li)), den);
                sts32f(L_CST + la, odd ? a1.y : a1.x); sts32f(L_CST + 256 + la, odd ? a4.y : a4.x);
                const cpx a1s = sgn(a1);
#pragma unroll
                for (int t = 0; t < 5; t++) { sts32f(L_CST + (2 + t) * 256 + la, co); if (t < 4) co = lmad(a1s, co, 0.0f); }
                *(LAS unsigned*)(uintptr_t)L_TAG = tg;
            }
            post(F_COMM + lw * 4, 1);
            STAMP(1);
            cpx wn[8];
            {
                const cpx r128 = cmul(r64, r64), r256 = cmul(r128, r128);
                wn[0] = lw ? r128 : cpx{1.0f, 0.0f};
#pragma unroll
                for (int n = 1; n < 8; n++) wn[n] = cmul(wn[n - 1], r256);
#pragma unroll
                for (int n = 0; n < 8; n++) { wn[n] = sgn(wn[n]); asm volatile("" : "+v"(wn[n].x), "+v"(wn[n].y)); }
            }
            unsigned gro = WS_GRAN + j * 8192 + lane * 8, gro2 = gro + i * 512;
            unsigned to0 = lw ? min((unsigned)(lane & 31) * 0xb0000u + idx * 0x4000u, WS_END - 4) : (lane & 3) * 0x100000 + idx * 0x4000;
            unsigned to1 = (448 * kq + 28 * i + min(lane, 27)) * 1024 + (2 * q + lw) * 128, to2 = (128 * q + 8 * i + p8) * 1024 + (2 * kq + (q8 & 1)) * 128;
            asm volatile("" : "+v"(gro), "+v"(gro2), "+v"(to0), "+v"(to1), "+v"(to2));
            wait_ge(fa, 4);
            STAMP(4);
            if (lw) {
                float e[4];
#pragma unroll
                for (int s = 0; s < 4; s++) e[s] = lds32f(L_TOT + s * 256 + la);
                const float p2 = lmad(a32, e[0], e[1]), p3 = lmad(a32, p2, e[2]);
                __builtin_amdgcn_raw_buffer_store_b64(v2u{__float_as_uint(lmad(a32, p3, e[3])), tg}, rws, gro2, 0, 16);
                sts32f(L_PRE + la, 0.0f); sts32f(L_PRE + 256 + la, e[0]); sts32f(L_PRE + 512 + la, p2); sts32f(L_PRE + 768 + la, p3);
            }
            STAMP(5);
            touch(sink, lw ? (const void*)p.ws : (const void*)p.out, to0);
            __builtin_amdgcn_s_sleep(LB_FIRST);
            {
                const int top = i - 1 - lw;
                v2u gr[8] = {};
                NOUNROLL for (int spin = 0; spin < SPIN_MAX; spin++) {
                    fence_c();
#pragma unroll
                    for (int n = 0; n < 8; n++) gr[n] = __builtin_amdgcn_raw_buffer_load_b64(rws, gro, ((top - 2 * n) & 15) * 512, 16);
                    unsigned long long bad = 0;
#pragma unroll
                    for (int n = 0; n < 8; n++) if (top - 2 * n >= 0) bad |= __builtin_amdgcn_ballot_w64(gr[n].y != tg);
                    if (bad == 0) break;
                    __builtin_amdgcn_s_sleep(LB_SLEEP);
                }
                float h = 0;
#pragma unroll
                for (int n = 7; n >= 0; n--) if (top - 2 * n >= 0) h = lmad(wn[n], __uint_as_float(gr[n].x), h);
                sts32f(L_XCH + 256 + lw * 256 + la, h);
            }
            postc<4>(F_COMM + lw * 4, lane);
            STAMP(6);
            touch(sink, p.wd, to1);
            touch(sink, lw ? p.Cim : p.Cre, to2);
            waitv<0>();
            asm volatile("" ::"v"(sink));
            STAMP(7);
            poll16(lw);
        }
        f32x4 base[4];
        {
            const int d = 128 * q + 32 * kq + 4 * p8;
            const v2u dv = *(const GAS v2u*)(p.Dsk + d);
            const float df[4] = {bflo(dv.x), bfhi(dv.x), bflo(dv.y), bfhi(dv.y)};
            v2u xv[4];
#pragma unroll
            for (int e = 0; e < 4; e++) xv[e] = *(const GAS v2u*)(p.x + (size_t)(128 * i + 32 * pr + 8 * e + q8) * DM + d);
#pragma unroll
            for (int e = 0; e < 4; e++) {
                const unsigned tp = (32 * pr + 8 * e + 4 * ((q8 >> 1) & 1) + 2 * (q8 & 1) + (q8 >> 2)) * 4;
                const float rs = lds32f(L_RSTD + tp), nm = smul(lds32f(L_MEAN + tp), rs);
                const float xf[4] = {bflo(xv[e].x), bfhi(xv[e].x), bflo(xv[e].y), bfhi(xv[e].y)};
#pragma unroll
                for (int k = 0; k < 4; k++) base[e][k] = sfma(sfma(xf[k], rs, nm), df[k], xf[k]);
            }
        }
        STAMP(8);
        const unsigned t16 = ((tg & 0x3fffu) + 0x2000u) << 16;
        const unsigned xi = xo + kq * 36864u + lane * 16;
        auto bad3 = [&](const v4u* in) { return ((in[0].y ^ t16) | (in[0].w ^ t16) | (in[1].y ^ t16)) | ((in[1].w ^ t16) | (in[2].y ^ t16) | (in[2].w ^ t16)); };
        auto val = [&](const v4u* in, int k) { const unsigned dw = in[k / 6][2 * ((k / 3) & 1) + (k % 3 == 2)]; return (k % 3 == 1) ? bfhi(dw) : bflo(dw); };
        const unsigned sc = L_O + pr * 4096;
        unsigned wa = sc + (8 * (c >> 2) + 4 * (c & 1) + ((c >> 1) & 1)) * 128 + ((g ^ (c & 7)) << 4), ra = sc + q8 * 128 + ((p8 ^ (2 * (q8 & 1) + (q8 >> 2))) << 4);
        GAS float* op = (GAS float*)p.out + (size_t)(128 * i + 32 * pr + q8) * DM + 128 * q + 32 * kq + 4 * p8;
        asm volatile("" : "+v"(wa), "+v"(ra), "+v"(op));
        wait_ge(F_SENT + pr * 4, 3);
        __builtin_amdgcn_s_sleep(X_FIRST);
        v4u in[9];
        f32x4 own[4];
        NOUNROLL for (int spin = 0; spin < SPIN_MAX; spin++) {
            fence_c();
#pragma unroll
            for (int n = 0; n < 9; n++) in[n] = __builtin_amdgcn_raw_buffer_load_b128(rws, xi + (n / 3) * 12288 + (n % 3) * 1024, 0, 16);
            SB();
            wait_ge(F_SENT + pr * 4, 4);
#pragma unroll
            for (int e = 0; e < 4; e++) own[e] = lds128f(sc + e * 1024 + lane * 16);
            if (__builtin_amdgcn_ballot_w64((bad3(in) | bad3(in + 3) | bad3(in + 6)) > 0xffffu) == 0) break;
        }
        STAMP(11);
#pragma unroll
        for (int e = 0; e < 4; e++) {
            f32x4 t = own[e];
#pragma unroll
            for (int sr = 0; sr < 9; sr += 3) t += f32x4{val(in + sr, 4 * e), val(in + sr, 4 * e + 1), val(in + sr, 4 * e + 2), val(in + sr, 4 * e + 3)};
            own[e] = t;
        }
        asm volatile("" : "+v"(own[0]), "+v"(own[1]), "+v"(own[2]), "+v"(own[3]));
#pragma unroll
        for (int e = 0; e < 4; e++) sts128f((wa ^ (unsigned)((e >> 1) << 6)) + (e & 1) * 256, own[e]);
        fence_c();
        f32x4 ta[4];
#pragma unroll
        for (int e = 0; e < 4; e++) ta[e] = lds128f((ra ^ (unsigned)((e & 1) << 6)) + e * 1024);
        SB();
#pragma unroll
        for (int e = 0; e < 4; e++) *(GAS f32x4*)(op + e * 8 * DM) = base[e] + ta[e];
        if (b == 255 && w == 4 && lane == 0) __builtin_amdgcn_raw_buffer_store_b32(tg, rws, WS_EPOCH, 0, 16);
        STAMP(12); STAMP_DUMP();
        return;
    }
    constexpr int PD = PDV, XL = XLV, EG = EGATE;
    unsigned stall = 0; (void)stall;
    v4u xa[16][2];
#define AF(u, k) xa[2 * (u) + ((k) >> 1)][(k) & 1]
    v4u sws = make_srd(p.ws);
    unsigned oG = WS_A2 + (unsigned)(i * 44 + 7 * kq) * 16384u + w * 4096, oC = WS_A2 + (unsigned)(i * 44 + 28 + 2 * kq) * 16384u + w * 4096, oF = fo + w * 8;
    asm volatile("" : "+s"(sws), "+s"(oG), "+s"(oC), "+s"(oF));
    auto uoff = [&](int v) __attribute__((always_inline)) { return v < 4 ? oG + v * 16384 : v < 8 ? oC + (8 * ((v >> 1) & 1) + (v & 1)) * 16384 : oG + (v - 4) * 16384; };
    LANE_VARS;
    unsigned fdone = F_DONE + w * 4, l16 = lane * 16;
    asm volatile("" : "+v"(fdone), "+v"(l16));
    unsigned tgs = 0; int raised = 0;
    auto raise = [&](int level) __attribute__((always_inline)) {
        asm volatile("s_mov_b64 exec, 1\n\tbuffer_store_dwordx2 %0, off, %1, %2 offset:%3 sc1\n\ts_mov_b64 exec, -1" ::"v"(v2u{tgs, ~tgs}), "s"(sws), "s"(oF), "n"((level - 1) * 128) : "memory");
        post(fdone + (F_OUT - F_DONE), level);
        raised = level;

        if (level == 1) STAMP(17); else STAMP(18);
    };
    auto try_raise = [&](int level) __attribute__((always_inline)) {
        unsigned v; asm volatile("s_getreg_b32 %0, hwreg(HW_REG_IB_STS, 0, 4)" : "=s"(v)::"memory");
        if (__builtin_expect((int)(v + raised) == level - 1, 0)) raise(level);
    };
    unsigned oX[3];
#pragma unroll
    for (int sl = 1; sl < 4; sl++) { oX[sl - 1] = xo + (kq ^ sl) * 36864u + (sl - 1) * 12288; asm volatile("" : "+s"(oX[sl - 1])); }
    unsigned cbs[2], tbs[4];
    v4u bf[PD];
    const unsigned ffl = F_FULL + (lane & 1) * 4;
    auto set2 = [&](auto kk) __attribute__((always_inline)) {
        constexpr int k = decltype(kk)::value;
        if constexpr (k == 0) tbs[0] = L_P + ((l16 << 1) & 0x780) + ((l16 >> 1) & 0x18) + (((l16 >> 2) & 0x40) | ((l16 >> 2) & 0x20));
        else if constexpr (k < 4) tbs[k] = tbs[0] ^ (unsigned)(k << 5);
        else if constexpr (k == 4) cbs[0] = L_W + ((l16 << 3) & 0x780) + (((l16 >> 4) ^ (l16 >> 1)) & 0x70);
        else cbs[1] = cbs[0] ^ 64u;
        if constexpr (k < 4) asm volatile("" : "+v"(tbs[k])); else asm volatile("" : "+v"(cbs[k - 4]));
    };
    auto prime2 = [&](auto mm) __attribute__((always_inline)) { constexpr int m = decltype(mm)::value; const v2u lo = ldstr(tbs[m]), hi = ldstr(tbs[m] + 2048); bf[m] = v4u{lo.x, lo.y, hi.x, hi.y}; };
    {
        STAMP_INIT();
        STAMP(0);
        unsigned cb[2];
        cb[0] = (unsigned)c * 128u + (unsigned)((g ^ (c >> 1)) << 4); cb[1] = cb[0] ^ 64u;
        f32x4 eX;
        unsigned tb[4];
        f32x4 nm4[2];
        {
            const unsigned voffX = (8 * (q8 >> 2) + 4 * (q8 & 1) + ((q8 >> 1) & 1)) * 1024 + ((p8 ^ (q8 >> 1)) << 4), voffX1 = voffX ^ 64u;
            const v4u srd = make_srd((const unsigned char*)p.x + (size_t)(128 * i + 32 * w) * 1024);
            auto issue_x = [&](int ln) __attribute__((always_inline)) {
                const unsigned s0 = ((ln + rot) & 7) * 128;
                dma4(srd, voffX, voffX1, s0, s0 + 15360, s0, s0 + 15360, sl_lo + (ln & 3) * 4096);
            };
            {
                const unsigned voffS = (8 * (q8 >> 2) + (q8 & 3)) * 1024 + ((p8 ^ (q8 >> 1)) << 4), so = 128 * (((w >> 1) + rot) & 7);
                dma4(make_srd((const unsigned char*)(lw ? p.Bim : p.Bre) + (size_t)(32 * j) * 1024), voffS, voffS ^ 64u, so, so + 15360, so + 2048, so + 17408, L_W + lw * 8192 + (w >> 1) * 4096);
            }
#pragma unroll
            for (int ln = 0; ln < XL; ln++) issue_x(ln);
            v4u ones = {0x3f803f80u, 0x3f803f80u, 0x3f803f80u, 0x3f803f80u};
            unsigned xl[2] = {cb[0] + sl_lo, cb[1] + sl_lo};
            cb[0] += L_W; cb[1] += L_W;
            asm volatile("" : "+v"(ones), "+v"(cb[0]), "+v"(cb[1]), "+v"(xl[0]), "+v"(xl[1]));
            f32x4 sm[2], sq[2], bsum;
            v4u bx[4];
            unsigned cbw[2] = {cb[0] + (w >> 1) * 8192 + (w & 1) * 2048, cb[1] + (w >> 1) * 8192 + (w & 1) * 2048};
            asm volatile("" : "+v"(cbw[0]), "+v"(cbw[1]));
            f32x4 acc[4][2];
            unsigned fl = 0; bool lack = false;
            const unsigned fcs = (lane & 4) ? F_COMM + (lane & 1) * 4 : F_BS + (lane & 3) * 4;
            static_assert(PD == 4 && XL <= 3, "prologue schedule");
            waitv<4 * (XL - 1)>();
#pragma unroll
            for (int f = 0; f < 4; f++) xa[f >> 1][f & 1] = lds128(xl[f >> 1] + (f & 1) * 2048);
            asm volatile("s_barrier" ::: "memory");
            STAMP(1);
            sfor<-PD, 64>(LAMB(nn) {
                constexpr int n = decltype(nn)::value, m = n + PD, ks = n >> 2, ln = n >> 3;
                v4u cur;
                if constexpr (n >= 0) { cur = bf[n % PD]; mma<(n < 4)>(acc[n & 3][0], xa[ks][0], cur); SB(); }
                if constexpr (m > 0 && (m & 15) == 0 && m < 64) need_full(lack, ffl, (m >> 4) + 1);
                if constexpr (m < 64) bf[m % PD] = lds128(cb[(m >> 2) & 1] + wslot(m >> 4) + (((m >> 1) & 1) * 4 + ((m >> 3) & 1) * 2 + (m & 1)) * 2048);
                SB();
                if constexpr (n >= 0) { mma<(n < 4)>(acc[n & 3][1], xa[ks][1], cur); SB(); }
                if constexpr (n >= 0 && (n & 7) < 4 && ln + XL < 8)
                    dma1(srd, (n & 1) ? voffX1 : voffX, ((ln + XL + rot) & 7) * 128 + (n & 1) * 16384 + (n & 2) * 1024, sl_lo + ((ln + XL) & 3) * 4096 + (n & 3) * 1024);
                if constexpr (n >= 0 && (n & 7) == 4 && ln < 7) waitv<4 * ((ln + XL < 7 ? ln + XL : 7) - ln - 1)>();
                if constexpr (n >= 0 && (n & 7) >= 4 && ln < 7) xa[2 * ln + 2 + ((n >> 1) & 1)][n & 1] = lds128(xl[(n >> 1) & 1] + ((ln + 1) & 3) * 4096 + (n & 1) * 2048);
                if constexpr (n == 56) post(fdone + (F_XI - F_DONE), 3);
                if constexpr ((m & 15) == 10 && m < 48) fl = peek(ffl);
                if constexpr ((m & 15) == 14 && m < 48) lack = any_lt(fl, (m >> 4) + 2);
                if constexpr ((m & 15) == 15 && m < 63) post(fdone, (m >> 4) + 1);
                if constexpr (m < 64 && (m & 15) < 4) bx[m & 3] = lds128(cbw[m & 1] + wslot(m >> 4) + ((m >> 1) & 1) * 4096);
                if constexpr (n == 55) { sts32f(L_CST + 7 * 256 + c * 16 + (w & 1) * 8 + (w >> 1) * 4, bsum.x); post(fdone + (F_BS - F_DONE), 1); }
                if constexpr (n == 60) fl = peek(fcs);
                SB();
                if constexpr (n >= 0 && (n & 15) < 4) mma<(n == 0)>(bsum, ones, bx[n & 3]);
                if constexpr (n >= 0) { if constexpr (n & 1) mma<(n < 4)>(sq[(n >> 1) & 1], xa[ks][(n >> 1) & 1], xa[ks][(n >> 1) & 1]); else mma<(n < 4)>(sm[(n >> 1) & 1], ones, xa[ks][(n >> 1) & 1]); }
                SB();
            });
            need_full(any_lt(fl, 1), fcs, 1);
            const unsigned cl = L_CST + (l16 & 0xf0);
            const f32x4 ca = lds128f(cl), c4 = lds128f(cl + 256), co = lds128f(cl + 512), bs = lds128f(cl + 7 * 256), c8 = lds128f(L_PW + 256 + (l16 & 0xf0));
            mfma_done();
            STAMP(2); VAL(24, stall); stall = 0;
            {
                float dg[2];
#pragma unroll
                for (int bb = 0; bb < 2; bb++) {
                    nm4[bb] = sm[bb] * (-1.0f / DM);
                    const float s01 = (lane & 1) ? sq[bb].y : sq[bb].x, s23 = (lane & 1) ? sq[bb].w : sq[bb].z;
                    dg[bb] = __builtin_amdgcn_rsqf(fmaxf(((lane & 2) ? s23 : s01) * (1.0f / DM) - nm4[bb].x * nm4[bb].x, 0.0f) + 1e-5f);
                }
                if (g == (c >> 2)) {
                    const unsigned sa = L_RSTD + (32 * w + 8 * g + (c & 3)) * 4;
                    sts32f(sa, dg[0]); sts32f(sa + 16, dg[1]); sts32f(sa + L_MEAN, nm4[0].x); sts32f(sa + L_MEAN + 16, nm4[1].x);
                }
            }
            fence_c();
            const unsigned ra = L_RSTD + (32 * w + 8 * g) * 4;
            const f32x4 nt0 = lds128f(ra + L_MEAN), nt1 = lds128f(ra + L_MEAN + 16), rs0 = lds128f(ra), rs1 = lds128f(ra + 16);
            {
                const int q4 = c >> 2, p4 = c & 3;
                const unsigned tb1 = L_P + (unsigned)(8 * g + q4) * 128u + (unsigned)((((q4 >> 1) & 1) | ((g & 1) << 1)) << 5) + p4 * 8;
#pragma unroll
                for (int t = 0; t < 4; t++) { tb[t] = tb1 ^ (unsigned)(t << 5); asm volatile("" : "+v"(tb[t])); }
            }
            SB();
            STAMP(3);
            f32x4 ev;
            f32x2 sr[2][4], si[2][4];
#pragma unroll
            for (int st = 0; st < 2; st++) {
                const f32x2 b2 = st ? hi2(bs) : lo2(bs);
#pragma unroll
                for (int t = 0; t < 4; t++) {
                    const f32x4 nt = (t & 2) ? nt1 : nt0;
                    const f32x2 n2 = (t & 1) ? hi2(nt) : lo2(nt);
                    sr[st][t] = bfma<0, 0>(b2, n2, (t & 1) ? hi2(acc[st][t >> 1]) : lo2(acc[st][t >> 1]));
                    si[st][t] = bfma<1, 0>(b2, n2, (t & 1) ? hi2(acc[2 + st][t >> 1]) : lo2(acc[2 + st][t >> 1]));
                }
            }
            SB();
#pragma unroll
            for (int st = 0; st < 2; st++)
#pragma unroll
                for (int t = 0; t < 4; t++) {
                    const f32x4 rs = (t & 2) ? rs1 : rs0;
                    const f32x2 r2 = (t & 1) ? hi2(rs) : lo2(rs);
                    sr[st][t] *= r2; si[st][t] *= r2;
                }
            SB();
            const f32x2 a1[2] = {lo2(ca), hi2(ca)}, cf[2] = {lo2(co), hi2(co)};
#pragma unroll
            for (int t = 1; t < 4; t++) {
                f32x2 tr[2], ti[2];
#pragma unroll
                for (int st = 0; st < 2; st++) { tr[st] = bfma<1, 1>(a1[st], si[st][t - 1], sr[st][t]); ti[st] = bfma<1, 0>(a1[st], sr[st][t - 1], si[st][t]); }
                SB();
#pragma unroll
                for (int st = 0; st < 2; st++) { sr[st][t] = bfma<0, 0>(a1[st], sr[st][t - 1], tr[st]); si[st][t] = bfma<0, 0>(a1[st], si[st][t - 1], ti[st]); }
                SB();
            }
#pragma unroll
            for (int st = 0; st < 2; st++) {
                ev[2 * st] = sfma(c4[2 * st], sr[st][3].x, sfnma(c4[2 * st + 1], si[st][3].x, sr[st][3].y));
                ev[2 * st + 1] = sfma(c4[2 * st], si[st][3].x, sfma(c4[2 * st + 1], sr[st][3].x, si[st][3].y));
                eX[2 * st] = sr[st][3].x; eX[2 * st + 1] = si[st][3].x;
            }
            sts128f(L_SEG + w * 1024 + l16, ev);
            fence_c();
            f32x4 es[4];
#pragma unroll
            for (int s = 0; s < 4; s++) es[s] = lds128f(L_SEG + w * 1024 + s * 256 + (l16 & 0xf0));
            SB();
            unsigned ya = L_O + w * 4096 + l16;
            asm volatile("" : "+v"(ya), "+v"(eX));
#pragma unroll
            for (int st = 0; st < 2; st++) {
                f32x2 tr[4], ti[4];
#pragma unroll
                for (int t = 0; t < 4; t++) { tr[t] = bmul<1, 1>(cf[st], si[st][t]); ti[t] = bmul<1, 1>(cf[st], sr[st][t]); }
                SB();
#pragma unroll
                for (int t = 0; t < 4; t++) { tr[t] = bfma<0, 0>(cf[st], sr[st][t], tr[t]); ti[t] = bfma<0, 1>(cf[st], si[st][t], ti[t]); }
                SB();
                sts128(ya + st * 1024, v4u{pk2(tr[0]), pk2(tr[1]), pk2(tr[2]), pk2(tr[3])});
                sts128(ya + 2048 + st * 1024, v4u{pk2(ti[0]), pk2(ti[1]), pk2(ti[2]), pk2(ti[3])});
                SB();
                if (st == 0) {
#pragma unroll
                    for (int s = 1; s < 4; s++) { const f32x2 u0 = cmadp(lo2(c8), lo2(es[s - 1]), lo2(es[s])), u1 = cmadp(hi2(c8), hi2(es[s - 1]), hi2(es[s])); es[s] = f32x4{u0.x, u0.y, u1.x, u1.y}; }
                    sts128f(L_TOT + w * 256 + (l16 & 0xf0), es[3]);
                    post(fdone, 4);
                    f32x4 pin;
#pragma unroll
                    for (int k = 0; k < 4; k++) { const float lo = g == 1 ? es[0][k] : 0.0f, hi = g == 3 ? es[2][k] : es[1][k]; pin[k] = g >= 2 ? hi : lo; }
                    sts128f(L_SEG + w * 1024 + l16, pin);
                    SB();
                }
            }
        }
        STAMP(4);
        {
            const v4u sel0 = lds128(L_SEL + l16), sel1 = lds128(L_SEL + 1024 + l16);
            f32x4 ct[6];
            unsigned fl = 0;
            sfor<0, 68>(LAMB(kk) {
                constexpr int k = decltype(kk)::value, e = k - 4;
                if constexpr (k < 64) mma_c(ct[k % 6], (k & 1) ? sel1 : sel0, xa[k >> 2][(k >> 1) & 1], nm4[(k >> 1) & 1]);
                if constexpr (k >= 64 || k < 4) asm volatile("s_nop 1");
                if constexpr (e >= 0) {
                    xa[e >> 2][(e >> 1) & 1][2 * (e & 1)] = cvtv(ct[e % 6].x, ct[e % 6].y);
                    xa[e >> 2][(e >> 1) & 1][2 * (e & 1) + 1] = cvtv(ct[e % 6].z, ct[e % 6].w);
                }
                if constexpr (k == 40) fl = peek(ffl);
                if constexpr (k == 52) need_full(any_lt(fl, 5), ffl, 5);
                if constexpr (k >= 53 && k < 53 + PD) { constexpr int m = k - 53; const unsigned a = tb[(m >> 1) & 3] + (m & 1) * 8192; const v2u lo = ldstr(a), hi = ldstr(a + 512); bf[(64 + m) % PD] = v4u{lo.x, lo.y, hi.x, hi.y}; }
                SB();
            });
        }
        STAMP(5);
        unsigned go = oG + q * 16384, so = oC + (q >> 1) * 16384 + (q & 1) * 2048;
        asm volatile("" : "+s"(go), "+s"(so));
        f32x4 acc[4][2][2];
        f32x2 r12;
        unsigned fc = 0;
        auto gpass = [&](auto pp, auto n0, auto n1) __attribute__((always_inline)) {
            constexpr int pass = decltype(pp)::value;
            unsigned fl = 0, dn = 0; bool lack = false;
            sfor<decltype(n0)::value, decltype(n1)::value>(LAMB(nn) {
                constexpr int n = decltype(nn)::value, m = n + PD;
                constexpr bool pre = pass == 0 || m < 128;
                constexpr unsigned off = wslot(m >> 4) + (m & 1) * 8192 + ((m >> 3) & 1) * 4096;
                const v4u cur = bf[(n + 64 + 128 * pass) % PD];
                mma<(n < 8)>(acc[(n >> 1) & 3][n & 1][0], cur, xa[n >> 3][0]);
                SB();
                if constexpr ((m & 15) == 0) post(fdone, dn);
                if constexpr ((m & 15) == 0) need_full(lack, ffl, 5 + 8 * pass + (m >> 4));
                v2u lo, hi;
                if constexpr (pre) lo = ldstr(tb[(m >> 1) & 3] + off); else lo = ldstr(tbs[m & 3]);
                if constexpr (pass == 1 && n >= 69 && n < 81 && (n & 1)) set2(IC<(n - 69) / 2>{});
                if constexpr ((m & 15) == 10) fl = peek(ffl);
                if constexpr ((m & 15) == 15) lack = any_lt(fl, m == 127 && pass == 0 ? N1 - 7 : 6 + 8 * pass + (m >> 4));
                if constexpr ((m & 15) == 13) { dn = 5 + 8 * pass + (m >> 4); asm volatile("" : "+v"(dn)); }
                if constexpr (n == EG - 8 && pass == 1) fc = peek(F_RDY);
                if constexpr (n == EG - 2 && pass == 1) { if (__builtin_expect(any_lt(fc, 1), 0)) { wait_ge(F_RDY, 1); } fence_c(); }
                if constexpr (n >= EG && n < EG + 24 && pass == 1 && (n & 1) == 0) { constexpr int f = (n - EG) >> 1; reldA<(f & 3) * 1024>(AF(f >> 2, f & 3), l16, sws, uoff(f >> 2)); }
                if constexpr (pass == 1 && (n == 33 || n == 41 || n == 49 || n == 57)) try_raise(1);
                if constexpr (pass == 1 && n == 67) { if (raised < 1) { waitv<0>(); raise(1); } }
                if constexpr (n == 120 && pass == 0) fc = peek(ffl + (F_COMM - F_FULL));
                if constexpr (n == 121 && pass == 0) { const unsigned ra = L_RSTD + w * 128 + (((l16 >> 1) & 0x60) | ((l16 >> 2) & 0xc)); r12 = f32x2{lds32f(ra), lds32f(ra + 16)}; }
                SB();
                mma<(n < 8)>(acc[(n >> 1) & 3][n & 1][1], cur, xa[n >> 3][1]);
                SB();
                if constexpr (pre) hi = ldstr(tb[(m >> 1) & 3] + off + 512); else hi = ldstr(tbs[m & 3] + 2048);
                bf[n % PD] = v4u{lo.x, lo.y, hi.x, hi.y};
                if constexpr ((m & 57) == 57 && (pass == 0 || m < 64)) {
                    tb[(m >> 1) & 3] += (m & 64) ? L_P - L_W : L_W - L_P; asm volatile("" : "+v"(tb[(m >> 1) & 3]));
                }
                SB();
            });
        };
        auto gated = [&](auto ff) __attribute__((always_inline)) {
            constexpr int uu = decltype(ff)::value >> 1, bb = decltype(ff)::value & 1;
            const f32x4 h0 = acc[2 * uu][0][bb] * acc[2 * uu][1][bb], h1 = acc[2 * uu + 1][0][bb] * acc[2 * uu + 1][1][bb];
            return v4u{pk2(bmul<bb, 0>(r12, lo2(h0))), pk2(bmul<bb, 0>(r12, hi2(h0))), pk2(bmul<bb, 0>(r12, lo2(h1))), pk2(bmul<bb, 0>(r12, hi2(h1)))};
        };
        gpass(IC<0>{}, IC<0>{}, IC<128>{});
        {
            r12 = r12 * r12;
            mfma_done();
            STAMP(6); VAL(25, stall); stall = 0;
            sfor<0, 2>(LAMB(ff) { stA<decltype(ff)::value * 1024>(gated(ff), l16, sws, go); });
            SB();
            need_full(any_lt(fc, 4), ffl + (F_COMM - F_FULL), 4);
            const unsigned tg = peek(L_TAG);
            const unsigned cl = L_CST + (l16 & 0xf0);
            unsigned ya = L_O + w * 4096 + l16;
            asm volatile("" : "+v"(ya));
            const f32x4 h0 = lds128f(L_XCH + 256 + (l16 & 0xf0)), h1 = lds128f(L_XCH + 512 + (l16 & 0xf0));
            const f32x4 aw = lds128f(L_PW + 1024 + w * 256 + (l16 & 0xf0)), pb = lds128f(L_PRE + w * 256 + (l16 & 0xf0));
            const f32x4 ag = lds128f(L_PW + l16), pin = lds128f(L_SEG + w * 1024 + l16);
            const v4u sel[2] = {lds128(L_SEL + l16), lds128(L_SEL + 1024 + l16)};
            const v4u yr[2] = {lds128(ya), lds128(ya + 1024)}, yi[2] = {lds128(ya + 2048), lds128(ya + 3072)};
            const f32x4 c4 = lds128f(cl + 256);
            f32x4 cw[4];
            SB();
            sfor<2, 4>(LAMB(ff) { stA<decltype(ff)::value * 1024>(gated(ff), l16, sws, go); });
            tgs = __builtin_amdgcn_readfirstlane(tg);
#pragma unroll
            for (int t = 0; t < 4; t++) cw[t] = lds128f(cl + (3 + t) * 256);
            SB();
            f32x4 zr[2][2], zi[2][2];
#pragma unroll
            for (int st = 0; st < 2; st++)
#pragma unroll
                for (int bb = 0; bb < 2; bb++) { mma<true>(zr[st][bb], yr[st], sel[bb]); mma<true>(zi[st][bb], yi[st], sel[bb]); }
            SB();
            f32x2 hr[2], hi[2];
            {
                const f32x4 cy = h0 + h1;
                const f32x2 k0 = cmadp(lo2(ag), cmadp(lo2(aw), lo2(cy), lo2(pb)), lo2(pin)), k1 = cmadp(hi2(ag), cmadp(hi2(aw), hi2(cy), hi2(pb)), hi2(pin));
                const f32x4 hin = {k0.x, k0.y, k1.x, k1.y};
                float t0[2], t1[2];
#pragma unroll
                for (int st = 0; st < 2; st++) { t0[st] = sfnma(c4[2 * st + 1], hin[2 * st + 1], eX[2 * st]); t1[st] = sfma(c4[2 * st + 1], hin[2 * st], eX[2 * st + 1]); }
                SB();
#pragma unroll
                for (int st = 0; st < 2; st++) { hr[st] = f32x2{hin[2 * st], sfma(c4[2 * st], hin[2 * st], t0[st])}; hi[st] = f32x2{hin[2 * st + 1], sfma(c4[2 * st], hin[2 * st + 1], t1[st])}; }
                SB();
            }
#pragma unroll
            for (int st = 0; st < 2; st++) {
                f32x2 tr[4], ti[4];
#pragma unroll
                for (int t = 0; t < 4; t++) { const f32x2 wv = st ? hi2(cw[t]) : lo2(cw[t]); tr[t] = bmul<1, 1>(wv, hi[st]); ti[t] = bmul<1, 1>(wv, hr[st]); }
                SB();
#pragma unroll
                for (int t = 0; t < 4; t++) { const f32x2 wv = st ? hi2(cw[t]) : lo2(cw[t]); tr[t] = bfma<0, 0>(wv, hr[st], tr[t]); ti[t] = bfma<0, 1>(wv, hi[st], ti[t]); }
                SB();
                v4u fr = {pk2(tr[0]), pk2(tr[1]), pk2(tr[2]), pk2(tr[3])}, fi = {pk2(ti[0]), pk2(ti[1]), pk2(ti[2]), pk2(ti[3])};
                asm volatile("s_nop 1" : "+v"(fr), "+v"(fi));
#pragma unroll
                for (int bb = 0; bb < 2; bb++) { mma<false>(zr[st][bb], fr, sel[bb]); mma<false>(zi[st][bb], fi, sel[bb]); }
                SB();
            }
            mfma_done();
            stA<0>(pk8(zr[0][0], zr[1][0]), l16, sws, so); stA<0>(pk8(zi[0][0], zi[1][0]), l16, sws, so + 8 * 16384);
            stA<1024>(pk8(zr[0][1], zr[1][1]), l16, sws, so); stA<1024>(pk8(zi[0][1], zi[1][1]), l16, sws, so + 8 * 16384);
            SB();
            STAMP(7);
        }
        if (heavy) {
            gpass(IC<1>{}, IC<0>{}, IC<128>{});
            mfma_done();
            STAMP(8); VAL(26, stall); stall = 0;
            sfor<0, 4>(LAMB(ff) { stA<decltype(ff)::value * 1024>(gated(ff), l16, sws, go + 4 * 16384); });
            SB();
        }
        int q2 = q;
        asm volatile("" : "+s"(q2));
        if (q2 == 3) {
            waitv<0>(); raise(1); raise(2);
            sfor<0, 6>(set2);
            wait_ge(ffl, N1 + 1);
            sfor<0, PD>(prime2);
            wait_ge(F_RDY, 1);
            sfor<0, 12>(LAMB(ff) { constexpr int f = decltype(ff)::value; reldA<(f & 3) * 1024>(AF(f >> 2, f & 3), l16, sws, uoff(f >> 2)); });
#pragma unroll
            for (int u = 0; u < 3; u++) asm volatile("s_waitcnt vmcnt(0)" : "+v"(AF(u, 0)), "+v"(AF(u, 1)), "+v"(AF(u, 2)), "+v"(AF(u, 3))::"memory");
        }
    }
    STAMP(9);
    {
        f32x4 acc[8][2];
        v4u pk[3];
        unsigned fl = 0, fr = 0, dn = 0; bool lack = false;
        float tagf = 0;
        constexpr auto fv = [](int n) { return n < 48 ? n >> 4 : n < 108 ? 3 + (n - 48) / 12 : n < 144 ? 8 + ((n - 108) % 12) / 4 : 3 + (n - 144) / 4; };
        constexpr auto fk = [](int n) { return n < 48 ? (n >> 3) & 1 : n < 108 ? ((n - 48) % 12) / 6 : (n >> 1) & 1; };
        constexpr auto fd = [](int n) { return n < 48 ? n & 7 : n < 108 ? 2 + (n - 48) % 6 : n < 144 ? 2 * ((n - 108) / 12 + 1) + (n & 1) : n & 1; };
        constexpr auto first = [](int n) { return n >= 0 && (n < 48 ? (n & 15) == 0 : n < 108 ? (n - 48) % 12 == 0 : n < 120 && (n & 3) == 0); };
        constexpr int LG = LGATE;
        sfor<0, 176>(LAMB(nn) {
            constexpr int n = decltype(nn)::value, m = n + PD;
            constexpr int v = fv(n), ks = fk(n), dt = fd(n);
            constexpr int mv = fv(m), mk = fk(m), md = fd(m);
            constexpr bool mc = (mv & ~3) == 4;
            constexpr unsigned moff = (mv & 3) * SLOT + (mc ? md * 2048 : (md >> 2) * 8192 + mk * 4096);
            constexpr auto chk = [](int q) { return q == 16 ? 2 : q == 32 ? 4 : q == 60 ? 8 : q == 108 ? 11 : 0; };
            v4u cur; v2u lo, hi;
            if constexpr (n >= 0) {
                if constexpr (first(n)) {
                    constexpr int mid = n <= 32 ? 0 : 4 + ((n - 31) / 2 < 16 ? (n - 31) / 2 : 16), late = n <= LG ? 0 : n - LG < 12 ? n - LG : 12;
                    constexpr int NW = v == 0 ? 12 : v == 1 ? 8 : v == 2 ? 63 : v < 8 ? mid - 4 * (v - 2) + late : late - 4 * (v - 7);
                    static_assert(NW >= 0, "load schedule");
                    asm volatile("s_waitcnt vmcnt(%4)" : "+v"(AF(v & 7, 0)), "+v"(AF(v & 7, 1)), "+v"(AF(v & 7, 2)), "+v"(AF(v & 7, 3)) : "n"(NW) : "memory");
                }
                cur = bf[n % PD];
                mma<(n < 8)>(acc[dt][0], cur, AF(v & 7, 2 * ks));
                SB();
            }
            if constexpr (m > 0 && (m & 15) == 0 && m <= 48) post(fdone, dn);
            if constexpr (chk(m) > 0) need_full(lack, ffl, N1 + chk(m));
            if constexpr (m < 176) { if constexpr (mc) bf[m % PD] = lds128(cbs[mk] + moff); else lo = ldstr(tbs[md & 3] + moff); }
            if constexpr (chk(m + 6) > 0) fl = peek(ffl);
            if constexpr (chk(m + 1) > 0) lack = any_lt(fl, N1 + chk(m + 1));
            if constexpr ((m & 15) == 13 && m < 48) { dn = N1 + mv + 1; asm volatile("" : "+v"(dn)); }
            if constexpr (n == 17 || n == 21) try_raise(2);
            if constexpr (n == 20) fr = peek(F_RDY);
            if constexpr (n == 26) { if (raised < 2) { waitv<0>(); raise(2); } }
            if constexpr (n == 27) need_full(any_lt(fr, 2), F_RDY, 2);
            if constexpr (n >= 28 && n < 32) reldA<(n & 3) * 1024>(AF(3, n & 3), l16, sws, uoff(3));
            if constexpr (n >= 32 && n < 64 && (n & 1) == 0) { constexpr int f = (n - 32) >> 1; reldA<(f & 3) * 1024>(AF(4 + (f >> 2), f & 3), l16, sws, uoff(4 + (f >> 2))); }
            if constexpr (n == LG - 6) fr = peek(F_RDY + 4);
            if constexpr (n == LG - 1) need_full(any_lt(fr, 1), F_RDY + 4, 1);
            if constexpr (n >= LG && n < LG + 12) { constexpr int f = n - LG; reldA<(f & 3) * 1024>(AF(f >> 2, f & 3), l16, sws, uoff(8 + (f >> 2))); }
            if constexpr (n == 70) { tagf = __uint_as_float(((tgs & 0x3fffu) + 0x2000u) << 16); asm volatile("" : "+v"(tagf)); }
            SB();
            if constexpr (n >= 0) { mma<(n < 8)>(acc[dt][1], cur, AF(v & 7, 2 * ks + 1)); SB(); }
            if constexpr (m < 176 && !mc) { hi = ldstr(tbs[md & 3] + moff + 2048); bf[m % PD] = v4u{lo.x, lo.y, hi.x, hi.y}; }
            if constexpr (n >= 120 && n < 156) {
                constexpr int sl = (n - 108) / 12, r = (n - 108) % 12 - 2;
                auto val = [&](int k) { return acc[2 * sl + (k >> 3)][(k >> 2) & 1][k & 3]; };
                if constexpr (r >= 0 && r < 9 && r % 3 < 2) {
                    constexpr int gi = 2 * (r / 3) + r % 3;
                    pk[gi >> 1][2 * (gi & 1)] = cvtv(val(3 * gi), val(gi < 5 ? 3 * gi + 1 : 15));
                    pk[gi >> 1][2 * (gi & 1) + 1] = cvtv(val(gi < 5 ? 3 * gi + 2 : 15), tagf);
                }
                if constexpr (r >= 0 && r % 3 == 2)
                    stX<(r / 3) * 1024>(pk[r / 3], l16, sws, oX[sl - 1]);
                if constexpr (r == 9 && sl == 3) post(fdone + (F_SENT - F_DONE), 3);
            }
            if constexpr (n == 107) { STAMP(10); VAL(28, stall); stall = 0; }
            SB();
        });
        mfma_done();
        STAMP(11); VAL(29, stall);
#pragma unroll
        for (int e = 0; e < 4; e++) sts128f(L_O + w * 4096 + e * 1024 + l16, acc[e >> 1][e & 1]);
        post(fdone + (F_SENT - F_DONE), 4);
    }
    STAMP(14); STAMP_DUMP();
}

extern "C" void kernel_launch(void* const* d_in, const int* in_sizes, int n_in, void* d_out, int out_size, void* d_ws, size_t ws_size,
                              hipStream_t stream) {
    Params p;
    p.x = (const u16*)d_in[0]; p.lre = (const u16*)d_in[1]; p.lim = (const u16*)d_in[2];
    p.Bre = (const u16*)d_in[3]; p.Bim = (const u16*)d_in[4]; p.Cre = (const u16*)d_in[5]; p.Cim = (const u16*)d_in[6];
    p.ldt = (const u16*)d_in[7]; p.Dsk = (const u16*)d_in[8];
    p.wg = (const u16*)d_in[10]; p.wu = (const u16*)d_in[11]; p.wd = (const u16*)d_in[12];
    p.out = (float*)d_out; p.ws = (unsigned char*)d_ws + WS_SHIFT;
    (void)hipFuncSetAttribute((const void*)fused, hipFuncAttributeMaxDynamicSharedMemorySize, L_TOTAL);
    fused<<<256, 512, L_TOTAL, stream>>>(p);
}
```

```cpp
#define PROBE 0
#define PROBEW 0
#define PROBEC heavy
#include <hip/hip_runtime.h>
#include <stdint.h>

typedef unsigned short u16;
typedef unsigned v4u __attribute__((ext_vector_type(4)));
typedef unsigned v2u __attribute__((ext_vector_type(2)));
typedef short s16x4 __attribute__((ext_vector_type(4)));
typedef __bf16 bf16x2 __attribute__((ext_vector_type(2)));
typedef __bf16 bf16x8 __attribute__((ext_vector_type(8)));
typedef float f32x2 __attribute__((ext_vector_type(2)));
typedef float f32x4 __attribute__((ext_vector_type(4)));
#define GAS __attribute__((address_space(1)))
#define LAS __attribute__((address_space(3)))
#define DEVI __device__ __forceinline__
#define NOUNROLL _Pragma("nounroll")

#define SPIN_SLEEP 0
constexpr int DM = 512, FF = 1792;
constexpr unsigned WS_A2 = 0;
constexpr unsigned WS_XB = 16u * 44u * 16384u;
constexpr unsigned WS_GRAN = WS_XB + 256u * 36864u;
constexpr unsigned WS_FLAG = WS_GRAN + 16u * 16u * 512u;
constexpr unsigned WS_EPOCH = WS_FLAG + 1024u * 128u;
constexpr unsigned WS_END = WS_EPOCH + 256u;
constexpr unsigned SLOT = 16384;
constexpr unsigned L_RSTD = 0, L_MEAN = 512;
constexpr unsigned F_DONE = 1024, F_SENT = F_DONE + 16, F_FULL = F_DONE + 32, F_COMM = F_DONE + 40, F_RDY = F_DONE + 48, F_OUT = F_DONE + 64, F_XI = F_DONE + 80, F_BS = F_DONE + 96, L_TAG = F_DONE + 128;
constexpr unsigned L_CST = 1280;
constexpr unsigned L_XCH = L_CST + 8 * 256;
constexpr unsigned L_SEG = 4096, L_PW = 8192, L_TOT = L_PW + 2048, L_PRE = L_TOT + 1024, L_SEL = 12288;
constexpr unsigned L_P = SLOT, L_O = 5 * SLOT, L_W = 6 * SLOT, L_TOTAL = 10 * SLOT;
constexpr int SPIN_MAX = 1 << 18;
constexpr int LSPIN_MAX = 1 << 22;

struct Params {
    const u16 *x, *lre, *lim, *Bre, *Bim, *Cre, *Cim, *ldt, *Dsk, *wg, *wu, *wd;
    float* out;
    unsigned char* ws;
};

DEVI float bf2f(u16 v) { return __uint_as_float(((unsigned)v) << 16); }
DEVI float bflo(unsigned v) { return __uint_as_float(v << 16); }
DEVI float bfhi(unsigned v) { return __uint_as_float(v & 0xffff0000u); }
DEVI unsigned pk2(float a, float b) { f32x2 p = {a, b}; return __builtin_bit_cast(unsigned, __builtin_convertvector(p, bf16x2)); }
DEVI unsigned pk2(f32x2 p) { return __builtin_bit_cast(unsigned, __builtin_convertvector(p, bf16x2)); }
DEVI v4u pk8(f32x4 a, f32x4 b) { return v4u{pk2(a.x, a.y), pk2(a.z, a.w), pk2(b.x, b.y), pk2(b.z, b.w)}; }
DEVI float dot2(unsigned a, unsigned b, float c) { return __builtin_amdgcn_fdot2_f32_bf16(__builtin_bit_cast(bf16x2, a), __builtin_bit_cast(bf16x2, b), c, false); }
template <int CTRL> DEVI float dppf(float v) { return __builtin_bit_cast(float, __builtin_amdgcn_update_dpp(0, __builtin_bit_cast(int, v), CTRL, 0xf, 0xf, true)); }
DEVI f32x4 mfma(v4u a, v4u b, f32x4 c) { return __builtin_amdgcn_mfma_f32_16x16x32_bf16(__builtin_bit_cast(bf16x8, a), __builtin_bit_cast(bf16x8, b), c, 0, 0, 0); }
template <bool FIRST> DEVI void mma(f32x4& c, const v4u& a, const v4u& b) {
    if constexpr (FIRST) asm volatile("v_mfma_f32_16x16x32_bf16 %0, %1, %2, 0" : "=&v"(c) : "v"(a), "v"(b));
    else asm volatile("v_mfma_f32_16x16x32_bf16 %0, %1, %2, %0" : "+v"(c) : "v"(a), "v"(b));
}
DEVI void mfma_done() { __builtin_amdgcn_sched_barrier(0); asm volatile("s_nop 7"); __builtin_amdgcn_sched_barrier(0); }
DEVI v4u lds128(unsigned a) { return *(LAS v4u*)(uintptr_t)a; }
DEVI f32x4 lds128f(unsigned a) { return *(LAS f32x4*)(uintptr_t)a; }
DEVI float lds32f(unsigned a) { return *(LAS float*)(uintptr_t)a; }
DEVI void sts32f(unsigned a, float v) { *(LAS float*)(uintptr_t)a = v; }
DEVI void sts128(unsigned a, v4u v) { *(LAS v4u*)(uintptr_t)a = v; }
DEVI void sts128f(unsigned a, f32x4 v) { *(LAS f32x4*)(uintptr_t)a = v; }
DEVI v2u ldstr(unsigned a) { return __builtin_bit_cast(v2u, __builtin_amdgcn_ds_read_tr16_b64_v4i16((LAS s16x4*)(uintptr_t)a)); }
DEVI void fence_c() { asm volatile("" ::: "memory"); }
DEVI void bar_l() { asm volatile("s_waitcnt lgkmcnt(0)\n\ts_barrier" ::: "memory"); }
template <int N> DEVI void waitv() { asm volatile("s_waitcnt vmcnt(%0)" ::"n"(N) : "memory"); }
DEVI void dma16(const void* sbase, unsigned voff, unsigned ldst) {
    asm volatile("s_mov_b32 m0, %2\n\ts_nop 0\n\tglobal_load_lds_dwordx4 %0, %1" ::"v"(voff), "s"(sbase), "s"(ldst) : "memory");
}
DEVI void post(unsigned a, unsigned v) { fence_c(); *(LAS unsigned*)(uintptr_t)a = v; fence_c(); }
DEVI unsigned peek(unsigned a) { fence_c(); return *(LAS unsigned*)(uintptr_t)a; }
DEVI bool any_lt(unsigned v, unsigned need) { return __builtin_amdgcn_ballot_w64(v < need) != 0; }
DEVI void wait_ge(unsigned a, unsigned need) {
    int s = LSPIN_MAX;
    NOUNROLL while (any_lt(peek(a), need)) { asm volatile("s_sub_i32 %0, %0, 1" : "+s"(s) :: "scc"); if (s < 0) break; if (SPIN_SLEEP) __builtin_amdgcn_s_sleep(SPIN_SLEEP); }
    fence_c();
}
template <int V> DEVI void postm(unsigned a, int lane) {
    asm volatile("s_mov_b64 exec, %2\n\tds_max_u32 %0, %1\n\ts_mov_b64 exec, -1" ::"v"(a), "v"(lane), "n"(1 << V) : "memory");
}
DEVI unsigned vm_outstanding() {
    unsigned v; asm volatile("s_getreg_b32 %0, hwreg(HW_REG_IB_STS)" : "=s"(v)::"memory");
    return (v & 15u) | ((v >> 18) & 0x30u);
}
DEVI void postv(unsigned a, int lane, unsigned v) {
    asm volatile("s_lshl_b64 exec, 1, %2\n\tds_max_u32 %0, %1\n\ts_mov_b64 exec, -1" ::"v"(a), "v"(lane), "s"(v) : "memory", "scc");
}
template <int V> DEVI void postc(unsigned a, int lane) {
    asm volatile("s_mov_b64 exec, %2\n\tds_write_b32 %0, %1\n\ts_mov_b64 exec, -1" ::"v"(a), "v"(lane), "n"(1 << V) : "memory");
}
DEVI void need_full(bool lack, unsigned a, unsigned need) { if (__builtin_expect(lack, 0)) wait_ge(a, need); fence_c(); }
#define VAL(id, v)
DEVI float sfma(float a, float b, float c) { float d; asm("v_fma_f32 %0, %1, %2, %3" : "=v"(d) : "v"(a), "v"(b), "v"(c)); return d; }
DEVI float sfms(float a, float b, float c) { float d; asm("v_fma_f32 %0, %1, %2, -%3" : "=v"(d) : "v"(a), "v"(b), "v"(c)); return d; }
DEVI float sfnma(float a, float b, float c) { float d; asm("v_fma_f32 %0, -%1, %2, %3" : "=v"(d) : "v"(a), "v"(b), "v"(c)); return d; }
DEVI float sadd(float a, float b) { float d; asm("v_add_f32 %0, %1, %2" : "=v"(d) : "v"(a), "v"(b)); return d; }
DEVI float smul(float a, float b) { float d; asm("v_mul_f32 %0, %1, %2" : "=v"(d) : "v"(a), "v"(b)); return d; }
DEVI float smul_t(float a, float b) { float d; asm("s_nop 0\n\tv_mul_f32 %0, %1, %2" : "=v"(d) : "v"(a), "v"(b)); return d; }
struct cpx { float x, y; };
DEVI cpx cmul(cpx a, cpx b) { return cpx{sfms(a.x, b.x, smul(a.y, b.y)), sfma(a.x, b.y, smul(a.y, b.x))}; }
DEVI float lmad(cpx a, float h, float u) {
    float d;
    asm("v_fma_f32 %0, %1, %3, %4\n\ts_nop 0\n\tv_fmac_f32_dpp %0, %3, %2 quad_perm:[1,0,3,2] row_mask:0xf bank_mask:0xf" : "=&v"(d) : "v"(a.x), "v"(a.y), "v"(h), "v"(u));
    return d;
}
DEVI f32x2 dup(float v) { f32x2 d = {v, v}; asm("" : "+v"(d)); return d; }
DEVI f32x2 pfma(f32x2 a, f32x2 b, f32x2 c) { return __builtin_elementwise_fma(a, b, c); }
DEVI f32x2 lo2(f32x4 v) { return f32x2{v.x, v.y}; }
DEVI f32x2 hi2(f32x4 v) { return f32x2{v.z, v.w}; }
#define SB() __builtin_amdgcn_sched_barrier(0)
template <int V> struct IC { static constexpr int value = V; };
template <int B, int E, class F> DEVI void sfor(F&& f) { if constexpr (B < E) { f(IC<B>{}); sfor<B + 1, E>(f); } }
#define LAMB(nn) [&](auto nn) __attribute__((always_inline))
#define STAMP(id) do { if ((id) == PROBE && w == PROBEW) { SB(); unsigned long long t_; asm volatile("s_memrealtime %0\n\ts_waitcnt lgkmcnt(0)" : "=s"(t_) :: "memory"); *(LAS unsigned*)(uintptr_t)14336 = (unsigned)t_; SB(); } } while (0)
#define STAMP_INIT()
#define STAMP_DUMP() do { if (w == 4 && (PROBEC)) { const unsigned d_ = 4000u + __builtin_amdgcn_readfirstlane(peek(14336)) - (unsigned)tstart; \
    while ((unsigned)__builtin_amdgcn_s_memrealtime() - (unsigned)tstart < min(d_, 9000u)) __builtin_amdgcn_s_sleep(4); } } while (0)
DEVI int fresh_lane() { int l; asm volatile("v_mbcnt_lo_u32_b32 %0, -1, 0\n\tv_mbcnt_hi_u32_b32 %0, -1, %0" : "=v"(l)); return l; }
DEVI v4u make_sel(int lane, int bb) {
    const int c = lane & 15, tk = 8 * (c >> 2) + 4 * bb + (c & 3) - 8 * (lane >> 4);
    v4u s;
#pragma unroll
    for (int e = 0; e < 4; e++) s[e] = (tk == 2 * e ? 0x3f80u : 0u) | (tk == 2 * e + 1 ? 0x3f800000u : 0u);
    return s;
}
#define LANE_VARS const int lane = fresh_lane(), c = lane & 15, g = lane >> 4, q8 = lane >> 3, p8 = lane & 7; (void)c; (void)g; (void)q8; (void)p8
constexpr unsigned wslot(int n) { return (unsigned)(n & 3) * SLOT; }

#define ROT1 (idx & 7)
#define ROT2 0
#define XI_STEP 8
#define TOUCH_GATE 1
#define LB_SLEEP 8
#define LB_FIRST 10
#define FL_FIRST 20
#define FL_SLEEP 6
#define MIDV 64
#define X_FIRST 10
#define X_SLEEP 1
#define PDV 4
#define INFL 8
#define TSL 6
#define TW __builtin_amdgcn_s_sleep(TSL);
#define PACE 14
#define PACE2 8
#define XLV 3
#define LGATE 84
#define TGN 36
#define EGATE 100
#define WS_SHIFT 0
#define SLEEP0 12
constexpr unsigned CODE_LINES = 288;
template <int OFF> DEVI void reldA(v4u& d, unsigned voff, v4u srd, unsigned so) {
    asm volatile("buffer_load_dwordx4 %0, %1, %2, %3 offen offset:%4 sc1" : "+v"(d) : "v"(voff), "s"(srd), "s"(so), "n"(OFF) : "memory");
}
template <int OFF> DEVI void stA(const v4u& d, unsigned voff, v4u srd, unsigned so) {
    asm volatile("buffer_store_dwordx4 %0, %1, %2, %3 offen offset:%4 sc1\n\ts_nop 1" ::"v"(d), "v"(voff), "s"(srd), "s"(so), "n"(OFF) : "memory");
}
template <int OFF> DEVI void stX(const v4u& d, unsigned voff, v4u srd, unsigned so) {
    asm volatile("buffer_store_dwordx4 %0, %1, %2, %3 offen offset:%4 sc1" ::"v"(d), "v"(voff), "s"(srd), "s"(so), "n"(OFF) : "memory");
}
DEVI void touch(unsigned& sink, const void* tp, unsigned to) { asm volatile("global_load_dword %0, %1, %2" : "+v"(sink) : "v"(to), "s"(tp) : "memory"); }

DEVI v4u make_srd(const void* base) { const unsigned long long a = (unsigned long long)base; return v4u{(unsigned)a, (unsigned)(a >> 32) & 0xffffu, 0x7fffffffu, 0x00020000u}; }
DEVI void dma4(v4u srd, unsigned ve, unsigned vo, unsigned s0, unsigned s1, unsigned s2, unsigned s3, unsigned ldst) {
    asm volatile("s_mov_b32 m0, %7\n\ts_nop 0\n\tbuffer_load_dwordx4 %0, %2, %3 offen lds\n\tbuffer_load_dwordx4 %1, %2, %4 offen offset:1024 lds\n\t"
                 "buffer_load_dwordx4 %0, %2, %5 offen offset:2048 lds\n\tbuffer_load_dwordx4 %1, %2, %6 offen offset:3072 lds"
                 ::"v"(ve), "v"(vo), "s"(srd), "s"(s0), "s"(s1), "s"(s2), "s"(s3), "s"(ldst) : "memory");
}
DEVI void dma1(v4u srd, unsigned v, unsigned so, unsigned ldst) {
    asm volatile("s_mov_b32 m0, %3\n\ts_nop 0\n\tbuffer_load_dwordx4 %0, %1, %2 offen lds" ::"v"(v), "s"(srd), "s"(so), "s"(ldst) : "memory");
}
template <int H, int NEG> DEVI f32x2 bmul(f32x2 a, f32x2 b) {
    f32x2 d; asm("v_pk_mul_f32 %0, %1, %2 op_sel:[%3,0] op_sel_hi:[%3,1] neg_lo:[%4,0] neg_hi:[%4,0]" : "=v"(d) : "v"(a), "v"(b), "n"(H), "n"(NEG)); return d;
}
template <int H, int NEG> DEVI f32x2 bfma(f32x2 a, f32x2 b, f32x2 c) {
    f32x2 d; asm("v_pk_fma_f32 %0, %1, %2, %3 op_sel:[%4,0,0] op_sel_hi:[%4,1,1] neg_lo:[%5,0,0] neg_hi:[%5,0,0]" : "=v"(d) : "v"(a), "v"(b), "v"(c), "n"(H), "n"(NEG)); return d;
}
DEVI f32x2 cmadp(f32x2 a, f32x2 h, f32x2 u) {
    f32x2 t, d;
    asm("v_pk_fma_f32 %0, %1, %2, %3 op_sel:[0,0,0] op_sel_hi:[0,1,1]" : "=v"(t) : "v"(h), "v"(a), "v"(u));
    asm("v_pk_fma_f32 %0, %1, %2, %3 op_sel:[1,0,0] op_sel_hi:[0,0,1] neg_lo:[1,0,0]" : "=v"(d) : "v"(a), "v"(f32x2{h.y, h.y}), "v"(t));
    return d;
}
DEVI void mma_c(f32x4& d, const v4u& a, const v4u& b, const f32x4& c) { asm volatile("v_mfma_f32_16x16x32_bf16 %0, %1, %2, %3" : "=&v"(d) : "v"(a), "v"(b), "v"(c)); }
DEVI unsigned cvtv(float a, float b) { unsigned d; asm volatile("v_cvt_pk_bf16_f32 %0, %1, %2" : "=v"(d) : "v"(a), "v"(b)); return d; }

__global__ __launch_bounds__(512, 2) void fused(Params p) {
    const unsigned char* pc0;
    asm volatile("s_getpc_b64 %0" : "=s"(pc0));
    const unsigned long long tstart = __builtin_amdgcn_s_memrealtime();
    const int w = __builtin_amdgcn_readfirstlane(threadIdx.x >> 6);
    const int b = blockIdx.x, xcd = b & 7, idx = b >> 3;
    const int i = 8 * (xcd & 1) + (idx & 7), kq = xcd >> 1, q = idx >> 3, j = 4 * kq + q;
    const bool heavy = q != 3;
    const int N1 = heavy ? 20 : 12;
    const int lw = w & 1, pr = w & 3;
    const int rot = ROT1;
    const __amdgpu_buffer_rsrc_t rws = __builtin_amdgcn_make_buffer_rsrc(p.ws, 0, WS_END, 0x00020000);
    auto unit_of = [&](int v) { return v < 4 ? 7 * kq + v : v < 8 ? 28 + 8 * ((v >> 1) & 1) + 2 * kq + (v & 1) : 7 * kq + v - 4; };
    const unsigned sl_lo = L_P + (unsigned)pr * SLOT;
    const unsigned xo = WS_XB + (unsigned)((i * 4 + q) * 4) * 36864u + pr * 3072;
    const unsigned fo = WS_FLAG + (unsigned)(i * 16 + j) * 512u;

    if (w >= 4) {
        LANE_VARS;
        STAMP_INIT();
        STAMP(0);
        const unsigned swz = (unsigned)((p8 ^ (q8 >> 1)) << 4);
        const unsigned fa = F_DONE + (lane & 3) * 4;
        unsigned sink = 0, tg;
        auto poll16 = [&](int which) __attribute__((always_inline)) {
            wait_ge(F_OUT + (lane & 3) * 4, which + 1);
            STAMP(16);
            const unsigned pf = WS_FLAG + (unsigned)(i * 16 + 4 * kq + (lane & 3)) * 512u + which * 128 + ((lane >> 2) & 3) * 8;
            NOUNROLL for (int spin = 0; spin < SPIN_MAX; spin++) {
                fence_c();
                const v2u f = __builtin_amdgcn_raw_buffer_load_b64(rws, pf, 0, 16);
                const unsigned bad = (unsigned)__builtin_amdgcn_ballot_w64(f.x != tg || f.y != ~tg);
                if (bad == 0) break;
                if ((bad & 0x7777u) == 0) { if (which) break; postc<1>(F_RDY, lane); }
                __builtin_amdgcn_s_sleep(FL_SLEEP);
            }
            postc<2>(F_RDY + which * 4, lane);
            STAMP(17);
        };
        if (w < 6) {
            unsigned ffull = F_FULL + lw * 4;
            asm volatile("" : "+v"(ffull));
            {
                const unsigned voffS = (8 * (q8 >> 2) + (q8 & 3)) * 1024 + swz;
                const v4u srd = make_srd((const unsigned char*)(lw ? p.Bim : p.Bre) + (size_t)(32 * j) * 1024);
                NOUNROLL for (int n = 2; n < 8; n++) {
                    const unsigned so = 128 * ((n + rot) & 7);
                    dma4(srd, voffS, voffS ^ 64u, so, so + 15360, so + 2048, so + 17408, L_W + (n >> 1) * SLOT + lw * 8192 + (n & 1) * 4096);
                    if (n == 3) {
                        touch(sink, p.x, (128 * i + 8 * j + 4 * lw) * 1024 + (lane & 31) * 128);
                        touch(sink, lw ? p.Bim : p.Bre, (32 * j + 2 * i) * 1024 + c * 128);
                        bar_l();
                    }
                    if (n == 4) { waitv<6>(); postc<2>(ffull, lane); }
                    if (n >= 5) { waitv<4>(); postv(ffull, lane, n >> 1); }
                }
                waitv<0>(); postc<4>(ffull, lane);
                asm volatile("" ::"v"(sink));
            }
            STAMP(1);
            const unsigned voffG = q8 * (FF * 2) + (((p8 >> 1) ^ ((q8 >> 1) & 1)) << 5) + ((p8 & 1) << 4), voffG1 = voffG ^ 64u;
            const unsigned voffC = q8 * 1024 + swz, voffC1 = voffC ^ 64u;
            const unsigned voffD = q8 * 1024 + (((p8 >> 1) ^ (q8 >> 1) ^ (2 * (kq & 1))) << 5) + ((p8 & 1) << 4);
            const unsigned char* gb = (const unsigned char*)(lw ? p.wu : p.wg) + (7 * kq + q) * 128;
            unsigned thr = (lane >> 2) + 1;
            int credit = 0, cbase = 0;
            auto item = [&](int n, const unsigned char* b0, const unsigned char* b1, unsigned ve, unsigned vo, unsigned st) __attribute__((always_inline)) {
                v4u s0 = make_srd(b0), s1 = make_srd(b1);
                unsigned dst = ((n & 4) ? L_P : L_W) + (n & 3) * SLOT + lw * 8192, dst1 = dst + 4096;
                asm volatile("" : "+s"(s0), "+s"(s1), "+s"(dst), "+s"(dst1));
                int sp = LSPIN_MAX;
                NOUNROLL while (n - 7 > credit) {
                    const unsigned dn = peek(fa);
                    postv(ffull, lane, n - ((vm_outstanding() + 7) >> 3));
                    unsigned long long mk = __builtin_amdgcn_ballot_w64(dn >= thr);
                    mk &= mk >> 1; mk &= mk >> 2;
                    credit = __builtin_popcountll(mk & 0x1111111111111111ull) + cbase;
                    asm volatile("s_sub_i32 %0, %0, 1" : "+s"(sp)::"scc");
                    if (sp < 0) break;
                }
                waitv<INFL>();
                dma4(s0, ve, vo, 0, st - 1024, 2 * st - 2048, 3 * st - 3072, dst);
                postv(ffull, lane, n - (INFL + 7) / 8);
                waitv<INFL>();
                dma4(s1, ve, vo, 0, st - 1024, 2 * st - 2048, 3 * st - 3072, dst1);
            };
            wait_ge(F_XI + (lane & 3) * 4, 3);
            NOUNROLL for (int n = 4; n < N1; n++) {
                const int t = n - 4;
                const unsigned char* bp = gb + (size_t)(((t + rot) & 7) * 64) * (FF * 2) + (t >> 3) * 512;
                item(n, bp, bp + 32 * FF * 2, voffG, voffG1, 8 * FF * 2);
            }
            cbase = N1 - 8; thr += cbase;
            const unsigned char* dp = (const unsigned char*)p.wd + (size_t)(7 * kq) * 65536 + q * 256 + (lw ^ (kq >> 1)) * 128;
            NOUNROLL for (int v = 0; v < 11; v++) {
                if ((v & ~3) == 4) {
                    const unsigned char* cbp = (const unsigned char*)((v & 2) ? p.Cim : p.Cre) + (size_t)q * 131072 + (2 * kq + (v & 1)) * 128;
                    item(N1 + v, cbp + ((2 * lw) ^ kq) * 32768, cbp + ((2 * lw + 1) ^ kq) * 32768, voffC, voffC1, 8192);
                } else {
                    item(N1 + v, dp, dp + 32768, voffD, voffD, 8192);
                    dp += 65536;
                }
            }
            waitv<16>(); postv(ffull, lane, N1 + 9);
            waitv<8>(); postv(ffull, lane, N1 + 10);
            waitv<0>(); postv(ffull, lane, N1 + 11);
            tg = peek(L_TAG);
            STAMP(7);
        } else {
            unsigned tag0, prm0, prm1, prm2;
            const unsigned cto = min(idx * 9 + 4 * lw + p8, (int)CODE_LINES - 1) * 128;
            touch(sink, pc0, cto);
            NOUNROLL for (int t = heavy ? 4 : 0; t < 4; t++)
                touch(sink, lw ? p.wu : p.wg, (32 * i + (lane & 31)) * (FF * 2) + (7 * kq + min(2 * t + (lane >> 5), 6)) * 128);
            const unsigned po = 2 * (32 * j + 8 * (lane >> 4) + 4 * ((lane >> 1) & 1) + ((lane >> 2) & 3));
            asm volatile("global_load_ushort %0, %4, %5\n\tglobal_load_ushort %1, %4, %6\n\tglobal_load_ushort %2, %4, %7\n\tglobal_load_dword %3, %8, %9 sc1"
                         : "=&v"(prm0), "=&v"(prm1), "=&v"(prm2), "=&v"(tag0) : "v"(po), "s"(p.lre), "s"(p.lim), "s"(p.ldt), "v"(WS_EPOCH), "s"(p.ws) : "memory");
            *(LAS unsigned*)(uintptr_t)(F_DONE + (lane & 31) * 4) = 0u;
            bar_l();
            sts128(L_SEL + lw * 1024 + lane * 16, make_sel(lane, lw));
            asm volatile("s_waitcnt vmcnt(0)" : "+v"(prm0), "+v"(prm1), "+v"(prm2), "+v"(tag0)::"memory");
            tg = tag0 + 1u;
            const bool odd = lane & 1;
            const unsigned la = lane * 4;
            const float lr = bflo(prm0), li = bflo(prm1);
            float dt = __expf(bflo(prm2));
            dt = bflo(pk2(dt, dt));
            const float ex = __expf(dt * lr);
            const float fr = __builtin_amdgcn_fractf(dt * li * 0.15915494309189535f);
            const float ar = smul_t(ex, __builtin_amdgcn_cosf(fr)), ai = smul_t(ex, __builtin_amdgcn_sinf(fr));
            const cpx a1 = {ar, ai};
            const cpx a2 = cmul(a1, a1), a4 = cmul(a2, a2);
            const unsigned sg = odd ? 0u : 0x80000000u;
            auto sgn = [&](cpx v) { return cpx{v.x, __uint_as_float(__float_as_uint(v.y) ^ sg)}; };
            const cpx r8 = cmul(a4, a4), r16 = cmul(r8, r8), r32 = cmul(r16, r16), r64 = cmul(r32, r32);
            const cpx a32 = sgn(r32);
            {
                const cpx r = lw ? r32 : r8, rs = sgn(r);
                float v = odd ? r.y : r.x;
                sts32f(L_PW + lw * 1024 + la, odd ? 0.0f : 1.0f);
#pragma unroll
                for (int s = 1; s < 4; s++) { sts32f(L_PW + lw * 1024 + s * 256 + la, v); if (s < 3) v = lmad(rs, v, 0.0f); }
            }
            if (lw == 0) {
                const float nr = ar - 1.0f, den = smul_t(__builtin_amdgcn_rcpf(sfma(lr, lr, smul(li, li))), 2.0f);
                float co = smul(odd ? sfms(ai, lr, smul(nr, li)) : sfma(nr, lr, smul(ai, li)), den);
                sts32f(L_CST + la, odd ? a1.y : a1.x); sts32f(L_CST + 256 + la, odd ? a4.y : a4.x);
                const cpx a1s = sgn(a1);
#pragma unroll
                for (int t = 0; t < 5; t++) { sts32f(L_CST + (2 + t) * 256 + la, co); if (t < 4) co = lmad(a1s, co, 0.0f); }
                *(LAS unsigned*)(uintptr_t)L_TAG = tg;
            }
            post(F_COMM + lw * 4, 1);
            STAMP(1);
            cpx wn[8];
            {
                const cpx r128 = cmul(r64, r64), r256 = cmul(r128, r128);
                wn[0] = lw ? r128 : cpx{1.0f, 0.0f};
#pragma unroll
                for (int n = 1; n < 8; n++) wn[n] = cmul(wn[n - 1], r256);
#pragma unroll
                for (int n = 0; n < 8; n++) { wn[n] = sgn(wn[n]); asm volatile("" : "+v"(wn[n].x), "+v"(wn[n].y)); }
            }
            unsigned gro = WS_GRAN + j * 8192 + lane * 8, gro2 = gro + i * 512;
            unsigned to0 = lw ? min((unsigned)(lane & 31) * 0xb0000u + idx * 0x4000u, WS_END - 4) : (lane & 3) * 0x100000 + idx * 0x4000;
            unsigned to1 = (448 * kq + 28 * i + min(lane, 27)) * 1024 + (2 * q + lw) * 128, to2 = (128 * q + 8 * i + p8) * 1024 + (2 * kq + (q8 & 1)) * 128;
            asm volatile("" : "+v"(gro), "+v"(gro2), "+v"(to0), "+v"(to1), "+v"(to2));
            wait_ge(fa, 4);
            STAMP(4);
            if (lw) {
                float e[4];
#pragma unroll
                for (int s = 0; s < 4; s++) e[s] = lds32f(L_TOT + s * 256 + la);
                const float p2 = lmad(a32, e[0], e[1]), p3 = lmad(a32, p2, e[2]);
                __builtin_amdgcn_raw_buffer_store_b64(v2u{__float_as_uint(lmad(a32, p3, e[3])), tg}, rws, gro2, 0, 16);
                sts32f(L_PRE + la, 0.0f); sts32f(L_PRE + 256 + la, e[0]); sts32f(L_PRE + 512 + la, p2); sts32f(L_PRE + 768 + la, p3);
            }
            STAMP(5);
            touch(sink, lw ? (const void*)p.ws : (const void*)p.out, to0);
            __builtin_amdgcn_s_sleep(LB_FIRST);
            {
                const int top = i - 1 - lw;
                v2u gr[8] = {};
                NOUNROLL for (int spin = 0; spin < SPIN_MAX; spin++) {
                    fence_c();
#pragma unroll
                    for (int n = 0; n < 8; n++) gr[n] = __builtin_amdgcn_raw_buffer_load_b64(rws, gro, ((top - 2 * n) & 15) * 512, 16);
                    unsigned long long bad = 0;
#pragma unroll
                    for (int n = 0; n < 8; n++) if (top - 2 * n >= 0) bad |= __builtin_amdgcn_ballot_w64(gr[n].y != tg);
                    if (bad == 0) break;
                    __builtin_amdgcn_s_sleep(LB_SLEEP);
                }
                float h = 0;
#pragma unroll
                for (int n = 7; n >= 0; n--) if (top - 2 * n >= 0) h = lmad(wn[n], __uint_as_float(gr[n].x), h);
                sts32f(L_XCH + 256 + lw * 256 + la, h);
            }
            postc<4>(F_COMM + lw * 4, lane);
            STAMP(6);
            touch(sink, p.wd, to1);
            touch(sink, lw ? p.Cim : p.Cre, to2);
            waitv<0>();
            asm volatile("" ::"v"(sink));
            STAMP(7);
            poll16(lw);
        }
        f32x4 base[4];
        {
            const int d = 128 * q + 32 * kq + 4 * p8;
            const v2u dv = *(const GAS v2u*)(p.Dsk + d);
            const float df[4] = {bflo(dv.x), bfhi(dv.x), bflo(dv.y), bfhi(dv.y)};
            v2u xv[4];
#pragma unroll
            for (int e = 0; e < 4; e++) xv[e] = *(const GAS v2u*)(p.x + (size_t)(128 * i + 32 * pr + 8 * e + q8) * DM + d);
#pragma unroll
            for (int e = 0; e < 4; e++) {
                const unsigned tp = (32 * pr + 8 * e + 4 * ((q8 >> 1) & 1) + 2 * (q8 & 1) + (q8 >> 2)) * 4;
                const float rs = lds32f(L_RSTD + tp), nm = smul(lds32f(L_MEAN + tp), rs);
                const float xf[4] = {bflo(xv[e].x), bfhi(xv[e].x), bflo(xv[e].y), bfhi(xv[e].y)};
#pragma unroll
                for (int k = 0; k < 4; k++) base[e][k] = sfma(sfma(xf[k], rs, nm), df[k], xf[k]);
            }
        }
        STAMP(8);
        const unsigned t16 = ((tg & 0x3fffu) + 0x2000u) << 16;
        const unsigned xi = xo + kq * 36864u + lane * 16;
        auto bad3 = [&](const v4u* in) { return ((in[0].y ^ t16) | (in[0].w ^ t16) | (in[1].y ^ t16)) | ((in[1].w ^ t16) | (in[2].y ^ t16) | (in[2].w ^ t16)); };
        auto val = [&](const v4u* in, int k) { const unsigned dw = in[k / 6][2 * ((k / 3) & 1) + (k % 3 == 2)]; return (k % 3 == 1) ? bfhi(dw) : bflo(dw); };
        const unsigned sc = L_O + pr * 4096;
        unsigned wa = sc + (8 * (c >> 2) + 4 * (c & 1) + ((c >> 1) & 1)) * 128 + ((g ^ (c & 7)) << 4), ra = sc + q8 * 128 + ((p8 ^ (2 * (q8 & 1) + (q8 >> 2))) << 4);
        GAS float* op = (GAS float*)p.out + (size_t)(128 * i + 32 * pr + q8) * DM + 128 * q + 32 * kq + 4 * p8;
        asm volatile("" : "+v"(wa), "+v"(ra), "+v"(op));
        wait_ge(F_SENT + pr * 4, 3);
        __builtin_amdgcn_s_sleep(X_FIRST);
        v4u in[9];
        f32x4 own[4];
        NOUNROLL for (int spin = 0; spin < SPIN_MAX; spin++) {
            fence_c();
#pragma unroll
            for (int n = 0; n < 9; n++) in[n] = __builtin_amdgcn_raw_buffer_load_b128(rws, xi + (n / 3) * 12288 + (n % 3) * 1024, 0, 16);
            SB();
            wait_ge(F_SENT + pr * 4, 4);
#pragma unroll
            for (int e = 0; e < 4; e++) own[e] = lds128f(sc + e * 1024 + lane * 16);
            if (__builtin_amdgcn_ballot_w64((bad3(in) | bad3(in + 3) | bad3(in + 6)) > 0xffffu) == 0) break;
        }
        STAMP(11);
#pragma unroll
        for (int e = 0; e < 4; e++) {
            f32x4 t = own[e];
#pragma unroll
            for (int sr = 0; sr < 9; sr += 3) t += f32x4{val(in + sr, 4 * e), val(in + sr, 4 * e + 1), val(in + sr, 4 * e + 2), val(in + sr, 4 * e + 3)};
            own[e] = t;
        }
        asm volatile("" : "+v"(own[0]), "+v"(own[1]), "+v"(own[2]), "+v"(own[3]));
#pragma unroll
        for (int e = 0; e < 4; e++) sts128f((wa ^ (unsigned)((e >> 1) << 6)) + (e & 1) * 256, own[e]);
        fence_c();
        f32x4 ta[4];
#pragma unroll
        for (int e = 0; e < 4; e++) ta[e] = lds128f((ra ^ (unsigned)((e & 1) << 6)) + e * 1024);
        SB();
#pragma unroll
        for (int e = 0; e < 4; e++) *(GAS f32x4*)(op + e * 8 * DM) = base[e] + ta[e];
        if (b == 255 && w == 4 && lane == 0) __builtin_amdgcn_raw_buffer_store_b32(tg, rws, WS_EPOCH, 0, 16);
        STAMP(12); STAMP_DUMP();
        return;
    }
    constexpr int PD = PDV, XL = XLV, EG = EGATE;
    unsigned stall = 0; (void)stall;
    v4u xa[16][2];
#define AF(u, k) xa[2 * (u) + ((k) >> 1)][(k) & 1]
    v4u sws = make_srd(p.ws);
    unsigned oG = WS_A2 + (unsigned)(i * 44 + 7 * kq) * 16384u + w * 4096, oC = WS_A2 + (unsigned)(i * 44 + 28 + 2 * kq) * 16384u + w * 4096, oF = fo + w * 8;
    asm volatile("" : "+s"(sws), "+s"(oG), "+s"(oC), "+s"(oF));
    auto uoff = [&](int v) __attribute__((always_inline)) { return v < 4 ? oG + v * 16384 : v < 8 ? oC + (8 * ((v >> 1) & 1) + (v & 1)) * 16384 : oG + (v - 4) * 16384; };
    LANE_VARS;
    unsigned fdone = F_DONE + w * 4, l16 = lane * 16;
    asm volatile("" : "+v"(fdone), "+v"(l16));
    unsigned tgs = 0; int raised = 0;
    auto raise = [&](int level) __attribute__((always_inline)) {
        asm volatile("s_mov_b64 exec, 1\n\tbuffer_store_dwordx2 %0, off, %1, %2 offset:%3 sc1\n\ts_mov_b64 exec, -1" ::"v"(v2u{tgs, ~tgs}), "s"(sws), "s"(oF), "n"((level - 1) * 128) : "memory");
        post(fdone + (F_OUT - F_DONE), level);
        raised = level;

        if (level == 1) STAMP(17); else STAMP(18);
    };
    auto try_raise = [&](int level) __attribute__((always_inline)) {
        unsigned v; asm volatile("s_getreg_b32 %0, hwreg(HW_REG_IB_STS, 0, 4)" : "=s"(v)::"memory");
        if (__builtin_expect((int)(v + raised) == level - 1, 0)) raise(level);
    };
    unsigned oX[3];
#pragma unroll
    for (int sl = 1; sl < 4; sl++) { oX[sl - 1] = xo + (kq ^ sl) * 36864u + (sl - 1) * 12288; asm volatile("" : "+s"(oX[sl - 1])); }
    unsigned cbs[2], tbs[4];
    v4u bf[PD];
    const unsigned ffl = F_FULL + (lane & 1) * 4;
    auto set2 = [&](auto kk) __attribute__((always_inline)) {
        constexpr int k = decltype(kk)::value;
        if constexpr (k == 0) tbs[0] = L_P + ((l16 << 1) & 0x780) + ((l16 >> 1) & 0x18) + (((l16 >> 2) & 0x40) | ((l16 >> 2) & 0x20));
        else if constexpr (k < 4) tbs[k] = tbs[0] ^ (unsigned)(k << 5);
        else if constexpr (k == 4) cbs[0] = L_W + ((l16 << 3) & 0x780) + (((l16 >> 4) ^ (l16 >> 1)) & 0x70);
        else cbs[1] = cbs[0] ^ 64u;
        if constexpr (k < 4) asm volatile("" : "+v"(tbs[k])); else asm volatile("" : "+v"(cbs[k - 4]));
    };
    auto prime2 = [&](auto mm) __attribute__((always_inline)) { constexpr int m = decltype(mm)::value; const v2u lo = ldstr(tbs[m]), hi = ldstr(tbs[m] + 2048); bf[m] = v4u{lo.x, lo.y, hi.x, hi.y}; };
    {
        STAMP_INIT();
        STAMP(0);
        unsigned cb[2];
        cb[0] = (unsigned)c * 128u + (unsigned)((g ^ (c >> 1)) << 4); cb[1] = cb[0] ^ 64u;
        f32x4 eX;
        unsigned tb[4];
        f32x4 nm4[2];
        {
            const unsigned voffX = (8 * (q8 >> 2) + 4 * (q8 & 1) + ((q8 >> 1) & 1)) * 1024 + ((p8 ^ (q8 >> 1)) << 4), voffX1 = voffX ^ 64u;
            const v4u srd = make_srd((const unsigned char*)p.x + (size_t)(128 * i + 32 * w) * 1024);
            auto issue_x = [&](int ln) __attribute__((always_inline)) {
                const unsigned s0 = ((ln + rot) & 7) * 128;
                dma4(srd, voffX, voffX1, s0, s0 + 15360, s0, s0 + 15360, sl_lo + (ln & 3) * 4096);
            };
            {
                const unsigned voffS = (8 * (q8 >> 2) + (q8 & 3)) * 1024 + ((p8 ^ (q8 >> 1)) << 4), so = 128 * (((w >> 1) + rot) & 7);
                dma4(make_srd((const unsigned char*)(lw ? p.Bim : p.Bre) + (size_t)(32 * j) * 1024), voffS, voffS ^ 64u, so, so + 15360, so + 2048, so + 17408, L_W + lw * 8192 + (w >> 1) * 4096);
            }
#pragma unroll
            for (int ln = 0; ln < XL; ln++) issue_x(ln);
            v4u ones = {0x3f803f80u, 0x3f803f80u, 0x3f803f80u, 0x3f803f80u};
            unsigned xl[2] = {cb[0] + sl_lo, cb[1] + sl_lo};
            cb[0] += L_W; cb[1] += L_W;
            asm volatile("" : "+v"(ones), "+v"(cb[0]), "+v"(cb[1]), "+v"(xl[0]), "+v"(xl[1]));
            f32x4 sm[2], sq[2], bsum;
            v4u bx[4];
            unsigned cbw[2] = {cb[0] + (w >> 1) * 8192 + (w & 1) * 2048, cb[1] + (w >> 1) * 8192 + (w & 1) * 2048};
            asm volatile("" : "+v"(cbw[0]), "+v"(cbw[1]));
            f32x4 acc[4][2];
            unsigned fl = 0; bool lack = false;
            const unsigned fcs = (lane & 4) ? F_COMM + (lane & 1) * 4 : F_BS + (lane & 3) * 4;
            static_assert(PD == 4 && XL <= 3, "prologue schedule");
            waitv<4 * (XL - 1)>();
#pragma unroll
            for (int f = 0; f < 4; f++) xa[f >> 1][f & 1] = lds128(xl[f >> 1] + (f & 1) * 2048);
            asm volatile("s_barrier" ::: "memory");
            STAMP(1);
            sfor<-PD, 64>(LAMB(nn) {
                constexpr int n = decltype(nn)::value, m = n + PD, ks = n >> 2, ln = n >> 3;
                v4u cur;
                if constexpr (n >= 0) { cur = bf[n % PD]; mma<(n < 4)>(acc[n & 3][0], xa[ks][0], cur); SB(); }
                if constexpr (m > 0 && (m & 15) == 0 && m < 64) need_full(lack, ffl, (m >> 4) + 1);
                if constexpr (m < 64) bf[m % PD] = lds128(cb[(m >> 2) & 1] + wslot(m >> 4) + (((m >> 1) & 1) * 4 + ((m >> 3) & 1) * 2 + (m & 1)) * 2048);
                SB();
                if constexpr (n >= 0) { mma<(n < 4)>(acc[n & 3][1], xa[ks][1], cur); SB(); }
                if constexpr (n >= 0 && (n & 7) < 4 && ln + XL < 8)
                    dma1(srd, (n & 1) ? voffX1 : voffX, ((ln + XL + rot) & 7) * 128 + (n & 1) * 16384 + (n & 2) * 1024, sl_lo + ((ln + XL) & 3) * 4096 + (n & 3) * 1024);
                if constexpr (n >= 0 && (n & 7) == 4 && ln < 7) waitv<4 * ((ln + XL < 7 ? ln + XL : 7) - ln - 1)>();
                if constexpr (n >= 0 && (n & 7) >= 4 && ln < 7) xa[2 * ln + 2 + ((n >> 1) & 1)][n & 1] = lds128(xl[(n >> 1) & 1] + ((ln + 1) & 3) * 4096 + (n & 1) * 2048);
                if constexpr (n == 56) post(fdone + (F_XI - F_DONE), 3);
                if constexpr ((m & 15) == 10 && m < 48) fl = peek(ffl);
                if constexpr ((m & 15) == 14 && m < 48) lack = any_lt(fl, (m >> 4) + 2);
                if constexpr ((m & 15) == 15 && m < 63) post(fdone, (m >> 4) + 1);
                if constexpr (m < 64 && (m & 15) < 4) bx[m & 3] = lds128(cbw[m & 1] + wslot(m >> 4) + ((m >> 1) & 1) * 4096);
                if constexpr (n == 55) { sts32f(L_CST + 7 * 256 + c * 16 + (w & 1) * 8 + (w >> 1) * 4, bsum.x); post(fdone + (F_BS - F_DONE), 1); }
                if constexpr (n == 60) fl = peek(fcs);
                SB();
                if constexpr (n >= 0 && (n & 15) < 4) mma<(n == 0)>(bsum, ones, bx[n & 3]);
                if constexpr (n >= 0) { if constexpr (n & 1) mma<(n < 4)>(sq[(n >> 1) & 1], xa[ks][(n >> 1) & 1], xa[ks][(n >> 1) & 1]); else mma<(n < 4)>(sm[(n >> 1) & 1], ones, xa[ks][(n >> 1) & 1]); }
                SB();
            });
            need_full(any_lt(fl, 1), fcs, 1);
            const unsigned cl = L_CST + (l16 & 0xf0);
            const f32x4 ca = lds128f(cl), c4 = lds128f(cl + 256), co = lds128f(cl + 512), bs = lds128f(cl + 7 * 256), c8 = lds128f(L_PW + 256 + (l16 & 0xf0));
            mfma_done();
            STAMP(2); VAL(24, stall); stall = 0;
            {
                float dg[2];
#pragma unroll
                for (int bb = 0; bb < 2; bb++) {
                    nm4[bb] = sm[bb] * (-1.0f / DM);
                    const float s01 = (lane & 1) ? sq[bb].y : sq[bb].x, s23 = (lane & 1) ? sq[bb].w : sq[bb].z;
                    dg[bb] = __builtin_amdgcn_rsqf(fmaxf(((lane & 2) ? s23 : s01) * (1.0f / DM) - nm4[bb].x * nm4[bb].x, 0.0f) + 1e-5f);
                }
                if (g == (c >> 2)) {
                    const unsigned sa = L_RSTD + (32 * w + 8 * g + (c & 3)) * 4;
                    sts32f(sa, dg[0]); sts32f(sa + 16, dg[1]); sts32f(sa + L_MEAN, nm4[0].x); sts32f(sa + L_MEAN + 16, nm4[1].x);
                }
            }
            fence_c();
            const unsigned ra = L_RSTD + (32 * w + 8 * g) * 4;
            const f32x4 nt0 = lds128f(ra + L_MEAN), nt1 = lds128f(ra + L_MEAN + 16), rs0 = lds128f(ra), rs1 = lds128f(ra + 16);
            {
                const int q4 = c >> 2, p4 = c & 3;
                const unsigned tb1 = L_P + (unsigned)(8 * g + q4) * 128u + (unsigned)((((q4 >> 1) & 1) | ((g & 1) << 1)) << 5) + p4 * 8;
#pragma unroll
                for (int t = 0; t < 4; t++) { tb[t] = tb1 ^ (unsigned)(t << 5); asm volatile("" : "+v"(tb[t])); }
            }
            SB();
            STAMP(3);
            f32x4 ev;
            f32x2 sr[2][4], si[2][4];
#pragma unroll
            for (int st = 0; st < 2; st++) {
                const f32x2 b2 = st ? hi2(bs) : lo2(bs);
#pragma unroll
                for (int t = 0; t < 4; t++) {
                    const f32x4 nt = (t & 2) ? nt1 : nt0;
                    const f32x2 n2 = (t & 1) ? hi2(nt) : lo2(nt);
                    sr[st][t] = bfma<0, 0>(b2, n2, (t & 1) ? hi2(acc[st][t >> 1]) : lo2(acc[st][t >> 1]));
                    si[st][t] = bfma<1, 0>(b2, n2, (t & 1) ? hi2(acc[2 + st][t >> 1]) : lo2(acc[2 + st][t >> 1]));
                }
            }
            SB();
#pragma unroll
            for (int st = 0; st < 2; st++)
#pragma unroll
                for (int t = 0; t < 4; t++) {
                    const f32x4 rs = (t & 2) ? rs1 : rs0;
                    const f32x2 r2 = (t & 1) ? hi2(rs) : lo2(rs);
                    sr[st][t] *= r2; si[st][t] *= r2;
                }
            SB();
            const f32x2 a1[2] = {lo2(ca), hi2(ca)}, cf[2] = {lo2(co), hi2(co)};
#pragma unroll
            for (int t = 1; t < 4; t++) {
                f32x2 tr[2], ti[2];
#pragma unroll
                for (int st = 0; st < 2; st++) { tr[st] = bfma<1, 1>(a1[st], si[st][t - 1], sr[st][t]); ti[st] = bfma<1, 0>(a1[st], sr[st][t - 1], si[st][t]); }
                SB();
#pragma unroll
                for (int st = 0; st < 2; st++) { sr[st][t] = bfma<0, 0>(a1[st], sr[st][t - 1], tr[st]); si[st][t] = bfma<0, 0>(a1[st], si[st][t - 1], ti[st]); }
                SB();
            }
#pragma unroll
            for (int st = 0; st < 2; st++) {
                ev[2 * st] = sfma(c4[2 * st], sr[st][3].x, sfnma(c4[2 * st + 1], si[st][3].x, sr[st][3].y));
                ev[2 * st + 1] = sfma(c4[2 * st], si[st][3].x, sfma(c4[2 * st + 1], sr[st][3].x, si[st][3].y));
                eX[2 * st] = sr[st][3].x; eX[2 * st + 1] = si[st][3].x;
            }
            sts128f(L_SEG + w * 1024 + l16, ev);
            fence_c();
            f32x4 es[4];
#pragma unroll
            for (int s = 0; s < 4; s++) es[s] = lds128f(L_SEG + w * 1024 + s * 256 + (l16 & 0xf0));
            SB();
            unsigned ya = L_O + w * 4096 + l16;
            asm volatile("" : "+v"(ya), "+v"(eX));
#pragma unroll
            for (int st = 0; st < 2; st++) {
                f32x2 tr[4], ti[4];
#pragma unroll
                for (int t = 0; t < 4; t++) { tr[t] = bmul<1, 1>(cf[st], si[st][t]); ti[t] = bmul<1, 1>(cf[st], sr[st][t]); }
                SB();
#pragma unroll
                for (int t = 0; t < 4; t++) { tr[t] = bfma<0, 0>(cf[st], sr[st][t], tr[t]); ti[t] = bfma<0, 1>(cf[st], si[st][t], ti[t]); }
                SB();
                sts128(ya + st * 1024, v4u{pk2(tr[0]), pk2(tr[1]), pk2(tr[2]), pk2(tr[3])});
                sts128(ya + 2048 + st * 1024, v4u{pk2(ti[0]), pk2(ti[1]), pk2(ti[2]), pk2(ti[3])});
                SB();
                if (st == 0) {
#pragma unroll
                    for (int s = 1; s < 4; s++) { const f32x2 u0 = cmadp(lo2(c8), lo2(es[s - 1]), lo2(es[s])), u1 = cmadp(hi2(c8), hi2(es[s - 1]), hi2(es[s])); es[s] = f32x4{u0.x, u0.y, u1.x, u1.y}; }
                    sts128f(L_TOT + w * 256 + (l16 & 0xf0), es[3]);
                    post(fdone, 4);
                    f32x4 pin;
#pragma unroll
                    for (int k = 0; k < 4; k++) { const float lo = g == 1 ? es[0][k] : 0.0f, hi = g == 3 ? es[2][k] : es[1][k]; pin[k] = g >= 2 ? hi : lo; }
                    sts128f(L_SEG + w * 1024 + l16, pin);
                    SB();
                }
            }
        }
        STAMP(4);
        {
            const v4u sel0 = lds128(L_SEL + l16), sel1 = lds128(L_SEL + 1024 + l16);
            f32x4 ct[6];
            unsigned fl = 0;
            sfor<0, 68>(LAMB(kk) {
                constexpr int k = decltype(kk)::value, e = k - 4;
                if constexpr (k < 64) mma_c(ct[k % 6], (k & 1) ? sel1 : sel0, xa[k >> 2][(k >> 1) & 1], nm4[(k >> 1) & 1]);
                if constexpr (k >= 64 || k < 4) asm volatile("s_nop 1");
                if constexpr (e >= 0) {
                    xa[e >> 2][(e >> 1) & 1][2 * (e & 1)] = cvtv(ct[e % 6].x, ct[e % 6].y);
                    xa[e >> 2][(e >> 1) & 1][2 * (e & 1) + 1] = cvtv(ct[e % 6].z, ct[e % 6].w);
                }
                if constexpr (k == 40) fl = peek(ffl);
                if constexpr (k == 52) need_full(any_lt(fl, 5), ffl, 5);
                if constexpr (k >= 53 && k < 53 + PD) { constexpr int m = k - 53; const unsigned a = tb[(m >> 1) & 3] + (m & 1) * 8192; const v2u lo = ldstr(a), hi = ldstr(a + 512); bf[(64 + m) % PD] = v4u{lo.x, lo.y, hi.x, hi.y}; }
                SB();
            });
        }
        STAMP(5);
        unsigned go = oG + q * 16384, so = oC + (q >> 1) * 16384 + (q & 1) * 2048;
        asm volatile("" : "+s"(go), "+s"(so));
        f32x4 acc[4][2][2];
        f32x2 r12;
        unsigned fc = 0;
        auto gpass = [&](auto pp, auto n0, auto n1) __attribute__((always_inline)) {
            constexpr int pass = decltype(pp)::value;
            unsigned fl = 0, dn = 0; bool lack = false;
            sfor<decltype(n0)::value, decltype(n1)::value>(LAMB(nn) {
                constexpr int n = decltype(nn)::value, m = n + PD;
                constexpr bool pre = pass == 0 || m < 128;
                constexpr unsigned off = wslot(m >> 4) + (m & 1) * 8192 + ((m >> 3) & 1) * 4096;
                const v4u cur = bf[(n + 64 + 128 * pass) % PD];
                mma<(n < 8)>(acc[(n >> 1) & 3][n & 1][0], cur, xa[n >> 3][0]);
                SB();
                if constexpr ((m & 15) == 0) post(fdone, dn);
                if constexpr ((m & 15) == 0) need_full(lack, ffl, 5 + 8 * pass + (m >> 4));
                v2u lo, hi;
                if constexpr (pre) lo = ldstr(tb[(m >> 1) & 3] + off); else lo = ldstr(tbs[m & 3]);
                if constexpr (pass == 1 && n >= 69 && n < 81 && (n & 1)) set2(IC<(n - 69) / 2>{});
                if constexpr ((m & 15) == 10) fl = peek(ffl);
                if constexpr ((m & 15) == 15) lack = any_lt(fl, m == 127 && pass == 0 ? N1 - 7 : 6 + 8 * pass + (m >> 4));
                if constexpr ((m & 15) == 13) { dn = 5 + 8 * pass + (m >> 4); asm volatile("" : "+v"(dn)); }
                if constexpr (n == EG - 8 && pass == 1) fc = peek(F_RDY);
                if constexpr (n == EG - 2 && pass == 1) { if (__builtin_expect(any_lt(fc, 1), 0)) { wait_ge(F_RDY, 1); } fence_c(); }
                if constexpr (n >= EG && n < EG + 24 && pass == 1 && (n & 1) == 0) { constexpr int f = (n - EG) >> 1; reldA<(f & 3) * 1024>(AF(f >> 2, f & 3), l16, sws, uoff(f >> 2)); }
                if constexpr (pass == 1 && (n == 33 || n == 41 || n == 49 || n == 57)) try_raise(1);
                if constexpr (pass == 1 && n == 67) { if (raised < 1) { waitv<0>(); raise(1); } }
                if constexpr (n == 120 && pass == 0) fc = peek(ffl + (F_COMM - F_FULL));
                if constexpr (n == 121 && pass == 0) { const unsigned ra = L_RSTD + w * 128 + (((l16 >> 1) & 0x60) | ((l16 >> 2) & 0xc)); r12 = f32x2{lds32f(ra), lds32f(ra + 16)}; }
                SB();
                mma<(n < 8)>(acc[(n >> 1) & 3][n & 1][1], cur, xa[n >> 3][1]);
                SB();
                if constexpr (pre) hi = ldstr(tb[(m >> 1) & 3] + off + 512); else hi = ldstr(tbs[m & 3] + 2048);
                bf[n % PD] = v4u{lo.x, lo.y, hi.x, hi.y};
                if constexpr ((m & 57) == 57 && (pass == 0 || m < 64)) {
                    tb[(m >> 1) & 3] += (m & 64) ? L_P - L_W : L_W - L_P; asm volatile("" : "+v"(tb[(m >> 1) & 3]));
                }
                SB();
            });
        };
        auto gated = [&](auto ff) __attribute__((always_inline)) {
            constexpr int uu = decltype(ff)::value >> 1, bb = decltype(ff)::value & 1;
            const f32x4 h0 = acc[2 * uu][0][bb] * acc[2 * uu][1][bb], h1 = acc[2 * uu + 1][0][bb] * acc[2 * uu + 1][1][bb];
            return v4u{pk2(bmul<bb, 0>(r12, lo2(h0))), pk2(bmul<bb, 0>(r12, hi2(h0))), pk2(bmul<bb, 0>(r12, lo2(h1))), pk2(bmul<bb, 0>(r12, hi2(h1)))};
        };
        gpass(IC<0>{}, IC<0>{}, IC<128>{});
        {
            r12 = r12 * r12;
            mfma_done();
            STAMP(6); VAL(25, stall); stall = 0;
            sfor<0, 2>(LAMB(ff) { stA<decltype(ff)::value * 1024>(gated(ff), l16, sws, go); });
            SB();
            need_full(any_lt(fc, 4), ffl + (F_COMM - F_FULL), 4);
            const unsigned tg = peek(L_TAG);
            const unsigned cl = L_CST + (l16 & 0xf0);
            unsigned ya = L_O + w * 4096 + l16;
            asm volatile("" : "+v"(ya));
            const f32x4 h0 = lds128f(L_XCH + 256 + (l16 & 0xf0)), h1 = lds128f(L_XCH + 512 + (l16 & 0xf0));
            const f32x4 aw = lds128f(L_PW + 1024 + w * 256 + (l16 & 0xf0)), pb = lds128f(L_PRE + w * 256 + (l16 & 0xf0));
            const f32x4 ag = lds128f(L_PW + l16), pin = lds128f(L_SEG + w * 1024 + l16);
            const v4u sel[2] = {lds128(L_SEL + l16), lds128(L_SEL + 1024 + l16)};
            const v4u yr[2] = {lds128(ya), lds128(ya + 1024)}, yi[2] = {lds128(ya + 2048), lds128(ya + 3072)};
            const f32x4 c4 = lds128f(cl + 256);
            f32x4 cw[4];
            SB();
            sfor<2, 4>(LAMB(ff) { stA<decltype(ff)::value * 1024>(gated(ff), l16, sws, go); });
            tgs = __builtin_amdgcn_readfirstlane(tg);
#pragma unroll
            for (int t = 0; t < 4; t++) cw[t] = lds128f(cl + (3 + t) * 256);
            SB();
            f32x4 zr[2][2], zi[2][2];
#pragma unroll
            for (int st = 0; st < 2; st++)
#pragma unroll
                for (int bb = 0; bb < 2; bb++) { mma<true>(zr[st][bb], yr[st], sel[bb]); mma<true>(zi[st][bb], yi[st], sel[bb]); }
            SB();
            f32x2 hr[2], hi[2];
            {
                const f32x4 cy = h0 + h1;
                const f32x2 k0 = cmadp(lo2(ag), cmadp(lo2(aw), lo2(cy), lo2(pb)), lo2(pin)), k1 = cmadp(hi2(ag), cmadp(hi2(aw), hi2(cy), hi2(pb)), hi2(pin));
                const f32x4 hin = {k0.x, k0.y, k1.x, k1.y};
                float t0[2], t1[2];
#pragma unroll
                for (int st = 0; st < 2; st++) { t0[st] = sfnma(c4[2 * st + 1], hin[2 * st + 1], eX[2 * st]); t1[st] = sfma(c4[2 * st + 1], hin[2 * st], eX[2 * st + 1]); }
                SB();
#pragma unroll
                for (int st = 0; st < 2; st++) { hr[st] = f32x2{hin[2 * st], sfma(c4[2 * st], hin[2 * st], t0[st])}; hi[st] = f32x2{hin[2 * st + 1], sfma(c4[2 * st], hin[2 * st + 1], t1[st])}; }
                SB();
            }
#pragma unroll
            for (int st = 0; st < 2; st++) {
                f32x2 tr[4], ti[4];
#pragma unroll
                for (int t = 0; t < 4; t++) { const f32x2 wv = st ? hi2(cw[t]) : lo2(cw[t]); tr[t] = bmul<1, 1>(wv, hi[st]); ti[t] = bmul<1, 1>(wv, hr[st]); }
                SB();
#pragma unroll
                for (int t = 0; t < 4; t++) { const f32x2 wv = st ? hi2(cw[t]) : lo2(cw[t]); tr[t] = bfma<0, 0>(wv, hr[st], tr[t]); ti[t] = bfma<0, 1>(wv, hi[st], ti[t]); }
                SB();
                v4u fr = {pk2(tr[0]), pk2(tr[1]), pk2(tr[2]), pk2(tr[3])}, fi = {pk2(ti[0]), pk2(ti[1]), pk2(ti[2]), pk2(ti[3])};
                asm volatile("s_nop 1" : "+v"(fr), "+v"(fi));
#pragma unroll
                for (int bb = 0; bb < 2; bb++) { mma<false>(zr[st][bb], fr, sel[bb]); mma<false>(zi[st][bb], fi, sel[bb]); }
                SB();
            }
            mfma_done();
            stA<0>(pk8(zr[0][0], zr[1][0]), l16, sws, so); stA<0>(pk8(zi[0][0], zi[1][0]), l16, sws, so + 8 * 16384);
            stA<1024>(pk8(zr[0][1], zr[1][1]), l16, sws, so); stA<1024>(pk8(zi[0][1], zi[1][1]), l16, sws, so + 8 * 16384);
            SB();
            STAMP(7);
        }
        if (heavy) {
            gpass(IC<1>{}, IC<0>{}, IC<128>{});
            mfma_done();
            STAMP(8); VAL(26, stall); stall = 0;
            sfor<0, 4>(LAMB(ff) { stA<decltype(ff)::value * 1024>(gated(ff), l16, sws, go + 4 * 16384); });
            SB();
        }
        int q2 = q;
        asm volatile("" : "+s"(q2));
        if (q2 == 3) {
            waitv<0>(); raise(1); raise(2);
            sfor<0, 6>(set2);
            wait_ge(ffl, N1 + 1);
            sfor<0, PD>(prime2);
            wait_ge(F_RDY, 1);
            sfor<0, 12>(LAMB(ff) { constexpr int f = decltype(ff)::value; reldA<(f & 3) * 1024>(AF(f >> 2, f & 3), l16, sws, uoff(f >> 2)); });
#pragma unroll
            for (int u = 0; u < 3; u++) asm volatile("s_waitcnt vmcnt(0)" : "+v"(AF(u, 0)), "+v"(AF(u, 1)), "+v"(AF(u, 2)), "+v"(AF(u, 3))::"memory");
        }
    }
    STAMP(9);
    {
        f32x4 acc[8][2];
        v4u pk[3];
        unsigned fl = 0, fr = 0, dn = 0; bool lack = false;
        float tagf = 0;
        constexpr auto fv = [](int n) { return n < 48 ? n >> 4 : n < 108 ? 3 + (n - 48) / 12 : n < 144 ? 8 + ((n - 108) % 12) / 4 : 3 + (n - 144) / 4; };
        constexpr auto fk = [](int n) { return n < 48 ? (n >> 3) & 1 : n < 108 ? ((n - 48) % 12) / 6 : (n >> 1) & 1; };
        constexpr auto fd = [](int n) { return n < 48 ? n & 7 : n < 108 ? 2 + (n - 48) % 6 : n < 144 ? 2 * ((n - 108) / 12 + 1) + (n & 1) : n & 1; };
        constexpr auto first = [](int n) { return n >= 0 && (n < 48 ? (n & 15) == 0 : n < 108 ? (n - 48) % 12 == 0 : n < 120 && (n & 3) == 0); };
        constexpr int LG = LGATE;
        sfor<0, 176>(LAMB(nn) {
            constexpr int n = decltype(nn)::value, m = n + PD;
            constexpr int v = fv(n), ks = fk(n), dt = fd(n);
            constexpr int mv = fv(m), mk = fk(m), md = fd(m);
            constexpr bool mc = (mv & ~3) == 4;
            constexpr unsigned moff = (mv & 3) * SLOT + (mc ? md * 2048 : (md >> 2) * 8192 + mk * 4096);
            constexpr auto chk = [](int q) { return q == 16 ? 2 : q == 32 ? 4 : q == 60 ? 8 : q == 108 ? 11 : 0; };
            v4u cur; v2u lo, hi;
            if constexpr (n >= 0) {
                if constexpr (first(n)) {
                    constexpr int mid = n <= 32 ? 0 : 4 + ((n - 31) / 2 < 16 ? (n - 31) / 2 : 16), late = n <= LG ? 0 : n - LG < 12 ? n - LG : 12;
                    constexpr int NW = v == 0 ? 12 : v == 1 ? 8 : v == 2 ? 63 : v < 8 ? mid - 4 * (v - 2) + late : late - 4 * (v - 7);
                    static_assert(NW >= 0, "load schedule");
                    asm volatile("s_waitcnt vmcnt(%4)" : "+v"(AF(v & 7, 0)), "+v"(AF(v & 7, 1)), "+v"(AF(v & 7, 2)), "+v"(AF(v & 7, 3)) : "n"(NW) : "memory");
                }
                cur = bf[n % PD];
                mma<(n < 8)>(acc[dt][0], cur, AF(v & 7, 2 * ks));
                SB();
            }
            if constexpr (m > 0 && (m & 15) == 0 && m <= 48) post(fdone, dn);
            if constexpr (chk(m) > 0) need_full(lack, ffl, N1 + chk(m));
            if constexpr (m < 176) { if constexpr (mc) bf[m % PD] = lds128(cbs[mk] + moff); else lo = ldstr(tbs[md & 3] + moff); }
            if constexpr (chk(m + 6) > 0) fl = peek(ffl);
            if constexpr (chk(m + 1) > 0) lack = any_lt(fl, N1 + chk(m + 1));
            if constexpr ((m & 15) == 13 && m < 48) { dn = N1 + mv + 1; asm volatile("" : "+v"(dn)); }
            if constexpr (n == 17 || n == 21) try_raise(2);
            if constexpr (n == 20) fr = peek(F_RDY);
            if constexpr (n == 26) { if (raised < 2) { waitv<0>(); raise(2); } }
            if constexpr (n == 27) need_full(any_lt(fr, 2), F_RDY, 2);
            if constexpr (n >= 28 && n < 32) reldA<(n & 3) * 1024>(AF(3, n & 3), l16, sws, uoff(3));
            if constexpr (n >= 32 && n < 64 && (n & 1) == 0) { constexpr int f = (n - 32) >> 1; reldA<(f & 3) * 1024>(AF(4 + (f >> 2), f & 3), l16, sws, uoff(4 + (f >> 2))); }
            if constexpr (n == LG - 6) fr = peek(F_RDY + 4);
            if constexpr (n == LG - 1) need_full(any_lt(fr, 1), F_RDY + 4, 1);
            if constexpr (n >= LG && n < LG + 12) { constexpr int f = n - LG; reldA<(f & 3) * 1024>(AF(f >> 2, f & 3), l16, sws, uoff(8 + (f >> 2))); }
            if constexpr (n == 70) { tagf = __uint_as_float(((tgs & 0x3fffu) + 0x2000u) << 16); asm volatile("" : "+v"(tagf)); }
            SB();
            if constexpr (n >= 0) { mma<(n < 8)>(acc[dt][1], cur, AF(v & 7, 2 * ks + 1)); SB(); }
            if constexpr (m < 176 && !mc) { hi = ldstr(tbs[md & 3] + moff + 2048); bf[m % PD] = v4u{lo.x, lo.y, hi.x, hi.y}; }
            if constexpr (n >= 120 && n < 156) {
                constexpr int sl = (n - 108) / 12, r = (n - 108) % 12 - 2;
                auto val = [&](int k) { return acc[2 * sl + (k >> 3)][(k >> 2) & 1][k & 3]; };
                if constexpr (r >= 0 && r < 9 && r % 3 < 2) {
                    constexpr int gi = 2 * (r / 3) + r % 3;
                    pk[gi >> 1][2 * (gi & 1)] = cvtv(val(3 * gi), val(gi < 5 ? 3 * gi + 1 : 15));
                    pk[gi >> 1][2 * (gi & 1) + 1] = cvtv(val(gi < 5 ? 3 * gi + 2 : 15), tagf);
                }
                if constexpr (r >= 0 && r % 3 == 2)
                    stX<(r / 3) * 1024>(pk[r / 3], l16, sws, oX[sl - 1]);
                if constexpr (r == 9 && sl == 3) post(fdone + (F_SENT - F_DONE), 3);
            }
            if constexpr (n == 107) { STAMP(10); VAL(28, stall); stall = 0; }
            SB();
        });
        mfma_done();
        STAMP(11); VAL(29, stall);
#pragma unroll
        for (int e = 0; e < 4; e++) sts128f(L_O + w * 4096 + e * 1024 + l16, acc[e >> 1][e & 1]);
        post(fdone + (F_SENT - F_DONE), 4);
    }
    STAMP(14); STAMP_DUMP();
}

extern "C" void kernel_launch(void* const* d_in, const int* in_sizes, int n_in, void* d_out, int out_size, void* d_ws, size_t ws_size,
                              hipStream_t stream) {
    Params p;
    p.x = (const u16*)d_in[0]; p.lre = (const u16*)d_in[1]; p.lim = (const u16*)d_in[2];
    p.Bre = (const u16*)d_in[3]; p.Bim = (const u16*)d_in[4]; p.Cre = (const u16*)d_in[5]; p.Cim = (const u16*)d_in[6];
    p.ldt = (const u16*)d_in[7]; p.Dsk = (const u16*)d_in[8];
    p.wg = (const u16*)d_in[10]; p.wu = (const u16*)d_in[11]; p.wd = (const u16*)d_in[12];
    p.out = (float*)d_out; p.ws = (unsigned char*)d_ws + WS_SHIFT;
    (void)hipFuncSetAttribute((const void*)fused, hipFuncAttributeMaxDynamicSharedMemorySize, L_TOTAL);
    fused<<<256, 512, L_TOTAL, stream>>>(p);
}
```

```cpp
#include <hip/hip_runtime.h>
#include <stdint.h>

typedef unsigned short u16;
typedef unsigned v4u __attribute__((ext_vector_type(4)));
typedef unsigned v2u __attribute__((ext_vector_type(2)));
typedef short s16x4 __attribute__((ext_vector_type(4)));
typedef __bf16 bf16x2 __attribute__((ext_vector_type(2)));
typedef __bf16 bf16x8 __attribute__((ext_vector_type(8)));
typedef float f32x2 __attribute__((ext_vector_type(2)));
typedef float f32x4 __attribute__((ext_vector_type(4)));
#define GAS __attribute__((address_space(1)))
#define LAS __attribute__((address_space(3)))
#define DEVI __device__ __forceinline__
#define NOUNROLL _Pragma("nounroll")

#define SPIN_SLEEP 0
constexpr int DM = 512, FF = 1792;
constexpr unsigned WS_A2 = 0;
constexpr unsigned WS_XB = 16u * 44u * 16384u;
constexpr unsigned WS_GRAN = WS_XB + 256u * 24576u;
constexpr unsigned WS_FLAG = WS_GRAN + 16u * 16u * 512u;
constexpr unsigned WS_EPOCH = WS_FLAG + 1024u * 128u;
constexpr unsigned WS_END = WS_EPOCH + 256u;
constexpr unsigned SLOT = 16384;
constexpr unsigned L_RSTD = 0, L_MEAN = 512;
constexpr unsigned F_DONE = 1024, F_SENT = F_DONE + 16, F_FULL = F_DONE + 32, F_COMM = F_DONE + 40, F_RDY = F_DONE + 48, F_OUT = F_DONE + 64, F_XI = F_DONE + 80, F_BS = F_DONE + 96, L_TAG = F_DONE + 128;
constexpr unsigned L_CST = 1280;
constexpr unsigned L_XCH = L_CST + 8 * 256;
constexpr unsigned L_SEG = 4096, L_PW = 8192, L_TOT = L_PW + 2048, L_PRE = L_TOT + 1024, L_SEL = 12288;
constexpr unsigned L_P = SLOT, L_O = 5 * SLOT, L_W = 6 * SLOT, L_TOTAL = 10 * SLOT;
constexpr int SPIN_MAX = 1 << 18;
constexpr int LSPIN_MAX = 1 << 22;

struct Params {
    const u16 *x, *lre, *lim, *Bre, *Bim, *Cre, *Cim, *ldt, *Dsk, *wg, *wu, *wd;
    float* out;
    unsigned char* ws;
};

DEVI float bf2f(u16 v) { return __uint_as_float(((unsigned)v) << 16); }
DEVI float bflo(unsigned v) { return __uint_as_float(v << 16); }
DEVI float bfhi(unsigned v) { return __uint_as_float(v & 0xffff0000u); }
DEVI unsigned pk2(float a, float b) { f32x2 p = {a, b}; return __builtin_bit_cast(unsigned, __builtin_convertvector(p, bf16x2)); }
DEVI unsigned pk2(f32x2 p) { return __builtin_bit_cast(unsigned, __builtin_convertvector(p, bf16x2)); }
DEVI v4u pk8(f32x4 a, f32x4 b) { return v4u{pk2(a.x, a.y), pk2(a.z, a.w), pk2(b.x, b.y), pk2(b.z, b.w)}; }
DEVI float dot2(unsigned a, unsigned b, float c) { return __builtin_amdgcn_fdot2_f32_bf16(__builtin_bit_cast(bf16x2, a), __builtin_bit_cast(bf16x2, b), c, false); }
template <int CTRL> DEVI float dppf(float v) { return __builtin_bit_cast(float, __builtin_amdgcn_update_dpp(0, __builtin_bit_cast(int, v), CTRL, 0xf, 0xf, true)); }
DEVI f32x4 mfma(v4u a, v4u b, f32x4 c) { return __builtin_amdgcn_mfma_f32_16x16x32_bf16(__builtin_bit_cast(bf16x8, a), __builtin_bit_cast(bf16x8, b), c, 0, 0, 0); }
template <bool FIRST> DEVI void mma(f32x4& c, const v4u& a, const v4u& b) {
    if constexpr (FIRST) asm volatile("v_mfma_f32_16x16x32_bf16 %0, %1, %2, 0" : "=&v"(c) : "v"(a), "v"(b));
    else asm volatile("v_mfma_f32_16x16x32_bf16 %0, %1, %2, %0" : "+v"(c) : "v"(a), "v"(b));
}
DEVI void mfma_done() { __builtin_amdgcn_sched_barrier(0); asm volatile("s_nop 7"); __builtin_amdgcn_sched_barrier(0); }
DEVI v4u lds128(unsigned a) { return *(LAS v4u*)(uintptr_t)a; }
DEVI f32x4 lds128f(unsigned a) { return *(LAS f32x4*)(uintptr_t)a; }
DEVI float lds32f(unsigned a) { return *(LAS float*)(uintptr_t)a; }
DEVI void sts32f(unsigned a, float v) { *(LAS float*)(uintptr_t)a = v; }
DEVI void sts128(unsigned a, v4u v) { *(LAS v4u*)(uintptr_t)a = v; }
DEVI void sts128f(unsigned a, f32x4 v) { *(LAS f32x4*)(uintptr_t)a = v; }
DEVI v2u ldstr(unsigned a) { return __builtin_bit_cast(v2u, __builtin_amdgcn_ds_read_tr16_b64_v4i16((LAS s16x4*)(uintptr_t)a)); }
DEVI void fence_c() { asm volatile("" ::: "memory"); }
DEVI void bar_l() { asm volatile("s_waitcnt lgkmcnt(0)\n\ts_barrier" ::: "memory"); }
template <int N> DEVI void waitv() { asm volatile("s_waitcnt vmcnt(%0)" ::"n"(N) : "memory"); }
DEVI void dma16(const void* sbase, unsigned voff, unsigned ldst) {
    asm volatile("s_mov_b32 m0, %2\n\ts_nop 0\n\tglobal_load_lds_dwordx4 %0, %1" ::"v"(voff), "s"(sbase), "s"(ldst) : "memory");
}
DEVI void post(unsigned a, unsigned v) { fence_c(); *(LAS unsigned*)(uintptr_t)a = v; fence_c(); }
DEVI unsigned peek(unsigned a) { fence_c(); return *(LAS unsigned*)(uintptr_t)a; }
DEVI bool any_lt(unsigned v, unsigned need) { return __builtin_amdgcn_ballot_w64(v < need) != 0; }
DEVI void wait_ge(unsigned a, unsigned need) {
    int s = LSPIN_MAX;
    NOUNROLL while (any_lt(peek(a), need)) { asm volatile("s_sub_i32 %0, %0, 1" : "+s"(s) :: "scc"); if (s < 0) break; if (SPIN_SLEEP) __builtin_amdgcn_s_sleep(SPIN_SLEEP); }
    fence_c();
}
template <int V> DEVI void postm(unsigned a, int lane) {
    asm volatile("s_mov_b64 exec, %2\n\tds_max_u32 %0, %1\n\ts_mov_b64 exec, -1" ::"v"(a), "v"(lane), "n"(1 << V) : "memory");
}
DEVI unsigned vm_outstanding() {
    unsigned v; asm volatile("s_getreg_b32 %0, hwreg(HW_REG_IB_STS)" : "=s"(v)::"memory");
    return (v & 15u) | ((v >> 18) & 0x30u);
}
DEVI void postv(unsigned a, int lane, unsigned v) {
    asm volatile("s_lshl_b64 exec, 1, %2\n\tds_max_u32 %0, %1\n\ts_mov_b64 exec, -1" ::"v"(a), "v"(lane), "s"(v) : "memory", "scc");
}
template <int V> DEVI void postc(unsigned a, int lane) {
    asm volatile("s_mov_b64 exec, %2\n\tds_write_b32 %0, %1\n\ts_mov_b64 exec, -1" ::"v"(a), "v"(lane), "n"(1 << V) : "memory");
}
DEVI void need_full(bool lack, unsigned a, unsigned need) { if (__builtin_expect(lack, 0)) wait_ge(a, need); fence_c(); }
#define VAL(id, v)
DEVI float sfma(float a, float b, float c) { float d; asm("v_fma_f32 %0, %1, %2, %3" : "=v"(d) : "v"(a), "v"(b), "v"(c)); return d; }
DEVI float sfms(float a, float b, float c) { float d; asm("v_fma_f32 %0, %1, %2, -%3" : "=v"(d) : "v"(a), "v"(b), "v"(c)); return d; }
DEVI float sfnma(float a, float b, float c) { float d; asm("v_fma_f32 %0, -%1, %2, %3" : "=v"(d) : "v"(a), "v"(b), "v"(c)); return d; }
DEVI float sadd(float a, float b) { float d; asm("v_add_f32 %0, %1, %2" : "=v"(d) : "v"(a), "v"(b)); return d; }
DEVI float smul(float a, float b) { float d; asm("v_mul_f32 %0, %1, %2" : "=v"(d) : "v"(a), "v"(b)); return d; }
DEVI float smul_t(float a, float b) { float d; asm("s_nop 0\n\tv_mul_f32 %0, %1, %2" : "=v"(d) : "v"(a), "v"(b)); return d; }
struct cpx { float x, y; };
DEVI cpx cmul(cpx a, cpx b) { return cpx{sfms(a.x, b.x, smul(a.y, b.y)), sfma(a.x, b.y, smul(a.y, b.x))}; }
DEVI float lmad(cpx a, float h, float u) {
    float d;
    asm("v_fma_f32 %0, %1, %3, %4\n\ts_nop 0\n\tv_fmac_f32_dpp %0, %3, %2 quad_perm:[1,0,3,2] row_mask:0xf bank_mask:0xf" : "=&v"(d) : "v"(a.x), "v"(a.y), "v"(h), "v"(u));
    return d;
}
DEVI f32x2 dup(float v) { f32x2 d = {v, v}; asm("" : "+v"(d)); return d; }
DEVI f32x2 pfma(f32x2 a, f32x2 b, f32x2 c) { return __builtin_elementwise_fma(a, b, c); }
DEVI f32x2 lo2(f32x4 v) { return f32x2{v.x, v.y}; }
DEVI f32x2 hi2(f32x4 v) { return f32x2{v.z, v.w}; }
#define SB() __builtin_amdgcn_sched_barrier(0)
template <int V> struct IC { static constexpr int value = V; };
template <int B, int E, class F> DEVI void sfor(F&& f) { if constexpr (B < E) { f(IC<B>{}); sfor<B + 1, E>(f); } }
#define LAMB(nn) [&](auto nn) __attribute__((always_inline))
#define STAMP(id)
#define STAMP_INIT()
#define STAMP_DUMP()
DEVI int fresh_lane() { int l; asm volatile("v_mbcnt_lo_u32_b32 %0, -1, 0\n\tv_mbcnt_hi_u32_b32 %0, -1, %0" : "=v"(l)); return l; }
DEVI v4u make_sel(int lane, int bb) {
    const int c = lane & 15, tk = 8 * (c >> 2) + 4 * bb + (c & 3) - 8 * (lane >> 4);
    v4u s;
#pragma unroll
    for (int e = 0; e < 4; e++) s[e] = (tk == 2 * e ? 0x3f80u : 0u) | (tk == 2 * e + 1 ? 0x3f800000u : 0u);
    return s;
}
#define LANE_VARS const int lane = fresh_lane(), c = lane & 15, g = lane >> 4, q8 = lane >> 3, p8 = lane & 7; (void)c; (void)g; (void)q8; (void)p8
constexpr unsigned wslot(int n) { return (unsigned)(n & 3) * SLOT; }

#define ROT1 (idx & 7)
#define ROT2 0
#define XI_STEP 8
#define TOUCH_GATE 1
#define LB_SLEEP 8
#define LB_FIRST 4
#define T2_SLEEP 6
#define FL_FIRST 20
#define FL_SLEEP 6
#define MIDV 64
#define X_FIRST 14
#define X_SLEEP 1
#define PDV 4
#define INFL 8
#define TSL 6
#define TW __builtin_amdgcn_s_sleep(TSL);
#define PACE 14
#define PACE2 8
#define XLV 3
#define LGATE 84
#define TGN 37
#define EGATE 100
#define X_LAST 6
#define CLAG 4
#define CRING 8
#define PF_SLEEP 1
#define WS_SHIFT 0
#define SLEEP0 12
constexpr unsigned CODE_LINES = 295;
template <int OFF> DEVI void reldA(v4u& d, unsigned voff, v4u srd, unsigned so) {
    asm volatile("buffer_load_dwordx4 %0, %1, %2, %3 offen offset:%4 sc1" : "+v"(d) : "v"(voff), "s"(srd), "s"(so), "n"(OFF) : "memory");
}
template <int OFF> DEVI void stA(const v4u& d, unsigned voff, v4u srd, unsigned so) {
    asm volatile("buffer_store_dwordx4 %0, %1, %2, %3 offen offset:%4 sc1\n\ts_nop 1" ::"v"(d), "v"(voff), "s"(srd), "s"(so), "n"(OFF) : "memory");
}
template <int OFF> DEVI void stX(const v4u& d, unsigned voff, v4u srd, unsigned so) {
    asm volatile("buffer_store_dwordx4 %0, %1, %2, %3 offen offset:%4 sc1" ::"v"(d), "v"(voff), "s"(srd), "s"(so), "n"(OFF) : "memory");
}
DEVI void touch(unsigned& sink, const void* tp, unsigned to) { asm volatile("global_load_dword %0, %1, %2" : "+v"(sink) : "v"(to), "s"(tp) : "memory"); }

DEVI v4u make_srd(const void* base) { const unsigned long long a = (unsigned long long)base; return v4u{(unsigned)a, (unsigned)(a >> 32) & 0xffffu, 0x7fffffffu, 0x00020000u}; }
DEVI void dma4(v4u srd, unsigned ve, unsigned vo, unsigned s0, unsigned s1, unsigned s2, unsigned s3, unsigned ldst) {
    asm volatile("s_mov_b32 m0, %7\n\ts_nop 0\n\tbuffer_load_dwordx4 %0, %2, %3 offen lds\n\tbuffer_load_dwordx4 %1, %2, %4 offen offset:1024 lds\n\t"
                 "buffer_load_dwordx4 %0, %2, %5 offen offset:2048 lds\n\tbuffer_load_dwordx4 %1, %2, %6 offen offset:3072 lds"
                 ::"v"(ve), "v"(vo), "s"(srd), "s"(s0), "s"(s1), "s"(s2), "s"(s3), "s"(ldst) : "memory");
}
DEVI void dma1(v4u srd, unsigned v, unsigned so, unsigned ldst) {
    asm volatile("s_mov_b32 m0, %3\n\ts_nop 0\n\tbuffer_load_dwordx4 %0, %1, %2 offen lds" ::"v"(v), "s"(srd), "s"(so), "s"(ldst) : "memory");
}
template <int H, int NEG> DEVI f32x2 bmul(f32x2 a, f32x2 b) {
    f32x2 d; asm("v_pk_mul_f32 %0, %1, %2 op_sel:[%3,0] op_sel_hi:[%3,1] neg_lo:[%4,0] neg_hi:[%4,0]" : "=v"(d) : "v"(a), "v"(b), "n"(H), "n"(NEG)); return d;
}
template <int H, int NEG> DEVI f32x2 bfma(f32x2 a, f32x2 b, f32x2 c) {
    f32x2 d; asm("v_pk_fma_f32 %0, %1, %2, %3 op_sel:[%4,0,0] op_sel_hi:[%4,1,1] neg_lo:[%5,0,0] neg_hi:[%5,0,0]" : "=v"(d) : "v"(a), "v"(b), "v"(c), "n"(H), "n"(NEG)); return d;
}
DEVI f32x2 cmadp(f32x2 a, f32x2 h, f32x2 u) {
    f32x2 t, d;
    asm("v_pk_fma_f32 %0, %1, %2, %3 op_sel:[0,0,0] op_sel_hi:[0,1,1]" : "=v"(t) : "v"(h), "v"(a), "v"(u));
    asm("v_pk_fma_f32 %0, %1, %2, %3 op_sel:[1,0,0] op_sel_hi:[0,0,1] neg_lo:[1,0,0]" : "=v"(d) : "v"(a), "v"(f32x2{h.y, h.y}), "v"(t));
    return d;
}
DEVI void mma_c(f32x4& d, const v4u& a, const v4u& b, const f32x4& c) { asm volatile("v_mfma_f32_16x16x32_bf16 %0, %1, %2, %3" : "=&v"(d) : "v"(a), "v"(b), "v"(c)); }
DEVI void mma_t(f32x4& d, const v4u& a, const v4u& b, const f32x4& c) { asm volatile("v_mfma_f32_16x16x32_bf16 %0, %1, %2, %3" : "+v"(d) : "v"(a), "v"(b), "v"(c)); }
DEVI unsigned cvtv(float a, float b) { unsigned d; asm volatile("v_cvt_pk_bf16_f32 %0, %1, %2" : "=v"(d) : "v"(a), "v"(b)); return d; }

__global__ __launch_bounds__(512, 2) void fused(Params p) {
    const unsigned char* pc0;
    asm volatile("s_getpc_b64 %0" : "=s"(pc0));
    const int w = __builtin_amdgcn_readfirstlane(threadIdx.x >> 6);
    const int b = blockIdx.x, xcd = b & 7, idx = b >> 3;
    const int i = 8 * (xcd & 1) + (idx & 7), kq = xcd >> 1, q = idx >> 3, j = 4 * kq + q;
    const bool heavy = q != 3;
    const int N1 = heavy ? 20 : 12;
    const int lw = w & 1, pr = w & 3;
    const int rot = ROT1;
    const __amdgpu_buffer_rsrc_t rws = __builtin_amdgcn_make_buffer_rsrc(p.ws, 0, WS_END, 0x00020000);
    auto unit_of = [&](int v) { return v < 4 ? 7 * kq + v : v < 8 ? 28 + 8 * ((v >> 1) & 1) + 2 * kq + (v & 1) : 7 * kq + v - 4; };
    const unsigned sl_lo = L_P + (unsigned)pr * SLOT;
    const unsigned xo = WS_XB + (unsigned)((i * 4 + q) * 4) * 24576u + pr * 6144;
    const unsigned fo = WS_FLAG + (unsigned)(i * 16 + j) * 512u;

    if (w >= 4) {
        LANE_VARS;
        STAMP_INIT();
        STAMP(0);
        const unsigned swz = (unsigned)((p8 ^ (q8 >> 1)) << 4);
        const unsigned fa = F_DONE + (lane & 3) * 4;
        unsigned sink = 0, tg;
        auto poll16 = [&](int which) __attribute__((always_inline)) {
            wait_ge(F_OUT + (lane & 3) * 4, which + 1);
            STAMP(16);
            const unsigned pf = WS_FLAG + (unsigned)(i * 16 + 4 * kq + (lane & 3)) * 512u + which * 128 + ((lane >> 2) & 3) * 8;
            NOUNROLL for (int spin = 0; spin < SPIN_MAX; spin++) {
                fence_c();
                const v2u f = __builtin_amdgcn_raw_buffer_load_b64(rws, pf, 0, 16);
                const unsigned bad = (unsigned)__builtin_amdgcn_ballot_w64(f.x != tg || f.y != ~tg);
                if (bad == 0) break;
                if ((bad & 0x7777u) == 0) { if (which) break; postc<1>(F_RDY, lane); }
                __builtin_amdgcn_s_sleep(FL_SLEEP);
            }
            postc<2>(F_RDY + which * 4, lane);
            STAMP(17);
        };
        if (w < 6) {
            unsigned ffull = F_FULL + lw * 4;
            asm volatile("" : "+v"(ffull));
            {
                const unsigned voffS = (8 * (q8 >> 2) + (q8 & 3)) * 1024 + swz;
                const v4u srd = make_srd((const unsigned char*)(lw ? p.Bim : p.Bre) + (size_t)(32 * j) * 1024);
                NOUNROLL for (int n = 2; n < 8; n++) {
                    const unsigned so = 128 * ((n + rot) & 7);
                    dma4(srd, voffS, voffS ^ 64u, so, so + 15360, so + 2048, so + 17408, L_W + (n >> 1) * SLOT + lw * 8192 + (n & 1) * 4096);
                    if (n == 3) {
                        touch(sink, p.x, (128 * i + 8 * j + 4 * lw) * 1024 + (lane & 31) * 128);
                        touch(sink, lw ? p.Bim : p.Bre, (32 * j + 2 * i) * 1024 + c * 128);
                        bar_l();
                    }
                    if (n == 4) { waitv<6>(); postc<2>(ffull, lane); }
                    if (n >= 5) { waitv<4>(); postv(ffull, lane, n >> 1); }
                }
                waitv<0>(); postc<4>(ffull, lane);
                asm volatile("" ::"v"(sink));
            }
            STAMP(1);
            const unsigned voffG = q8 * (FF * 2) + (((p8 >> 1) ^ ((q8 >> 1) & 1)) << 5) + ((p8 & 1) << 4), voffG1 = voffG ^ 64u;
            const unsigned voffC = q8 * 1024 + swz, voffC1 = voffC ^ 64u;
            const unsigned voffD = q8 * 1024 + (((p8 >> 1) ^ (q8 >> 1) ^ (2 * (kq & 1))) << 5) + ((p8 & 1) << 4);
            const unsigned char* gb = (const unsigned char*)(lw ? p.wu : p.wg) + (7 * kq + q) * 128;
            unsigned thr = (lane >> 2) + 1;
            int credit = 0, cbase = 0;
            auto item = [&](int n, const unsigned char* b0, const unsigned char* b1, unsigned ve, unsigned vo, unsigned st) __attribute__((always_inline)) {
                v4u s0 = make_srd(b0), s1 = make_srd(b1);
                unsigned dst = ((n & 4) ? L_P : L_W) + (n & 3) * SLOT + lw * 8192, dst1 = dst + 4096;
                asm volatile("" : "+s"(s0), "+s"(s1), "+s"(dst), "+s"(dst1));
                int sp = LSPIN_MAX;
                NOUNROLL while (n - 7 > credit) {
                    const unsigned dn = peek(fa);
                    postv(ffull, lane, n - ((vm_outstanding() + 7) >> 3));
                    unsigned long long mk = __builtin_amdgcn_ballot_w64(dn >= thr);
                    mk &= mk >> 1; mk &= mk >> 2;
                    credit = __builtin_popcountll(mk & 0x1111111111111111ull) + cbase;
                    asm volatile("s_sub_i32 %0, %0, 1" : "+s"(sp)::"scc");
                    if (sp < 0) break;
                }
                waitv<INFL>();
                dma4(s0, ve, vo, 0, st - 1024, 2 * st - 2048, 3 * st - 3072, dst);
                postv(ffull, lane, n - (INFL + 7) / 8);
                waitv<INFL>();
                dma4(s1, ve, vo, 0, st - 1024, 2 * st - 2048, 3 * st - 3072, dst1);
            };
            wait_ge(F_XI + (lane & 3) * 4, 3);
            NOUNROLL for (int n = 4; n < N1; n++) {
                const int t = n - 4;
                const unsigned char* bp = gb + (size_t)(((t + rot) & 7) * 64) * (FF * 2) + (t >> 3) * 512;
                item(n, bp, bp + 32 * FF * 2, voffG, voffG1, 8 * FF * 2);
            }
            cbase = N1 - 8; thr += cbase;
            const unsigned char* dp = (const unsigned char*)p.wd + (size_t)(7 * kq) * 65536 + q * 256 + (lw ^ (kq >> 1)) * 128;
            NOUNROLL for (int v = 0; v < 11; v++) {
                if ((v & ~3) == 4) {
                    const unsigned char* cbp = (const unsigned char*)((v & 2) ? p.Cim : p.Cre) + (size_t)q * 131072 + (2 * kq + (v & 1)) * 128;
                    item(N1 + v, cbp + ((2 * lw) ^ kq) * 32768, cbp + ((2 * lw + 1) ^ kq) * 32768, voffC, voffC1, 8192);
                } else {
                    item(N1 + v, dp, dp + 32768, voffD, voffD, 8192);
                    dp += 65536;
                }
            }
            waitv<16>(); postv(ffull, lane, N1 + 9);
            waitv<8>(); postv(ffull, lane, N1 + 10);
            waitv<0>(); postv(ffull, lane, N1 + 11);
            tg = peek(L_TAG);
            STAMP(7);
        } else {
            unsigned tag0, prm0, prm1, prm2;
            const unsigned cto = min(idx * 11 + 4 * lw + p8, (int)CODE_LINES - 1) * 128;
            touch(sink, pc0, cto);
            touch(sink, lw ? (const void*)p.ws : (const void*)p.Dsk, lw ? ((lane & 4) ? WS_GRAN + (j * 16 + i) * 512 : fo) + (lane & 3) * 128 : p8 * 128);
            NOUNROLL for (int t = heavy ? 4 : 0; t < 4; t++)
                touch(sink, lw ? p.wu : p.wg, (32 * i + (lane & 31)) * (FF * 2) + (7 * kq + min(2 * t + (lane >> 5), 6)) * 128);
            const unsigned po = 2 * (32 * j + 8 * (lane >> 4) + 4 * ((lane >> 1) & 1) + ((lane >> 2) & 3));
            asm volatile("global_load_ushort %0, %4, %5\n\tglobal_load_ushort %1, %4, %6\n\tglobal_load_ushort %2, %4, %7\n\tglobal_load_dword %3, %8, %9 sc1"
                         : "=&v"(prm0), "=&v"(prm1), "=&v"(prm2), "=&v"(tag0) : "v"(po), "s"(p.lre), "s"(p.lim), "s"(p.ldt), "v"(WS_EPOCH), "s"(p.ws) : "memory");
            *(LAS unsigned*)(uintptr_t)(F_DONE + (lane & 31) * 4) = 0u;
            bar_l();
            sts128(L_SEL + lw * 1024 + lane * 16, make_sel(lane, lw));
            asm volatile("s_waitcnt vmcnt(0)" : "+v"(prm0), "+v"(prm1), "+v"(prm2), "+v"(tag0)::"memory");
            tg = tag0 + 1u;
            const bool odd = lane & 1;
            const unsigned la = lane * 4;
            const float lr = bflo(prm0), li = bflo(prm1);
            float dt = __expf(bflo(prm2));
            dt = bflo(pk2(dt, dt));
            const float ex = __expf(dt * lr);
            const float fr = __builtin_amdgcn_fractf(dt * li * 0.15915494309189535f);
            const float ar = smul_t(ex, __builtin_amdgcn_cosf(fr)), ai = smul_t(ex, __builtin_amdgcn_sinf(fr));
            const cpx a1 = {ar, ai};
            const cpx a2 = cmul(a1, a1), a4 = cmul(a2, a2);
            const unsigned sg = odd ? 0u : 0x80000000u;
            auto sgn = [&](cpx v) { return cpx{v.x, __uint_as_float(__float_as_uint(v.y) ^ sg)}; };
            const cpx r8 = cmul(a4, a4), r16 = cmul(r8, r8), r32 = cmul(r16, r16), r64 = cmul(r32, r32);
            const cpx a32 = sgn(r32);
            {
                const cpx r = lw ? r32 : r8, rs = sgn(r);
                float v = odd ? r.y : r.x;
                sts32f(L_PW + lw * 1024 + la, odd ? 0.0f : 1.0f);
#pragma unroll
                for (int s = 1; s < 4; s++) { sts32f(L_PW + lw * 1024 + s * 256 + la, v); if (s < 3) v = lmad(rs, v, 0.0f); }
            }
            if (lw == 0) {
                const float nr = ar - 1.0f, den = smul_t(__builtin_amdgcn_rcpf(sfma(lr, lr, smul(li, li))), 2.0f);
                float co = smul(odd ? sfms(ai, lr, smul(nr, li)) : sfma(nr, lr, smul(ai, li)), den);
                sts32f(L_CST + la, odd ? a1.y : a1.x); sts32f(L_CST + 256 + la, odd ? a4.y : a4.x);
                const cpx a1s = sgn(a1);
#pragma unroll
                for (int t = 0; t < 5; t++) { sts32f(L_CST + (2 + t) * 256 + la, co); if (t < 4) co = lmad(a1s, co, 0.0f); }
                *(LAS unsigned*)(uintptr_t)L_TAG = tg;
            }
            post(F_COMM + lw * 4, 1);
            STAMP(1);
            cpx wn[8];
            {
                const cpx r128 = cmul(r64, r64), r256 = cmul(r128, r128);
                wn[0] = lw ? r128 : cpx{1.0f, 0.0f};
#pragma unroll
                for (int n = 1; n < 8; n++) wn[n] = cmul(wn[n - 1], r256);
#pragma unroll
                for (int n = 0; n < 8; n++) { wn[n] = sgn(wn[n]); asm volatile("" : "+v"(wn[n].x), "+v"(wn[n].y)); }
            }
            unsigned gro = WS_GRAN + j * 8192 + lane * 8, gro2 = gro + i * 512;
            unsigned to0 = lw ? min((unsigned)(lane & 31) * 0x98000u + idx * 0x4000u, WS_END - 4) : (lane & 3) * 0x100000 + idx * 0x4000;
            unsigned to1 = (448 * kq + 28 * i + min(lane, 27)) * 1024 + (2 * q + lw) * 128, to2 = (128 * q + 8 * i + p8) * 1024 + (2 * kq + (q8 & 1)) * 128;
            unsigned po2 = WS_XB + (unsigned)((i * 4 + q) * 4 + kq) * 24576u + lane * 16;
            asm volatile("" : "+v"(gro), "+v"(gro2), "+v"(to0), "+v"(to1), "+v"(to2), "+v"(po2));
            wait_ge(F_XI + (lane & 3) * 4, heavy ? 1 : 0);
            touch(sink, p.wd, to1);
            touch(sink, lw ? p.Cim : p.Cre, to2);
            touch(sink, lw ? (const void*)p.ws : (const void*)p.out, to0);
            wait_ge(fa, 4);
            STAMP(4);
            if (lw) {
                float e[4];
#pragma unroll
                for (int s = 0; s < 4; s++) e[s] = lds32f(L_TOT + s * 256 + la);
                const float p2 = lmad(a32, e[0], e[1]), p3 = lmad(a32, p2, e[2]);
                __builtin_amdgcn_raw_buffer_store_b64(v2u{__float_as_uint(lmad(a32, p3, e[3])), tg}, rws, gro2, 0, 16);
                sts32f(L_PRE + la, 0.0f); sts32f(L_PRE + 256 + la, e[0]); sts32f(L_PRE + 512 + la, p2); sts32f(L_PRE + 768 + la, p3);
            }
            STAMP(5);
            __builtin_amdgcn_s_sleep(T2_SLEEP);
            if (!lw) {
                v4u none = {~0u, ~0u, ~0u, ~0u};
                NOUNROLL for (int n = 0; n < 24; n++) { __builtin_amdgcn_raw_buffer_store_b128(none, rws, po2, n * 1024, 16); __builtin_amdgcn_s_sleep(PF_SLEEP); }
            }
            __builtin_amdgcn_s_sleep(LB_FIRST);
            {
                const int top = i - 1 - lw;
                v2u gr[8] = {};
                NOUNROLL for (int spin = 0; spin < SPIN_MAX; spin++) {
                    fence_c();
#pragma unroll
                    for (int n = 0; n < 8; n++) gr[n] = __builtin_amdgcn_raw_buffer_load_b64(rws, gro, ((top - 2 * n) & 15) * 512, 16);
                    unsigned long long bad = 0;
#pragma unroll
                    for (int n = 0; n < 8; n++) if (top - 2 * n >= 0) bad |= __builtin_amdgcn_ballot_w64(gr[n].y != tg);
                    if (bad == 0) break;
                    __builtin_amdgcn_s_sleep(LB_SLEEP);
                }
                float h = 0;
#pragma unroll
                for (int n = 7; n >= 0; n--) if (top - 2 * n >= 0) h = lmad(wn[n], __uint_as_float(gr[n].x), h);
                sts32f(L_XCH + 256 + lw * 256 + la, h);
            }
            postc<4>(F_COMM + lw * 4, lane);
            STAMP(6);
            waitv<0>();
            asm volatile("" ::"v"(sink));
            if (w == 6 && lane == 0) __builtin_amdgcn_raw_buffer_store_b64(v2u{tg, ~tg}, rws, fo + 256, 0, 16);
            STAMP(7);
            if (lw) {
                wait_ge(F_OUT + (lane & 3) * 4, 1);
                __builtin_amdgcn_s_sleep(16);
                const unsigned pf = WS_FLAG + (unsigned)(i * 16 + 4 * (lane & 3) + q) * 512u + 256;
                NOUNROLL for (int spin = 0; spin < SPIN_MAX; spin++) {
                    fence_c();
                    const v2u f = __builtin_amdgcn_raw_buffer_load_b64(rws, pf, 0, 16);
                    if (__builtin_amdgcn_ballot_w64(f.x != tg || f.y != ~tg) == 0) break;
                    __builtin_amdgcn_s_sleep(16);
                }
            }
            poll16(lw);
        }
        f32x4 base[4];
        {
            const int d = 128 * q + 32 * kq + 4 * p8;
            const v2u dv = *(const GAS v2u*)(p.Dsk + d);
            const float df[4] = {bflo(dv.x), bfhi(dv.x), bflo(dv.y), bfhi(dv.y)};
            v2u xv[4];
#pragma unroll
            for (int e = 0; e < 4; e++) xv[e] = *(const GAS v2u*)(p.x + (size_t)(128 * i + 32 * pr + 8 * e + q8) * DM + d);
#pragma unroll
            for (int e = 0; e < 4; e++) {
                const unsigned tp = (32 * pr + 8 * e + 4 * ((q8 >> 1) & 1) + 2 * (q8 & 1) + (q8 >> 2)) * 4;
                const float rs = lds32f(L_RSTD + tp), nm = smul(lds32f(L_MEAN + tp), rs);
                const float xf[4] = {bflo(xv[e].x), bfhi(xv[e].x), bflo(xv[e].y), bfhi(xv[e].y)};
#pragma unroll
                for (int k = 0; k < 4; k++) base[e][k] = sfma(sfma(xf[k], rs, nm), df[k], xf[k]);
            }
        }
        STAMP(8);
        const unsigned xi = xo + kq * 24576u + lane * 16;
        const unsigned sc = L_O + pr * 4096;
        unsigned wa = sc + (8 * (c >> 2) + 4 * (c & 1) + ((c >> 1) & 1)) * 128 + ((g ^ (c & 7)) << 4), ra = sc + q8 * 128 + ((p8 ^ (2 * (q8 & 1) + (q8 >> 2))) << 4);
        GAS float* op = (GAS float*)p.out + (size_t)(128 * i + 32 * pr + q8) * DM + 128 * q + 32 * kq + 4 * p8;
        asm volatile("" : "+v"(wa), "+v"(ra), "+v"(op));
        wait_ge(F_SENT + pr * 4, 3);
        __builtin_amdgcn_s_sleep(X_FIRST);
        v4u in[6];
        f32x4 own[4];
        NOUNROLL for (int spin = 0; spin < SPIN_MAX; spin++) {
            fence_c();
#pragma unroll
            for (int n = 0; n < 6; n++) in[n] = __builtin_amdgcn_raw_buffer_load_b128(rws, xi + n * 1024, 0, 16);
            SB();
            wait_ge(F_SENT + pr * 4, 4);
#pragma unroll
            for (int e = 0; e < 4; e++) own[e] = lds128f(sc + e * 1024 + lane * 16);
            unsigned mx = 0;
#pragma unroll
            for (int n = 0; n < 6; n++) mx = max(mx, max(max(in[n].x, in[n].y), max(in[n].z, in[n].w)));
            if (__builtin_amdgcn_ballot_w64(mx == ~0u) == 0) break;
        }
        STAMP(11);
#pragma unroll
        for (int e = 0; e < 4; e++)
#pragma unroll
            for (int sr = 0; sr < 3; sr++) { const unsigned d0 = in[2 * sr + (e >> 1)][2 * (e & 1)], d1 = in[2 * sr + (e >> 1)][2 * (e & 1) + 1]; own[e] += f32x4{bflo(d0), bfhi(d0), bflo(d1), bfhi(d1)}; }
        asm volatile("" : "+v"(own[0]), "+v"(own[1]), "+v"(own[2]), "+v"(own[3]));
#pragma unroll
        for (int e = 0; e < 4; e++) sts128f((wa ^ (unsigned)((e >> 1) << 6)) + (e & 1) * 256, own[e]);
        fence_c();
        f32x4 ta[4];
#pragma unroll
        for (int e = 0; e < 4; e++) ta[e] = lds128f((ra ^ (unsigned)((e & 1) << 6)) + e * 1024);
        SB();
#pragma unroll
        for (int e = 0; e < 4; e++) *(GAS f32x4*)(op + e * 8 * DM) = base[e] + ta[e];
        if (b == 255 && w == 4 && lane == 0) __builtin_amdgcn_raw_buffer_store_b32(tg, rws, WS_EPOCH, 0, 16);
        STAMP(12); STAMP_DUMP();
        return;
    }
    constexpr int PD = PDV, XL = XLV, EG = EGATE;
    unsigned stall = 0; (void)stall;
    v4u xa[16][2];
#define AF(u, k) xa[2 * (u) + ((k) >> 1)][(k) & 1]
    v4u sws = make_srd(p.ws);
    unsigned oG = WS_A2 + (unsigned)(i * 44 + 7 * kq) * 16384u + w * 4096, oC = WS_A2 + (unsigned)(i * 44 + 28 + 2 * kq) * 16384u + w * 4096, oF = fo + w * 8;
    asm volatile("" : "+s"(sws), "+s"(oG), "+s"(oC), "+s"(oF));
    auto uoff = [&](int v) __attribute__((always_inline)) { return v < 4 ? oG + v * 16384 : v < 8 ? oC + (8 * ((v >> 1) & 1) + (v & 1)) * 16384 : oG + (v - 4) * 16384; };
    LANE_VARS;
    unsigned fdone = F_DONE + w * 4, l16 = lane * 16;
    asm volatile("" : "+v"(fdone), "+v"(l16));
    unsigned tgs = 0; int raised = 0;
    auto raise = [&](int level) __attribute__((always_inline)) {
        asm volatile("s_mov_b64 exec, 1\n\tbuffer_store_dwordx2 %0, off, %1, %2 offset:%3 sc1\n\ts_mov_b64 exec, -1" ::"v"(v2u{tgs, ~tgs}), "s"(sws), "s"(oF), "n"((level - 1) * 128) : "memory");
        post(fdone + (F_OUT - F_DONE), level);
        raised = level;

        if (level == 1) STAMP(17); else STAMP(18);
    };
    auto try_raise = [&](int level) __attribute__((always_inline)) {
        unsigned v; asm volatile("s_getreg_b32 %0, hwreg(HW_REG_IB_STS, 0, 4)" : "=s"(v)::"memory");
        if (__builtin_expect((int)(v + raised) == level - 1, 0)) raise(level);
    };
    unsigned oX[3];
#pragma unroll
    for (int sl = 1; sl < 4; sl++) { oX[sl - 1] = xo + (kq ^ sl) * 24576u + (sl - 1) * 2048; asm volatile("" : "+s"(oX[sl - 1])); }
    unsigned cbs[2], tbs[4];
    v4u bf[PD];
    const unsigned ffl = F_FULL + (lane & 1) * 4;
    auto set2 = [&](auto kk) __attribute__((always_inline)) {
        constexpr int k = decltype(kk)::value;
        if constexpr (k == 0) tbs[0] = L_P + ((l16 << 1) & 0x780) + ((l16 >> 1) & 0x18) + (((l16 >> 2) & 0x40) | ((l16 >> 2) & 0x20));
        else if constexpr (k < 4) tbs[k] = tbs[0] ^ (unsigned)(k << 5);
        else if constexpr (k == 4) cbs[0] = L_W + ((l16 << 3) & 0x780) + (((l16 >> 4) ^ (l16 >> 1)) & 0x70);
        else cbs[1] = cbs[0] ^ 64u;
        if constexpr (k < 4) asm volatile("" : "+v"(tbs[k])); else asm volatile("" : "+v"(cbs[k - 4]));
    };
    auto prime2 = [&](auto mm) __attribute__((always_inline)) { constexpr int m = decltype(mm)::value; const v2u lo = ldstr(tbs[m]), hi = ldstr(tbs[m] + 2048); bf[m] = v4u{lo.x, lo.y, hi.x, hi.y}; };
    {
        STAMP_INIT();
        STAMP(0);
        unsigned cb[2];
        cb[0] = (unsigned)c * 128u + (unsigned)((g ^ (c >> 1)) << 4); cb[1] = cb[0] ^ 64u;
        f32x4 eX;
        unsigned tb[4];
        f32x4 nm4[2];
        {
            const unsigned voffX = (8 * (q8 >> 2) + 4 * (q8 & 1) + ((q8 >> 1) & 1)) * 1024 + ((p8 ^ (q8 >> 1)) << 4), voffX1 = voffX ^ 64u;
            const v4u srd = make_srd((const unsigned char*)p.x + (size_t)(128 * i + 32 * w) * 1024);
            auto issue_x = [&](int ln) __attribute__((always_inline)) {
                const unsigned s0 = ((ln + rot) & 7) * 128;
                dma4(srd, voffX, voffX1, s0, s0 + 15360, s0, s0 + 15360, sl_lo + (ln & 3) * 4096);
            };
            {
                const unsigned voffS = (8 * (q8 >> 2) + (q8 & 3)) * 1024 + ((p8 ^ (q8 >> 1)) << 4), so = 128 * (((w >> 1) + rot) & 7);
                dma4(make_srd((const unsigned char*)(lw ? p.Bim : p.Bre) + (size_t)(32 * j) * 1024), voffS, voffS ^ 64u, so, so + 15360, so + 2048, so + 17408, L_W + lw * 8192 + (w >> 1) * 4096);
            }
#pragma unroll
            for (int ln = 0; ln < XL; ln++) issue_x(ln);
            v4u ones = {0x3f803f80u, 0x3f803f80u, 0x3f803f80u, 0x3f803f80u};
            unsigned xl[2] = {cb[0] + sl_lo, cb[1] + sl_lo};
            cb[0] += L_W; cb[1] += L_W;
            asm volatile("" : "+v"(ones), "+v"(cb[0]), "+v"(cb[1]), "+v"(xl[0]), "+v"(xl[1]));
            f32x4 sm[2], sq[2], bsum;
            v4u bx[4];
            unsigned cbw[2] = {cb[0] + (w >> 1) * 8192 + (w & 1) * 2048, cb[1] + (w >> 1) * 8192 + (w & 1) * 2048};
            asm volatile("" : "+v"(cbw[0]), "+v"(cbw[1]));
            f32x4 acc[4][2];
            unsigned fl = 0; bool lack = false;
            const unsigned fcs = (lane & 4) ? F_COMM + (lane & 1) * 4 : F_BS + (lane & 3) * 4;
            static_assert(PD == 4 && XL <= 3, "prologue schedule");
            waitv<4 * (XL - 1)>();
#pragma unroll
            for (int f = 0; f < 4; f++) xa[f >> 1][f & 1] = lds128(xl[f >> 1] + (f & 1) * 2048);
            asm volatile("s_barrier" ::: "memory");
            STAMP(1);
            sfor<-PD, 64>(LAMB(nn) {
                constexpr int n = decltype(nn)::value, m = n + PD, ks = n >> 2, ln = n >> 3;
                v4u cur;
                if constexpr (n >= 0) { cur = bf[n % PD]; mma<(n < 4)>(acc[n & 3][0], xa[ks][0], cur); SB(); }
                if constexpr (m > 0 && (m & 15) == 0 && m < 64) need_full(lack, ffl, (m >> 4) + 1);
                if constexpr (m < 64) bf[m % PD] = lds128(cb[(m >> 2) & 1] + wslot(m >> 4) + (((m >> 1) & 1) * 4 + ((m >> 3) & 1) * 2 + (m & 1)) * 2048);
                SB();
                if constexpr (n >= 0) { mma<(n < 4)>(acc[n & 3][1], xa[ks][1], cur); SB(); }
                if constexpr (n >= 0 && (n & 7) < 4 && ln + XL < 8)
                    dma1(srd, (n & 1) ? voffX1 : voffX, ((ln + XL + rot) & 7) * 128 + (n & 1) * 16384 + (n & 2) * 1024, sl_lo + ((ln + XL) & 3) * 4096 + (n & 3) * 1024);
                if constexpr (n >= 0 && (n & 7) == 4 && ln < 7) waitv<4 * ((ln + XL < 7 ? ln + XL : 7) - ln - 1)>();
                if constexpr (n >= 0 && (n & 7) >= 4 && ln < 7) xa[2 * ln + 2 + ((n >> 1) & 1)][n & 1] = lds128(xl[(n >> 1) & 1] + ((ln + 1) & 3) * 4096 + (n & 1) * 2048);
                if constexpr (n == TGN) post(fdone + (F_XI - F_DONE), 1);
                if constexpr (n == 56) post(fdone + (F_XI - F_DONE), 3);
                if constexpr ((m & 15) == 10 && m < 48) fl = peek(ffl);
                if constexpr ((m & 15) == 14 && m < 48) lack = any_lt(fl, (m >> 4) + 2);
                if constexpr ((m & 15) == 15 && m < 63) post(fdone, (m >> 4) + 1);
                if constexpr (m < 64 && (m & 15) < 4) bx[m & 3] = lds128(cbw[m & 1] + wslot(m >> 4) + ((m >> 1) & 1) * 4096);
                if constexpr (n == 55) { asm volatile("" : "+v"(bsum)); sts32f(L_CST + 7 * 256 + c * 16 + (w & 1) * 8 + (w >> 1) * 4, bsum.x); post(fdone + (F_BS - F_DONE), 1); }
                if constexpr (n == 60) fl = peek(fcs);
                SB();
                if constexpr (n >= 0 && (n & 15) < 4) mma<(n == 0)>(bsum, ones, bx[n & 3]);
                if constexpr (n >= 0) { if constexpr (n & 1) mma<(n < 4)>(sq[(n >> 1) & 1], xa[ks][(n >> 1) & 1], xa[ks][(n >> 1) & 1]); else mma<(n < 4)>(sm[(n >> 1) & 1], ones, xa[ks][(n >> 1) & 1]); }
                SB();
            });
            need_full(any_lt(fl, 1), fcs, 1);
            const unsigned cl = L_CST + (l16 & 0xf0);
            const f32x4 ca = lds128f(cl), c4 = lds128f(cl + 256), co = lds128f(cl + 512), bs = lds128f(cl + 7 * 256), c8 = lds128f(L_PW + 256 + (l16 & 0xf0));
            mfma_done();
            STAMP(2); VAL(24, stall); stall = 0;
            {
                float dg[2];
#pragma unroll
                for (int bb = 0; bb < 2; bb++) {
                    nm4[bb] = sm[bb] * (-1.0f / DM);
                    const float s01 = (lane & 1) ? sq[bb].y : sq[bb].x, s23 = (lane & 1) ? sq[bb].w : sq[bb].z;
                    dg[bb] = __builtin_amdgcn_rsqf(fmaxf(((lane & 2) ? s23 : s01) * (1.0f / DM) - nm4[bb].x * nm4[bb].x, 0.0f) + 1e-5f);
                }
                if (g == (c >> 2)) {
                    const unsigned sa = L_RSTD + (32 * w + 8 * g + (c & 3)) * 4;
                    sts32f(sa, dg[0]); sts32f(sa + 16, dg[1]); sts32f(sa + L_MEAN, nm4[0].x); sts32f(sa + L_MEAN + 16, nm4[1].x);
                }
            }
            fence_c();
            const unsigned ra = L_RSTD + (32 * w + 8 * g) * 4;
            const f32x4 nt0 = lds128f(ra + L_MEAN), nt1 = lds128f(ra + L_MEAN + 16), rs0 = lds128f(ra), rs1 = lds128f(ra + 16);
            {
                const int q4 = c >> 2, p4 = c & 3;
                const unsigned tb1 = L_P + (unsigned)(8 * g + q4) * 128u + (unsigned)((((q4 >> 1) & 1) | ((g & 1) << 1)) << 5) + p4 * 8;
#pragma unroll
                for (int t = 0; t < 4; t++) { tb[t] = tb1 ^ (unsigned)(t << 5); asm volatile("" : "+v"(tb[t])); }
            }
            SB();
            STAMP(3);
            f32x4 ev;
            f32x2 sr[2][4], si[2][4];
#pragma unroll
            for (int st = 0; st < 2; st++) {
                const f32x2 b2 = st ? hi2(bs) : lo2(bs);
#pragma unroll
                for (int t = 0; t < 4; t++) {
                    const f32x4 nt = (t & 2) ? nt1 : nt0;
                    const f32x2 n2 = (t & 1) ? hi2(nt) : lo2(nt);
                    sr[st][t] = bfma<0, 0>(b2, n2, (t & 1) ? hi2(acc[st][t >> 1]) : lo2(acc[st][t >> 1]));
                    si[st][t] = bfma<1, 0>(b2, n2, (t & 1) ? hi2(acc[2 + st][t >> 1]) : lo2(acc[2 + st][t >> 1]));
                }
            }
            SB();
#pragma unroll
            for (int st = 0; st < 2; st++)
#pragma unroll
                for (int t = 0; t < 4; t++) {
                    const f32x4 rs = (t & 2) ? rs1 : rs0;
                    const f32x2 r2 = (t & 1) ? hi2(rs) : lo2(rs);
                    sr[st][t] *= r2; si[st][t] *= r2;
                }
            SB();
            const f32x2 a1[2] = {lo2(ca), hi2(ca)}, cf[2] = {lo2(co), hi2(co)};
#pragma unroll
            for (int t = 1; t < 4; t++) {
                f32x2 tr[2], ti[2];
#pragma unroll
                for (int st = 0; st < 2; st++) { tr[st] = bfma<1, 1>(a1[st], si[st][t - 1], sr[st][t]); ti[st] = bfma<1, 0>(a1[st], sr[st][t - 1], si[st][t]); }
                SB();
#pragma unroll
                for (int st = 0; st < 2; st++) { sr[st][t] = bfma<0, 0>(a1[st], sr[st][t - 1], tr[st]); si[st][t] = bfma<0, 0>(a1[st], si[st][t - 1], ti[st]); }
                SB();
            }
#pragma unroll
            for (int st = 0; st < 2; st++) {
                ev[2 * st] = sfma(c4[2 * st], sr[st][3].x, sfnma(c4[2 * st + 1], si[st][3].x, sr[st][3].y));
                ev[2 * st + 1] = sfma(c4[2 * st], si[st][3].x, sfma(c4[2 * st + 1], sr[st][3].x, si[st][3].y));
                eX[2 * st] = sr[st][3].x; eX[2 * st + 1] = si[st][3].x;
            }
            sts128f(L_SEG + w * 1024 + l16, ev);
            fence_c();
            f32x4 es[4];
#pragma unroll
            for (int s = 0; s < 4; s++) es[s] = lds128f(L_SEG + w * 1024 + s * 256 + (l16 & 0xf0));
            SB();
            unsigned ya = L_O + w * 4096 + l16;
            asm volatile("" : "+v"(ya), "+v"(eX));
#pragma unroll
            for (int st = 0; st < 2; st++) {
                f32x2 tr[4], ti[4];
#pragma unroll
                for (int t = 0; t < 4; t++) { tr[t] = bmul<1, 1>(cf[st], si[st][t]); ti[t] = bmul<1, 1>(cf[st], sr[st][t]); }
                SB();
#pragma unroll
                for (int t = 0; t < 4; t++) { tr[t] = bfma<0, 0>(cf[st], sr[st][t], tr[t]); ti[t] = bfma<0, 1>(cf[st], si[st][t], ti[t]); }
                SB();
                sts128(ya + st * 1024, v4u{pk2(tr[0]), pk2(tr[1]), pk2(tr[2]), pk2(tr[3])});
                sts128(ya + 2048 + st * 1024, v4u{pk2(ti[0]), pk2(ti[1]), pk2(ti[2]), pk2(ti[3])});
                SB();
                if (st == 0) {
#pragma unroll
                    for (int s = 1; s < 4; s++) { const f32x2 u0 = cmadp(lo2(c8), lo2(es[s - 1]), lo2(es[s])), u1 = cmadp(hi2(c8), hi2(es[s - 1]), hi2(es[s])); es[s] = f32x4{u0.x, u0.y, u1.x, u1.y}; }
                    sts128f(L_TOT + w * 256 + (l16 & 0xf0), es[3]);
                    post(fdone, 4);
                    f32x4 pin;
#pragma unroll
                    for (int k = 0; k < 4; k++) { const float lo = g == 1 ? es[0][k] : 0.0f, hi = g == 3 ? es[2][k] : es[1][k]; pin[k] = g >= 2 ? hi : lo; }
                    sts128f(L_SEG + w * 1024 + l16, pin);
                    SB();
                }
            }
        }
        STAMP(4);
        {
            const v4u sel0 = lds128(L_SEL + l16), sel1 = lds128(L_SEL + 1024 + l16);
            unsigned fl = 0;
            f32x4 ct[CRING];
#pragma unroll
            for (int k = 0; k < CRING; k++) asm volatile("" : "=v"(ct[k]));
            sfor<0, 64 + CLAG>(LAMB(kk) {
                constexpr int k = decltype(kk)::value, e = k - CLAG;
                if constexpr (k < 64) mma_t(ct[k % CRING], (k & 1) ? sel1 : sel0, xa[k >> 2][(k >> 1) & 1], nm4[(k >> 1) & 1]);
                if constexpr (k >= 64 || k < 4) asm volatile("s_nop 1");
                if constexpr (e >= 0) {
                    xa[e >> 2][(e >> 1) & 1][2 * (e & 1)] = cvtv(ct[e % CRING].x, ct[e % CRING].y);
                    xa[e >> 2][(e >> 1) & 1][2 * (e & 1) + 1] = cvtv(ct[e % CRING].z, ct[e % CRING].w);
                }
                if constexpr (k == 40) fl = peek(ffl);
                if constexpr (k == 52) need_full(any_lt(fl, 5), ffl, 5);
                if constexpr (k >= 53 && k < 53 + PD) { constexpr int m = k - 53; const unsigned a = tb[(m >> 1) & 3] + (m & 1) * 8192; const v2u lo = ldstr(a), hi = ldstr(a + 512); bf[(64 + m) % PD] = v4u{lo.x, lo.y, hi.x, hi.y}; }
                SB();
            });
        }
        STAMP(5);
        unsigned go = oG + q * 16384, so = oC + (q >> 1) * 16384 + (q & 1) * 2048;
        asm volatile("" : "+s"(go), "+s"(so));
        f32x4 acc[4][2][2];
        f32x2 r12;
        unsigned fc = 0;
        auto gpass = [&](auto pp, auto n0, auto n1) __attribute__((always_inline)) {
            constexpr int pass = decltype(pp)::value;
            unsigned fl = 0, dn = 0; bool lack = false;
            sfor<decltype(n0)::value, decltype(n1)::value>(LAMB(nn) {
                constexpr int n = decltype(nn)::value, m = n + PD;
                constexpr bool pre = pass == 0 || m < 128;
                constexpr unsigned off = wslot(m >> 4) + (m & 1) * 8192 + ((m >> 3) & 1) * 4096;
                const v4u cur = bf[(n + 64 + 128 * pass) % PD];
                mma<(n < 8)>(acc[(n >> 1) & 3][n & 1][0], cur, xa[n >> 3][0]);
                SB();
                if constexpr ((m & 15) == 0) post(fdone, dn);
                if constexpr ((m & 15) == 0) need_full(lack, ffl, 5 + 8 * pass + (m >> 4));
                v2u lo, hi;
                if constexpr (pre) lo = ldstr(tb[(m >> 1) & 3] + off); else lo = ldstr(tbs[m & 3]);
                if constexpr (pass == 1 && n >= 69 && n < 81 && (n & 1)) set2(IC<(n - 69) / 2>{});
                if constexpr ((m & 15) == 10) fl = peek(ffl);
                if constexpr ((m & 15) == 15) lack = any_lt(fl, m == 127 && pass == 0 ? N1 - 7 : 6 + 8 * pass + (m >> 4));
                if constexpr ((m & 15) == 13) { dn = 5 + 8 * pass + (m >> 4); asm volatile("" : "+v"(dn)); }
                if constexpr (n == EG - 8 && pass == 1) fc = peek(F_RDY);
                if constexpr (n == EG - 2 && pass == 1) { if (__builtin_expect(any_lt(fc, 1), 0)) { wait_ge(F_RDY, 1); } fence_c(); }
                if constexpr (n >= EG && n < EG + 24 && pass == 1 && (n & 1) == 0) { constexpr int f = (n - EG) >> 1; reldA<(f & 3) * 1024>(AF(f >> 2, f & 3), l16, sws, uoff(f >> 2)); }
                if constexpr (pass == 1 && (n == 33 || n == 41 || n == 49 || n == 57)) try_raise(1);
                if constexpr (pass == 1 && n == 67) { if (raised < 1) { waitv<0>(); raise(1); } }
                if constexpr (n == 120 && pass == 0) fc = peek(ffl + (F_COMM - F_FULL));
                if constexpr (n == 121 && pass == 0) { const unsigned ra = L_RSTD + w * 128 + (((l16 >> 1) & 0x60) | ((l16 >> 2) & 0xc)); r12 = f32x2{lds32f(ra), lds32f(ra + 16)}; }
                SB();
                mma<(n < 8)>(acc[(n >> 1) & 3][n & 1][1], cur, xa[n >> 3][1]);
                SB();
                if constexpr (pre) hi = ldstr(tb[(m >> 1) & 3] + off + 512); else hi = ldstr(tbs[m & 3] + 2048);
                bf[n % PD] = v4u{lo.x, lo.y, hi.x, hi.y};
                if constexpr ((m & 57) == 57 && (pass == 0 || m < 64)) {
                    tb[(m >> 1) & 3] += (m & 64) ? L_P - L_W : L_W - L_P; asm volatile("" : "+v"(tb[(m >> 1) & 3]));
                }
                SB();
            });
        };
        auto gated = [&](auto ff) __attribute__((always_inline)) {
            constexpr int uu = decltype(ff)::value >> 1, bb = decltype(ff)::value & 1;
            const f32x4 h0 = acc[2 * uu][0][bb] * acc[2 * uu][1][bb], h1 = acc[2 * uu + 1][0][bb] * acc[2 * uu + 1][1][bb];
            return v4u{pk2(bmul<bb, 0>(r12, lo2(h0))), pk2(bmul<bb, 0>(r12, hi2(h0))), pk2(bmul<bb, 0>(r12, lo2(h1))), pk2(bmul<bb, 0>(r12, hi2(h1)))};
        };
        gpass(IC<0>{}, IC<0>{}, IC<128>{});
        {
            r12 = r12 * r12;
            mfma_done();
            STAMP(6); VAL(25, stall); stall = 0;
            sfor<0, 2>(LAMB(ff) { stA<decltype(ff)::value * 1024>(gated(ff), l16, sws, go); });
            SB();
            need_full(any_lt(fc, 4), ffl + (F_COMM - F_FULL), 4);
            const unsigned tg = peek(L_TAG);
            const unsigned cl = L_CST + (l16 & 0xf0);
            unsigned ya = L_O + w * 4096 + l16;
            asm volatile("" : "+v"(ya));
            const f32x4 h0 = lds128f(L_XCH + 256 + (l16 & 0xf0)), h1 = lds128f(L_XCH + 512 + (l16 & 0xf0));
            const f32x4 aw = lds128f(L_PW + 1024 + w * 256 + (l16 & 0xf0)), pb = lds128f(L_PRE + w * 256 + (l16 & 0xf0));
            const f32x4 ag = lds128f(L_PW + l16), pin = lds128f(L_SEG + w * 1024 + l16);
            const v4u sel[2] = {lds128(L_SEL + l16), lds128(L_SEL + 1024 + l16)};
            const v4u yr[2] = {lds128(ya), lds128(ya + 1024)}, yi[2] = {lds128(ya + 2048), lds128(ya + 3072)};
            const f32x4 c4 = lds128f(cl + 256);
            f32x4 cw[4];
            SB();
            sfor<2, 4>(LAMB(ff) { stA<decltype(ff)::value * 1024>(gated(ff), l16, sws, go); });
            tgs = __builtin_amdgcn_readfirstlane(tg);
#pragma unroll
            for (int t = 0; t < 4; t++) cw[t] = lds128f(cl + (3 + t) * 256);
            SB();
            f32x4 zr[2][2], zi[2][2];
#pragma unroll
            for (int st = 0; st < 2; st++)
#pragma unroll
                for (int bb = 0; bb < 2; bb++) { mma<true>(zr[st][bb], yr[st], sel[bb]); mma<true>(zi[st][bb], yi[st], sel[bb]); }
            SB();
            f32x2 hr[2], hi[2];
            {
                const f32x4 cy = h0 + h1;
                const f32x2 k0 = cmadp(lo2(ag), cmadp(lo2(aw), lo2(cy), lo2(pb)), lo2(pin)), k1 = cmadp(hi2(ag), cmadp(hi2(aw), hi2(cy), hi2(pb)), hi2(pin));
                const f32x4 hin = {k0.x, k0.y, k1.x, k1.y};
                float t0[2], t1[2];
#pragma unroll
                for (int st = 0; st < 2; st++) { t0[st] = sfnma(c4[2 * st + 1], hin[2 * st + 1], eX[2 * st]); t1[st] = sfma(c4[2 * st + 1], hin[2 * st], eX[2 * st + 1]); }
                SB();
#pragma unroll
                for (int st = 0; st < 2; st++) { hr[st] = f32x2{hin[2 * st], sfma(c4[2 * st], hin[2 * st], t0[st])}; hi[st] = f32x2{hin[2 * st + 1], sfma(c4[2 * st], hin[2 * st + 1], t1[st])}; }
                SB();
            }
#pragma unroll
            for (int st = 0; st < 2; st++) {
                f32x2 tr[4], ti[4];
#pragma unroll
                for (int t = 0; t < 4; t++) { const f32x2 wv = st ? hi2(cw[t]) : lo2(cw[t]); tr[t] = bmul<1, 1>(wv, hi[st]); ti[t] = bmul<1, 1>(wv, hr[st]); }
                SB();
#pragma unroll
                for (int t = 0; t < 4; t++) { const f32x2 wv = st ? hi2(cw[t]) : lo2(cw[t]); tr[t] = bfma<0, 0>(wv, hr[st], tr[t]); ti[t] = bfma<0, 1>(wv, hi[st], ti[t]); }
                SB();
                v4u fr = {pk2(tr[0]), pk2(tr[1]), pk2(tr[2]), pk2(tr[3])}, fi = {pk2(ti[0]), pk2(ti[1]), pk2(ti[2]), pk2(ti[3])};
                asm volatile("s_nop 1" : "+v"(fr), "+v"(fi));
#pragma unroll
                for (int bb = 0; bb < 2; bb++) { mma<false>(zr[st][bb], fr, sel[bb]); mma<false>(zi[st][bb], fi, sel[bb]); }
                SB();
            }
            mfma_done();
            stA<0>(pk8(zr[0][0], zr[1][0]), l16, sws, so); stA<0>(pk8(zi[0][0], zi[1][0]), l16, sws, so + 8 * 16384);
            stA<1024>(pk8(zr[0][1], zr[1][1]), l16, sws, so); stA<1024>(pk8(zi[0][1], zi[1][1]), l16, sws, so + 8 * 16384);
            SB();
            STAMP(7);
        }
        if (heavy) {
            gpass(IC<1>{}, IC<0>{}, IC<128>{});
            mfma_done();
            STAMP(8); VAL(26, stall); stall = 0;
            sfor<0, 4>(LAMB(ff) { stA<decltype(ff)::value * 1024>(gated(ff), l16, sws, go + 4 * 16384); });
            SB();
        }
        int q2 = q;
        asm volatile("" : "+s"(q2));
        if (q2 == 3) {
            waitv<0>(); raise(1); raise(2);
            sfor<0, 6>(set2);
            wait_ge(ffl, N1 + 1);
            sfor<0, PD>(prime2);
            wait_ge(F_RDY, 1);
            sfor<0, 12>(LAMB(ff) { constexpr int f = decltype(ff)::value; reldA<(f & 3) * 1024>(AF(f >> 2, f & 3), l16, sws, uoff(f >> 2)); });
#pragma unroll
            for (int u = 0; u < 3; u++) asm volatile("s_waitcnt vmcnt(0)" : "+v"(AF(u, 0)), "+v"(AF(u, 1)), "+v"(AF(u, 2)), "+v"(AF(u, 3))::"memory");
        }
    }
    STAMP(9);
    {
        f32x4 acc[8][2];
        v4u pk[2];
        unsigned fl = 0, fr = 0, dn = 0; bool lack = false;
        constexpr auto fv = [](int n) { return n < 48 ? n >> 4 : n < 108 ? 3 + (n - 48) / 12 : n < 144 ? 8 + ((n - 108) % 12) / 4 : 3 + (n - 144) / 4; };
        constexpr auto fk = [](int n) { return n < 48 ? (n >> 3) & 1 : n < 108 ? ((n - 48) % 12) / 6 : (n >> 1) & 1; };
        constexpr auto fd = [](int n) { return n < 48 ? n & 7 : n < 108 ? 2 + (n - 48) % 6 : n < 144 ? 2 * ((n - 108) / 12 + 1) + (n & 1) : n & 1; };
        constexpr auto first = [](int n) { return n >= 0 && (n < 48 ? (n & 15) == 0 : n < 108 ? (n - 48) % 12 == 0 : n < 120 && (n & 3) == 0); };
        constexpr int LG = LGATE;
        sfor<0, 176>(LAMB(nn) {
            constexpr int n = decltype(nn)::value, m = n + PD;
            constexpr int v = fv(n), ks = fk(n), dt = fd(n);
            constexpr int mv = fv(m), mk = fk(m), md = fd(m);
            constexpr bool mc = (mv & ~3) == 4;
            constexpr unsigned moff = (mv & 3) * SLOT + (mc ? md * 2048 : (md >> 2) * 8192 + mk * 4096);
            constexpr auto chk = [](int q) { return q == 16 ? 2 : q == 32 ? 4 : q == 60 ? 8 : q == 108 ? 11 : 0; };
            v4u cur; v2u lo, hi;
            if constexpr (n >= 0) {
                if constexpr (first(n)) {
                    constexpr int mid = n <= 32 ? 0 : 4 + ((n - 31) / 2 < 16 ? (n - 31) / 2 : 16), late = n <= LG ? 0 : n - LG < 12 ? n - LG : 12;
                    constexpr int NW = v == 0 ? 12 : v == 1 ? 8 : v == 2 ? 63 : v < 8 ? mid - 4 * (v - 2) + late : late - 4 * (v - 7);
                    static_assert(NW >= 0, "load schedule");
                    asm volatile("s_waitcnt vmcnt(%4)" : "+v"(AF(v & 7, 0)), "+v"(AF(v & 7, 1)), "+v"(AF(v & 7, 2)), "+v"(AF(v & 7, 3)) : "n"(NW) : "memory");
                }
                cur = bf[n % PD];
                mma<(n < 8)>(acc[dt][0], cur, AF(v & 7, 2 * ks));
                SB();
            }
            if constexpr (m > 0 && (m & 15) == 0 && m <= 48) post(fdone, dn);
            if constexpr (chk(m) > 0) need_full(lack, ffl, N1 + chk(m));
            if constexpr (m < 176) { if constexpr (mc) bf[m % PD] = lds128(cbs[mk] + moff); else lo = ldstr(tbs[md & 3] + moff); }
            if constexpr (chk(m + 6) > 0) fl = peek(ffl);
            if constexpr (chk(m + 1) > 0) lack = any_lt(fl, N1 + chk(m + 1));
            if constexpr ((m & 15) == 13 && m < 48) { dn = N1 + mv + 1; asm volatile("" : "+v"(dn)); }
            if constexpr (n == 17 || n == 21) try_raise(2);
            if constexpr (n == 20) fr = peek(F_RDY);
            if constexpr (n == 26) { if (raised < 2) { waitv<0>(); raise(2); } }
            if constexpr (n == 27) need_full(any_lt(fr, 2), F_RDY, 2);
            if constexpr (n >= 28 && n < 32) reldA<(n & 3) * 1024>(AF(3, n & 3), l16, sws, uoff(3));
            if constexpr (n >= 32 && n < 64 && (n & 1) == 0) { constexpr int f = (n - 32) >> 1; reldA<(f & 3) * 1024>(AF(4 + (f >> 2), f & 3), l16, sws, uoff(4 + (f >> 2))); }
            if constexpr (n == LG - 6) fr = peek(F_RDY + 4);
            if constexpr (n == LG - 1) need_full(any_lt(fr, 1), F_RDY + 4, 1);
            if constexpr (n >= LG && n < LG + 12) { constexpr int f = n - LG; reldA<(f & 3) * 1024>(AF(f >> 2, f & 3), l16, sws, uoff(8 + (f >> 2))); }
            SB();
            if constexpr (n >= 0) { mma<(n < 8)>(acc[dt][1], cur, AF(v & 7, 2 * ks + 1)); SB(); }
            if constexpr (m < 176 && !mc) { hi = ldstr(tbs[md & 3] + moff + 2048); bf[m % PD] = v4u{lo.x, lo.y, hi.x, hi.y}; }
            if constexpr (n >= 120 && n < 156) {
                constexpr int sl = (n - 108) / 12, r = (n - 108) % 12 - 2;
                if constexpr (r >= 0 && r < 10 && r % 5 < 4) { const f32x4& a = acc[2 * sl + r / 5][(r % 5) >> 1]; pk[r / 5][r % 5] = cvtv(a[2 * (r % 5 & 1)], a[2 * (r % 5 & 1) + 1]); }
                if constexpr (r >= 0 && r % 5 == 4) stX<(r / 5) * 1024>(pk[r / 5], l16, sws, oX[sl - 1]);
            }
            if constexpr (n == 156) post(fdone + (F_SENT - F_DONE), 3);
            if constexpr (n == 107) { STAMP(10); VAL(28, stall); stall = 0; }
            SB();
        });
        mfma_done();
        STAMP(11); VAL(29, stall);
#pragma unroll
        for (int e = 0; e < 4; e++) sts128f(L_O + w * 4096 + e * 1024 + l16, acc[e >> 1][e & 1]);
        post(fdone + (F_SENT - F_DONE), 4);
    }
    STAMP(14); STAMP_DUMP();
}

extern "C" void kernel_launch(void* const* d_in, const int* in_sizes, int n_in, void* d_out, int out_size, void* d_ws, size_t ws_size,
                              hipStream_t stream) {
    Params p;
    p.x = (const u16*)d_in[0]; p.lre = (const u16*)d_in[1]; p.lim = (const u16*)d_in[2];
    p.Bre = (const u16*)d_in[3]; p.Bim = (const u16*)d_in[4]; p.Cre = (const u16*)d_in[5]; p.Cim = (const u16*)d_in[6];
    p.ldt = (const u16*)d_in[7]; p.Dsk = (const u16*)d_in[8];
    p.wg = (const u16*)d_in[10]; p.wu = (const u16*)d_in[11]; p.wd = (const u16*)d_in[12];
    p.out = (float*)d_out; p.ws = (unsigned char*)d_ws + WS_SHIFT;
    (void)hipFuncSetAttribute((const void*)fused, hipFuncAttributeMaxDynamicSharedMemorySize, L_TOTAL);
    fused<<<256, 512, L_TOTAL, stream>>>(p);
}
```

```cpp
#define ROT1 ((i + 2 * kq) & 7)
#define NOXT
#include <hip/hip_runtime.h>
#include <stdint.h>

typedef unsigned short u16;
typedef unsigned v4u __attribute__((ext_vector_type(4)));
typedef unsigned v2u __attribute__((ext_vector_type(2)));
typedef short s16x4 __attribute__((ext_vector_type(4)));
typedef __bf16 bf16x2 __attribute__((ext_vector_type(2)));
typedef __bf16 bf16x8 __attribute__((ext_vector_type(8)));
typedef float f32x2 __attribute__((ext_vector_type(2)));
typedef float f32x4 __attribute__((ext_vector_type(4)));
#define GAS __attribute__((address_space(1)))
#define LAS __attribute__((address_space(3)))
#define DEVI __device__ __forceinline__
#define NOUNROLL _Pragma("nounroll")

#define SPIN_SLEEP 0
constexpr int DM = 512, FF = 1792;
constexpr unsigned WS_A2 = 0;
constexpr unsigned WS_XB = 16u * 44u * 16384u;
constexpr unsigned WS_GRAN = WS_XB + 256u * 24576u;
constexpr unsigned WS_FLAG = WS_GRAN + 16u * 16u * 512u;
constexpr unsigned WS_EPOCH = WS_FLAG + 1024u * 128u;
constexpr unsigned WS_END = WS_EPOCH + 256u;
constexpr unsigned SLOT = 16384;
constexpr unsigned L_RSTD = 0, L_MEAN = 512;
constexpr unsigned F_DONE = 1024, F_SENT = F_DONE + 16, F_FULL = F_DONE + 32, F_COMM = F_DONE + 40, F_RDY = F_DONE + 48, F_OUT = F_DONE + 64, F_XI = F_DONE + 80, F_BS = F_DONE + 96, L_TAG = F_DONE + 128;
constexpr unsigned L_CST = 1280;
constexpr unsigned L_XCH = L_CST + 8 * 256;
constexpr unsigned L_SEG = 4096, L_PW = 8192, L_TOT = L_PW + 2048, L_PRE = L_TOT + 1024, L_SEL = 12288;
constexpr unsigned L_P = SLOT, L_O = 5 * SLOT, L_W = 6 * SLOT, L_TOTAL = 10 * SLOT;
constexpr int SPIN_MAX = 1 << 18;
constexpr int LSPIN_MAX = 1 << 22;

struct Params {
    const u16 *x, *lre, *lim, *Bre, *Bim, *Cre, *Cim, *ldt, *Dsk, *wg, *wu, *wd;
    float* out;
    unsigned char* ws;
};

DEVI float bf2f(u16 v) { return __uint_as_float(((unsigned)v) << 16); }
DEVI float bflo(unsigned v) { return __uint_as_float(v << 16); }
DEVI float bfhi(unsigned v) { return __uint_as_float(v & 0xffff0000u); }
DEVI unsigned pk2(float a, float b) { f32x2 p = {a, b}; return __builtin_bit_cast(unsigned, __builtin_convertvector(p, bf16x2)); }
DEVI unsigned pk2(f32x2 p) { return __builtin_bit_cast(unsigned, __builtin_convertvector(p, bf16x2)); }
DEVI v4u pk8(f32x4 a, f32x4 b) { return v4u{pk2(a.x, a.y), pk2(a.z, a.w), pk2(b.x, b.y), pk2(b.z, b.w)}; }
DEVI float dot2(unsigned a, unsigned b, float c) { return __builtin_amdgcn_fdot2_f32_bf16(__builtin_bit_cast(bf16x2, a), __builtin_bit_cast(bf16x2, b), c, false); }
template <int CTRL> DEVI float dppf(float v) { return __builtin_bit_cast(float, __builtin_amdgcn_update_dpp(0, __builtin_bit_cast(int, v), CTRL, 0xf, 0xf, true)); }
DEVI f32x4 mfma(v4u a, v4u b, f32x4 c) { return __builtin_amdgcn_mfma_f32_16x16x32_bf16(__builtin_bit_cast(bf16x8, a), __builtin_bit_cast(bf16x8, b), c, 0, 0, 0); }
template <bool FIRST> DEVI void mma(f32x4& c, const v4u& a, const v4u& b) {
    if constexpr (FIRST) asm volatile("v_mfma_f32_16x16x32_bf16 %0, %1, %2, 0" : "=&v"(c) : "v"(a), "v"(b));
    else asm volatile("v_mfma_f32_16x16x32_bf16 %0, %1, %2, %0" : "+v"(c) : "v"(a), "v"(b));
}
DEVI void mfma_done() { __builtin_amdgcn_sched_barrier(0); asm volatile("s_nop 7"); __builtin_amdgcn_sched_barrier(0); }
DEVI v4u lds128(unsigned a) { return *(LAS v4u*)(uintptr_t)a; }
DEVI f32x4 lds128f(unsigned a) { return *(LAS f32x4*)(uintptr_t)a; }
DEVI float lds32f(unsigned a) { return *(LAS float*)(uintptr_t)a; }
DEVI void sts32f(unsigned a, float v) { *(LAS float*)(uintptr_t)a = v; }
DEVI void sts128(unsigned a, v4u v) { *(LAS v4u*)(uintptr_t)a = v; }
DEVI void sts128f(unsigned a, f32x4 v) { *(LAS f32x4*)(uintptr_t)a = v; }
DEVI v2u ldstr(unsigned a) { return __builtin_bit_cast(v2u, __builtin_amdgcn_ds_read_tr16_b64_v4i16((LAS s16x4*)(uintptr_t)a)); }
DEVI void fence_c() { asm volatile("" ::: "memory"); }
DEVI void bar_l() { asm volatile("s_waitcnt lgkmcnt(0)\n\ts_barrier" ::: "memory"); }
template <int N> DEVI void waitv() { asm volatile("s_waitcnt vmcnt(%0)" ::"n"(N) : "memory"); }
DEVI void dma16(const void* sbase, unsigned voff, unsigned ldst) {
    asm volatile("s_mov_b32 m0, %2\n\ts_nop 0\n\tglobal_load_lds_dwordx4 %0, %1" ::"v"(voff), "s"(sbase), "s"(ldst) : "memory");
}
DEVI void post(unsigned a, unsigned v) { fence_c(); *(LAS unsigned*)(uintptr_t)a = v; fence_c(); }
DEVI unsigned peek(unsigned a) { fence_c(); return *(LAS unsigned*)(uintptr_t)a; }
DEVI bool any_lt(unsigned v, unsigned need) { return __builtin_amdgcn_ballot_w64(v < need) != 0; }
DEVI void wait_ge(unsigned a, unsigned need) {
    int s = LSPIN_MAX;
    NOUNROLL while (any_lt(peek(a), need)) { asm volatile("s_sub_i32 %0, %0, 1" : "+s"(s) :: "scc"); if (s < 0) break; if (SPIN_SLEEP) __builtin_amdgcn_s_sleep(SPIN_SLEEP); }
    fence_c();
}
template <int V> DEVI void postm(unsigned a, int lane) {
    asm volatile("s_mov_b64 exec, %2\n\tds_max_u32 %0, %1\n\ts_mov_b64 exec, -1" ::"v"(a), "v"(lane), "n"(1 << V) : "memory");
}
DEVI unsigned vm_outstanding() {
    unsigned v; asm volatile("s_getreg_b32 %0, hwreg(HW_REG_IB_STS)" : "=s"(v)::"memory");
    return (v & 15u) | ((v >> 18) & 0x30u);
}
DEVI void postv(unsigned a, int lane, unsigned v) {
    asm volatile("s_lshl_b64 exec, 1, %2\n\tds_max_u32 %0, %1\n\ts_mov_b64 exec, -1" ::"v"(a), "v"(lane), "s"(v) : "memory", "scc");
}
template <int V> DEVI void postc(unsigned a, int lane) {
    asm volatile("s_mov_b64 exec, %2\n\tds_write_b32 %0, %1\n\ts_mov_b64 exec, -1" ::"v"(a), "v"(lane), "n"(1 << V) : "memory");
}
DEVI void need_full(bool lack, unsigned a, unsigned need) { if (__builtin_expect(lack, 0)) wait_ge(a, need); fence_c(); }
#define VAL(id, v)
DEVI float sfma(float a, float b, float c) { float d; asm("v_fma_f32 %0, %1, %2, %3" : "=v"(d) : "v"(a), "v"(b), "v"(c)); return d; }
DEVI float sfms(float a, float b, float c) { float d; asm("v_fma_f32 %0, %1, %2, -%3" : "=v"(d) : "v"(a), "v"(b), "v"(c)); return d; }
DEVI float sfnma(float a, float b, float c) { float d; asm("v_fma_f32 %0, -%1, %2, %3" : "=v"(d) : "v"(a), "v"(b), "v"(c)); return d; }
DEVI float sadd(float a, float b) { float d; asm("v_add_f32 %0, %1, %2" : "=v"(d) : "v"(a), "v"(b)); return d; }
DEVI float smul(float a, float b) { float d; asm("v_mul_f32 %0, %1, %2" : "=v"(d) : "v"(a), "v"(b)); return d; }
DEVI float smul_t(float a, float b) { float d; asm("s_nop 0\n\tv_mul_f32 %0, %1, %2" : "=v"(d) : "v"(a), "v"(b)); return d; }
struct cpx { float x, y; };
DEVI cpx cmul(cpx a, cpx b) { return cpx{sfms(a.x, b.x, smul(a.y, b.y)), sfma(a.x, b.y, smul(a.y, b.x))}; }
DEVI float lmad(cpx a, float h, float u) {
    float d;
    asm("v_fma_f32 %0, %1, %3, %4\n\ts_nop 0\n\tv_fmac_f32_dpp %0, %3, %2 quad_perm:[1,0,3,2] row_mask:0xf bank_mask:0xf" : "=&v"(d) : "v"(a.x), "v"(a.y), "v"(h), "v"(u));
    return d;
}
DEVI f32x2 dup(float v) { f32x2 d = {v, v}; asm("" : "+v"(d)); return d; }
DEVI f32x2 pfma(f32x2 a, f32x2 b, f32x2 c) { return __builtin_elementwise_fma(a, b, c); }
DEVI f32x2 lo2(f32x4 v) { return f32x2{v.x, v.y}; }
DEVI f32x2 hi2(f32x4 v) { return f32x2{v.z, v.w}; }
#define SB() __builtin_amdgcn_sched_barrier(0)
template <int V> struct IC { static constexpr int value = V; };
template <int B, int E, class F> DEVI void sfor(F&& f) { if constexpr (B < E) { f(IC<B>{}); sfor<B + 1, E>(f); } }
#define LAMB(nn) [&](auto nn) __attribute__((always_inline))
#define STAMP(id)
#define STAMP_INIT()
#define STAMP_DUMP()
DEVI int fresh_lane() { int l; asm volatile("v_mbcnt_lo_u32_b32 %0, -1, 0\n\tv_mbcnt_hi_u32_b32 %0, -1, %0" : "=v"(l)); return l; }
DEVI v4u make_sel(int lane, int bb) {
    const int c = lane & 15, tk = 8 * (c >> 2) + 4 * bb + (c & 3) - 8 * (lane >> 4);
    v4u s;
#pragma unroll
    for (int e = 0; e < 4; e++) s[e] = (tk == 2 * e ? 0x3f80u : 0u) | (tk == 2 * e + 1 ? 0x3f800000u : 0u);
    return s;
}
#define LANE_VARS const int lane = fresh_lane(), c = lane & 15, g = lane >> 4, q8 = lane >> 3, p8 = lane & 7; (void)c; (void)g; (void)q8; (void)p8
constexpr unsigned wslot(int n) { return (unsigned)(n & 3) * SLOT; }

#define ROT2 0
#define XI_STEP 8
#define TOUCH_GATE 1
#define LB_SLEEP 8
#define LB_FIRST 4
#define T2_SLEEP 6
#define FL_FIRST 20
#define FL_SLEEP 6
#define MIDV 64
#define X_FIRST 14
#define X_SLEEP 1
#define PDV 4
#define INFL 8
#define TSL 6
#define TW __builtin_amdgcn_s_sleep(TSL);
#define PACE 14
#define PACE2 8
#define XLV 3
#define LGATE 84
#define TGN 37
#define EGATE 100
#define X_LAST 6
#define CLAG 4
#define CRING 8
#define PF_SLEEP 1
#define WS_SHIFT 0
#define SLEEP0 12
constexpr unsigned CODE_LINES = 295;
template <int OFF> DEVI void reldA(v4u& d, unsigned voff, v4u srd, unsigned so) {
    asm volatile("buffer_load_dwordx4 %0, %1, %2, %3 offen offset:%4 sc1" : "+v"(d) : "v"(voff), "s"(srd), "s"(so), "n"(OFF) : "memory");
}
template <int OFF> DEVI void stA(const v4u& d, unsigned voff, v4u srd, unsigned so) {
    asm volatile("buffer_store_dwordx4 %0, %1, %2, %3 offen offset:%4 sc1\n\ts_nop 1" ::"v"(d), "v"(voff), "s"(srd), "s"(so), "n"(OFF) : "memory");
}
template <int OFF> DEVI void stX(const v4u& d, unsigned voff, v4u srd, unsigned so) {
    asm volatile("buffer_store_dwordx4 %0, %1, %2, %3 offen offset:%4 sc1" ::"v"(d), "v"(voff), "s"(srd), "s"(so), "n"(OFF) : "memory");
}
DEVI void touch(unsigned& sink, const void* tp, unsigned to) { asm volatile("global_load_dword %0, %1, %2" : "+v"(sink) : "v"(to), "s"(tp) : "memory"); }

DEVI v4u make_srd(const void* base) { const unsigned long long a = (unsigned long long)base; return v4u{(unsigned)a, (unsigned)(a >> 32) & 0xffffu, 0x7fffffffu, 0x00020000u}; }
DEVI void dma4(v4u srd, unsigned ve, unsigned vo, unsigned s0, unsigned s1, unsigned s2, unsigned s3, unsigned ldst) {
    asm volatile("s_mov_b32 m0, %7\n\ts_nop 0\n\tbuffer_load_dwordx4 %0, %2, %3 offen lds\n\tbuffer_load_dwordx4 %1, %2, %4 offen offset:1024 lds\n\t"
                 "buffer_load_dwordx4 %0, %2, %5 offen offset:2048 lds\n\tbuffer_load_dwordx4 %1, %2, %6 offen offset:3072 lds"
                 ::"v"(ve), "v"(vo), "s"(srd), "s"(s0), "s"(s1), "s"(s2), "s"(s3), "s"(ldst) : "memory");
}
DEVI void dma1(v4u srd, unsigned v, unsigned so, unsigned ldst) {
    asm volatile("s_mov_b32 m0, %3\n\ts_nop 0\n\tbuffer_load_dwordx4 %0, %1, %2 offen lds" ::"v"(v), "s"(srd), "s"(so), "s"(ldst) : "memory");
}
template <int H, int NEG> DEVI f32x2 bmul(f32x2 a, f32x2 b) {
    f32x2 d; asm("v_pk_mul_f32 %0, %1, %2 op_sel:[%3,0] op_sel_hi:[%3,1] neg_lo:[%4,0] neg_hi:[%4,0]" : "=v"(d) : "v"(a), "v"(b), "n"(H), "n"(NEG)); return d;
}
template <int H, int NEG> DEVI f32x2 bfma(f32x2 a, f32x2 b, f32x2 c) {
    f32x2 d; asm("v_pk_fma_f32 %0, %1, %2, %3 op_sel:[%4,0,0] op_sel_hi:[%4,1,1] neg_lo:[%5,0,0] neg_hi:[%5,0,0]" : "=v"(d) : "v"(a), "v"(b), "v"(c), "n"(H), "n"(NEG)); return d;
}
DEVI f32x2 cmadp(f32x2 a, f32x2 h, f32x2 u) {
    f32x2 t, d;
    asm("v_pk_fma_f32 %0, %1, %2, %3 op_sel:[0,0,0] op_sel_hi:[0,1,1]" : "=v"(t) : "v"(h), "v"(a), "v"(u));
    asm("v_pk_fma_f32 %0, %1, %2, %3 op_sel:[1,0,0] op_sel_hi:[0,0,1] neg_lo:[1,0,0]" : "=v"(d) : "v"(a), "v"(f32x2{h.y, h.y}), "v"(t));
    return d;
}
DEVI void mma_c(f32x4& d, const v4u& a, const v4u& b, const f32x4& c) { asm volatile("v_mfma_f32_16x16x32_bf16 %0, %1, %2, %3" : "=&v"(d) : "v"(a), "v"(b), "v"(c)); }
DEVI void mma_t(f32x4& d, const v4u& a, const v4u& b, const f32x4& c) { asm volatile("v_mfma_f32_16x16x32_bf16 %0, %1, %2, %3" : "+v"(d) : "v"(a), "v"(b), "v"(c)); }
DEVI unsigned cvtv(float a, float b) { unsigned d; asm volatile("v_cvt_pk_bf16_f32 %0, %1, %2" : "=v"(d) : "v"(a), "v"(b)); return d; }

__global__ __launch_bounds__(512, 2) void fused(Params p) {
    const unsigned char* pc0;
    asm volatile("s_getpc_b64 %0" : "=s"(pc0));
    const int w = __builtin_amdgcn_readfirstlane(threadIdx.x >> 6);
    const int b = blockIdx.x, xcd = b & 7, idx = b >> 3;
    const int i = 8 * (xcd & 1) + (idx & 7), kq = xcd >> 1, q = idx >> 3, j = 4 * kq + q;
    const bool heavy = q != 3;
    const int N1 = heavy ? 20 : 12;
    const int lw = w & 1, pr = w & 3;
    const int rot = ROT1;
    const __amdgpu_buffer_rsrc_t rws = __builtin_amdgcn_make_buffer_rsrc(p.ws, 0, WS_END, 0x00020000);
    auto unit_of = [&](int v) { return v < 4 ? 7 * kq + v : v < 8 ? 28 + 8 * ((v >> 1) & 1) + 2 * kq + (v & 1) : 7 * kq + v - 4; };
    const unsigned sl_lo = L_P + (unsigned)pr * SLOT;
    const unsigned xo = WS_XB + (unsigned)((i * 4 + q) * 4) * 24576u + pr * 6144;
    const unsigned fo = WS_FLAG + (unsigned)(i * 16 + j) * 512u;

    if (w >= 4) {
        LANE_VARS;
        STAMP_INIT();
        STAMP(0);
        const unsigned swz = (unsigned)((p8 ^ (q8 >> 1)) << 4);
        const unsigned fa = F_DONE + (lane & 3) * 4;
        unsigned sink = 0, tg;
        auto poll16 = [&](int which) __attribute__((always_inline)) {
            wait_ge(F_OUT + (lane & 3) * 4, which + 1);
            STAMP(16);
            const unsigned pf = WS_FLAG + (unsigned)(i * 16 + 4 * kq + (lane & 3)) * 512u + which * 128 + ((lane >> 2) & 3) * 8;
            NOUNROLL for (int spin = 0; spin < SPIN_MAX; spin++) {
                fence_c();
                const v2u f = __builtin_amdgcn_raw_buffer_load_b64(rws, pf, 0, 16);
                const unsigned bad = (unsigned)__builtin_amdgcn_ballot_w64(f.x != tg || f.y != ~tg);
                if (bad == 0) break;
                if ((bad & 0x7777u) == 0) { if (which) break; postc<1>(F_RDY, lane); }
                __builtin_amdgcn_s_sleep(FL_SLEEP);
            }
            postc<2>(F_RDY + which * 4, lane);
            STAMP(17);
        };
        if (w < 6) {
            unsigned ffull = F_FULL + lw * 4;
            asm volatile("" : "+v"(ffull));
            {
                const unsigned voffS = (8 * (q8 >> 2) + (q8 & 3)) * 1024 + swz;
                const v4u srd = make_srd((const unsigned char*)(lw ? p.Bim : p.Bre) + (size_t)(32 * j) * 1024);
                NOUNROLL for (int n = 2; n < 8; n++) {
                    const unsigned so = 128 * ((n + rot) & 7);
                    dma4(srd, voffS, voffS ^ 64u, so, so + 15360, so + 2048, so + 17408, L_W + (n >> 1) * SLOT + lw * 8192 + (n & 1) * 4096);
                    if (n == 3) {
                        bar_l();
                    }
                    if (n == 4) { waitv<4>(); postc<2>(ffull, lane); }
                    if (n >= 5) { waitv<4>(); postv(ffull, lane, n >> 1); }
                }
                waitv<0>(); postc<4>(ffull, lane);
                asm volatile("" ::"v"(sink));
            }
            STAMP(1);
            const unsigned voffG = q8 * (FF * 2) + (((p8 >> 1) ^ ((q8 >> 1) & 1)) << 5) + ((p8 & 1) << 4), voffG1 = voffG ^ 64u;
            const unsigned voffC = q8 * 1024 + swz, voffC1 = voffC ^ 64u;
            const unsigned voffD = q8 * 1024 + (((p8 >> 1) ^ (q8 >> 1) ^ (2 * (kq & 1))) << 5) + ((p8 & 1) << 4);
            const unsigned char* gb = (const unsigned char*)(lw ? p.wu : p.wg) + (7 * kq + q) * 128;
            unsigned thr = (lane >> 2) + 1;
            int credit = 0, cbase = 0;
            auto item = [&](int n, const unsigned char* b0, const unsigned char* b1, unsigned ve, unsigned vo, unsigned st) __attribute__((always_inline)) {
                v4u s0 = make_srd(b0), s1 = make_srd(b1);
                unsigned dst = ((n & 4) ? L_P : L_W) + (n & 3) * SLOT + lw * 8192, dst1 = dst + 4096;
                asm volatile("" : "+s"(s0), "+s"(s1), "+s"(dst), "+s"(dst1));
                int sp = LSPIN_MAX;
                NOUNROLL while (n - 7 > credit) {
                    const unsigned dn = peek(fa);
                    postv(ffull, lane, n - ((vm_outstanding() + 7) >> 3));
                    unsigned long long mk = __builtin_amdgcn_ballot_w64(dn >= thr);
                    mk &= mk >> 1; mk &= mk >> 2;
                    credit = __builtin_popcountll(mk & 0x1111111111111111ull) + cbase;
                    asm volatile("s_sub_i32 %0, %0, 1" : "+s"(sp)::"scc");
                    if (sp < 0) break;
                }
                waitv<INFL>();
                dma4(s0, ve, vo, 0, st - 1024, 2 * st - 2048, 3 * st - 3072, dst);
                postv(ffull, lane, n - (INFL + 7) / 8);
                waitv<INFL>();
                dma4(s1, ve, vo, 0, st - 1024, 2 * st - 2048, 3 * st - 3072, dst1);
            };
            wait_ge(F_XI + (lane & 3) * 4, 3);
            NOUNROLL for (int n = 4; n < N1; n++) {
                const int t = n - 4;
                const unsigned char* bp = gb + (size_t)(((t + rot) & 7) * 64) * (FF * 2) + (t >> 3) * 512;
                item(n, bp, bp + 32 * FF * 2, voffG, voffG1, 8 * FF * 2);
            }
            cbase = N1 - 8; thr += cbase;
            const unsigned char* dp = (const unsigned char*)p.wd + (size_t)(7 * kq) * 65536 + q * 256 + (lw ^ (kq >> 1)) * 128;
            NOUNROLL for (int v = 0; v < 11; v++) {
                if ((v & ~3) == 4) {
                    const unsigned char* cbp = (const unsigned char*)((v & 2) ? p.Cim : p.Cre) + (size_t)q * 131072 + (2 * kq + (v & 1)) * 128;
                    item(N1 + v, cbp + ((2 * lw) ^ kq) * 32768, cbp + ((2 * lw + 1) ^ kq) * 32768, voffC, voffC1, 8192);
                } else {
                    item(N1 + v, dp, dp + 32768, voffD, voffD, 8192);
                    dp += 65536;
                }
            }
            waitv<16>(); postv(ffull, lane, N1 + 9);
            waitv<8>(); postv(ffull, lane, N1 + 10);
            waitv<0>(); postv(ffull, lane, N1 + 11);
            tg = peek(L_TAG);
            STAMP(7);
        } else {
            unsigned tag0, prm0, prm1, prm2;
            const unsigned cto = min(idx * 11 + 4 * lw + p8, (int)CODE_LINES - 1) * 128;
            touch(sink, pc0, cto);
            touch(sink, lw ? (const void*)p.ws : (const void*)p.Dsk, lw ? ((lane & 4) ? WS_GRAN + (j * 16 + i) * 512 : fo) + (lane & 3) * 128 : p8 * 128);
            NOUNROLL for (int t = heavy ? 4 : 0; t < 4; t++)
                touch(sink, lw ? p.wu : p.wg, (32 * i + (lane & 31)) * (FF * 2) + (7 * kq + min(2 * t + (lane >> 5), 6)) * 128);
            const unsigned po = 2 * (32 * j + 8 * (lane >> 4) + 4 * ((lane >> 1) & 1) + ((lane >> 2) & 3));
            asm volatile("global_load_ushort %0, %4, %5\n\tglobal_load_ushort %1, %4, %6\n\tglobal_load_ushort %2, %4, %7\n\tglobal_load_dword %3, %8, %9 sc1"
                         : "=&v"(prm0), "=&v"(prm1), "=&v"(prm2), "=&v"(tag0) : "v"(po), "s"(p.lre), "s"(p.lim), "s"(p.ldt), "v"(WS_EPOCH), "s"(p.ws) : "memory");
            *(LAS unsigned*)(uintptr_t)(F_DONE + (lane & 31) * 4) = 0u;
            bar_l();
            sts128(L_SEL + lw * 1024 + lane * 16, make_sel(lane, lw));
            asm volatile("s_waitcnt vmcnt(0)" : "+v"(prm0), "+v"(prm1), "+v"(prm2), "+v"(tag0)::"memory");
            tg = tag0 + 1u;
            const bool odd = lane & 1;
            const unsigned la = lane * 4;
            const float lr = bflo(prm0), li = bflo(prm1);
            float dt = __expf(bflo(prm2));
            dt = bflo(pk2(dt, dt));
            const float ex = __expf(dt * lr);
            const float fr = __builtin_amdgcn_fractf(dt * li * 0.15915494309189535f);
            const float ar = smul_t(ex, __builtin_amdgcn_cosf(fr)), ai = smul_t(ex, __builtin_amdgcn_sinf(fr));
            const cpx a1 = {ar, ai};
            const cpx a2 = cmul(a1, a1), a4 = cmul(a2, a2);
            const unsigned sg = odd ? 0u : 0x80000000u;
            auto sgn = [&](cpx v) { return cpx{v.x, __uint_as_float(__float_as_uint(v.y) ^ sg)}; };
            const cpx r8 = cmul(a4, a4), r16 = cmul(r8, r8), r32 = cmul(r16, r16), r64 = cmul(r32, r32);
            const cpx a32 = sgn(r32);
            {
                const cpx r = lw ? r32 : r8, rs = sgn(r);
                float v = odd ? r.y : r.x;
                sts32f(L_PW + lw * 1024 + la, odd ? 0.0f : 1.0f);
#pragma unroll
                for (int s = 1; s < 4; s++) { sts32f(L_PW + lw * 1024 + s * 256 + la, v); if (s < 3) v = lmad(rs, v, 0.0f); }
            }
            if (lw == 0) {
                const float nr = ar - 1.0f, den = smul_t(__builtin_amdgcn_rcpf(sfma(lr, lr, smul(li, li))), 2.0f);
                float co = smul(odd ? sfms(ai, lr, smul(nr, li)) : sfma(nr, lr, smul(ai, li)), den);
                sts32f(L_CST + la, odd ? a1.y : a1.x); sts32f(L_CST + 256 + la, odd ? a4.y : a4.x);
                const cpx a1s = sgn(a1);
#pragma unroll
                for (int t = 0; t < 5; t++) { sts32f(L_CST + (2 + t) * 256 + la, co); if (t < 4) co = lmad(a1s, co, 0.0f); }
                *(LAS unsigned*)(uintptr_t)L_TAG = tg;
            }
            post(F_COMM + lw * 4, 1);
            STAMP(1);
            cpx wn[8];
            {
                const cpx r128 = cmul(r64, r64), r256 = cmul(r128, r128);
                wn[0] = lw ? r128 : cpx{1.0f, 0.0f};
#pragma unroll
                for (int n = 1; n < 8; n++) wn[n] = cmul(wn[n - 1], r256);
#pragma unroll
                for (int n = 0; n < 8; n++) { wn[n] = sgn(wn[n]); asm volatile("" : "+v"(wn[n].x), "+v"(wn[n].y)); }
            }
            unsigned gro = WS_GRAN + j * 8192 + lane * 8, gro2 = gro + i * 512;
            unsigned to0 = lw ? min((unsigned)(lane & 31) * 0x98000u + idx * 0x4000u, WS_END - 4) : (lane & 3) * 0x100000 + idx * 0x4000;
            unsigned to1 = (448 * kq + 28 * i + min(lane, 27)) * 1024 + (2 * q + lw) * 128, to2 = (128 * q + 8 * i + p8) * 1024 + (2 * kq + (q8 & 1)) * 128;
            unsigned po2 = WS_XB + (unsigned)((i * 4 + q) * 4 + kq) * 24576u + lane * 16;
            asm volatile("" : "+v"(gro), "+v"(gro2), "+v"(to0), "+v"(to1), "+v"(to2), "+v"(po2));
            wait_ge(F_XI + (lane & 3) * 4, heavy ? 1 : 0);
            touch(sink, p.wd, to1);
            touch(sink, lw ? p.Cim : p.Cre, to2);
            touch(sink, lw ? (const void*)p.ws : (const void*)p.out, to0);
            wait_ge(fa, 4);
            STAMP(4);
            if (lw) {
                float e[4];
#pragma unroll
                for (int s = 0; s < 4; s++) e[s] = lds32f(L_TOT + s * 256 + la);
                const float p2 = lmad(a32, e[0], e[1]), p3 = lmad(a32, p2, e[2]);
                __builtin_amdgcn_raw_buffer_store_b64(v2u{__float_as_uint(lmad(a32, p3, e[3])), tg}, rws, gro2, 0, 16);
                sts32f(L_PRE + la, 0.0f); sts32f(L_PRE + 256 + la, e[0]); sts32f(L_PRE + 512 + la, p2); sts32f(L_PRE + 768 + la, p3);
            }
            STAMP(5);
            __builtin_amdgcn_s_sleep(T2_SLEEP);
            if (!lw) {
                v4u none = {~0u, ~0u, ~0u, ~0u};
                NOUNROLL for (int n = 0; n < 24; n++) { __builtin_amdgcn_raw_buffer_store_b128(none, rws, po2, n * 1024, 16); __builtin_amdgcn_s_sleep(PF_SLEEP); }
            }
            __builtin_amdgcn_s_sleep(LB_FIRST);
            {
                const int top = i - 1 - lw;
                v2u gr[8] = {};
                NOUNROLL for (int spin = 0; spin < SPIN_MAX; spin++) {
                    fence_c();
#pragma unroll
                    for (int n = 0; n < 8; n++) gr[n] = __builtin_amdgcn_raw_buffer_load_b64(rws, gro, ((top - 2 * n) & 15) * 512, 16);
                    unsigned long long bad = 0;
#pragma unroll
                    for (int n = 0; n < 8; n++) if (top - 2 * n >= 0) bad |= __builtin_amdgcn_ballot_w64(gr[n].y != tg);
                    if (bad == 0) break;
                    __builtin_amdgcn_s_sleep(LB_SLEEP);
                }
                float h = 0;
#pragma unroll
                for (int n = 7; n >= 0; n--) if (top - 2 * n >= 0) h = lmad(wn[n], __uint_as_float(gr[n].x), h);
                sts32f(L_XCH + 256 + lw * 256 + la, h);
            }
            postc<4>(F_COMM + lw * 4, lane);
            STAMP(6);
            waitv<0>();
            asm volatile("" ::"v"(sink));
            if (w == 6 && lane == 0) __builtin_amdgcn_raw_buffer_store_b64(v2u{tg, ~tg}, rws, fo + 256, 0, 16);
            STAMP(7);
            if (lw) {
                wait_ge(F_OUT + (lane & 3) * 4, 1);
                __builtin_amdgcn_s_sleep(16);
                const unsigned pf = WS_FLAG + (unsigned)(i * 16 + 4 * (lane & 3) + q) * 512u + 256;
                NOUNROLL for (int spin = 0; spin < SPIN_MAX; spin++) {
                    fence_c();
                    const v2u f = __builtin_amdgcn_raw_buffer_load_b64(rws, pf, 0, 16);
                    if (__builtin_amdgcn_ballot_w64(f.x != tg || f.y != ~tg) == 0) break;
                    __builtin_amdgcn_s_sleep(16);
                }
            }
            poll16(lw);
        }
        f32x4 base[4];
        {
            const int d = 128 * q + 32 * kq + 4 * p8;
            const v2u dv = *(const GAS v2u*)(p.Dsk + d);
            const float df[4] = {bflo(dv.x), bfhi(dv.x), bflo(dv.y), bfhi(dv.y)};
            v2u xv[4];
#pragma unroll
            for (int e = 0; e < 4; e++) xv[e] = *(const GAS v2u*)(p.x + (size_t)(128 * i + 32 * pr + 8 * e + q8) * DM + d);
#pragma unroll
            for (int e = 0; e < 4; e++) {
                const unsigned tp = (32 * pr + 8 * e + 4 * ((q8 >> 1) & 1) + 2 * (q8 & 1) + (q8 >> 2)) * 4;
                const float rs = lds32f(L_RSTD + tp), nm = smul(lds32f(L_MEAN + tp), rs);
                const float xf[4] = {bflo(xv[e].x), bfhi(xv[e].x), bflo(xv[e].y), bfhi(xv[e].y)};
#pragma unroll
                for (int k = 0; k < 4; k++) base[e][k] = sfma(sfma(xf[k], rs, nm), df[k], xf[k]);
            }
        }
        STAMP(8);
        const unsigned xi = xo + kq * 24576u + lane * 16;
        const unsigned sc = L_O + pr * 4096;
        unsigned wa = sc + (8 * (c >> 2) + 4 * (c & 1) + ((c >> 1) & 1)) * 128 + ((g ^ (c & 7)) << 4), ra = sc + q8 * 128 + ((p8 ^ (2 * (q8 & 1) + (q8 >> 2))) << 4);
        GAS float* op = (GAS float*)p.out + (size_t)(128 * i + 32 * pr + q8) * DM + 128 * q + 32 * kq + 4 * p8;
        asm volatile("" : "+v"(wa), "+v"(ra), "+v"(op));
        wait_ge(F_SENT + pr * 4, 3);
        __builtin_amdgcn_s_sleep(X_FIRST);
        v4u in[6];
        f32x4 own[4];
        NOUNROLL for (int spin = 0; spin < SPIN_MAX; spin++) {
            fence_c();
#pragma unroll
            for (int n = 0; n < 6; n++) in[n] = __builtin_amdgcn_raw_buffer_load_b128(rws, xi + n * 1024, 0, 16);
            SB();
            wait_ge(F_SENT + pr * 4, 4);
#pragma unroll
            for (int e = 0; e < 4; e++) own[e] = lds128f(sc + e * 1024 + lane * 16);
            unsigned mx = 0;
#pragma unroll
            for (int n = 0; n < 6; n++) mx = max(mx, max(max(in[n].x, in[n].y), max(in[n].z, in[n].w)));
            if (__builtin_amdgcn_ballot_w64(mx == ~0u) == 0) break;
        }
        STAMP(11);
#pragma unroll
        for (int e = 0; e < 4; e++)
#pragma unroll
            for (int sr = 0; sr < 3; sr++) { const unsigned d0 = in[2 * sr + (e >> 1)][2 * (e & 1)], d1 = in[2 * sr + (e >> 1)][2 * (e & 1) + 1]; own[e] += f32x4{bflo(d0), bfhi(d0), bflo(d1), bfhi(d1)}; }
        asm volatile("" : "+v"(own[0]), "+v"(own[1]), "+v"(own[2]), "+v"(own[3]));
#pragma unroll
        for (int e = 0; e < 4; e++) sts128f((wa ^ (unsigned)((e >> 1) << 6)) + (e & 1) * 256, own[e]);
        fence_c();
        f32x4 ta[4];
#pragma unroll
        for (int e = 0; e < 4; e++) ta[e] = lds128f((ra ^ (unsigned)((e & 1) << 6)) + e * 1024);
        SB();
#pragma unroll
        for (int e = 0; e < 4; e++) *(GAS f32x4*)(op + e * 8 * DM) = base[e] + ta[e];
        if (b == 255 && w == 4 && lane == 0) __builtin_amdgcn_raw_buffer_store_b32(tg, rws, WS_EPOCH, 0, 16);
        STAMP(12); STAMP_DUMP();
        return;
    }
    constexpr int PD = PDV, XL = XLV, EG = EGATE;
    unsigned stall = 0; (void)stall;
    v4u xa[16][2];
#define AF(u, k) xa[2 * (u) + ((k) >> 1)][(k) & 1]
    v4u sws = make_srd(p.ws);
    unsigned oG = WS_A2 + (unsigned)(i * 44 + 7 * kq) * 16384u + w * 4096, oC = WS_A2 + (unsigned)(i * 44 + 28 + 2 * kq) * 16384u + w * 4096, oF = fo + w * 8;
    asm volatile("" : "+s"(sws), "+s"(oG), "+s"(oC), "+s"(oF));
    auto uoff = [&](int v) __attribute__((always_inline)) { return v < 4 ? oG + v * 16384 : v < 8 ? oC + (8 * ((v >> 1) & 1) + (v & 1)) * 16384 : oG + (v - 4) * 16384; };
    LANE_VARS;
    unsigned fdone = F_DONE + w * 4, l16 = lane * 16;
    asm volatile("" : "+v"(fdone), "+v"(l16));
    unsigned tgs = 0; int raised = 0;
    auto raise = [&](int level) __attribute__((always_inline)) {
        asm volatile("s_mov_b64 exec, 1\n\tbuffer_store_dwordx2 %0, off, %1, %2 offset:%3 sc1\n\ts_mov_b64 exec, -1" ::"v"(v2u{tgs, ~tgs}), "s"(sws), "s"(oF), "n"((level - 1) * 128) : "memory");
        post(fdone + (F_OUT - F_DONE), level);
        raised = level;

        if (level == 1) STAMP(17); else STAMP(18);
    };
    auto try_raise = [&](int level) __attribute__((always_inline)) {
        unsigned v; asm volatile("s_getreg_b32 %0, hwreg(HW_REG_IB_STS, 0, 4)" : "=s"(v)::"memory");
        if (__builtin_expect((int)(v + raised) == level - 1, 0)) raise(level);
    };
    unsigned oX[3];
#pragma unroll
    for (int sl = 1; sl < 4; sl++) { oX[sl - 1] = xo + (kq ^ sl) * 24576u + (sl - 1) * 2048; asm volatile("" : "+s"(oX[sl - 1])); }
    unsigned cbs[2], tbs[4];
    v4u bf[PD];
    const unsigned ffl = F_FULL + (lane & 1) * 4;
    auto set2 = [&](auto kk) __attribute__((always_inline)) {
        constexpr int k = decltype(kk)::value;
        if constexpr (k == 0) tbs[0] = L_P + ((l16 << 1) & 0x780) + ((l16 >> 1) & 0x18) + (((l16 >> 2) & 0x40) | ((l16 >> 2) & 0x20));
        else if constexpr (k < 4) tbs[k] = tbs[0] ^ (unsigned)(k << 5);
        else if constexpr (k == 4) cbs[0] = L_W + ((l16 << 3) & 0x780) + (((l16 >> 4) ^ (l16 >> 1)) & 0x70);
        else cbs[1] = cbs[0] ^ 64u;
        if constexpr (k < 4) asm volatile("" : "+v"(tbs[k])); else asm volatile("" : "+v"(cbs[k - 4]));
    };
    auto prime2 = [&](auto mm) __attribute__((always_inline)) { constexpr int m = decltype(mm)::value; const v2u lo = ldstr(tbs[m]), hi = ldstr(tbs[m] + 2048); bf[m] = v4u{lo.x, lo.y, hi.x, hi.y}; };
    {
        STAMP_INIT();
        STAMP(0);
        unsigned cb[2];
        cb[0] = (unsigned)c * 128u + (unsigned)((g ^ (c >> 1)) << 4); cb[1] = cb[0] ^ 64u;
        f32x4 eX;
        unsigned tb[4];
        f32x4 nm4[2];
        {
            const unsigned voffX = (8 * (q8 >> 2) + 4 * (q8 & 1) + ((q8 >> 1) & 1)) * 1024 + ((p8 ^ (q8 >> 1)) << 4), voffX1 = voffX ^ 64u;
            const v4u srd = make_srd((const unsigned char*)p.x + (size_t)(128 * i + 32 * w) * 1024);
            auto issue_x = [&](int ln) __attribute__((always_inline)) {
                const unsigned s0 = ((ln + rot) & 7) * 128;
                dma4(srd, voffX, voffX1, s0, s0 + 15360, s0, s0 + 15360, sl_lo + (ln & 3) * 4096);
            };
            {
                const unsigned voffS = (8 * (q8 >> 2) + (q8 & 3)) * 1024 + ((p8 ^ (q8 >> 1)) << 4), so = 128 * (((w >> 1) + rot) & 7);
                dma4(make_srd((const unsigned char*)(lw ? p.Bim : p.Bre) + (size_t)(32 * j) * 1024), voffS, voffS ^ 64u, so, so + 15360, so + 2048, so + 17408, L_W + lw * 8192 + (w >> 1) * 4096);
            }
#pragma unroll
            for (int ln = 0; ln < XL; ln++) issue_x(ln);
            v4u ones = {0x3f803f80u, 0x3f803f80u, 0x3f803f80u, 0x3f803f80u};
            unsigned xl[2] = {cb[0] + sl_lo, cb[1] + sl_lo};
            cb[0] += L_W; cb[1] += L_W;
            asm volatile("" : "+v"(ones), "+v"(cb[0]), "+v"(cb[1]), "+v"(xl[0]), "+v"(xl[1]));
            f32x4 sm[2], sq[2], bsum;
            v4u bx[4];
            unsigned cbw[2] = {cb[0] + (w >> 1) * 8192 + (w & 1) * 2048, cb[1] + (w >> 1) * 8192 + (w & 1) * 2048};
            asm volatile("" : "+v"(cbw[0]), "+v"(cbw[1]));
            f32x4 acc[4][2];
            unsigned fl = 0; bool lack = false;
            const unsigned fcs = (lane & 4) ? F_COMM + (lane & 1) * 4 : F_BS + (lane & 3) * 4;
            static_assert(PD == 4 && XL <= 3, "prologue schedule");
            waitv<4 * (XL - 1)>();
#pragma unroll
            for (int f = 0; f < 4; f++) xa[f >> 1][f & 1] = lds128(xl[f >> 1] + (f & 1) * 2048);
            asm volatile("s_barrier" ::: "memory");
            STAMP(1);
            sfor<-PD, 64>(LAMB(nn) {
                constexpr int n = decltype(nn)::value, m = n + PD, ks = n >> 2, ln = n >> 3;
                v4u cur;
                if constexpr (n >= 0) { cur = bf[n % PD]; mma<(n < 4)>(acc[n & 3][0], xa[ks][0], cur); SB(); }
                if constexpr (m > 0 && (m & 15) == 0 && m < 64) need_full(lack, ffl, (m >> 4) + 1);
                if constexpr (m < 64) bf[m % PD] = lds128(cb[(m >> 2) & 1] + wslot(m >> 4) + (((m >> 1) & 1) * 4 + ((m >> 3) & 1) * 2 + (m & 1)) * 2048);
                SB();
                if constexpr (n >= 0) { mma<(n < 4)>(acc[n & 3][1], xa[ks][1], cur); SB(); }
                if constexpr (n >= 0 && (n & 7) < 4 && ln + XL < 8)
                    dma1(srd, (n & 1) ? voffX1 : voffX, ((ln + XL + rot) & 7) * 128 + (n & 1) * 16384 + (n & 2) * 1024, sl_lo + ((ln + XL) & 3) * 4096 + (n & 3) * 1024);
                if constexpr (n >= 0 && (n & 7) == 4 && ln < 7) waitv<4 * ((ln + XL < 7 ? ln + XL : 7) - ln - 1)>();
                if constexpr (n >= 0 && (n & 7) >= 4 && ln < 7) xa[2 * ln + 2 + ((n >> 1) & 1)][n & 1] = lds128(xl[(n >> 1) & 1] + ((ln + 1) & 3) * 4096 + (n & 1) * 2048);
                if constexpr (n == TGN) post(fdone + (F_XI - F_DONE), 1);
                if constexpr (n == 56) post(fdone + (F_XI - F_DONE), 3);
                if constexpr ((m & 15) == 10 && m < 48) fl = peek(ffl);
                if constexpr ((m & 15) == 14 && m < 48) lack = any_lt(fl, (m >> 4) + 2);
                if constexpr ((m & 15) == 15 && m < 63) post(fdone, (m >> 4) + 1);
                if constexpr (m < 64 && (m & 15) < 4) bx[m & 3] = lds128(cbw[m & 1] + wslot(m >> 4) + ((m >> 1) & 1) * 4096);
                if constexpr (n == 55) { asm volatile("" : "+v"(bsum)); sts32f(L_CST + 7 * 256 + c * 16 + (w & 1) * 8 + (w >> 1) * 4, bsum.x); post(fdone + (F_BS - F_DONE), 1); }
                if constexpr (n == 60) fl = peek(fcs);
                SB();
                if constexpr (n >= 0 && (n & 15) < 4) mma<(n == 0)>(bsum, ones, bx[n & 3]);
                if constexpr (n >= 0) { if constexpr (n & 1) mma<(n < 4)>(sq[(n >> 1) & 1], xa[ks][(n >> 1) & 1], xa[ks][(n >> 1) & 1]); else mma<(n < 4)>(sm[(n >> 1) & 1], ones, xa[ks][(n >> 1) & 1]); }
                SB();
            });
            need_full(any_lt(fl, 1), fcs, 1);
            const unsigned cl = L_CST + (l16 & 0xf0);
            const f32x4 ca = lds128f(cl), c4 = lds128f(cl + 256), co = lds128f(cl + 512), bs = lds128f(cl + 7 * 256), c8 = lds128f(L_PW + 256 + (l16 & 0xf0));
            mfma_done();
            STAMP(2); VAL(24, stall); stall = 0;
            {
                float dg[2];
#pragma unroll
                for (int bb = 0; bb < 2; bb++) {
                    nm4[bb] = sm[bb] * (-1.0f / DM);
                    const float s01 = (lane & 1) ? sq[bb].y : sq[bb].x, s23 = (lane & 1) ? sq[bb].w : sq[bb].z;
                    dg[bb] = __builtin_amdgcn_rsqf(fmaxf(((lane & 2) ? s23 : s01) * (1.0f / DM) - nm4[bb].x * nm4[bb].x, 0.0f) + 1e-5f);
                }
                if (g == (c >> 2)) {
                    const unsigned sa = L_RSTD + (32 * w + 8 * g + (c & 3)) * 4;
                    sts32f(sa, dg[0]); sts32f(sa + 16, dg[1]); sts32f(sa + L_MEAN, nm4[0].x); sts32f(sa + L_MEAN + 16, nm4[1].x);
                }
            }
            fence_c();
            const unsigned ra = L_RSTD + (32 * w + 8 * g) * 4;
            const f32x4 nt0 = lds128f(ra + L_MEAN), nt1 = lds128f(ra + L_MEAN + 16), rs0 = lds128f(ra), rs1 = lds128f(ra + 16);
            {
                const int q4 = c >> 2, p4 = c & 3;
                const unsigned tb1 = L_P + (unsigned)(8 * g + q4) * 128u + (unsigned)((((q4 >> 1) & 1) | ((g & 1) << 1)) << 5) + p4 * 8;
#pragma unroll
                for (int t = 0; t < 4; t++) { tb[t] = tb1 ^ (unsigned)(t << 5); asm volatile("" : "+v"(tb[t])); }
            }
            SB();
            STAMP(3);
            f32x4 ev;
            f32x2 sr[2][4], si[2][4];
#pragma unroll
            for (int st = 0; st < 2; st++) {
                const f32x2 b2 = st ? hi2(bs) : lo2(bs);
#pragma unroll
                for (int t = 0; t < 4; t++) {
                    const f32x4 nt = (t & 2) ? nt1 : nt0;
                    const f32x2 n2 = (t & 1) ? hi2(nt) : lo2(nt);
                    sr[st][t] = bfma<0, 0>(b2, n2, (t & 1) ? hi2(acc[st][t >> 1]) : lo2(acc[st][t >> 1]));
                    si[st][t] = bfma<1, 0>(b2, n2, (t & 1) ? hi2(acc[2 + st][t >> 1]) : lo2(acc[2 + st][t >> 1]));
                }
            }
            SB();
#pragma unroll
            for (int st = 0; st < 2; st++)
#pragma unroll
                for (int t = 0; t < 4; t++) {
                    const f32x4 rs = (t & 2) ? rs1 : rs0;
                    const f32x2 r2 = (t & 1) ? hi2(rs) : lo2(rs);
                    sr[st][t] *= r2; si[st][t] *= r2;
                }
            SB();
            const f32x2 a1[2] = {lo2(ca), hi2(ca)}, cf[2] = {lo2(co), hi2(co)};
#pragma unroll
            for (int t = 1; t < 4; t++) {
                f32x2 tr[2], ti[2];
#pragma unroll
                for (int st = 0; st < 2; st++) { tr[st] = bfma<1, 1>(a1[st], si[st][t - 1], sr[st][t]); ti[st] = bfma<1, 0>(a1[st], sr[st][t - 1], si[st][t]); }
                SB();
#pragma unroll
                for (int st = 0; st < 2; st++) { sr[st][t] = bfma<0, 0>(a1[st], sr[st][t - 1], tr[st]); si[st][t] = bfma<0, 0>(a1[st], si[st][t - 1], ti[st]); }
                SB();
            }
#pragma unroll
            for (int st = 0; st < 2; st++) {
                ev[2 * st] = sfma(c4[2 * st], sr[st][3].x, sfnma(c4[2 * st + 1], si[st][3].x, sr[st][3].y));
                ev[2 * st + 1] = sfma(c4[2 * st], si[st][3].x, sfma(c4[2 * st + 1], sr[st][3].x, si[st][3].y));
                eX[2 * st] = sr[st][3].x; eX[2 * st + 1] = si[st][3].x;
            }
            sts128f(L_SEG + w * 1024 + l16, ev);
            fence_c();
            f32x4 es[4];
#pragma unroll
            for (int s = 0; s < 4; s++) es[s] = lds128f(L_SEG + w * 1024 + s * 256 + (l16 & 0xf0));
            SB();
            unsigned ya = L_O + w * 4096 + l16;
            asm volatile("" : "+v"(ya), "+v"(eX));
#pragma unroll
            for (int st = 0; st < 2; st++) {
                f32x2 tr[4], ti[4];
#pragma unroll
                for (int t = 0; t < 4; t++) { tr[t] = bmul<1, 1>(cf[st], si[st][t]); ti[t] = bmul<1, 1>(cf[st], sr[st][t]); }
                SB();
#pragma unroll
                for (int t = 0; t < 4; t++) { tr[t] = bfma<0, 0>(cf[st], sr[st][t], tr[t]); ti[t] = bfma<0, 1>(cf[st], si[st][t], ti[t]); }
                SB();
                sts128(ya + st * 1024, v4u{pk2(tr[0]), pk2(tr[1]), pk2(tr[2]), pk2(tr[3])});
                sts128(ya + 2048 + st * 1024, v4u{pk2(ti[0]), pk2(ti[1]), pk2(ti[2]), pk2(ti[3])});
                SB();
                if (st == 0) {
#pragma unroll
                    for (int s = 1; s < 4; s++) { const f32x2 u0 = cmadp(lo2(c8), lo2(es[s - 1]), lo2(es[s])), u1 = cmadp(hi2(c8), hi2(es[s - 1]), hi2(es[s])); es[s] = f32x4{u0.x, u0.y, u1.x, u1.y}; }
                    sts128f(L_TOT + w * 256 + (l16 & 0xf0), es[3]);
                    post(fdone, 4);
                    f32x4 pin;
#pragma unroll
                    for (int k = 0; k < 4; k++) { const float lo = g == 1 ? es[0][k] : 0.0f, hi = g == 3 ? es[2][k] : es[1][k]; pin[k] = g >= 2 ? hi : lo; }
                    sts128f(L_SEG + w * 1024 + l16, pin);
                    SB();
                }
            }
        }
        STAMP(4);
        {
            const v4u sel0 = lds128(L_SEL + l16), sel1 = lds128(L_SEL + 1024 + l16);
            unsigned fl = 0;
            f32x4 ct[CRING];
#pragma unroll
            for (int k = 0; k < CRING; k++) asm volatile("" : "=v"(ct[k]));
            sfor<0, 64 + CLAG>(LAMB(kk) {
                constexpr int k = decltype(kk)::value, e = k - CLAG;
                if constexpr (k < 64) mma_t(ct[k % CRING], (k & 1) ? sel1 : sel0, xa[k >> 2][(k >> 1) & 1], nm4[(k >> 1) & 1]);
                if constexpr (k >= 64 || k < 4) asm volatile("s_nop 1");
                if constexpr (e >= 0) {
                    xa[e >> 2][(e >> 1) & 1][2 * (e & 1)] = cvtv(ct[e % CRING].x, ct[e % CRING].y);
                    xa[e >> 2][(e >> 1) & 1][2 * (e & 1) + 1] = cvtv(ct[e % CRING].z, ct[e % CRING].w);
                }
                if constexpr (k == 40) fl = peek(ffl);
                if constexpr (k == 52) need_full(any_lt(fl, 5), ffl, 5);
                if constexpr (k >= 53 && k < 53 + PD) { constexpr int m = k - 53; const unsigned a = tb[(m >> 1) & 3] + (m & 1) * 8192; const v2u lo = ldstr(a), hi = ldstr(a + 512); bf[(64 + m) % PD] = v4u{lo.x, lo.y, hi.x, hi.y}; }
                SB();
            });
        }
        STAMP(5);
        unsigned go = oG + q * 16384, so = oC + (q >> 1) * 16384 + (q & 1) * 2048;
        asm volatile("" : "+s"(go), "+s"(so));
        f32x4 acc[4][2][2];
        f32x2 r12;
        unsigned fc = 0;
        auto gpass = [&](auto pp, auto n0, auto n1) __attribute__((always_inline)) {
            constexpr int pass = decltype(pp)::value;
            unsigned fl = 0, dn = 0; bool lack = false;
            sfor<decltype(n0)::value, decltype(n1)::value>(LAMB(nn) {
                constexpr int n = decltype(nn)::value, m = n + PD;
                constexpr bool pre = pass == 0 || m < 128;
                constexpr unsigned off = wslot(m >> 4) + (m & 1) * 8192 + ((m >> 3) & 1) * 4096;
                const v4u cur = bf[(n + 64 + 128 * pass) % PD];
                mma<(n < 8)>(acc[(n >> 1) & 3][n & 1][0], cur, xa[n >> 3][0]);
                SB();
                if constexpr ((m & 15) == 0) post(fdone, dn);
                if constexpr ((m & 15) == 0) need_full(lack, ffl, 5 + 8 * pass + (m >> 4));
                v2u lo, hi;
                if constexpr (pre) lo = ldstr(tb[(m >> 1) & 3] + off); else lo = ldstr(tbs[m & 3]);
                if constexpr (pass == 1 && n >= 69 && n < 81 && (n & 1)) set2(IC<(n - 69) / 2>{});
                if constexpr ((m & 15) == 10) fl = peek(ffl);
                if constexpr ((m & 15) == 15) lack = any_lt(fl, m == 127 && pass == 0 ? N1 - 7 : 6 + 8 * pass + (m >> 4));
                if constexpr ((m & 15) == 13) { dn = 5 + 8 * pass + (m >> 4); asm volatile("" : "+v"(dn)); }
                if constexpr (n == EG - 8 && pass == 1) fc = peek(F_RDY);
                if constexpr (n == EG - 2 && pass == 1) { if (__builtin_expect(any_lt(fc, 1), 0)) { wait_ge(F_RDY, 1); } fence_c(); }
                if constexpr (n >= EG && n < EG + 24 && pass == 1 && (n & 1) == 0) { constexpr int f = (n - EG) >> 1; reldA<(f & 3) * 1024>(AF(f >> 2, f & 3), l16, sws, uoff(f >> 2)); }
                if constexpr (pass == 1 && (n == 33 || n == 41 || n == 49 || n == 57)) try_raise(1);
                if constexpr (pass == 1 && n == 67) { if (raised < 1) { waitv<0>(); raise(1); } }
                if constexpr (n == 120 && pass == 0) fc = peek(ffl + (F_COMM - F_FULL));
                if constexpr (n == 121 && pass == 0) { const unsigned ra = L_RSTD + w * 128 + (((l16 >> 1) & 0x60) | ((l16 >> 2) & 0xc)); r12 = f32x2{lds32f(ra), lds32f(ra + 16)}; }
                SB();
                mma<(n < 8)>(acc[(n >> 1) & 3][n & 1][1], cur, xa[n >> 3][1]);
                SB();
                if constexpr (pre) hi = ldstr(tb[(m >> 1) & 3] + off + 512); else hi = ldstr(tbs[m & 3] + 2048);
                bf[n % PD] = v4u{lo.x, lo.y, hi.x, hi.y};
                if constexpr ((m & 57) == 57 && (pass == 0 || m < 64)) {
                    tb[(m >> 1) & 3] += (m & 64) ? L_P - L_W : L_W - L_P; asm volatile("" : "+v"(tb[(m >> 1) & 3]));
                }
                SB();
            });
        };
        auto gated = [&](auto ff) __attribute__((always_inline)) {
            constexpr int uu = decltype(ff)::value >> 1, bb = decltype(ff)::value & 1;
            const f32x4 h0 = acc[2 * uu][0][bb] * acc[2 * uu][1][bb], h1 = acc[2 * uu + 1][0][bb] * acc[2 * uu + 1][1][bb];
            return v4u{pk2(bmul<bb, 0>(r12, lo2(h0))), pk2(bmul<bb, 0>(r12, hi2(h0))), pk2(bmul<bb, 0>(r12, lo2(h1))), pk2(bmul<bb, 0>(r12, hi2(h1)))};
        };
        gpass(IC<0>{}, IC<0>{}, IC<128>{});
        {
            r12 = r12 * r12;
            mfma_done();
            STAMP(6); VAL(25, stall); stall = 0;
            sfor<0, 2>(LAMB(ff) { stA<decltype(ff)::value * 1024>(gated(ff), l16, sws, go); });
            SB();
            need_full(any_lt(fc, 4), ffl + (F_COMM - F_FULL), 4);
            const unsigned tg = peek(L_TAG);
            const unsigned cl = L_CST + (l16 & 0xf0);
            unsigned ya = L_O + w * 4096 + l16;
            asm volatile("" : "+v"(ya));
            const f32x4 h0 = lds128f(L_XCH + 256 + (l16 & 0xf0)), h1 = lds128f(L_XCH + 512 + (l16 & 0xf0));
            const f32x4 aw = lds128f(L_PW + 1024 + w * 256 + (l16 & 0xf0)), pb = lds128f(L_PRE + w * 256 + (l16 & 0xf0));
            const f32x4 ag = lds128f(L_PW + l16), pin = lds128f(L_SEG + w * 1024 + l16);
            const v4u sel[2] = {lds128(L_SEL + l16), lds128(L_SEL + 1024 + l16)};
            const v4u yr[2] = {lds128(ya), lds128(ya + 1024)}, yi[2] = {lds128(ya + 2048), lds128(ya + 3072)};
            const f32x4 c4 = lds128f(cl + 256);
            f32x4 cw[4];
            SB();
            sfor<2, 4>(LAMB(ff) { stA<decltype(ff)::value * 1024>(gated(ff), l16, sws, go); });
            tgs = __builtin_amdgcn_readfirstlane(tg);
#pragma unroll
            for (int t = 0; t < 4; t++) cw[t] = lds128f(cl + (3 + t) * 256);
            SB();
            f32x4 zr[2][2], zi[2][2];
#pragma unroll
            for (int st = 0; st < 2; st++)
#pragma unroll
                for (int bb = 0; bb < 2; bb++) { mma<true>(zr[st][bb], yr[st], sel[bb]); mma<true>(zi[st][bb], yi[st], sel[bb]); }
            SB();
            f32x2 hr[2], hi[2];
            {
                const f32x4 cy = h0 + h1;
                const f32x2 k0 = cmadp(lo2(ag), cmadp(lo2(aw), lo2(cy), lo2(pb)), lo2(pin)), k1 = cmadp(hi2(ag), cmadp(hi2(aw), hi2(cy), hi2(pb)), hi2(pin));
                const f32x4 hin = {k0.x, k0.y, k1.x, k1.y};
                float t0[2], t1[2];
#pragma unroll
                for (int st = 0; st < 2; st++) { t0[st] = sfnma(c4[2 * st + 1], hin[2 * st + 1], eX[2 * st]); t1[st] = sfma(c4[2 * st + 1], hin[2 * st], eX[2 * st + 1]); }
                SB();
#pragma unroll
                for (int st = 0; st < 2; st++) { hr[st] = f32x2{hin[2 * st], sfma(c4[2 * st], hin[2 * st], t0[st])}; hi[st] = f32x2{hin[2 * st + 1], sfma(c4[2 * st], hin[2 * st + 1], t1[st])}; }
                SB();
            }
#pragma unroll
            for (int st = 0; st < 2; st++) {
                f32x2 tr[4], ti[4];
#pragma unroll
                for (int t = 0; t < 4; t++) { const f32x2 wv = st ? hi2(cw[t]) : lo2(cw[t]); tr[t] = bmul<1, 1>(wv, hi[st]); ti[t] = bmul<1, 1>(wv, hr[st]); }
                SB();
#pragma unroll
                for (int t = 0; t < 4; t++) { const f32x2 wv = st ? hi2(cw[t]) : lo2(cw[t]); tr[t] = bfma<0, 0>(wv, hr[st], tr[t]); ti[t] = bfma<0, 1>(wv, hi[st], ti[t]); }
                SB();
                v4u fr = {pk2(tr[0]), pk2(tr[1]), pk2(tr[2]), pk2(tr[3])}, fi = {pk2(ti[0]), pk2(ti[1]), pk2(ti[2]), pk2(ti[3])};
                asm volatile("s_nop 1" : "+v"(fr), "+v"(fi));
#pragma unroll
                for (int bb = 0; bb < 2; bb++) { mma<false>(zr[st][bb], fr, sel[bb]); mma<false>(zi[st][bb], fi, sel[bb]); }
                SB();
            }
            mfma_done();
            stA<0>(pk8(zr[0][0], zr[1][0]), l16, sws, so); stA<0>(pk8(zi[0][0], zi[1][0]), l16, sws, so + 8 * 16384);
            stA<1024>(pk8(zr[0][1], zr[1][1]), l16, sws, so); stA<1024>(pk8(zi[0][1], zi[1][1]), l16, sws, so + 8 * 16384);
            SB();
            STAMP(7);
        }
        if (heavy) {
            gpass(IC<1>{}, IC<0>{}, IC<128>{});
            mfma_done();
            STAMP(8); VAL(26, stall); stall = 0;
            sfor<0, 4>(LAMB(ff) { stA<decltype(ff)::value * 1024>(gated(ff), l16, sws, go + 4 * 16384); });
            SB();
        }
        int q2 = q;
        asm volatile("" : "+s"(q2));
        if (q2 == 3) {
            waitv<0>(); raise(1); raise(2);
            sfor<0, 6>(set2);
            wait_ge(ffl, N1 + 1);
            sfor<0, PD>(prime2);
            wait_ge(F_RDY, 1);
            sfor<0, 12>(LAMB(ff) { constexpr int f = decltype(ff)::value; reldA<(f & 3) * 1024>(AF(f >> 2, f & 3), l16, sws, uoff(f >> 2)); });
#pragma unroll
            for (int u = 0; u < 3; u++) asm volatile("s_waitcnt vmcnt(0)" : "+v"(AF(u, 0)), "+v"(AF(u, 1)), "+v"(AF(u, 2)), "+v"(AF(u, 3))::"memory");
        }
    }
    STAMP(9);
    {
        f32x4 acc[8][2];
        v4u pk[2];
        unsigned fl = 0, fr = 0, dn = 0; bool lack = false;
        constexpr auto fv = [](int n) { return n < 48 ? n >> 4 : n < 108 ? 3 + (n - 48) / 12 : n < 144 ? 8 + ((n - 108) % 12) / 4 : 3 + (n - 144) / 4; };
        constexpr auto fk = [](int n) { return n < 48 ? (n >> 3) & 1 : n < 108 ? ((n - 48) % 12) / 6 : (n >> 1) & 1; };
        constexpr auto fd = [](int n) { return n < 48 ? n & 7 : n < 108 ? 2 + (n - 48) % 6 : n < 144 ? 2 * ((n - 108) / 12 + 1) + (n & 1) : n & 1; };
        constexpr auto first = [](int n) { return n >= 0 && (n < 48 ? (n & 15) == 0 : n < 108 ? (n - 48) % 12 == 0 : n < 120 && (n & 3) == 0); };
        constexpr int LG = LGATE;
        sfor<0, 176>(LAMB(nn) {
            constexpr int n = decltype(nn)::value, m = n + PD;
            constexpr int v = fv(n), ks = fk(n), dt = fd(n);
            constexpr int mv = fv(m), mk = fk(m), md = fd(m);
            constexpr bool mc = (mv & ~3) == 4;
            constexpr unsigned moff = (mv & 3) * SLOT + (mc ? md * 2048 : (md >> 2) * 8192 + mk * 4096);
            constexpr auto chk = [](int q) { return q == 16 ? 2 : q == 32 ? 4 : q == 60 ? 8 : q == 108 ? 11 : 0; };
            v4u cur; v2u lo, hi;
            if constexpr (n >= 0) {
                if constexpr (first(n)) {
                    constexpr int mid = n <= 32 ? 0 : 4 + ((n - 31) / 2 < 16 ? (n - 31) / 2 : 16), late = n <= LG ? 0 : n - LG < 12 ? n - LG : 12;
                    constexpr int NW = v == 0 ? 12 : v == 1 ? 8 : v == 2 ? 63 : v < 8 ? mid - 4 * (v - 2) + late : late - 4 * (v - 7);
                    static_assert(NW >= 0, "load schedule");
                    asm volatile("s_waitcnt vmcnt(%4)" : "+v"(AF(v & 7, 0)), "+v"(AF(v & 7, 1)), "+v"(AF(v & 7, 2)), "+v"(AF(v & 7, 3)) : "n"(NW) : "memory");
                }
                cur = bf[n % PD];
                mma<(n < 8)>(acc[dt][0], cur, AF(v & 7, 2 * ks));
                SB();
            }
            if constexpr (m > 0 && (m & 15) == 0 && m <= 48) post(fdone, dn);
            if constexpr (chk(m) > 0) need_full(lack, ffl, N1 + chk(m));
            if constexpr (m < 176) { if constexpr (mc) bf[m % PD] = lds128(cbs[mk] + moff); else lo = ldstr(tbs[md & 3] + moff); }
            if constexpr (chk(m + 6) > 0) fl = peek(ffl);
            if constexpr (chk(m + 1) > 0) lack = any_lt(fl, N1 + chk(m + 1));
            if constexpr ((m & 15) == 13 && m < 48) { dn = N1 + mv + 1; asm volatile("" : "+v"(dn)); }
            if constexpr (n == 17 || n == 21) try_raise(2);
            if constexpr (n == 20) fr = peek(F_RDY);
            if constexpr (n == 26) { if (raised < 2) { waitv<0>(); raise(2); } }
            if constexpr (n == 27) need_full(any_lt(fr, 2), F_RDY, 2);
            if constexpr (n >= 28 && n < 32) reldA<(n & 3) * 1024>(AF(3, n & 3), l16, sws, uoff(3));
            if constexpr (n >= 32 && n < 64 && (n & 1) == 0) { constexpr int f = (n - 32) >> 1; reldA<(f & 3) * 1024>(AF(4 + (f >> 2), f & 3), l16, sws, uoff(4 + (f >> 2))); }
            if constexpr (n == LG - 6) fr = peek(F_RDY + 4);
            if constexpr (n == LG - 1) need_full(any_lt(fr, 1), F_RDY + 4, 1);
            if constexpr (n >= LG && n < LG + 12) { constexpr int f = n - LG; reldA<(f & 3) * 1024>(AF(f >> 2, f & 3), l16, sws, uoff(8 + (f >> 2))); }
            SB();
            if constexpr (n >= 0) { mma<(n < 8)>(acc[dt][1], cur, AF(v & 7, 2 * ks + 1)); SB(); }
            if constexpr (m < 176 && !mc) { hi = ldstr(tbs[md & 3] + moff + 2048); bf[m % PD] = v4u{lo.x, lo.y, hi.x, hi.y}; }
            if constexpr (n >= 120 && n < 156) {
                constexpr int sl = (n - 108) / 12, r = (n - 108) % 12 - 2;
                if constexpr (r >= 0 && r < 10 && r % 5 < 4) { const f32x4& a = acc[2 * sl + r / 5][(r % 5) >> 1]; pk[r / 5][r % 5] = cvtv(a[2 * (r % 5 & 1)], a[2 * (r % 5 & 1) + 1]); }
                if constexpr (r >= 0 && r % 5 == 4) stX<(r / 5) * 1024>(pk[r / 5], l16, sws, oX[sl - 1]);
            }
            if constexpr (n == 156) post(fdone + (F_SENT - F_DONE), 3);
            if constexpr (n == 107) { STAMP(10); VAL(28, stall); stall = 0; }
            SB();
        });
        mfma_done();
        STAMP(11); VAL(29, stall);
#pragma unroll
        for (int e = 0; e < 4; e++) sts128f(L_O + w * 4096 + e * 1024 + l16, acc[e >> 1][e & 1]);
        post(fdone + (F_SENT - F_DONE), 4);
    }
    STAMP(14); STAMP_DUMP();
}

extern "C" void kernel_launch(void* const* d_in, const int* in_sizes, int n_in, void* d_out, int out_size, void* d_ws, size_t ws_size,
                              hipStream_t stream) {
    Params p;
    p.x = (const u16*)d_in[0]; p.lre = (const u16*)d_in[1]; p.lim = (const u16*)d_in[2];
    p.Bre = (const u16*)d_in[3]; p.Bim = (const u16*)d_in[4]; p.Cre = (const u16*)d_in[5]; p.Cim = (const u16*)d_in[6];
    p.ldt = (const u16*)d_in[7]; p.Dsk = (const u16*)d_in[8];
    p.wg = (const u16*)d_in[10]; p.wu = (const u16*)d_in[11]; p.wd = (const u16*)d_in[12];
    p.out = (float*)d_out; p.ws = (unsigned char*)d_ws + WS_SHIFT;
    (void)hipFuncSetAttribute((const void*)fused, hipFuncAttributeMaxDynamicSharedMemorySize, L_TOTAL);
    fused<<<256, 512, L_TOTAL, stream>>>(p);
}
```

```cpp
#define T2_SLEEP 14
#define PF_SLEEP 2
#include <hip/hip_runtime.h>
#include <stdint.h>

typedef unsigned short u16;
typedef unsigned v4u __attribute__((ext_vector_type(4)));
typedef unsigned v2u __attribute__((ext_vector_type(2)));
typedef short s16x4 __attribute__((ext_vector_type(4)));
typedef __bf16 bf16x2 __attribute__((ext_vector_type(2)));
typedef __bf16 bf16x8 __attribute__((ext_vector_type(8)));
typedef float f32x2 __attribute__((ext_vector_type(2)));
typedef float f32x4 __attribute__((ext_vector_type(4)));
#define GAS __attribute__((address_space(1)))
#define LAS __attribute__((address_space(3)))
#define DEVI __device__ __forceinline__
#define NOUNROLL _Pragma("nounroll")

#define SPIN_SLEEP 0
constexpr int DM = 512, FF = 1792;
constexpr unsigned WS_A2 = 0;
constexpr unsigned WS_XB = 16u * 44u * 16384u;
constexpr unsigned WS_GRAN = WS_XB + 256u * 24576u;
constexpr unsigned WS_FLAG = WS_GRAN + 16u * 16u * 512u;
constexpr unsigned WS_EPOCH = WS_FLAG + 1024u * 128u;
constexpr unsigned WS_END = WS_EPOCH + 256u;
constexpr unsigned SLOT = 16384;
constexpr unsigned L_RSTD = 0, L_MEAN = 512;
constexpr unsigned F_DONE = 1024, F_SENT = F_DONE + 16, F_FULL = F_DONE + 32, F_COMM = F_DONE + 40, F_RDY = F_DONE + 48, F_OUT = F_DONE + 64, F_XI = F_DONE + 80, F_BS = F_DONE + 96, L_TAG = F_DONE + 128;
constexpr unsigned L_CST = 1280;
constexpr unsigned L_XCH = L_CST + 8 * 256;
constexpr unsigned L_SEG = 4096, L_PW = 8192, L_TOT = L_PW + 2048, L_PRE = L_TOT + 1024, L_SEL = 12288;
constexpr unsigned L_P = SLOT, L_O = 5 * SLOT, L_W = 6 * SLOT, L_TOTAL = 10 * SLOT;
constexpr int SPIN_MAX = 1 << 18;
constexpr int LSPIN_MAX = 1 << 22;

struct Params {
    const u16 *x, *lre, *lim, *Bre, *Bim, *Cre, *Cim, *ldt, *Dsk, *wg, *wu, *wd;
    float* out;
    unsigned char* ws;
};

DEVI float bf2f(u16 v) { return __uint_as_float(((unsigned)v) << 16); }
DEVI float bflo(unsigned v) { return __uint_as_float(v << 16); }
DEVI float bfhi(unsigned v) { return __uint_as_float(v & 0xffff0000u); }
DEVI unsigned pk2(float a, float b) { f32x2 p = {a, b}; return __builtin_bit_cast(unsigned, __builtin_convertvector(p, bf16x2)); }
DEVI unsigned pk2(f32x2 p) { return __builtin_bit_cast(unsigned, __builtin_convertvector(p, bf16x2)); }
DEVI v4u pk8(f32x4 a, f32x4 b) { return v4u{pk2(a.x, a.y), pk2(a.z, a.w), pk2(b.x, b.y), pk2(b.z, b.w)}; }
DEVI float dot2(unsigned a, unsigned b, float c) { return __builtin_amdgcn_fdot2_f32_bf16(__builtin_bit_cast(bf16x2, a), __builtin_bit_cast(bf16x2, b), c, false); }
template <int CTRL> DEVI float dppf(float v) { return __builtin_bit_cast(float, __builtin_amdgcn_update_dpp(0, __builtin_bit_cast(int, v), CTRL, 0xf, 0xf, true)); }
DEVI f32x4 mfma(v4u a, v4u b, f32x4 c) { return __builtin_amdgcn_mfma_f32_16x16x32_bf16(__builtin_bit_cast(bf16x8, a), __builtin_bit_cast(bf16x8, b), c, 0, 0, 0); }
template <bool FIRST> DEVI void mma(f32x4& c, const v4u& a, const v4u& b) {
    if constexpr (FIRST) asm volatile("v_mfma_f32_16x16x32_bf16 %0, %1, %2, 0" : "=&v"(c) : "v"(a), "v"(b));
    else asm volatile("v_mfma_f32_16x16x32_bf16 %0, %1, %2, %0" : "+v"(c) : "v"(a), "v"(b));
}
DEVI void mfma_done() { __builtin_amdgcn_sched_barrier(0); asm volatile("s_nop 7"); __builtin_amdgcn_sched_barrier(0); }
DEVI v4u lds128(unsigned a) { return *(LAS v4u*)(uintptr_t)a; }
DEVI f32x4 lds128f(unsigned a) { return *(LAS f32x4*)(uintptr_t)a; }
DEVI float lds32f(unsigned a) { return *(LAS float*)(uintptr_t)a; }
DEVI void sts32f(unsigned a, float v) { *(LAS float*)(uintptr_t)a = v; }
DEVI void sts128(unsigned a, v4u v) { *(LAS v4u*)(uintptr_t)a = v; }
DEVI void sts128f(unsigned a, f32x4 v) { *(LAS f32x4*)(uintptr_t)a = v; }
DEVI v2u ldstr(unsigned a) { return __builtin_bit_cast(v2u, __builtin_amdgcn_ds_read_tr16_b64_v4i16((LAS s16x4*)(uintptr_t)a)); }
DEVI void fence_c() { asm volatile("" ::: "memory"); }
DEVI void bar_l() { asm volatile("s_waitcnt lgkmcnt(0)\n\ts_barrier" ::: "memory"); }
template <int N> DEVI void waitv() { asm volatile("s_waitcnt vmcnt(%0)" ::"n"(N) : "memory"); }
DEVI void dma16(const void* sbase, unsigned voff, unsigned ldst) {
    asm volatile("s_mov_b32 m0, %2\n\ts_nop 0\n\tglobal_load_lds_dwordx4 %0, %1" ::"v"(voff), "s"(sbase), "s"(ldst) : "memory");
}
DEVI void post(unsigned a, unsigned v) { fence_c(); *(LAS unsigned*)(uintptr_t)a = v; fence_c(); }
DEVI unsigned peek(unsigned a) { fence_c(); return *(LAS unsigned*)(uintptr_t)a; }
DEVI bool any_lt(unsigned v, unsigned need) { return __builtin_amdgcn_ballot_w64(v < need) != 0; }
DEVI void wait_ge(unsigned a, unsigned need) {
    int s = LSPIN_MAX;
    NOUNROLL while (any_lt(peek(a), need)) { asm volatile("s_sub_i32 %0, %0, 1" : "+s"(s) :: "scc"); if (s < 0) break; if (SPIN_SLEEP) __builtin_amdgcn_s_sleep(SPIN_SLEEP); }
    fence_c();
}
template <int V> DEVI void postm(unsigned a, int lane) {
    asm volatile("s_mov_b64 exec, %2\n\tds_max_u32 %0, %1\n\ts_mov_b64 exec, -1" ::"v"(a), "v"(lane), "n"(1 << V) : "memory");
}
DEVI unsigned vm_outstanding() {
    unsigned v; asm volatile("s_getreg_b32 %0, hwreg(HW_REG_IB_STS)" : "=s"(v)::"memory");
    return (v & 15u) | ((v >> 18) & 0x30u);
}
DEVI void postv(unsigned a, int lane, unsigned v) {
    asm volatile("s_lshl_b64 exec, 1, %2\n\tds_max_u32 %0, %1\n\ts_mov_b64 exec, -1" ::"v"(a), "v"(lane), "s"(v) : "memory", "scc");
}
template <int V> DEVI void postc(unsigned a, int lane) {
    asm volatile("s_mov_b64 exec, %2\n\tds_write_b32 %0, %1\n\ts_mov_b64 exec, -1" ::"v"(a), "v"(lane), "n"(1 << V) : "memory");
}
DEVI void need_full(bool lack, unsigned a, unsigned need) { if (__builtin_expect(lack, 0)) wait_ge(a, need); fence_c(); }
#define VAL(id, v)
DEVI float sfma(float a, float b, float c) { float d; asm("v_fma_f32 %0, %1, %2, %3" : "=v"(d) : "v"(a), "v"(b), "v"(c)); return d; }
DEVI float sfms(float a, float b, float c) { float d; asm("v_fma_f32 %0, %1, %2, -%3" : "=v"(d) : "v"(a), "v"(b), "v"(c)); return d; }
DEVI float sfnma(float a, float b, float c) { float d; asm("v_fma_f32 %0, -%1, %2, %3" : "=v"(d) : "v"(a), "v"(b), "v"(c)); return d; }
DEVI float sadd(float a, float b) { float d; asm("v_add_f32 %0, %1, %2" : "=v"(d) : "v"(a), "v"(b)); return d; }
DEVI float smul(float a, float b) { float d; asm("v_mul_f32 %0, %1, %2" : "=v"(d) : "v"(a), "v"(b)); return d; }
DEVI float smul_t(float a, float b) { float d; asm("s_nop 0\n\tv_mul_f32 %0, %1, %2" : "=v"(d) : "v"(a), "v"(b)); return d; }
struct cpx { float x, y; };
DEVI cpx cmul(cpx a, cpx b) { return cpx{sfms(a.x, b.x, smul(a.y, b.y)), sfma(a.x, b.y, smul(a.y, b.x))}; }
DEVI float lmad(cpx a, float h, float u) {
    float d;
    asm("v_fma_f32 %0, %1, %3, %4\n\ts_nop 0\n\tv_fmac_f32_dpp %0, %3, %2 quad_perm:[1,0,3,2] row_mask:0xf bank_mask:0xf" : "=&v"(d) : "v"(a.x), "v"(a.y), "v"(h), "v"(u));
    return d;
}
DEVI f32x2 dup(float v) { f32x2 d = {v, v}; asm("" : "+v"(d)); return d; }
DEVI f32x2 pfma(f32x2 a, f32x2 b, f32x2 c) { return __builtin_elementwise_fma(a, b, c); }
DEVI f32x2 lo2(f32x4 v) { return f32x2{v.x, v.y}; }
DEVI f32x2 hi2(f32x4 v) { return f32x2{v.z, v.w}; }
#define SB() __builtin_amdgcn_sched_barrier(0)
template <int V> struct IC { static constexpr int value = V; };
template <int B, int E, class F> DEVI void sfor(F&& f) { if constexpr (B < E) { f(IC<B>{}); sfor<B + 1, E>(f); } }
#define LAMB(nn) [&](auto nn) __attribute__((always_inline))
#define STAMP(id)
#define STAMP_INIT()
#define STAMP_DUMP()
DEVI int fresh_lane() { int l; asm volatile("v_mbcnt_lo_u32_b32 %0, -1, 0\n\tv_mbcnt_hi_u32_b32 %0, -1, %0" : "=v"(l)); return l; }
DEVI v4u make_sel(int lane, int bb) {
    const int c = lane & 15, tk = 8 * (c >> 2) + 4 * bb + (c & 3) - 8 * (lane >> 4);
    v4u s;
#pragma unroll
    for (int e = 0; e < 4; e++) s[e] = (tk == 2 * e ? 0x3f80u : 0u) | (tk == 2 * e + 1 ? 0x3f800000u : 0u);
    return s;
}
#define LANE_VARS const int lane = fresh_lane(), c = lane & 15, g = lane >> 4, q8 = lane >> 3, p8 = lane & 7; (void)c; (void)g; (void)q8; (void)p8
constexpr unsigned wslot(int n) { return (unsigned)(n & 3) * SLOT; }

#define ROT1 ((i + 2 * kq) & 7)
#define ROT2 0
#define XI_STEP 8
#define TOUCH_GATE 1
#define LB_SLEEP 8
#define LB_FIRST 1
#define FL_FIRST 20
#define FL_SLEEP 6
#define MIDV 64
#define X_FIRST 14
#define X_SLEEP 1
#define PDV 4
#define INFL 8
#define TSL 6
#define TW __builtin_amdgcn_s_sleep(TSL);
#define PACE 14
#define PACE2 8
#define XLV 3
#define LGATE 80
#define RAISE2 23
#define TGN 37
#define EGATE 102
#define X_LAST 6
#define CLAG 4
#define CRING 8
#define WS_SHIFT 0
#define NOXT
#define LT_SLEEP 16
#define MG 18
#define ABLW(v) 0
#define WR_DEPTH 4
#define SLEEP0 12
constexpr unsigned CODE_LINES = 306;
template <int OFF> DEVI void reldA(v4u& d, unsigned voff, v4u srd, unsigned so) {
    asm volatile("buffer_load_dwordx4 %0, %1, %2, %3 offen offset:%4 sc1" : "+v"(d) : "v"(voff), "s"(srd), "s"(so), "n"(OFF) : "memory");
}
template <int OFF> DEVI void stA(const v4u& d, unsigned voff, v4u srd, unsigned so) {
    asm volatile("buffer_store_dwordx4 %0, %1, %2, %3 offen offset:%4 sc1\n\ts_nop 1" ::"v"(d), "v"(voff), "s"(srd), "s"(so), "n"(OFF) : "memory");
}
template <int OFF> DEVI void stX(const v4u& d, unsigned voff, v4u srd, unsigned so) {
    asm volatile("buffer_store_dwordx4 %0, %1, %2, %3 offen offset:%4 sc1" ::"v"(d), "v"(voff), "s"(srd), "s"(so), "n"(OFF) : "memory");
}
DEVI void ldW(v4u& d, unsigned voff, v4u srd, unsigned so) { asm volatile("buffer_load_dwordx4 %0, %1, %2, %3 offen" : "=v"(d) : "v"(voff), "s"(srd), "s"(so) : "memory"); }
DEVI void touch(unsigned& sink, const void* tp, unsigned to) { asm volatile("global_load_dword %0, %1, %2" : "+v"(sink) : "v"(to), "s"(tp) : "memory"); }

DEVI v4u make_srd(const void* base) { const unsigned long long a = (unsigned long long)base; return v4u{(unsigned)a, (unsigned)(a >> 32) & 0xffffu, 0x7fffffffu, 0x00020000u}; }
DEVI void dma4(v4u srd, unsigned ve, unsigned vo, unsigned s0, unsigned s1, unsigned s2, unsigned s3, unsigned ldst) {
    asm volatile("s_mov_b32 m0, %7\n\ts_nop 0\n\tbuffer_load_dwordx4 %0, %2, %3 offen lds\n\tbuffer_load_dwordx4 %1, %2, %4 offen offset:1024 lds\n\t"
                 "buffer_load_dwordx4 %0, %2, %5 offen offset:2048 lds\n\tbuffer_load_dwordx4 %1, %2, %6 offen offset:3072 lds"
                 ::"v"(ve), "v"(vo), "s"(srd), "s"(s0), "s"(s1), "s"(s2), "s"(s3), "s"(ldst) : "memory");
}
DEVI void dma1(v4u srd, unsigned v, unsigned so, unsigned ldst) {
    asm volatile("s_mov_b32 m0, %3\n\ts_nop 0\n\tbuffer_load_dwordx4 %0, %1, %2 offen lds" ::"v"(v), "s"(srd), "s"(so), "s"(ldst) : "memory");
}
template <int H, int NEG> DEVI f32x2 bmul(f32x2 a, f32x2 b) {
    f32x2 d; asm("v_pk_mul_f32 %0, %1, %2 op_sel:[%3,0] op_sel_hi:[%3,1] neg_lo:[%4,0] neg_hi:[%4,0]" : "=v"(d) : "v"(a), "v"(b), "n"(H), "n"(NEG)); return d;
}
template <int H, int NEG> DEVI f32x2 bfma(f32x2 a, f32x2 b, f32x2 c) {
    f32x2 d; asm("v_pk_fma_f32 %0, %1, %2, %3 op_sel:[%4,0,0] op_sel_hi:[%4,1,1] neg_lo:[%5,0,0] neg_hi:[%5,0,0]" : "=v"(d) : "v"(a), "v"(b), "v"(c), "n"(H), "n"(NEG)); return d;
}
DEVI f32x2 cmadp(f32x2 a, f32x2 h, f32x2 u) {
    f32x2 t, d;
    asm("v_pk_fma_f32 %0, %1, %2, %3 op_sel:[0,0,0] op_sel_hi:[0,1,1]" : "=v"(t) : "v"(h), "v"(a), "v"(u));
    asm("v_pk_fma_f32 %0, %1, %2, %3 op_sel:[1,0,0] op_sel_hi:[0,0,1] neg_lo:[1,0,0]" : "=v"(d) : "v"(a), "v"(f32x2{h.y, h.y}), "v"(t));
    return d;
}
DEVI void mma_c(f32x4& d, const v4u& a, const v4u& b, const f32x4& c) { asm volatile("v_mfma_f32_16x16x32_bf16 %0, %1, %2, %3" : "=&v"(d) : "v"(a), "v"(b), "v"(c)); }
DEVI void mma_t(f32x4& d, const v4u& a, const v4u& b, const f32x4& c) { asm volatile("v_mfma_f32_16x16x32_bf16 %0, %1, %2, %3" : "+v"(d) : "v"(a), "v"(b), "v"(c)); }
DEVI unsigned cvtv(float a, float b) { unsigned d; asm volatile("v_cvt_pk_bf16_f32 %0, %1, %2" : "=v"(d) : "v"(a), "v"(b)); return d; }

__global__ __launch_bounds__(512, 2) void fused(Params p) {
    const unsigned char* pc0;
    asm volatile("s_getpc_b64 %0" : "=s"(pc0));
    const int w = __builtin_amdgcn_readfirstlane(threadIdx.x >> 6);
    const int b = blockIdx.x, xcd = b & 7, idx = b >> 3;
    const int i = 8 * (xcd & 1) + (idx & 7), kq = xcd >> 1, q = idx >> 3, j = 4 * kq + q;
    const bool heavy = q != 3;
    const int N1 = heavy ? 20 : 12;
    const int lw = w & 1, pr = w & 3;
    const int rot = ROT1;
    const __amdgpu_buffer_rsrc_t rws = __builtin_amdgcn_make_buffer_rsrc(p.ws, 0, WS_END, 0x00020000);
    auto unit_of = [&](int v) { return v < 4 ? 7 * kq + v : v < 8 ? 28 + 8 * ((v >> 1) & 1) + 2 * kq + (v & 1) : 7 * kq + v - 4; };
    const unsigned sl_lo = L_P + (unsigned)pr * SLOT;
    const unsigned xo = WS_XB + (unsigned)((i * 4 + q) * 4) * 24576u + pr * 6144;
    const unsigned fo = WS_FLAG + (unsigned)(i * 16 + j) * 512u;

    if (w >= 4) {
        LANE_VARS;
        STAMP_INIT();
        STAMP(0);
        const unsigned swz = (unsigned)((p8 ^ (q8 >> 1)) << 4);
        const unsigned fa = F_DONE + (lane & 3) * 4;
        unsigned sink = 0, tg;
        auto poll16 = [&](int which) __attribute__((always_inline)) {
            wait_ge(F_OUT + (lane & 3) * 4, which + 1);
            STAMP(16);
            const unsigned pf = WS_FLAG + (unsigned)(i * 16 + 4 * kq + (lane & 3)) * 512u + which * 128 + ((lane >> 2) & 3) * 8;
            NOUNROLL for (int spin = 0; spin < SPIN_MAX; spin++) {
                fence_c();
                const v2u f = __builtin_amdgcn_raw_buffer_load_b64(rws, pf, 0, 16);
                const unsigned bad = (unsigned)__builtin_amdgcn_ballot_w64(f.x != tg || f.y != ~tg);
                if (bad == 0) break;
                if ((bad & 0x7777u) == 0) { if (which) break; postc<1>(F_RDY, lane); }
                __builtin_amdgcn_s_sleep(FL_SLEEP);
            }
            postc<2>(F_RDY + which * 4, lane);
            STAMP(17);
        };
        if (w < 6) {
            unsigned ffull = F_FULL + lw * 4;
            asm volatile("" : "+v"(ffull));
            {
                const unsigned voffS = (8 * (q8 >> 2) + (q8 & 3)) * 1024 + swz;
                const v4u srd = make_srd((const unsigned char*)(lw ? p.Bim : p.Bre) + (size_t)(32 * j) * 1024);
                NOUNROLL for (int n = 2; n < 8; n++) {
                    const unsigned so = 128 * ((n + rot) & 7);
                    dma4(srd, voffS, voffS ^ 64u, so, so + 15360, so + 2048, so + 17408, L_W + (n >> 1) * SLOT + lw * 8192 + (n & 1) * 4096);
                    if (n == 3) {
                        bar_l();
                    }
                    if (n == 4) { waitv<4>(); postc<2>(ffull, lane); }
                    if (n >= 5) { waitv<4>(); postv(ffull, lane, n >> 1); }
                }
                waitv<0>(); postc<4>(ffull, lane);
                asm volatile("" ::"v"(sink));
            }
            STAMP(1);
            const unsigned voffG = q8 * (FF * 2) + (((p8 >> 1) ^ ((q8 >> 1) & 1)) << 5) + ((p8 & 1) << 4), voffG1 = voffG ^ 64u;
            const unsigned voffC = q8 * 1024 + swz, voffC1 = voffC ^ 64u;
            const unsigned voffD = q8 * 1024 + (((p8 >> 1) ^ (q8 >> 1) ^ (2 * (kq & 1))) << 5) + ((p8 & 1) << 4);
            const unsigned char* gb = (const unsigned char*)(lw ? p.wu : p.wg) + (7 * kq + q) * 128;
            unsigned thr = (lane >> 2) + 1;
            int credit = 0, cbase = 0;
            auto item = [&](int n, const unsigned char* b0, const unsigned char* b1, unsigned ve, unsigned vo, unsigned st) __attribute__((always_inline)) {
                v4u s0 = make_srd(b0), s1 = make_srd(b1);
                unsigned dst = ((n & 4) ? L_P : L_W) + (n & 3) * SLOT + lw * 8192, dst1 = dst + 4096;
                asm volatile("" : "+s"(s0), "+s"(s1), "+s"(dst), "+s"(dst1));
                int sp = LSPIN_MAX;
                NOUNROLL while (n - 7 > credit) {
                    const unsigned dn = peek(fa);
                    postv(ffull, lane, n - ((vm_outstanding() + 7) >> 3));
                    unsigned long long mk = __builtin_amdgcn_ballot_w64(dn >= thr);
                    mk &= mk >> 1; mk &= mk >> 2;
                    credit = __builtin_popcountll(mk & 0x1111111111111111ull) + cbase;
                    asm volatile("s_sub_i32 %0, %0, 1" : "+s"(sp)::"scc");
                    if (sp < 0) break;
                }
                waitv<INFL>();
                dma4(s0, ve, vo, 0, st - 1024, 2 * st - 2048, 3 * st - 3072, dst);
                postv(ffull, lane, n - (INFL + 7) / 8);
                waitv<INFL>();
                dma4(s1, ve, vo, 0, st - 1024, 2 * st - 2048, 3 * st - 3072, dst1);
            };
            wait_ge(F_XI + (lane & 3) * 4, 3);
            NOUNROLL for (int n = 4; n < N1; n++) {
                const int t = n - 4;
                const unsigned char* bp = gb + (size_t)(((t + rot) & 7) * 64) * (FF * 2) + (t >> 3) * 512;
                item(n, bp, bp + 32 * FF * 2, voffG, voffG1, 8 * FF * 2);
            }
            cbase = N1 - 8; thr += cbase;
            const unsigned char* dp = (const unsigned char*)p.wd + (size_t)(7 * kq) * 65536 + q * 256 + (lw ^ (kq >> 1)) * 128;
            NOUNROLL for (int v = 0; v < 7; v++) {
                if (v >= 4) {
                    const unsigned char* cbp = (const unsigned char*)((v & 2) ? p.Cim : p.Cre) + (size_t)q * 131072 + (2 * kq + (v & 1)) * 128;
                    item(N1 + v, cbp + ((2 * lw) ^ kq) * 32768, cbp + ((2 * lw + 1) ^ kq) * 32768, voffC, voffC1, 8192);
                } else {
                    item(N1 + v, dp, dp + 32768, voffD, voffD, 8192);
                    dp += 65536;
                }
            }
            waitv<16>(); postv(ffull, lane, N1 + 5);
            waitv<8>(); postv(ffull, lane, N1 + 6);
            waitv<0>(); postv(ffull, lane, N1 + 11);
            tg = peek(L_TAG);
            STAMP(7);
            if (lw) {
                const unsigned pf = WS_FLAG + (unsigned)(i * 16 + 4 * (lane & 3) + q) * 512u + 256 + ((lane >> 2) & 1) * 8;
                NOUNROLL for (int spin = 0; spin < SPIN_MAX; spin++) {
                    fence_c();
                    const v2u f = __builtin_amdgcn_raw_buffer_load_b64(rws, pf, 0, 16);
                    if (__builtin_amdgcn_ballot_w64(f.x != tg || f.y != ~tg) == 0) break;
                    __builtin_amdgcn_s_sleep(16);
                }
            }
            if (lw) poll16(1);
        } else {
            unsigned tag0, prm0, prm1, prm2;
            const unsigned cto = min(idx * 11 + 4 * lw + p8, (int)CODE_LINES - 1) * 128;
            touch(sink, pc0, cto);
            touch(sink, lw ? (const void*)p.ws : (const void*)p.Dsk, lw ? ((lane & 4) ? WS_GRAN + (j * 16 + i) * 512 : fo) + (lane & 3) * 128 : p8 * 128);
            if (!heavy) __builtin_amdgcn_s_sleep(LT_SLEEP);
            NOUNROLL for (int t = heavy ? 4 : 0; t < 4; t++)
                touch(sink, lw ? p.wu : p.wg, (32 * i + (lane & 31)) * (FF * 2) + (7 * kq + min(2 * t + (lane >> 5), 6)) * 128);
            const unsigned po = 2 * (32 * j + 8 * (lane >> 4) + 4 * ((lane >> 1) & 1) + ((lane >> 2) & 3));
            asm volatile("global_load_ushort %0, %4, %5\n\tglobal_load_ushort %1, %4, %6\n\tglobal_load_ushort %2, %4, %7\n\tglobal_load_dword %3, %8, %9 sc1"
                         : "=&v"(prm0), "=&v"(prm1), "=&v"(prm2), "=&v"(tag0) : "v"(po), "s"(p.lre), "s"(p.lim), "s"(p.ldt), "v"(WS_EPOCH), "s"(p.ws) : "memory");
            *(LAS unsigned*)(uintptr_t)(F_DONE + (lane & 31) * 4) = 0u;
            bar_l();
            sts128(L_SEL + lw * 1024 + lane * 16, make_sel(lane, lw));
            asm volatile("s_waitcnt vmcnt(0)" : "+v"(prm0), "+v"(prm1), "+v"(prm2), "+v"(tag0)::"memory");
            tg = tag0 + 1u;
            const bool odd = lane & 1;
            const unsigned la = lane * 4;
            const float lr = bflo(prm0), li = bflo(prm1);
            float dt = __expf(bflo(prm2));
            dt = bflo(pk2(dt, dt));
            const float ex = __expf(dt * lr);
            const float fr = __builtin_amdgcn_fractf(dt * li * 0.15915494309189535f);
            const float ar = smul_t(ex, __builtin_amdgcn_cosf(fr)), ai = smul_t(ex, __builtin_amdgcn_sinf(fr));
            const cpx a1 = {ar, ai};
            const cpx a2 = cmul(a1, a1), a4 = cmul(a2, a2);
            const unsigned sg = odd ? 0u : 0x80000000u;
            auto sgn = [&](cpx v) { return cpx{v.x, __uint_as_float(__float_as_uint(v.y) ^ sg)}; };
            const cpx r8 = cmul(a4, a4), r16 = cmul(r8, r8), r32 = cmul(r16, r16), r64 = cmul(r32, r32);
            const cpx a32 = sgn(r32);
            {
                const cpx r = lw ? r32 : r8, rs = sgn(r);
                float v = odd ? r.y : r.x;
                sts32f(L_PW + lw * 1024 + la, odd ? 0.0f : 1.0f);
#pragma unroll
                for (int s = 1; s < 4; s++) { sts32f(L_PW + lw * 1024 + s * 256 + la, v); if (s < 3) v = lmad(rs, v, 0.0f); }
            }
            if (lw == 0) {
                const float nr = ar - 1.0f, den = smul_t(__builtin_amdgcn_rcpf(sfma(lr, lr, smul(li, li))), 2.0f);
                float co = smul(odd ? sfms(ai, lr, smul(nr, li)) : sfma(nr, lr, smul(ai, li)), den);
                sts32f(L_CST + la, odd ? a1.y : a1.x); sts32f(L_CST + 256 + la, odd ? a4.y : a4.x);
                const cpx a1s = sgn(a1);
#pragma unroll
                for (int t = 0; t < 5; t++) { sts32f(L_CST + (2 + t) * 256 + la, co); if (t < 4) co = lmad(a1s, co, 0.0f); }
                *(LAS unsigned*)(uintptr_t)L_TAG = tg;
            }
            post(F_COMM + lw * 4, 1);
            STAMP(1);
            cpx wn[8];
            {
                const cpx r128 = cmul(r64, r64), r256 = cmul(r128, r128);
                wn[0] = lw ? r128 : cpx{1.0f, 0.0f};
#pragma unroll
                for (int n = 1; n < 8; n++) wn[n] = cmul(wn[n - 1], r256);
#pragma unroll
                for (int n = 0; n < 8; n++) { wn[n] = sgn(wn[n]); asm volatile("" : "+v"(wn[n].x), "+v"(wn[n].y)); }
            }
            unsigned gro = WS_GRAN + j * 8192 + lane * 8, gro2 = gro + i * 512;
            unsigned to0 = lw ? min((unsigned)(lane & 31) * 0x98000u + idx * 0x4000u, WS_END - 4) : (lane & 3) * 0x100000 + idx * 0x4000;
            unsigned to1 = (448 * kq + 28 * i + min(lane, 27)) * 1024 + (2 * q + lw) * 128, to2 = (128 * q + 8 * i + p8) * 1024 + (2 * kq + (q8 & 1)) * 128;
            unsigned po2 = WS_XB + (unsigned)((i * 4 + q) * 4 + kq) * 24576u + lw * 12288 + lane * 16;
            unsigned voffC = q8 * 1024 + swz, voffC1 = voffC ^ 64u;
            unsigned voffD = q8 * 1024 + (((p8 >> 1) ^ (q8 >> 1) ^ (2 * (kq & 1))) << 5) + ((p8 & 1) << 4);
            v4u none = {~0u, ~0u, ~0u, ~0u};
            asm volatile("" : "+v"(gro), "+v"(gro2), "+v"(to0), "+v"(to1), "+v"(to2), "+v"(po2), "+v"(voffC), "+v"(voffC1), "+v"(voffD), "+v"(none));
            wait_ge(F_XI + (lane & 3) * 4, heavy ? 1 : 0);
            touch(sink, p.wd, to1);
            touch(sink, lw ? p.Cim : p.Cre, to2);
            touch(sink, lw ? (const void*)p.ws : (const void*)p.out, to0);
            wait_ge(fa, 4);
            STAMP(4);
            if (lw) {
                float e[4];
#pragma unroll
                for (int s = 0; s < 4; s++) e[s] = lds32f(L_TOT + s * 256 + la);
                const float p2 = lmad(a32, e[0], e[1]), p3 = lmad(a32, p2, e[2]);
                __builtin_amdgcn_raw_buffer_store_b64(v2u{__float_as_uint(lmad(a32, p3, e[3])), tg}, rws, gro2, 0, 16);
                sts32f(L_PRE + la, 0.0f); sts32f(L_PRE + 256 + la, e[0]); sts32f(L_PRE + 512 + la, p2); sts32f(L_PRE + 768 + la, p3);
            }
            STAMP(5);
            __builtin_amdgcn_s_sleep(T2_SLEEP);
            v4u wr[4][8];
            {
                const v4u sws = make_srd(p.ws);
                const v4u sc = make_srd((const unsigned char*)p.Cim + (size_t)q * 131072 + (2 * kq + 1) * 128);
                const v4u sd = make_srd((const unsigned char*)p.wd + (size_t)(7 * kq + 4) * 65536 + q * 256 + (lw ^ (kq >> 1)) * 128);
                sfor<0, 36>(LAMB(ff) {
                    constexpr int f = decltype(ff)::value, m = f & 7;
                    if constexpr (f < 8) ldW(wr[0][m], (m & 1) ? voffC1 : voffC, sc, ((2 * lw + (m >> 2)) ^ kq) * 32768 + (m & 3) * 8192);
                    else if constexpr (f < 32) ldW(wr[f >> 3][m], voffD, sd, (f - 8) * 8192);
                    if constexpr (f % 3 == 2) { stX<((f / 3) & 3) * 1024>(none, po2, sws, (f / 12) * 4096); __builtin_amdgcn_s_sleep(PF_SLEEP); }
                });
            }
            __builtin_amdgcn_s_sleep(LB_FIRST);
            {
                const int top = i - 1 - lw;
                v2u gr[8] = {};
                NOUNROLL for (int spin = 0; spin < SPIN_MAX; spin++) {
                    fence_c();
#pragma unroll
                    for (int n = 0; n < 8; n++) gr[n] = __builtin_amdgcn_raw_buffer_load_b64(rws, gro, ((top - 2 * n) & 15) * 512, 16);
                    unsigned long long bad = 0;
#pragma unroll
                    for (int n = 0; n < 8; n++) if (top - 2 * n >= 0) bad |= __builtin_amdgcn_ballot_w64(gr[n].y != tg);
                    if (bad == 0) break;
                    __builtin_amdgcn_s_sleep(LB_SLEEP);
                }
                float h = 0;
#pragma unroll
                for (int n = 7; n >= 0; n--) if (top - 2 * n >= 0) h = lmad(wn[n], __uint_as_float(gr[n].x), h);
                sts32f(L_XCH + 256 + lw * 256 + la, h);
            }
            postc<4>(F_COMM + lw * 4, lane);
            STAMP(6);
            waitv<0>();
            asm volatile("" ::"v"(sink));
            if (lane == 0) __builtin_amdgcn_raw_buffer_store_b64(v2u{tg, ~tg}, rws, fo + 256 + lw * 8, 0, 16);
            STAMP(7);
#pragma unroll
            for (int u = 0; u < 4; u++) asm volatile("" : "+v"(wr[u][0]), "+v"(wr[u][1]), "+v"(wr[u][2]), "+v"(wr[u][3]), "+v"(wr[u][4]), "+v"(wr[u][5]), "+v"(wr[u][6]), "+v"(wr[u][7]));
            STAMP(13);
            if (!lw) poll16(0);
            {
                unsigned wa0 = lw * 8192 + lane * 16 + L_P, wa1 = wa0 + (L_W + 3 * SLOT - L_P);
                asm volatile("" : "+v"(wa0), "+v"(wa1));
                sfor<0, 4>(LAMB(uu) {
                    constexpr int u = decltype(uu)::value;
                    wait_ge(fa, N1 + u);
#pragma unroll
                    for (int m = 0; m < 8; m++) sts128((u ? wa0 + (u - 1) * SLOT : wa1) + m * 1024, wr[u][m]);
                    postv(F_COMM + lw * 4, lane, N1 + 8 + u);
                });
            }
            STAMP(15);
        }
        f32x4 base[4];
        {
            const int d = 128 * q + 32 * kq + 4 * p8;
            const v2u dv = *(const GAS v2u*)(p.Dsk + d);
            const float df[4] = {bflo(dv.x), bfhi(dv.x), bflo(dv.y), bfhi(dv.y)};
            v2u xv[4];
#pragma unroll
            for (int e = 0; e < 4; e++) xv[e] = *(const GAS v2u*)(p.x + (size_t)(128 * i + 32 * pr + 8 * e + q8) * DM + d);
#pragma unroll
            for (int e = 0; e < 4; e++) {
                const unsigned tp = (32 * pr + 8 * e + 4 * ((q8 >> 1) & 1) + 2 * (q8 & 1) + (q8 >> 2)) * 4;
                const float rs = lds32f(L_RSTD + tp), nm = smul(lds32f(L_MEAN + tp), rs);
                const float xf[4] = {bflo(xv[e].x), bfhi(xv[e].x), bflo(xv[e].y), bfhi(xv[e].y)};
#pragma unroll
                for (int k = 0; k < 4; k++) base[e][k] = sfma(sfma(xf[k], rs, nm), df[k], xf[k]);
            }
        }
        STAMP(8);
        const unsigned xi = xo + kq * 24576u + lane * 16;
        const unsigned sc = L_O + pr * 4096;
        unsigned wa = sc + (8 * (c >> 2) + 4 * (c & 1) + ((c >> 1) & 1)) * 128 + ((g ^ (c & 7)) << 4), ra = sc + q8 * 128 + ((p8 ^ (2 * (q8 & 1) + (q8 >> 2))) << 4);
        GAS float* op = (GAS float*)p.out + (size_t)(128 * i + 32 * pr + q8) * DM + 128 * q + 32 * kq + 4 * p8;
        asm volatile("" : "+v"(wa), "+v"(ra), "+v"(op));
        wait_ge(F_SENT + pr * 4, 3);
        __builtin_amdgcn_s_sleep(X_FIRST);
        v4u in[6];
        f32x4 own[4];
        NOUNROLL for (int spin = 0; spin < SPIN_MAX; spin++) {
            fence_c();
#pragma unroll
            for (int n = 0; n < 6; n++) in[n] = __builtin_amdgcn_raw_buffer_load_b128(rws, xi + n * 1024, 0, 16);
            SB();
            wait_ge(F_SENT + pr * 4, 4);
#pragma unroll
            for (int e = 0; e < 4; e++) own[e] = lds128f(sc + e * 1024 + lane * 16);
            unsigned mx = 0;
#pragma unroll
            for (int n = 0; n < 6; n++) mx = max(mx, max(max(in[n].x, in[n].y), max(in[n].z, in[n].w)));
            if (__builtin_amdgcn_ballot_w64(mx == ~0u) == 0) break;
        }
        STAMP(11);
#pragma unroll
        for (int e = 0; e < 4; e++)
#pragma unroll
            for (int sr = 0; sr < 3; sr++) { const unsigned d0 = in[2 * sr + (e >> 1)][2 * (e & 1)], d1 = in[2 * sr + (e >> 1)][2 * (e & 1) + 1]; own[e] += f32x4{bflo(d0), bfhi(d0), bflo(d1), bfhi(d1)}; }
        asm volatile("" : "+v"(own[0]), "+v"(own[1]), "+v"(own[2]), "+v"(own[3]));
#pragma unroll
        for (int e = 0; e < 4; e++) sts128f((wa ^ (unsigned)((e >> 1) << 6)) + (e & 1) * 256, own[e]);
        fence_c();
        f32x4 ta[4];
#pragma unroll
        for (int e = 0; e < 4; e++) ta[e] = lds128f((ra ^ (unsigned)((e & 1) << 6)) + e * 1024);
        SB();
#pragma unroll
        for (int e = 0; e < 4; e++) *(GAS f32x4*)(op + e * 8 * DM) = base[e] + ta[e];
        if (b == 255 && w == 4 && lane == 0) __builtin_amdgcn_raw_buffer_store_b32(tg, rws, WS_EPOCH, 0, 16);
        STAMP(12); STAMP_DUMP();
        return;
    }
    constexpr int PD = PDV, XL = XLV, EG = EGATE;
    unsigned stall = 0; (void)stall;
    v4u xa[16][2];
#define AF(u, k) xa[2 * (u) + ((k) >> 1)][(k) & 1]
    v4u sws = make_srd(p.ws);
    unsigned oG = WS_A2 + (unsigned)(i * 44 + 7 * kq) * 16384u + w * 4096, oC = WS_A2 + (unsigned)(i * 44 + 28 + 2 * kq) * 16384u + w * 4096, oF = fo + w * 8;
    asm volatile("" : "+s"(sws), "+s"(oG), "+s"(oC), "+s"(oF));
    auto uoff = [&](int v) __attribute__((always_inline)) { return v < 4 ? oG + v * 16384 : v < 8 ? oC + (8 * ((v >> 1) & 1) + (v & 1)) * 16384 : oG + (v - 4) * 16384; };
    LANE_VARS;
    unsigned fdone = F_DONE + w * 4, l16 = lane * 16;
    asm volatile("" : "+v"(fdone), "+v"(l16));
    unsigned tgs = 0; int raised = 0;
    auto raise = [&](int level) __attribute__((always_inline)) {
        asm volatile("s_mov_b64 exec, 1\n\tbuffer_store_dwordx2 %0, off, %1, %2 offset:%3 sc1\n\ts_mov_b64 exec, -1" ::"v"(v2u{tgs, ~tgs}), "s"(sws), "s"(oF), "n"((level - 1) * 128) : "memory");
        post(fdone + (F_OUT - F_DONE), level);
        raised = level;

        if (level == 1) STAMP(17); else STAMP(18);
    };
    auto try_raise = [&](int level) __attribute__((always_inline)) {
        unsigned v; asm volatile("s_getreg_b32 %0, hwreg(HW_REG_IB_STS, 0, 4)" : "=s"(v)::"memory");
        if (__builtin_expect((int)(v + raised) == level - 1, 0)) raise(level);
    };
    unsigned oX[3];
#pragma unroll
    for (int sl = 1; sl < 4; sl++) { oX[sl - 1] = xo + (kq ^ sl) * 24576u + (sl - 1) * 2048; asm volatile("" : "+s"(oX[sl - 1])); }
    unsigned cbs[2], tbs[4];
    v4u bf[PD];
    const unsigned ffl = F_FULL + (lane & 1) * 4;
    auto set2 = [&](auto kk) __attribute__((always_inline)) {
        constexpr int k = decltype(kk)::value;
        if constexpr (k == 0) tbs[0] = L_P + ((l16 << 1) & 0x780) + ((l16 >> 1) & 0x18) + (((l16 >> 2) & 0x40) | ((l16 >> 2) & 0x20));
        else if constexpr (k < 4) tbs[k] = tbs[0] ^ (unsigned)(k << 5);
        else if constexpr (k == 4) cbs[0] = L_W + ((l16 << 3) & 0x780) + (((l16 >> 4) ^ (l16 >> 1)) & 0x70);
        else cbs[1] = cbs[0] ^ 64u;
        if constexpr (k < 4) asm volatile("" : "+v"(tbs[k])); else asm volatile("" : "+v"(cbs[k - 4]));
    };
    auto prime2 = [&](auto mm) __attribute__((always_inline)) { constexpr int m = decltype(mm)::value; const v2u lo = ldstr(tbs[m]), hi = ldstr(tbs[m] + 2048); bf[m] = v4u{lo.x, lo.y, hi.x, hi.y}; };
    {
        STAMP_INIT();
        STAMP(0);
        unsigned cb[2];
        cb[0] = (unsigned)c * 128u + (unsigned)((g ^ (c >> 1)) << 4); cb[1] = cb[0] ^ 64u;
        f32x4 eX;
        unsigned tb[4];
        f32x4 nm4[2];
        {
            const unsigned voffX = (8 * (q8 >> 2) + 4 * (q8 & 1) + ((q8 >> 1) & 1)) * 1024 + ((p8 ^ (q8 >> 1)) << 4), voffX1 = voffX ^ 64u;
            const v4u srd = make_srd((const unsigned char*)p.x + (size_t)(128 * i + 32 * w) * 1024);
            auto issue_x = [&](int ln) __attribute__((always_inline)) {
                const unsigned s0 = ((ln + rot) & 7) * 128;
                dma4(srd, voffX, voffX1, s0, s0 + 15360, s0, s0 + 15360, sl_lo + (ln & 3) * 4096);
            };
            {
                const unsigned voffS = (8 * (q8 >> 2) + (q8 & 3)) * 1024 + ((p8 ^ (q8 >> 1)) << 4), so = 128 * (((w >> 1) + rot) & 7);
                dma4(make_srd((const unsigned char*)(lw ? p.Bim : p.Bre) + (size_t)(32 * j) * 1024), voffS, voffS ^ 64u, so, so + 15360, so + 2048, so + 17408, L_W + lw * 8192 + (w >> 1) * 4096);
            }
#pragma unroll
            for (int ln = 0; ln < XL; ln++) issue_x(ln);
            v4u ones = {0x3f803f80u, 0x3f803f80u, 0x3f803f80u, 0x3f803f80u};
            unsigned xl[2] = {cb[0] + sl_lo, cb[1] + sl_lo};
            cb[0] += L_W; cb[1] += L_W;
            asm volatile("" : "+v"(ones), "+v"(cb[0]), "+v"(cb[1]), "+v"(xl[0]), "+v"(xl[1]));
            f32x4 sm[2], sq[2], bsum;
            v4u bx[4];
            unsigned cbw[2] = {cb[0] + (w >> 1) * 8192 + (w & 1) * 2048, cb[1] + (w >> 1) * 8192 + (w & 1) * 2048};
            asm volatile("" : "+v"(cbw[0]), "+v"(cbw[1]));
            f32x4 acc[4][2];
            unsigned fl = 0; bool lack = false;
            const unsigned fcs = (lane & 4) ? F_COMM + (lane & 1) * 4 : F_BS + (lane & 3) * 4;
            static_assert(PD == 4 && XL <= 3, "prologue schedule");
            waitv<4 * (XL - 1)>();
#pragma unroll
            for (int f = 0; f < 4; f++) xa[f >> 1][f & 1] = lds128(xl[f >> 1] + (f & 1) * 2048);
            asm volatile("s_barrier" ::: "memory");
            STAMP(1);
            sfor<-PD, 64>(LAMB(nn) {
                constexpr int n = decltype(nn)::value, m = n + PD, ks = n >> 2, ln = n >> 3;
                v4u cur;
                if constexpr (n >= 0) { cur = bf[n % PD]; mma<(n < 4)>(acc[n & 3][0], xa[ks][0], cur); SB(); }
                if constexpr (m > 0 && (m & 15) == 0 && m < 64) need_full(lack, ffl, (m >> 4) + 1);
                if constexpr (m < 64) bf[m % PD] = lds128(cb[(m >> 2) & 1] + wslot(m >> 4) + (((m >> 1) & 1) * 4 + ((m >> 3) & 1) * 2 + (m & 1)) * 2048);
                SB();
                if constexpr (n >= 0) { mma<(n < 4)>(acc[n & 3][1], xa[ks][1], cur); SB(); }
                if constexpr (n >= 0 && (n & 7) < 4 && ln + XL < 8)
                    dma1(srd, (n & 1) ? voffX1 : voffX, ((ln + XL + rot) & 7) * 128 + (n & 1) * 16384 + (n & 2) * 1024, sl_lo + ((ln + XL) & 3) * 4096 + (n & 3) * 1024);
                if constexpr (n >= 0 && (n & 7) == 4 && ln < 7) waitv<4 * ((ln + XL < 7 ? ln + XL : 7) - ln - 1)>();
                if constexpr (n >= 0 && (n & 7) >= 4 && ln < 7) xa[2 * ln + 2 + ((n >> 1) & 1)][n & 1] = lds128(xl[(n >> 1) & 1] + ((ln + 1) & 3) * 4096 + (n & 1) * 2048);
                if constexpr (n == TGN) post(fdone + (F_XI - F_DONE), 1);
                if constexpr (n == 56) post(fdone + (F_XI - F_DONE), 3);
                if constexpr ((m & 15) == 10 && m < 48) fl = peek(ffl);
                if constexpr ((m & 15) == 14 && m < 48) lack = any_lt(fl, (m >> 4) + 2);
                if constexpr ((m & 15) == 15 && m < 63) post(fdone, (m >> 4) + 1);
                if constexpr (m < 64 && (m & 15) < 4) bx[m & 3] = lds128(cbw[m & 1] + wslot(m >> 4) + ((m >> 1) & 1) * 4096);
                if constexpr (n == 55) { asm volatile("" : "+v"(bsum)); sts32f(L_CST + 7 * 256 + c * 16 + (w & 1) * 8 + (w >> 1) * 4, bsum.x); post(fdone + (F_BS - F_DONE), 1); }
                if constexpr (n == 60) fl = peek(fcs);
                SB();
                if constexpr (n >= 0 && (n & 15) < 4) mma<(n == 0)>(bsum, ones, bx[n & 3]);
                if constexpr (n >= 0) { if constexpr (n & 1) mma<(n < 4)>(sq[(n >> 1) & 1], xa[ks][(n >> 1) & 1], xa[ks][(n >> 1) & 1]); else mma<(n < 4)>(sm[(n >> 1) & 1], ones, xa[ks][(n >> 1) & 1]); }
                SB();
            });
            need_full(any_lt(fl, 1), fcs, 1);
            const unsigned cl = L_CST + (l16 & 0xf0);
            const f32x4 ca = lds128f(cl), c4 = lds128f(cl + 256), co = lds128f(cl + 512), bs = lds128f(cl + 7 * 256), c8 = lds128f(L_PW + 256 + (l16 & 0xf0));
            mfma_done();
            STAMP(2); VAL(24, stall); stall = 0;
            {
                float dg[2];
#pragma unroll
                for (int bb = 0; bb < 2; bb++) {
                    nm4[bb] = sm[bb] * (-1.0f / DM);
                    const float s01 = (lane & 1) ? sq[bb].y : sq[bb].x, s23 = (lane & 1) ? sq[bb].w : sq[bb].z;
                    dg[bb] = __builtin_amdgcn_rsqf(fmaxf(((lane & 2) ? s23 : s01) * (1.0f / DM) - nm4[bb].x * nm4[bb].x, 0.0f) + 1e-5f);
                }
                if (g == (c >> 2)) {
                    const unsigned sa = L_RSTD + (32 * w + 8 * g + (c & 3)) * 4;
                    sts32f(sa, dg[0]); sts32f(sa + 16, dg[1]); sts32f(sa + L_MEAN, nm4[0].x); sts32f(sa + L_MEAN + 16, nm4[1].x);
                }
            }
            fence_c();
            const unsigned ra = L_RSTD + (32 * w + 8 * g) * 4;
            const f32x4 nt0 = lds128f(ra + L_MEAN), nt1 = lds128f(ra + L_MEAN + 16), rs0 = lds128f(ra), rs1 = lds128f(ra + 16);
            {
                const int q4 = c >> 2, p4 = c & 3;
                const unsigned tb1 = L_P + (unsigned)(8 * g + q4) * 128u + (unsigned)((((q4 >> 1) & 1) | ((g & 1) << 1)) << 5) + p4 * 8;
#pragma unroll
                for (int t = 0; t < 4; t++) { tb[t] = tb1 ^ (unsigned)(t << 5); asm volatile("" : "+v"(tb[t])); }
            }
            SB();
            STAMP(3);
            f32x4 ev;
            f32x2 sr[2][4], si[2][4];
#pragma unroll
            for (int st = 0; st < 2; st++) {
                const f32x2 b2 = st ? hi2(bs) : lo2(bs);
#pragma unroll
                for (int t = 0; t < 4; t++) {
                    const f32x4 nt = (t & 2) ? nt1 : nt0;
                    const f32x2 n2 = (t & 1) ? hi2(nt) : lo2(nt);
                    sr[st][t] = bfma<0, 0>(b2, n2, (t & 1) ? hi2(acc[st][t >> 1]) : lo2(acc[st][t >> 1]));
                    si[st][t] = bfma<1, 0>(b2, n2, (t & 1) ? hi2(acc[2 + st][t >> 1]) : lo2(acc[2 + st][t >> 1]));
                }
            }
            SB();
#pragma unroll
            for (int st = 0; st < 2; st++)
#pragma unroll
                for (int t = 0; t < 4; t++) {
                    const f32x4 rs = (t & 2) ? rs1 : rs0;
                    const f32x2 r2 = (t & 1) ? hi2(rs) : lo2(rs);
                    sr[st][t] *= r2; si[st][t] *= r2;
                }
            SB();
            const f32x2 a1[2] = {lo2(ca), hi2(ca)}, cf[2] = {lo2(co), hi2(co)};
#pragma unroll
            for (int t = 1; t < 4; t++) {
                f32x2 tr[2], ti[2];
#pragma unroll
                for (int st = 0; st < 2; st++) { tr[st] = bfma<1, 1>(a1[st], si[st][t - 1], sr[st][t]); ti[st] = bfma<1, 0>(a1[st], sr[st][t - 1], si[st][t]); }
                SB();
#pragma unroll
                for (int st = 0; st < 2; st++) { sr[st][t] = bfma<0, 0>(a1[st], sr[st][t - 1], tr[st]); si[st][t] = bfma<0, 0>(a1[st], si[st][t - 1], ti[st]); }
                SB();
            }
#pragma unroll
            for (int st = 0; st < 2; st++) {
                ev[2 * st] = sfma(c4[2 * st], sr[st][3].x, sfnma(c4[2 * st + 1], si[st][3].x, sr[st][3].y));
                ev[2 * st + 1] = sfma(c4[2 * st], si[st][3].x, sfma(c4[2 * st + 1], sr[st][3].x, si[st][3].y));
                eX[2 * st] = sr[st][3].x; eX[2 * st + 1] = si[st][3].x;
            }
            sts128f(L_SEG + w * 1024 + l16, ev);
            fence_c();
            f32x4 es[4];
#pragma unroll
            for (int s = 0; s < 4; s++) es[s] = lds128f(L_SEG + w * 1024 + s * 256 + (l16 & 0xf0));
            SB();
            unsigned ya = L_O + w * 4096 + l16;
            asm volatile("" : "+v"(ya), "+v"(eX));
#pragma unroll
            for (int st = 0; st < 2; st++) {
                f32x2 tr[4], ti[4];
#pragma unroll
                for (int t = 0; t < 4; t++) { tr[t] = bmul<1, 1>(cf[st], si[st][t]); ti[t] = bmul<1, 1>(cf[st], sr[st][t]); }
                SB();
#pragma unroll
                for (int t = 0; t < 4; t++) { tr[t] = bfma<0, 0>(cf[st], sr[st][t], tr[t]); ti[t] = bfma<0, 1>(cf[st], si[st][t], ti[t]); }
                SB();
                sts128(ya + st * 1024, v4u{pk2(tr[0]), pk2(tr[1]), pk2(tr[2]), pk2(tr[3])});
                sts128(ya + 2048 + st * 1024, v4u{pk2(ti[0]), pk2(ti[1]), pk2(ti[2]), pk2(ti[3])});
                SB();
                if (st == 0) {
#pragma unroll
                    for (int s = 1; s < 4; s++) { const f32x2 u0 = cmadp(lo2(c8), lo2(es[s - 1]), lo2(es[s])), u1 = cmadp(hi2(c8), hi2(es[s - 1]), hi2(es[s])); es[s] = f32x4{u0.x, u0.y, u1.x, u1.y}; }
                    sts128f(L_TOT + w * 256 + (l16 & 0xf0), es[3]);
                    post(fdone, 4);
                    f32x4 pin;
#pragma unroll
                    for (int k = 0; k < 4; k++) { const float lo = g == 1 ? es[0][k] : 0.0f, hi = g == 3 ? es[2][k] : es[1][k]; pin[k] = g >= 2 ? hi : lo; }
                    sts128f(L_SEG + w * 1024 + l16, pin);
                    SB();
                }
            }
        }
        STAMP(4);
        {
            const v4u sel0 = lds128(L_SEL + l16), sel1 = lds128(L_SEL + 1024 + l16);
            unsigned fl = 0;
            f32x4 ct[CRING];
#pragma unroll
            for (int k = 0; k < CRING; k++) asm volatile("" : "=v"(ct[k]));
            sfor<0, 64 + CLAG>(LAMB(kk) {
                constexpr int k = decltype(kk)::value, e = k - CLAG;
                if constexpr (k < 64) mma_t(ct[k % CRING], (k & 1) ? sel1 : sel0, xa[k >> 2][(k >> 1) & 1], nm4[(k >> 1) & 1]);
                if constexpr (k >= 64 || k < 4) asm volatile("s_nop 1");
                if constexpr (e >= 0) {
                    xa[e >> 2][(e >> 1) & 1][2 * (e & 1)] = cvtv(ct[e % CRING].x, ct[e % CRING].y);
                    xa[e >> 2][(e >> 1) & 1][2 * (e & 1) + 1] = cvtv(ct[e % CRING].z, ct[e % CRING].w);
                }
                if constexpr (k == 40) fl = peek(ffl);
                if constexpr (k == 52) need_full(any_lt(fl, 5), ffl, 5);
                if constexpr (k >= 53 && k < 53 + PD) { constexpr int m = k - 53; const unsigned a = tb[(m >> 1) & 3] + (m & 1) * 8192; const v2u lo = ldstr(a), hi = ldstr(a + 512); bf[(64 + m) % PD] = v4u{lo.x, lo.y, hi.x, hi.y}; }
                SB();
            });
        }
        STAMP(5);
        unsigned go = oG + q * 16384, so = oC + (q >> 1) * 16384 + (q & 1) * 2048;
        asm volatile("" : "+s"(go), "+s"(so));
        f32x4 acc[4][2][2];
        f32x2 r12;
        unsigned fc = 0;
        auto gpass = [&](auto pp, auto n0, auto n1) __attribute__((always_inline)) {
            constexpr int pass = decltype(pp)::value;
            unsigned fl = 0, dn = 0; bool lack = false;
            sfor<decltype(n0)::value, decltype(n1)::value>(LAMB(nn) {
                constexpr int n = decltype(nn)::value, m = n + PD;
                constexpr bool pre = pass == 0 || m < 128;
                constexpr unsigned off = wslot(m >> 4) + (m & 1) * 8192 + ((m >> 3) & 1) * 4096;
                const v4u cur = bf[(n + 64 + 128 * pass) % PD];
                mma<(n < 8)>(acc[(n >> 1) & 3][n & 1][0], cur, xa[n >> 3][0]);
                SB();
                if constexpr ((m & 15) == 0) post(fdone, dn);
                if constexpr ((m & 15) == 0) need_full(lack, ffl, 5 + 8 * pass + (m >> 4));
                v2u lo, hi;
                if constexpr (pre) lo = ldstr(tb[(m >> 1) & 3] + off); else lo = ldstr(tbs[m & 3]);
                if constexpr (pass == 1 && n >= 69 && n < 81 && (n & 1)) set2(IC<(n - 69) / 2>{});
                if constexpr ((m & 15) == 10) fl = peek(ffl);
                if constexpr ((m & 15) == 15) lack = any_lt(fl, m == 127 && pass == 0 ? N1 - 7 : 6 + 8 * pass + (m >> 4));
                if constexpr ((m & 15) == 13) { dn = 5 + 8 * pass + (m >> 4); asm volatile("" : "+v"(dn)); }
                if constexpr (n == EG - 8 && pass == 1) fc = peek(F_RDY);
                if constexpr (n == EG - 2 && pass == 1) { if (__builtin_expect(any_lt(fc, 1), 0)) { wait_ge(F_RDY, 1); } fence_c(); }
                if constexpr (n >= EG && n < EG + 24 && pass == 1 && (n - EG) % 3 == 0) { constexpr int f = (n - EG) / 3; reldA<(f & 3) * 1024>(AF(f >> 2, f & 3), l16, sws, uoff(f >> 2)); }
                if constexpr (pass == 1 && (n == 33 || n == 41 || n == 49 || n == 57)) try_raise(1);
                if constexpr (pass == 1 && n == 67) { if (raised < 1) { waitv<0>(); raise(1); } }
                if constexpr (n == 120 && pass == 0) fc = peek(ffl + (F_COMM - F_FULL));
                if constexpr (n == 121 && pass == 0) { const unsigned ra = L_RSTD + w * 128 + (((l16 >> 1) & 0x60) | ((l16 >> 2) & 0xc)); r12 = f32x2{lds32f(ra), lds32f(ra + 16)}; }
                SB();
                mma<(n < 8)>(acc[(n >> 1) & 3][n & 1][1], cur, xa[n >> 3][1]);
                SB();
                if constexpr (pre) hi = ldstr(tb[(m >> 1) & 3] + off + 512); else hi = ldstr(tbs[m & 3] + 2048);
                bf[n % PD] = v4u{lo.x, lo.y, hi.x, hi.y};
                if constexpr ((m & 57) == 57 && (pass == 0 || m < 64)) {
                    tb[(m >> 1) & 3] += (m & 64) ? L_P - L_W : L_W - L_P; asm volatile("" : "+v"(tb[(m >> 1) & 3]));
                }
                SB();
            });
        };
        auto gated = [&](auto ff) __attribute__((always_inline)) {
            constexpr int uu = decltype(ff)::value >> 1, bb = decltype(ff)::value & 1;
            const f32x4 h0 = acc[2 * uu][0][bb] * acc[2 * uu][1][bb], h1 = acc[2 * uu + 1][0][bb] * acc[2 * uu + 1][1][bb];
            return v4u{pk2(bmul<bb, 0>(r12, lo2(h0))), pk2(bmul<bb, 0>(r12, hi2(h0))), pk2(bmul<bb, 0>(r12, lo2(h1))), pk2(bmul<bb, 0>(r12, hi2(h1)))};
        };
        gpass(IC<0>{}, IC<0>{}, IC<128>{});
        {
            r12 = r12 * r12;
            mfma_done();
            STAMP(6); VAL(25, stall); stall = 0;
            sfor<0, 2>(LAMB(ff) { stA<decltype(ff)::value * 1024>(gated(ff), l16, sws, go); });
            SB();
            need_full(any_lt(fc, 4), ffl + (F_COMM - F_FULL), 4);
            const unsigned tg = peek(L_TAG);
            const unsigned cl = L_CST + (l16 & 0xf0);
            unsigned ya = L_O + w * 4096 + l16;
            asm volatile("" : "+v"(ya));
            const f32x4 h0 = lds128f(L_XCH + 256 + (l16 & 0xf0)), h1 = lds128f(L_XCH + 512 + (l16 & 0xf0));
            const f32x4 aw = lds128f(L_PW + 1024 + w * 256 + (l16 & 0xf0)), pb = lds128f(L_PRE + w * 256 + (l16 & 0xf0));
            const f32x4 ag = lds128f(L_PW + l16), pin = lds128f(L_SEG + w * 1024 + l16);
            const v4u sel[2] = {lds128(L_SEL + l16), lds128(L_SEL + 1024 + l16)};
            const v4u yr[2] = {lds128(ya), lds128(ya + 1024)}, yi[2] = {lds128(ya + 2048), lds128(ya + 3072)};
            const f32x4 c4 = lds128f(cl + 256);
            f32x4 cw[4];
            SB();
            sfor<2, 4>(LAMB(ff) { stA<decltype(ff)::value * 1024>(gated(ff), l16, sws, go); });
            tgs = __builtin_amdgcn_readfirstlane(tg);
#pragma unroll
            for (int t = 0; t < 4; t++) cw[t] = lds128f(cl + (3 + t) * 256);
            SB();
            f32x4 zr[2][2], zi[2][2];
#pragma unroll
            for (int st = 0; st < 2; st++)
#pragma unroll
                for (int bb = 0; bb < 2; bb++) { mma<true>(zr[st][bb], yr[st], sel[bb]); mma<true>(zi[st][bb], yi[st], sel[bb]); }
            SB();
            f32x2 hr[2], hi[2];
            {
                const f32x4 cy = h0 + h1;
                const f32x2 k0 = cmadp(lo2(ag), cmadp(lo2(aw), lo2(cy), lo2(pb)), lo2(pin)), k1 = cmadp(hi2(ag), cmadp(hi2(aw), hi2(cy), hi2(pb)), hi2(pin));
                const f32x4 hin = {k0.x, k0.y, k1.x, k1.y};
                float t0[2], t1[2];
#pragma unroll
                for (int st = 0; st < 2; st++) { t0[st] = sfnma(c4[2 * st + 1], hin[2 * st + 1], eX[2 * st]); t1[st] = sfma(c4[2 * st + 1], hin[2 * st], eX[2 * st + 1]); }
                SB();
#pragma unroll
                for (int st = 0; st < 2; st++) { hr[st] = f32x2{hin[2 * st], sfma(c4[2 * st], hin[2 * st], t0[st])}; hi[st] = f32x2{hin[2 * st + 1], sfma(c4[2 * st], hin[2 * st + 1], t1[st])}; }
                SB();
            }
#pragma unroll
            for (int st = 0; st < 2; st++) {
                f32x2 tr[4], ti[4];
#pragma unroll
                for (int t = 0; t < 4; t++) { const f32x2 wv = st ? hi2(cw[t]) : lo2(cw[t]); tr[t] = bmul<1, 1>(wv, hi[st]); ti[t] = bmul<1, 1>(wv, hr[st]); }
                SB();
#pragma unroll
                for (int t = 0; t < 4; t++) { const f32x2 wv = st ? hi2(cw[t]) : lo2(cw[t]); tr[t] = bfma<0, 0>(wv, hr[st], tr[t]); ti[t] = bfma<0, 1>(wv, hi[st], ti[t]); }
                SB();
                v4u fr = {pk2(tr[0]), pk2(tr[1]), pk2(tr[2]), pk2(tr[3])}, fi = {pk2(ti[0]), pk2(ti[1]), pk2(ti[2]), pk2(ti[3])};
                asm volatile("s_nop 1" : "+v"(fr), "+v"(fi));
#pragma unroll
                for (int bb = 0; bb < 2; bb++) { mma<false>(zr[st][bb], fr, sel[bb]); mma<false>(zi[st][bb], fi, sel[bb]); }
                SB();
            }
            mfma_done();
            stA<0>(pk8(zr[0][0], zr[1][0]), l16, sws, so); stA<0>(pk8(zi[0][0], zi[1][0]), l16, sws, so + 8 * 16384);
            stA<1024>(pk8(zr[0][1], zr[1][1]), l16, sws, so); stA<1024>(pk8(zi[0][1], zi[1][1]), l16, sws, so + 8 * 16384);
            SB();
            STAMP(7);
        }
        if (heavy) {
            gpass(IC<1>{}, IC<0>{}, IC<128>{});
            sfor<0, 4>(LAMB(ff) { constexpr int f = decltype(ff)::value; reldA<f * 1024>(AF(2, f), l16, sws, uoff(2)); });
            mfma_done();
            STAMP(8); VAL(26, stall); stall = 0;
            sfor<0, 4>(LAMB(ff) { stA<decltype(ff)::value * 1024>(gated(ff), l16, sws, go + 4 * 16384); });
            SB();
        }
        int q2 = q;
        asm volatile("" : "+s"(q2));
        if (q2 == 3) {
            waitv<0>(); raise(1); raise(2);
            sfor<0, 6>(set2);
            wait_ge(ffl, N1 + 1);
            sfor<0, PD>(prime2);
            wait_ge(F_RDY, 1);
            sfor<0, 12>(LAMB(ff) { constexpr int f = decltype(ff)::value; reldA<(f & 3) * 1024>(AF(f >> 2, f & 3), l16, sws, uoff(f >> 2)); });
#pragma unroll
            for (int u = 0; u < 3; u++) asm volatile("s_waitcnt vmcnt(0)" : "+v"(AF(u, 0)), "+v"(AF(u, 1)), "+v"(AF(u, 2)), "+v"(AF(u, 3))::"memory");
        }
    }
    STAMP(9);
    {
        f32x4 acc[8][2];
        v4u pk[2];
        unsigned fl = 0, fr = 0, dn = 0; bool lack = false;
        constexpr auto fv = [](int n) { return n < 48 ? n >> 4 : n < 108 ? 3 + (n - 48) / 12 : n < 144 ? 8 + ((n - 108) % 12) / 4 : 3 + (n - 144) / 4; };
        constexpr auto fk = [](int n) { return n < 48 ? (n >> 3) & 1 : n < 108 ? ((n - 48) % 12) / 6 : (n >> 1) & 1; };
        constexpr auto fd = [](int n) { return n < 48 ? n & 7 : n < 108 ? 2 + (n - 48) % 6 : n < 144 ? 2 * ((n - 108) / 12 + 1) + (n & 1) : n & 1; };
        constexpr auto first = [](int n) { return n >= 0 && (n < 48 ? (n & 15) == 0 : n < 108 ? (n - 48) % 12 == 0 : n < 120 && (n & 3) == 0); };
        constexpr int LG = LGATE;
        constexpr auto nmid = [](int n) { return n <= MG ? 0 : (n - MG + 2) / 3 < 20 ? (n - MG + 2) / 3 : 20; };
        static_assert(MG + 57 < LG && LG + 22 < 108, "load schedule");
        sfor<0, 176>(LAMB(nn) {
            constexpr int n = decltype(nn)::value, m = n + PD;
            constexpr int v = fv(n), ks = fk(n), dt = fd(n);
            constexpr int mv = fv(m), mk = fk(m), md = fd(m);
            constexpr bool mc = (mv & ~3) == 4;
            constexpr unsigned moff = (mv & 3) * SLOT + (mc ? md * 2048 : (md >> 2) * 8192 + mk * 4096);
            constexpr auto chk = [](int q) { return q == 16 ? 2 : q == 32 ? 4 : q == 60 ? 7 : q == 96 ? 11 : 0; };
            v4u cur; v2u lo, hi;
            if constexpr (n >= 0) {
                if constexpr (first(n)) {
                    constexpr int mid = nmid(n), late = n <= LG ? 0 : (n - LG + 1) / 2 < 12 ? (n - LG + 1) / 2 : 12;
                    constexpr int NW = v == 0 ? 12 : v == 1 ? 8 : v == 2 ? 63 : v < 8 ? mid - 4 * (v - 2) + late : late - 4 * (v - 7);
                    static_assert(NW >= 0, "load schedule");
                    asm volatile("s_waitcnt vmcnt(%4)" : "+v"(AF(v & 7, 0)), "+v"(AF(v & 7, 1)), "+v"(AF(v & 7, 2)), "+v"(AF(v & 7, 3)) : "n"(ABLW(v) ? 63 : NW) : "memory");
                }
                cur = bf[n % PD];
                mma<(n < 8)>(acc[dt][0], cur, AF(v & 7, 2 * ks));
                SB();
            }
            if constexpr (m > 0 && (m & 15) == 0 && m <= 48) post(fdone, dn);
            if constexpr (chk(m) > 0) need_full(lack, m == 96 ? ffl + (F_COMM - F_FULL) : ffl, N1 + chk(m));
            if constexpr (m < 176) { if constexpr (mc) bf[m % PD] = lds128(cbs[mk] + moff); else lo = ldstr(tbs[md & 3] + moff); }
            if constexpr (chk(m + 6) > 0) fl = peek(m == 90 ? ffl + (F_COMM - F_FULL) : ffl);
            if constexpr (chk(m + 1) > 0) lack = any_lt(fl, N1 + chk(m + 1));
            if constexpr ((m & 15) == 13 && m < 48) { dn = N1 + mv + 1; asm volatile("" : "+v"(dn)); }
            if constexpr (n == MG - 7) fr = peek(F_RDY);
            if constexpr (n == MG - 1) { if (__builtin_expect(any_lt(fr, 2), 0)) { if (raised < 2) { waitv<0>(); raise(2); } wait_ge(F_RDY, 2); } fence_c(); }
            if constexpr (n == RAISE2) { if (raised < 2) { if (!ABLW(99)) waitv<nmid(n)>(); raise(2); } }
            if constexpr (n >= MG && n < MG + 60 && (n - MG) % 3 == 0) { constexpr int f = (n - MG) / 3; reldA<(f & 3) * 1024>(AF(3 + (f >> 2), f & 3), l16, sws, uoff(3 + (f >> 2))); }
            if constexpr (n == LG - 6) fr = peek(F_RDY + 4);
            if constexpr (n == LG - 1) need_full(any_lt(fr, 1), F_RDY + 4, 1);
            if constexpr (n >= LG && n < LG + 24 && (n - LG) % 2 == 0) { constexpr int f = (n - LG) / 2; reldA<(f & 3) * 1024>(AF(f >> 2, f & 3), l16, sws, uoff(8 + (f >> 2))); }
            SB();
            if constexpr (n >= 0) { mma<(n < 8)>(acc[dt][1], cur, AF(v & 7, 2 * ks + 1)); SB(); }
            if constexpr (m < 176 && !mc) { hi = ldstr(tbs[md & 3] + moff + 2048); bf[m % PD] = v4u{lo.x, lo.y, hi.x, hi.y}; }
            if constexpr (n >= 120 && n < 156) {
                constexpr int sl = (n - 108) / 12, r = (n - 108) % 12 - 2;
                if constexpr (r >= 0 && r < 10 && r % 5 < 4) { const f32x4& a = acc[2 * sl + r / 5][(r % 5) >> 1]; pk[r / 5][r % 5] = cvtv(a[2 * (r % 5 & 1)], a[2 * (r % 5 & 1) + 1]); }
                if constexpr (r >= 0 && r % 5 == 4) stX<(r / 5) * 1024>(pk[r / 5], l16, sws, oX[sl - 1]);
            }
            if constexpr (n == 156) post(fdone + (F_SENT - F_DONE), 3);
            if constexpr (n == 107) { STAMP(10); VAL(28, stall); stall = 0; }
            SB();
        });
        mfma_done();
        STAMP(11); VAL(29, stall);
#pragma unroll
        for (int e = 0; e < 4; e++) sts128f(L_O + w * 4096 + e * 1024 + l16, acc[e >> 1][e & 1]);
        post(fdone + (F_SENT - F_DONE), 4);
    }
    STAMP(14); STAMP_DUMP();
}

extern "C" void kernel_launch(void* const* d_in, const int* in_sizes, int n_in, void* d_out, int out_size, void* d_ws, size_t ws_size,
                              hipStream_t stream) {
    Params p;
    p.x = (const u16*)d_in[0]; p.lre = (const u16*)d_in[1]; p.lim = (const u16*)d_in[2];
    p.Bre = (const u16*)d_in[3]; p.Bim = (const u16*)d_in[4]; p.Cre = (const u16*)d_in[5]; p.Cim = (const u16*)d_in[6];
    p.ldt = (const u16*)d_in[7]; p.Dsk = (const u16*)d_in[8];
    p.wg = (const u16*)d_in[10]; p.wu = (const u16*)d_in[11]; p.wd = (const u16*)d_in[12];
    p.out = (float*)d_out; p.ws = (unsigned char*)d_ws + WS_SHIFT;
    (void)hipFuncSetAttribute((const void*)fused, hipFuncAttributeMaxDynamicSharedMemorySize, L_TOTAL);
    fused<<<256, 512, L_TOTAL, stream>>>(p);
}
```

```cpp
#define PFPOS 2
#include <hip/hip_runtime.h>
#include <stdint.h>

typedef unsigned short u16;
typedef unsigned v4u __attribute__((ext_vector_type(4)));
typedef unsigned v2u __attribute__((ext_vector_type(2)));
typedef short s16x4 __attribute__((ext_vector_type(4)));
typedef __bf16 bf16x2 __attribute__((ext_vector_type(2)));
typedef __bf16 bf16x8 __attribute__((ext_vector_type(8)));
typedef float f32x2 __attribute__((ext_vector_type(2)));
typedef float f32x4 __attribute__((ext_vector_type(4)));
#define GAS __attribute__((address_space(1)))
#define LAS __attribute__((address_space(3)))
#define DEVI __device__ __forceinline__
#define NOUNROLL _Pragma("nounroll")

#define SPIN_SLEEP 0
constexpr int DM = 512, FF = 1792;
constexpr unsigned WS_A2 = 0;
constexpr unsigned WS_XB = 16u * 44u * 16384u;
constexpr unsigned WS_GRAN = WS_XB + 256u * 24576u;
constexpr unsigned WS_FLAG = WS_GRAN + 16u * 16u * 512u;
constexpr unsigned WS_EPOCH = WS_FLAG + 1024u * 128u;
constexpr unsigned WS_END = WS_EPOCH + 256u;
constexpr unsigned SLOT = 16384;
constexpr unsigned L_RSTD = 0, L_MEAN = 512;
constexpr unsigned F_DONE = 1024, F_SENT = F_DONE + 16, F_FULL = F_DONE + 32, F_COMM = F_DONE + 40, F_RDY = F_DONE + 48, F_OUT = F_DONE + 64, F_XI = F_DONE + 80, F_BS = F_DONE + 96, L_TAG = F_DONE + 128;
constexpr unsigned L_CST = 1280;
constexpr unsigned L_XCH = L_CST + 8 * 256;
constexpr unsigned L_SEG = 4096, L_PW = 8192, L_TOT = L_PW + 2048, L_PRE = L_TOT + 1024, L_SEL = 12288;
constexpr unsigned L_P = SLOT, L_O = 5 * SLOT, L_W = 6 * SLOT, L_TOTAL = 10 * SLOT;
constexpr int SPIN_MAX = 1 << 18;
constexpr int LSPIN_MAX = 1 << 22;

struct Params {
    const u16 *x, *lre, *lim, *Bre, *Bim, *Cre, *Cim, *ldt, *Dsk, *wg, *wu, *wd;
    float* out;
    unsigned char* ws;
};

DEVI float bf2f(u16 v) { return __uint_as_float(((unsigned)v) << 16); }
DEVI float bflo(unsigned v) { return __uint_as_float(v << 16); }
DEVI float bfhi(unsigned v) { return __uint_as_float(v & 0xffff0000u); }
DEVI unsigned pk2(float a, float b) { f32x2 p = {a, b}; return __builtin_bit_cast(unsigned, __builtin_convertvector(p, bf16x2)); }
DEVI unsigned pk2(f32x2 p) { return __builtin_bit_cast(unsigned, __builtin_convertvector(p, bf16x2)); }
DEVI v4u pk8(f32x4 a, f32x4 b) { return v4u{pk2(a.x, a.y), pk2(a.z, a.w), pk2(b.x, b.y), pk2(b.z, b.w)}; }
DEVI float dot2(unsigned a, unsigned b, float c) { return __builtin_amdgcn_fdot2_f32_bf16(__builtin_bit_cast(bf16x2, a), __builtin_bit_cast(bf16x2, b), c, false); }
template <int CTRL> DEVI float dppf(float v) { return __builtin_bit_cast(float, __builtin_amdgcn_update_dpp(0, __builtin_bit_cast(int, v), CTRL, 0xf, 0xf, true)); }
DEVI f32x4 mfma(v4u a, v4u b, f32x4 c) { return __builtin_amdgcn_mfma_f32_16x16x32_bf16(__builtin_bit_cast(bf16x8, a), __builtin_bit_cast(bf16x8, b), c, 0, 0, 0); }
template <bool FIRST> DEVI void mma(f32x4& c, const v4u& a, const v4u& b) {
    if constexpr (FIRST) asm volatile("v_mfma_f32_16x16x32_bf16 %0, %1, %2, 0" : "=&v"(c) : "v"(a), "v"(b));
    else asm volatile("v_mfma_f32_16x16x32_bf16 %0, %1, %2, %0" : "+v"(c) : "v"(a), "v"(b));
}
DEVI void mfma_done() { __builtin_amdgcn_sched_barrier(0); asm volatile("s_nop 7"); __builtin_amdgcn_sched_barrier(0); }
DEVI v4u lds128(unsigned a) { return *(LAS v4u*)(uintptr_t)a; }
DEVI f32x4 lds128f(unsigned a) { return *(LAS f32x4*)(uintptr_t)a; }
DEVI float lds32f(unsigned a) { return *(LAS float*)(uintptr_t)a; }
DEVI void sts32f(unsigned a, float v) { *(LAS float*)(uintptr_t)a = v; }
DEVI void sts128(unsigned a, v4u v) { *(LAS v4u*)(uintptr_t)a = v; }
DEVI void sts128f(unsigned a, f32x4 v) { *(LAS f32x4*)(uintptr_t)a = v; }
DEVI v2u ldstr(unsigned a) { return __builtin_bit_cast(v2u, __builtin_amdgcn_ds_read_tr16_b64_v4i16((LAS s16x4*)(uintptr_t)a)); }
DEVI void fence_c() { asm volatile("" ::: "memory"); }
DEVI void bar_l() { asm volatile("s_waitcnt lgkmcnt(0)\n\ts_barrier" ::: "memory"); }
template <int N> DEVI void waitv() { asm volatile("s_waitcnt vmcnt(%0)" ::"n"(N) : "memory"); }
DEVI void dma16(const void* sbase, unsigned voff, unsigned ldst) {
    asm volatile("s_mov_b32 m0, %2\n\ts_nop 0\n\tglobal_load_lds_dwordx4 %0, %1" ::"v"(voff), "s"(sbase), "s"(ldst) : "memory");
}
DEVI void post(unsigned a, unsigned v) { fence_c(); *(LAS unsigned*)(uintptr_t)a = v; fence_c(); }
DEVI unsigned peek(unsigned a) { fence_c(); return *(LAS unsigned*)(uintptr_t)a; }
DEVI bool any_lt(unsigned v, unsigned need) { return __builtin_amdgcn_ballot_w64(v < need) != 0; }
DEVI void wait_ge(unsigned a, unsigned need) {
    int s = LSPIN_MAX;
    NOUNROLL while (any_lt(peek(a), need)) { asm volatile("s_sub_i32 %0, %0, 1" : "+s"(s) :: "scc"); if (s < 0) break; if (SPIN_SLEEP) __builtin_amdgcn_s_sleep(SPIN_SLEEP); }
    fence_c();
}
template <int V> DEVI void postm(unsigned a, int lane) {
    asm volatile("s_mov_b64 exec, %2\n\tds_max_u32 %0, %1\n\ts_mov_b64 exec, -1" ::"v"(a), "v"(lane), "n"(1 << V) : "memory");
}
DEVI unsigned vm_outstanding() {
    unsigned v; asm volatile("s_getreg_b32 %0, hwreg(HW_REG_IB_STS)" : "=s"(v)::"memory");
    return (v & 15u) | ((v >> 18) & 0x30u);
}
DEVI void postv(unsigned a, int lane, unsigned v) {
    asm volatile("s_lshl_b64 exec, 1, %2\n\tds_max_u32 %0, %1\n\ts_mov_b64 exec, -1" ::"v"(a), "v"(lane), "s"(v) : "memory", "scc");
}
template <int V> DEVI void postc(unsigned a, int lane) {
    asm volatile("s_mov_b64 exec, %2\n\tds_write_b32 %0, %1\n\ts_mov_b64 exec, -1" ::"v"(a), "v"(lane), "n"(1 << V) : "memory");
}
DEVI void need_full(bool lack, unsigned a, unsigned need) { if (__builtin_expect(lack, 0)) wait_ge(a, need); fence_c(); }
#define VAL(id, v)
DEVI float sfma(float a, float b, float c) { float d; asm("v_fma_f32 %0, %1, %2, %3" : "=v"(d) : "v"(a), "v"(b), "v"(c)); return d; }
DEVI float sfms(float a, float b, float c) { float d; asm("v_fma_f32 %0, %1, %2, -%3" : "=v"(d) : "v"(a), "v"(b), "v"(c)); return d; }
DEVI float sfnma(float a, float b, float c) { float d; asm("v_fma_f32 %0, -%1, %2, %3" : "=v"(d) : "v"(a), "v"(b), "v"(c)); return d; }
DEVI float sadd(float a, float b) { float d; asm("v_add_f32 %0, %1, %2" : "=v"(d) : "v"(a), "v"(b)); return d; }
DEVI float smul(float a, float b) { float d; asm("v_mul_f32 %0, %1, %2" : "=v"(d) : "v"(a), "v"(b)); return d; }
DEVI float smul_t(float a, float b) { float d; asm("s_nop 0\n\tv_mul_f32 %0, %1, %2" : "=v"(d) : "v"(a), "v"(b)); return d; }
struct cpx { float x, y; };
DEVI cpx cmul(cpx a, cpx b) { return cpx{sfms(a.x, b.x, smul(a.y, b.y)), sfma(a.x, b.y, smul(a.y, b.x))}; }
DEVI float lmad(cpx a, float h, float u) {
    float d;
    asm("v_fma_f32 %0, %1, %3, %4\n\ts_nop 0\n\tv_fmac_f32_dpp %0, %3, %2 quad_perm:[1,0,3,2] row_mask:0xf bank_mask:0xf" : "=&v"(d) : "v"(a.x), "v"(a.y), "v"(h), "v"(u));
    return d;
}
DEVI f32x2 dup(float v) { f32x2 d = {v, v}; asm("" : "+v"(d)); return d; }
DEVI f32x2 pfma(f32x2 a, f32x2 b, f32x2 c) { return __builtin_elementwise_fma(a, b, c); }
DEVI f32x2 lo2(f32x4 v) { return f32x2{v.x, v.y}; }
DEVI f32x2 hi2(f32x4 v) { return f32x2{v.z, v.w}; }
#define SB() __builtin_amdgcn_sched_barrier(0)
template <int V> struct IC { static constexpr int value = V; };
template <int B, int E, class F> DEVI void sfor(F&& f) { if constexpr (B < E) { f(IC<B>{}); sfor<B + 1, E>(f); } }
#define LAMB(nn) [&](auto nn) __attribute__((always_inline))
#define STAMP(id)
#define STAMP_INIT()
#define STAMP_DUMP()
DEVI int fresh_lane() { int l; asm volatile("v_mbcnt_lo_u32_b32 %0, -1, 0\n\tv_mbcnt_hi_u32_b32 %0, -1, %0" : "=v"(l)); return l; }
DEVI v4u make_sel(int lane, int bb) {
    const int c = lane & 15, tk = 8 * (c >> 2) + 4 * bb + (c & 3) - 8 * (lane >> 4);
    v4u s;
#pragma unroll
    for (int e = 0; e < 4; e++) s[e] = (tk == 2 * e ? 0x3f80u : 0u) | (tk == 2 * e + 1 ? 0x3f800000u : 0u);
    return s;
}
#define LANE_VARS const int lane = fresh_lane(), c = lane & 15, g = lane >> 4, q8 = lane >> 3, p8 = lane & 7; (void)c; (void)g; (void)q8; (void)p8
constexpr unsigned wslot(int n) { return (unsigned)(n & 3) * SLOT; }

#define ROT1 ((i + 2 * kq) & 7)
#define ROT2 0
#define XI_STEP 6
#define XI2_STEP 15
#define TOUCH_GATE 1
#define LB_SLEEP 8
#define LB_FIRST 10
#define T2_SLEEP 1
#define FL_FIRST 20
#define FL_SLEEP 6
#define MIDV 64
#define X_FIRST 14
#define X_SLEEP 1
#define PDV 4
#define INFL 8
#define TSL 6
#define TW __builtin_amdgcn_s_sleep(TSL);
#define PACE 14
#define PACE2 8
#define XLV 3
#define LGATE 80
#define RAISE2 23
#define TGN 37
#define EGATE 102
#define X_LAST 6
#define CLAG 4
#define CRING 8
#define PF_SLEEP 2
#define WS_SHIFT 0
#define NOXT
#define LT_SLEEP 16
#define MG 18
#define ABLW(v) 0
#define WR_DEPTH 4
#define SLEEP0 12
constexpr unsigned CODE_LINES = 306;
template <int OFF> DEVI void reldA(v4u& d, unsigned voff, v4u srd, unsigned so) {
    asm volatile("buffer_load_dwordx4 %0, %1, %2, %3 offen offset:%4 sc1" : "+v"(d) : "v"(voff), "s"(srd), "s"(so), "n"(OFF) : "memory");
}
template <int OFF> DEVI void stA(const v4u& d, unsigned voff, v4u srd, unsigned so) {
    asm volatile("buffer_store_dwordx4 %0, %1, %2, %3 offen offset:%4 sc1\n\ts_nop 1" ::"v"(d), "v"(voff), "s"(srd), "s"(so), "n"(OFF) : "memory");
}
template <int OFF> DEVI void stX(const v4u& d, unsigned voff, v4u srd, unsigned so) {
    asm volatile("buffer_store_dwordx4 %0, %1, %2, %3 offen offset:%4 sc1" ::"v"(d), "v"(voff), "s"(srd), "s"(so), "n"(OFF) : "memory");
}
DEVI void ldW(v4u& d, unsigned voff, v4u srd, unsigned so) { asm volatile("buffer_load_dwordx4 %0, %1, %2, %3 offen" : "=v"(d) : "v"(voff), "s"(srd), "s"(so) : "memory"); }
DEVI void touch(unsigned& sink, const void* tp, unsigned to) { asm volatile("global_load_dword %0, %1, %2" : "+v"(sink) : "v"(to), "s"(tp) : "memory"); }

DEVI v4u make_srd(const void* base) { const unsigned long long a = (unsigned long long)base; return v4u{(unsigned)a, (unsigned)(a >> 32) & 0xffffu, 0x7fffffffu, 0x00020000u}; }
DEVI void dma4(v4u srd, unsigned ve, unsigned vo, unsigned s0, unsigned s1, unsigned s2, unsigned s3, unsigned ldst) {
    asm volatile("s_mov_b32 m0, %7\n\ts_nop 0\n\tbuffer_load_dwordx4 %0, %2, %3 offen lds\n\tbuffer_load_dwordx4 %1, %2, %4 offen offset:1024 lds\n\t"
                 "buffer_load_dwordx4 %0, %2, %5 offen offset:2048 lds\n\tbuffer_load_dwordx4 %1, %2, %6 offen offset:3072 lds"
                 ::"v"(ve), "v"(vo), "s"(srd), "s"(s0), "s"(s1), "s"(s2), "s"(s3), "s"(ldst) : "memory");
}
DEVI void dma1(v4u srd, unsigned v, unsigned so, unsigned ldst) {
    asm volatile("s_mov_b32 m0, %3\n\ts_nop 0\n\tbuffer_load_dwordx4 %0, %1, %2 offen lds" ::"v"(v), "s"(srd), "s"(so), "s"(ldst) : "memory");
}
template <int H, int NEG> DEVI f32x2 bmul(f32x2 a, f32x2 b) {
    f32x2 d; asm("v_pk_mul_f32 %0, %1, %2 op_sel:[%3,0] op_sel_hi:[%3,1] neg_lo:[%4,0] neg_hi:[%4,0]" : "=v"(d) : "v"(a), "v"(b), "n"(H), "n"(NEG)); return d;
}
template <int H, int NEG> DEVI f32x2 bfma(f32x2 a, f32x2 b, f32x2 c) {
    f32x2 d; asm("v_pk_fma_f32 %0, %1, %2, %3 op_sel:[%4,0,0] op_sel_hi:[%4,1,1] neg_lo:[%5,0,0] neg_hi:[%5,0,0]" : "=v"(d) : "v"(a), "v"(b), "v"(c), "n"(H), "n"(NEG)); return d;
}
DEVI f32x2 cmadp(f32x2 a, f32x2 h, f32x2 u) {
    f32x2 t, d;
    asm("v_pk_fma_f32 %0, %1, %2, %3 op_sel:[0,0,0] op_sel_hi:[0,1,1]" : "=v"(t) : "v"(h), "v"(a), "v"(u));
    asm("v_pk_fma_f32 %0, %1, %2, %3 op_sel:[1,0,0] op_sel_hi:[0,0,1] neg_lo:[1,0,0]" : "=v"(d) : "v"(a), "v"(f32x2{h.y, h.y}), "v"(t));
    return d;
}
DEVI void mma_c(f32x4& d, const v4u& a, const v4u& b, const f32x4& c) { asm volatile("v_mfma_f32_16x16x32_bf16 %0, %1, %2, %3" : "=&v"(d) : "v"(a), "v"(b), "v"(c)); }
DEVI void mma_t(f32x4& d, const v4u& a, const v4u& b, const f32x4& c) { asm volatile("v_mfma_f32_16x16x32_bf16 %0, %1, %2, %3" : "+v"(d) : "v"(a), "v"(b), "v"(c)); }
DEVI unsigned cvtv(float a, float b) { unsigned d; asm volatile("v_cvt_pk_bf16_f32 %0, %1, %2" : "=v"(d) : "v"(a), "v"(b)); return d; }

__global__ __launch_bounds__(512, 2) void fused(Params p) {
    const unsigned char* pc0;
    asm volatile("s_getpc_b64 %0" : "=s"(pc0));
    const int w = __builtin_amdgcn_readfirstlane(threadIdx.x >> 6);
    const int b = blockIdx.x, xcd = b & 7, idx = b >> 3;
    const int i = 8 * (xcd & 1) + (idx & 7), kq = xcd >> 1, q = idx >> 3, j = 4 * kq + q;
    const bool heavy = q != 3;
    const int N1 = heavy ? 20 : 12;
    const int lw = w & 1, pr = w & 3;
    const int rot = ROT1;
    const __amdgpu_buffer_rsrc_t rws = __builtin_amdgcn_make_buffer_rsrc(p.ws, 0, WS_END, 0x00020000);
    auto unit_of = [&](int v) { return v < 4 ? 7 * kq + v : v < 8 ? 28 + 8 * ((v >> 1) & 1) + 2 * kq + (v & 1) : 7 * kq + v - 4; };
    const unsigned sl_lo = L_P + (unsigned)pr * SLOT;
    const unsigned xo = WS_XB + (unsigned)((i * 4 + q) * 4) * 24576u + pr * 6144;
    const unsigned fo = WS_FLAG + (unsigned)(i * 16 + j) * 512u;

    if (w >= 4) {
        LANE_VARS;
        STAMP_INIT();
        STAMP(0);
        const unsigned swz = (unsigned)((p8 ^ (q8 >> 1)) << 4);
        const unsigned fa = F_DONE + (lane & 3) * 4;
        unsigned sink = 0, tg;
        auto poll16 = [&](int which) __attribute__((always_inline)) {
            wait_ge(F_OUT + (lane & 3) * 4, which + 1);
            STAMP(16);
            const unsigned pf = WS_FLAG + (unsigned)(i * 16 + 4 * kq + (lane & 3)) * 512u + which * 128 + ((lane >> 2) & 3) * 8;
            NOUNROLL for (int spin = 0; spin < SPIN_MAX; spin++) {
                fence_c();
                const v2u f = __builtin_amdgcn_raw_buffer_load_b64(rws, pf, 0, 16);
                const unsigned bad = (unsigned)__builtin_amdgcn_ballot_w64(f.x != tg || f.y != ~tg);
                if (bad == 0) break;
                if ((bad & 0x7777u) == 0) { if (which) break; postc<1>(F_RDY, lane); }
                __builtin_amdgcn_s_sleep(FL_SLEEP);
            }
            postc<2>(F_RDY + which * 4, lane);
            STAMP(17);
        };
        if (w < 6) {
            unsigned ffull = F_FULL + lw * 4;
            asm volatile("" : "+v"(ffull));
            touch(sink, lw ? p.Bim : p.Bre, (32 * j + 2 * i) * 1024 + c * 128);
            bar_l();
            {
                const unsigned voffS = (8 * (q8 >> 2) + (q8 & 3)) * 1024 + swz;
                const v4u srd = make_srd((const unsigned char*)(lw ? p.Bim : p.Bre) + (size_t)(32 * j) * 1024);
                wait_ge(F_XI + (lane & 3) * 4, 1);
                NOUNROLL for (int n = 0; n < 8; n++) {
                    const unsigned so = 128 * ((n + rot) & 7);
                    dma4(srd, voffS, voffS ^ 64u, so, so + 15360, so + 2048, so + 17408, L_W + (n >> 1) * SLOT + lw * 8192 + (n & 1) * 4096);
                    waitv<8>(); postv(ffull, lane, max(n - 1, 0) >> 1);
                }
                asm volatile("" ::"v"(sink));
            }
            STAMP(1);
            const unsigned voffG = q8 * (FF * 2) + (((p8 >> 1) ^ ((q8 >> 1) & 1)) << 5) + ((p8 & 1) << 4), voffG1 = voffG ^ 64u;
            const unsigned voffC = q8 * 1024 + swz, voffC1 = voffC ^ 64u;
            const unsigned voffD = q8 * 1024 + (((p8 >> 1) ^ (q8 >> 1) ^ (2 * (kq & 1))) << 5) + ((p8 & 1) << 4);
            const unsigned char* gb = (const unsigned char*)(lw ? p.wu : p.wg) + (7 * kq + q) * 128;
            unsigned thr = (lane >> 2) + 1;
            int credit = 0, cbase = 0;
            auto item = [&](int n, const unsigned char* b0, const unsigned char* b1, unsigned ve, unsigned vo, unsigned st) __attribute__((always_inline)) {
                v4u s0 = make_srd(b0), s1 = make_srd(b1);
                unsigned dst = ((n & 4) ? L_P : L_W) + (n & 3) * SLOT + lw * 8192, dst1 = dst + 4096;
                asm volatile("" : "+s"(s0), "+s"(s1), "+s"(dst), "+s"(dst1));
                int sp = LSPIN_MAX;
                NOUNROLL while (n - 7 > credit) {
                    const unsigned dn = peek(fa);
                    postv(ffull, lane, n - ((vm_outstanding() + 7) >> 3));
                    unsigned long long mk = __builtin_amdgcn_ballot_w64(dn >= thr);
                    mk &= mk >> 1; mk &= mk >> 2;
                    credit = __builtin_popcountll(mk & 0x1111111111111111ull) + cbase;
                    asm volatile("s_sub_i32 %0, %0, 1" : "+s"(sp)::"scc");
                    if (sp < 0) break;
                }
                waitv<INFL>();
                dma4(s0, ve, vo, 0, st - 1024, 2 * st - 2048, 3 * st - 3072, dst);
                postv(ffull, lane, n - (INFL + 7) / 8);
                waitv<INFL>();
                dma4(s1, ve, vo, 0, st - 1024, 2 * st - 2048, 3 * st - 3072, dst1);
            };
            wait_ge(F_XI + (lane & 3) * 4, 3);
            NOUNROLL for (int n = 4; n < N1; n++) {
                const int t = n - 4;
                const unsigned char* bp = gb + (size_t)(((t + rot) & 7) * 64) * (FF * 2) + (t >> 3) * 512;
                item(n, bp, bp + 32 * FF * 2, voffG, voffG1, 8 * FF * 2);
            }
            cbase = N1 - 8; thr += cbase;
            const unsigned char* dp = (const unsigned char*)p.wd + (size_t)(7 * kq) * 65536 + q * 256 + (lw ^ (kq >> 1)) * 128;
            NOUNROLL for (int v = 0; v < 7; v++) {
                if (v >= 4) {
                    const unsigned char* cbp = (const unsigned char*)((v & 2) ? p.Cim : p.Cre) + (size_t)q * 131072 + (2 * kq + (v & 1)) * 128;
                    item(N1 + v, cbp + ((2 * lw) ^ kq) * 32768, cbp + ((2 * lw + 1) ^ kq) * 32768, voffC, voffC1, 8192);
                } else {
                    item(N1 + v, dp, dp + 32768, voffD, voffD, 8192);
                    dp += 65536;
                }
            }
            waitv<16>(); postv(ffull, lane, N1 + 5);
            waitv<8>(); postv(ffull, lane, N1 + 6);
            waitv<0>(); postv(ffull, lane, N1 + 11);
            tg = peek(L_TAG);
            STAMP(7);
            if (lw) {
                const unsigned pf = WS_FLAG + (unsigned)(i * 16 + 4 * (lane & 3) + q) * 512u + 256 + ((lane >> 2) & 1) * 8;
                NOUNROLL for (int spin = 0; spin < SPIN_MAX; spin++) {
                    fence_c();
                    const v2u f = __builtin_amdgcn_raw_buffer_load_b64(rws, pf, 0, 16);
                    if (__builtin_amdgcn_ballot_w64(f.x != tg || f.y != ~tg) == 0) break;
                    __builtin_amdgcn_s_sleep(16);
                }
            }
            if (lw) poll16(1);
        } else {
            unsigned tag0, prm0, prm1, prm2;
            const unsigned cto = min(idx * 11 + 4 * lw + p8, (int)CODE_LINES - 1) * 128;
            touch(sink, pc0, cto);
            touch(sink, lw ? (const void*)p.ws : (const void*)p.Dsk, lw ? ((lane & 4) ? WS_GRAN + (j * 16 + i) * 512 : fo) + (lane & 3) * 128 : p8 * 128);
            if (!heavy) __builtin_amdgcn_s_sleep(LT_SLEEP);
            NOUNROLL for (int t = heavy ? 4 : 0; t < 4; t++)
                touch(sink, lw ? p.wu : p.wg, (32 * i + (lane & 31)) * (FF * 2) + (7 * kq + min(2 * t + (lane >> 5), 6)) * 128);
            const unsigned po = 2 * (32 * j + 8 * (lane >> 4) + 4 * ((lane >> 1) & 1) + ((lane >> 2) & 3));
            asm volatile("global_load_ushort %0, %4, %5\n\tglobal_load_ushort %1, %4, %6\n\tglobal_load_ushort %2, %4, %7\n\tglobal_load_dword %3, %8, %9 sc1"
                         : "=&v"(prm0), "=&v"(prm1), "=&v"(prm2), "=&v"(tag0) : "v"(po), "s"(p.lre), "s"(p.lim), "s"(p.ldt), "v"(WS_EPOCH), "s"(p.ws) : "memory");
            *(LAS unsigned*)(uintptr_t)(F_DONE + (lane & 31) * 4) = 0u;
            bar_l();
            sts128(L_SEL + lw * 1024 + lane * 16, make_sel(lane, lw));
            asm volatile("s_waitcnt vmcnt(0)" : "+v"(prm0), "+v"(prm1), "+v"(prm2), "+v"(tag0)::"memory");
            tg = tag0 + 1u;
            const bool odd = lane & 1;
            const unsigned la = lane * 4;
            const float lr = bflo(prm0), li = bflo(prm1);
            float dt = __expf(bflo(prm2));
            dt = bflo(pk2(dt, dt));
            const float ex = __expf(dt * lr);
            const float fr = __builtin_amdgcn_fractf(dt * li * 0.15915494309189535f);
            const float ar = smul_t(ex, __builtin_amdgcn_cosf(fr)), ai = smul_t(ex, __builtin_amdgcn_sinf(fr));
            const cpx a1 = {ar, ai};
            const cpx a2 = cmul(a1, a1), a4 = cmul(a2, a2);
            const unsigned sg = odd ? 0u : 0x80000000u;
            auto sgn = [&](cpx v) { return cpx{v.x, __uint_as_float(__float_as_uint(v.y) ^ sg)}; };
            const cpx r8 = cmul(a4, a4), r16 = cmul(r8, r8), r32 = cmul(r16, r16), r64 = cmul(r32, r32);
            const cpx a32 = sgn(r32);
            {
                const cpx r = lw ? r32 : r8, rs = sgn(r);
                float v = odd ? r.y : r.x;
                sts32f(L_PW + lw * 1024 + la, odd ? 0.0f : 1.0f);
#pragma unroll
                for (int s = 1; s < 4; s++) { sts32f(L_PW + lw * 1024 + s * 256 + la, v); if (s < 3) v = lmad(rs, v, 0.0f); }
            }
            if (lw == 0) {
                const float nr = ar - 1.0f, den = smul_t(__builtin_amdgcn_rcpf(sfma(lr, lr, smul(li, li))), 2.0f);
                float co = smul(odd ? sfms(ai, lr, smul(nr, li)) : sfma(nr, lr, smul(ai, li)), den);
                sts32f(L_CST + la, odd ? a1.y : a1.x); sts32f(L_CST + 256 + la, odd ? a4.y : a4.x);
                const cpx a1s = sgn(a1);
#pragma unroll
                for (int t = 0; t < 5; t++) { sts32f(L_CST + (2 + t) * 256 + la, co); if (t < 4) co = lmad(a1s, co, 0.0f); }
                *(LAS unsigned*)(uintptr_t)L_TAG = tg;
            }
            post(F_COMM + lw * 4, 1);
            STAMP(1);
            cpx wn[8];
            {
                const cpx r128 = cmul(r64, r64), r256 = cmul(r128, r128);
                wn[0] = lw ? r128 : cpx{1.0f, 0.0f};
#pragma unroll
                for (int n = 1; n < 8; n++) wn[n] = cmul(wn[n - 1], r256);
#pragma unroll
                for (int n = 0; n < 8; n++) { wn[n] = sgn(wn[n]); asm volatile("" : "+v"(wn[n].x), "+v"(wn[n].y)); }
            }
            unsigned gro = WS_GRAN + j * 8192 + lane * 8, gro2 = gro + i * 512;
            unsigned to0 = lw ? min((unsigned)(lane & 31) * 0x98000u + idx * 0x4000u, WS_END - 4) : (lane & 3) * 0x100000 + idx * 0x4000;
            unsigned to1 = (448 * kq + 28 * i + min(lane, 27)) * 1024 + (2 * q + lw) * 128, to2 = (128 * q + 8 * i + p8) * 1024 + (2 * kq + (q8 & 1)) * 128;
            unsigned po2 = WS_XB + (unsigned)((i * 4 + q) * 4 + kq) * 24576u + lw * 12288 + lane * 16;
            unsigned voffC = q8 * 1024 + swz, voffC1 = voffC ^ 64u;
            unsigned voffD = q8 * 1024 + (((p8 >> 1) ^ (q8 >> 1) ^ (2 * (kq & 1))) << 5) + ((p8 & 1) << 4);
            v4u none = {~0u, ~0u, ~0u, ~0u};
            asm volatile("" : "+v"(gro), "+v"(gro2), "+v"(to0), "+v"(to1), "+v"(to2), "+v"(po2), "+v"(voffC), "+v"(voffC1), "+v"(voffD), "+v"(none));
            wait_ge(F_XI + (lane & 3) * 4, heavy ? 2 : 0);
            touch(sink, p.wd, to1);
            touch(sink, lw ? p.Cim : p.Cre, to2);
            touch(sink, lw ? (const void*)p.ws : (const void*)p.out, to0);
            v4u wr[4][8];
            auto paced = [&](auto dl, auto ds) __attribute__((always_inline)) {
                const v4u sws = make_srd(p.ws);
                const v4u sc = make_srd((const unsigned char*)p.Cim + (size_t)q * 131072 + (2 * kq + 1) * 128);
                const v4u sd = make_srd((const unsigned char*)p.wd + (size_t)(7 * kq + 4) * 65536 + q * 256 + (lw ^ (kq >> 1)) * 128);
                sfor<0, 36>(LAMB(ff) {
                    constexpr int f = decltype(ff)::value, m = f & 7;
                    if constexpr (decltype(dl)::value) {
                        if constexpr (f < 8) ldW(wr[0][m], (m & 1) ? voffC1 : voffC, sc, ((2 * lw + (m >> 2)) ^ kq) * 32768 + (m & 3) * 8192);
                        else if constexpr (f < 32) ldW(wr[f >> 3][m], voffD, sd, (f - 8) * 8192);
                    }
                    if constexpr (f % 3 == 2) { if constexpr (decltype(ds)::value) stX<((f / 3) & 3) * 1024>(none, po2, sws, (f / 12) * 4096); __builtin_amdgcn_s_sleep(PF_SLEEP); }
                });
            };
            if constexpr (PFPOS != 3) { waitv<0>(); __builtin_amdgcn_s_sleep(T2_SLEEP); paced(IC<!(PFPOS & 1)>{}, IC<!(PFPOS & 2)>{}); }
            STAMP(3);
            wait_ge(fa, 4);
            STAMP(4);
            if (lw) {
                float e[4];
#pragma unroll
                for (int s = 0; s < 4; s++) e[s] = lds32f(L_TOT + s * 256 + la);
                const float p2 = lmad(a32, e[0], e[1]), p3 = lmad(a32, p2, e[2]);
                __builtin_amdgcn_raw_buffer_store_b64(v2u{__float_as_uint(lmad(a32, p3, e[3])), tg}, rws, gro2, 0, 16);
                sts32f(L_PRE + la, 0.0f); sts32f(L_PRE + 256 + la, e[0]); sts32f(L_PRE + 512 + la, p2); sts32f(L_PRE + 768 + la, p3);
            }
            STAMP(5);
            __builtin_amdgcn_s_sleep(LB_FIRST);
            {
                const int top = i - 1 - lw;
                v2u gr[8] = {};
                NOUNROLL for (int spin = 0; spin < SPIN_MAX; spin++) {
                    fence_c();
#pragma unroll
                    for (int n = 0; n < 8; n++) gr[n] = __builtin_amdgcn_raw_buffer_load_b64(rws, gro, ((top - 2 * n) & 15) * 512, 16);
                    unsigned long long bad = 0;
#pragma unroll
                    for (int n = 0; n < 8; n++) if (top - 2 * n >= 0) bad |= __builtin_amdgcn_ballot_w64(gr[n].y != tg);
                    if (bad == 0) break;
                    __builtin_amdgcn_s_sleep(LB_SLEEP);
                }
                float h = 0;
#pragma unroll
                for (int n = 7; n >= 0; n--) if (top - 2 * n >= 0) h = lmad(wn[n], __uint_as_float(gr[n].x), h);
                sts32f(L_XCH + 256 + lw * 256 + la, h);
            }
            postc<4>(F_COMM + lw * 4, lane);
            STAMP(6);
            if constexpr (PFPOS != 0) paced(IC<(PFPOS & 1)>{}, IC<(PFPOS & 2)>{});
            waitv<0>();
            asm volatile("" ::"v"(sink));
            if (lane == 0) __builtin_amdgcn_raw_buffer_store_b64(v2u{tg, ~tg}, rws, fo + 256 + lw * 8, 0, 16);
            STAMP(7);
#pragma unroll
            for (int u = 0; u < 4; u++) asm volatile("" : "+v"(wr[u][0]), "+v"(wr[u][1]), "+v"(wr[u][2]), "+v"(wr[u][3]), "+v"(wr[u][4]), "+v"(wr[u][5]), "+v"(wr[u][6]), "+v"(wr[u][7]));
            STAMP(13);
            if (!lw) poll16(0);
            {
                unsigned wa0 = lw * 8192 + lane * 16 + L_P, wa1 = wa0 + (L_W + 3 * SLOT - L_P);
                asm volatile("" : "+v"(wa0), "+v"(wa1));
                sfor<0, 4>(LAMB(uu) {
                    constexpr int u = decltype(uu)::value;
                    wait_ge(fa, N1 + u);
#pragma unroll
                    for (int m = 0; m < 8; m++) sts128((u ? wa0 + (u - 1) * SLOT : wa1) + m * 1024, wr[u][m]);
                    postv(F_COMM + lw * 4, lane, N1 + 8 + u);
                });
            }
            STAMP(15);
        }
        f32x4 base[4];
        {
            const int d = 128 * q + 32 * kq + 4 * p8;
            const v2u dv = *(const GAS v2u*)(p.Dsk + d);
            const float df[4] = {bflo(dv.x), bfhi(dv.x), bflo(dv.y), bfhi(dv.y)};
            v2u xv[4];
#pragma unroll
            for (int e = 0; e < 4; e++) xv[e] = *(const GAS v2u*)(p.x + (size_t)(128 * i + 32 * pr + 8 * e + q8) * DM + d);
#pragma unroll
            for (int e = 0; e < 4; e++) {
                const unsigned tp = (32 * pr + 8 * e + 4 * ((q8 >> 1) & 1) + 2 * (q8 & 1) + (q8 >> 2)) * 4;
                const float rs = lds32f(L_RSTD + tp), nm = smul(lds32f(L_MEAN + tp), rs);
                const float xf[4] = {bflo(xv[e].x), bfhi(xv[e].x), bflo(xv[e].y), bfhi(xv[e].y)};
#pragma unroll
                for (int k = 0; k < 4; k++) base[e][k] = sfma(sfma(xf[k], rs, nm), df[k], xf[k]);
            }
        }
        STAMP(8);
        const unsigned xi = xo + kq * 24576u + lane * 16;
        const unsigned sc = L_O + pr * 4096;
        unsigned wa = sc + (8 * (c >> 2) + 4 * (c & 1) + ((c >> 1) & 1)) * 128 + ((g ^ (c & 7)) << 4), ra = sc + q8 * 128 + ((p8 ^ (2 * (q8 & 1) + (q8 >> 2))) << 4);
        GAS float* op = (GAS float*)p.out + (size_t)(128 * i + 32 * pr + q8) * DM + 128 * q + 32 * kq + 4 * p8;
        asm volatile("" : "+v"(wa), "+v"(ra), "+v"(op));
        wait_ge(F_SENT + pr * 4, 3);
        __builtin_amdgcn_s_sleep(X_FIRST);
        v4u in[6];
        f32x4 own[4];
        NOUNROLL for (int spin = 0; spin < SPIN_MAX; spin++) {
            fence_c();
#pragma unroll
            for (int n = 0; n < 6; n++) in[n] = __builtin_amdgcn_raw_buffer_load_b128(rws, xi + n * 1024, 0, 16);
            SB();
            wait_ge(F_SENT + pr * 4, 4);
#pragma unroll
            for (int e = 0; e < 4; e++) own[e] = lds128f(sc + e * 1024 + lane * 16);
            unsigned mx = 0;
#pragma unroll
            for (int n = 0; n < 6; n++) mx = max(mx, max(max(in[n].x, in[n].y), max(in[n].z, in[n].w)));
            if (__builtin_amdgcn_ballot_w64(mx == ~0u) == 0) break;
        }
        STAMP(11);
#pragma unroll
        for (int e = 0; e < 4; e++)
#pragma unroll
            for (int sr = 0; sr < 3; sr++) { const unsigned d0 = in[2 * sr + (e >> 1)][2 * (e & 1)], d1 = in[2 * sr + (e >> 1)][2 * (e & 1) + 1]; own[e] += f32x4{bflo(d0), bfhi(d0), bflo(d1), bfhi(d1)}; }
        asm volatile("" : "+v"(own[0]), "+v"(own[1]), "+v"(own[2]), "+v"(own[3]));
#pragma unroll
        for (int e = 0; e < 4; e++) sts128f((wa ^ (unsigned)((e >> 1) << 6)) + (e & 1) * 256, own[e]);
        fence_c();
        f32x4 ta[4];
#pragma unroll
        for (int e = 0; e < 4; e++) ta[e] = lds128f((ra ^ (unsigned)((e & 1) << 6)) + e * 1024);
        SB();
#pragma unroll
        for (int e = 0; e < 4; e++) *(GAS f32x4*)(op + e * 8 * DM) = base[e] + ta[e];
        if (b == 255 && w == 4 && lane == 0) __builtin_amdgcn_raw_buffer_store_b32(tg, rws, WS_EPOCH, 0, 16);
        STAMP(12); STAMP_DUMP();
        return;
    }
    constexpr int PD = PDV, XL = XLV, EG = EGATE;
    unsigned stall = 0; (void)stall;
    v4u xa[16][2];
#define AF(u, k) xa[2 * (u) + ((k) >> 1)][(k) & 1]
    v4u sws = make_srd(p.ws);
    unsigned oG = WS_A2 + (unsigned)(i * 44 + 7 * kq) * 16384u + w * 4096, oC = WS_A2 + (unsigned)(i * 44 + 28 + 2 * kq) * 16384u + w * 4096, oF = fo + w * 8;
    asm volatile("" : "+s"(sws), "+s"(oG), "+s"(oC), "+s"(oF));
    auto uoff = [&](int v) __attribute__((always_inline)) { return v < 4 ? oG + v * 16384 : v < 8 ? oC + (8 * ((v >> 1) & 1) + (v & 1)) * 16384 : oG + (v - 4) * 16384; };
    LANE_VARS;
    unsigned fdone = F_DONE + w * 4, l16 = lane * 16;
    asm volatile("" : "+v"(fdone), "+v"(l16));
    unsigned tgs = 0; int raised = 0;
    auto raise = [&](int level) __attribute__((always_inline)) {
        asm volatile("s_mov_b64 exec, 1\n\tbuffer_store_dwordx2 %0, off, %1, %2 offset:%3 sc1\n\ts_mov_b64 exec, -1" ::"v"(v2u{tgs, ~tgs}), "s"(sws), "s"(oF), "n"((level - 1) * 128) : "memory");
        post(fdone + (F_OUT - F_DONE), level);
        raised = level;

        if (level == 1) STAMP(17); else STAMP(18);
    };
    auto try_raise = [&](int level) __attribute__((always_inline)) {
        unsigned v; asm volatile("s_getreg_b32 %0, hwreg(HW_REG_IB_STS, 0, 4)" : "=s"(v)::"memory");
        if (__builtin_expect((int)(v + raised) == level - 1, 0)) raise(level);
    };
    unsigned oX[3];
#pragma unroll
    for (int sl = 1; sl < 4; sl++) { oX[sl - 1] = xo + (kq ^ sl) * 24576u + (sl - 1) * 2048; asm volatile("" : "+s"(oX[sl - 1])); }
    unsigned cbs[2], tbs[4];
    v4u bf[PD];
    const unsigned ffl = F_FULL + (lane & 1) * 4;
    auto set2 = [&](auto kk) __attribute__((always_inline)) {
        constexpr int k = decltype(kk)::value;
        if constexpr (k == 0) tbs[0] = L_P + ((l16 << 1) & 0x780) + ((l16 >> 1) & 0x18) + (((l16 >> 2) & 0x40) | ((l16 >> 2) & 0x20));
        else if constexpr (k < 4) tbs[k] = tbs[0] ^ (unsigned)(k << 5);
        else if constexpr (k == 4) cbs[0] = L_W + ((l16 << 3) & 0x780) + (((l16 >> 4) ^ (l16 >> 1)) & 0x70);
        else cbs[1] = cbs[0] ^ 64u;
        if constexpr (k < 4) asm volatile("" : "+v"(tbs[k])); else asm volatile("" : "+v"(cbs[k - 4]));
    };
    auto prime2 = [&](auto mm) __attribute__((always_inline)) { constexpr int m = decltype(mm)::value; const v2u lo = ldstr(tbs[m]), hi = ldstr(tbs[m] + 2048); bf[m] = v4u{lo.x, lo.y, hi.x, hi.y}; };
    {
        STAMP_INIT();
        STAMP(0);
        unsigned cb[2];
        cb[0] = (unsigned)c * 128u + (unsigned)((g ^ (c >> 1)) << 4); cb[1] = cb[0] ^ 64u;
        {
            const unsigned voffX = (8 * (q8 >> 2) + 4 * (q8 & 1) + ((q8 >> 1) & 1)) * 1024 + ((p8 ^ (q8 >> 1)) << 4), voffX1 = voffX ^ 64u;
            const v4u srd = make_srd((const unsigned char*)p.x + (size_t)(128 * i + 32 * w) * 1024 - 4096);
            auto issue_x = [&](int t) __attribute__((always_inline)) {
                const unsigned s0 = (t >> 3) * 2048 + ((t + rot) & 7) * 128, s1 = (t >> 3) * 2048 + ((t + 1 + rot) & 7) * 128 + 2048;
                dma4(srd, voffX, voffX1, s0 + 4096, s0 + 4096 + 15360, s1, s1 + 15360, sl_lo + (t & 7) * 2048);
            };
            issue_x(0); issue_x(2); issue_x(4); issue_x(6);
            bar_l();
            const v4u sel0 = make_sel(lane, 0), sel1 = make_sel(lane, 1);
            v4u ones = {0x3f803f80u, 0x3f803f80u, 0x3f803f80u, 0x3f803f80u};
            asm volatile("" : "+v"(ones));
            f32x4 sm[2], sq[2], nm4[2], ct[6];
            const unsigned xl0 = cb[0] + sl_lo, xl1 = cb[1] + sl_lo;
            unsigned sa = L_RSTD + (32 * w + 8 * g + (c & 3)) * 4;
            const unsigned long long dgm = __builtin_amdgcn_ballot_w64(g == (c >> 2));
            asm volatile("" : "+v"(sa));
            auto stats = [&](int bb) __attribute__((always_inline)) {
                float s01, s23, sd;
                asm("v_cndmask_b32 %0, %1, %2, %3" : "=v"(s01) : "v"(sq[bb].x), "v"(sq[bb].y), "s"(0xaaaaaaaaaaaaaaaaull));
                asm("v_cndmask_b32 %0, %1, %2, %3" : "=v"(s23) : "v"(sq[bb].z), "v"(sq[bb].w), "s"(0xaaaaaaaaaaaaaaaaull));
                asm("v_cndmask_b32 %0, %1, %2, %3" : "=v"(sd) : "v"(s01), "v"(s23), "s"(0xccccccccccccccccull));
                const float rstd = __builtin_amdgcn_rsqf(fmaxf(sd * (1.0f / DM) - nm4[bb].x * nm4[bb].x, 0.0f) + 1e-5f);
                asm volatile("s_mov_b64 exec, %3\n\tds_write2_b32 %0, %1, %2 offset0:%4 offset1:%5\n\ts_mov_b64 exec, -1" ::"v"(sa), "v"(rstd), "v"(nm4[bb].x), "s"(dgm), "n"(4 * bb), "n"(L_MEAN / 4 + 4 * bb) : "memory");
            };
            auto centre = [&](auto kk) __attribute__((always_inline)) {
                constexpr int k = decltype(kk)::value, e = k - 4;
                if constexpr (k < 64) mma_c(ct[k % 6], (k & 1) ? sel1 : sel0, xa[(k >> 1) & 15][k >> 5], nm4[k >> 5]);
                if constexpr (e >= 0) {
                    xa[(e >> 1) & 15][e >> 5][2 * (e & 1)] = cvtv(ct[e % 6].x, ct[e % 6].y);
                    xa[(e >> 1) & 15][e >> 5][2 * (e & 1) + 1] = cvtv(ct[e % 6].z, ct[e % 6].w);
                }
                SB();
            };
            SB();
            sfor<0, 17>(LAMB(tt) {
                constexpr int t = decltype(tt)::value, u = t - 1;
                if constexpr (t < 16) {
                    constexpr int t5 = ((t + 1) & ~1) + 5, top = t5 < 7 ? 7 : t5 > 15 ? 15 : t5;
                    waitv<2 * (top - t)>();
                    xa[2 * (t & 7)][t >> 3] = lds128(xl0 + (t & 7) * 2048); xa[2 * (t & 7) + 1][t >> 3] = lds128(xl1 + (t & 7) * 2048);
                }
                SB();
                if constexpr (t == 9) { asm volatile("s_nop 3"); SB(); nm4[0] = sm[0] * (-1.0f / DM); asm volatile("s_nop 1" : "+v"(nm4[0])); SB(); }
                if constexpr (t >= 9) { centre(IC<4 * (t - 9)>{}); centre(IC<4 * (t - 9) + 1>{}); centre(IC<4 * (t - 9) + 2>{}); centre(IC<4 * (t - 9) + 3>{}); }
                if constexpr (u >= 0) {
                    mma<(u & 7) == 0>(sm[u >> 3], ones, xa[2 * (u & 7)][u >> 3]); mma<(u & 7) == 0>(sq[u >> 3], xa[2 * (u & 7)][u >> 3], xa[2 * (u & 7)][u >> 3]);
                    mma<false>(sm[u >> 3], ones, xa[2 * (u & 7) + 1][u >> 3]); mma<false>(sq[u >> 3], xa[2 * (u & 7) + 1][u >> 3], xa[2 * (u & 7) + 1][u >> 3]);
                }
                if constexpr ((t & 1) == 0 && t >= 2 && t + 6 < 16) issue_x(t + 6);
                if constexpr (t == XI_STEP) post(fdone + (F_XI - F_DONE), 1);
                if constexpr (t == XI2_STEP) post(fdone + (F_XI - F_DONE), 2);
                SB();
            });
            post(fdone + (F_XI - F_DONE), 3);
            STAMP(1);
            stats(0);
            SB();
            nm4[1] = sm[1] * (-1.0f / DM); asm volatile("s_nop 1" : "+v"(nm4[1]));
            SB();
            sfor<32, 64>(LAMB(kk) { centre(kk); });
            stats(1);
            SB();
            sfor<64, 68>(LAMB(kk) { centre(kk); });
        }
        STAMP(2);
        f32x4 eX;
        unsigned tb[4];
        {
            cb[0] += L_W; cb[1] += L_W;
            asm volatile("" : "+v"(cb[0]), "+v"(cb[1]));
            f32x4 acc[4][2];
            unsigned fl = 0; bool lack = false;
            {
                const int q4 = c >> 2, p4 = c & 3;
                const unsigned tb1 = L_P + (unsigned)(8 * g + q4) * 128u + (unsigned)((((q4 >> 1) & 1) | ((g & 1) << 1)) << 5) + p4 * 8;
#pragma unroll
                for (int t = 0; t < 4; t++) { tb[t] = tb1 ^ (unsigned)(t << 5); asm volatile("" : "+v"(tb[t])); }
            }
            f32x4 ca, c4, co, c8, rs0, rs1;
            wait_ge(F_FULL + (lane & 3) * 4, 1);
            STAMP(3);
            sfor<-PD, 64>(LAMB(nn) {
                constexpr int n = decltype(nn)::value, m = n + PD;
                v4u cur;
                if constexpr (n >= 0) { cur = bf[n % PD]; mma<(n < 4)>(acc[n & 3][0], xa[n >> 2][0], cur); SB(); }
                if constexpr (m > 0 && (m & 15) == 0) need_full(lack, ffl, (m >> 4) + 1);
                if constexpr (m < 64) bf[m % PD] = lds128(cb[(m >> 2) & 1] + wslot(m >> 4) + (((m >> 1) & 1) * 4 + ((m >> 3) & 1) * 2 + (m & 1)) * 2048);
                else { const unsigned a = tb[(m >> 1) & 3] + wslot(0) + (m & 1) * 8192; const v2u lo = ldstr(a), hi = ldstr(a + 512); bf[m % PD] = v4u{lo.x, lo.y, hi.x, hi.y}; }
                SB();
                if constexpr (n >= 0) { mma<(n < 4)>(acc[n & 3][1], xa[n >> 2][1], cur); SB(); }
                if constexpr ((m & 15) == 10) fl = peek(ffl);
                if constexpr ((m & 15) == 14) lack = any_lt(fl, (m >> 4) + 2);
                if constexpr ((m & 15) == 15 && m < 63) post(fdone, (m >> 4) + 1);
                if constexpr (m == 52) ca = lds128f(L_CST + c * 16);
                if constexpr (m == 53) c4 = lds128f(L_CST + 256 + c * 16);
                if constexpr (m == 54) co = lds128f(L_CST + 512 + c * 16);
                if constexpr (m == 57) c8 = lds128f(L_PW + 256 + c * 16);
                if constexpr (m == 55) rs0 = lds128f(L_RSTD + (32 * w + 8 * g) * 4);
                if constexpr (m == 56) rs1 = lds128f(L_RSTD + (32 * w + 8 * g + 4) * 4);
                SB();
            });
            mfma_done();
            STAMP(4); VAL(24, stall); stall = 0;
            f32x4 ev;
            f32x2 sr[2][4], si[2][4];
#pragma unroll
            for (int st = 0; st < 2; st++) {
                const f32x4 r0 = acc[st][0] * rs0, r1 = acc[st][1] * rs1, i0 = acc[2 + st][0] * rs0, i1 = acc[2 + st][1] * rs1;
                sr[st][0] = lo2(r0); sr[st][1] = hi2(r0); sr[st][2] = lo2(r1); sr[st][3] = hi2(r1);
                si[st][0] = lo2(i0); si[st][1] = hi2(i0); si[st][2] = lo2(i1); si[st][3] = hi2(i1);
            }
            const f32x2 a1[2] = {lo2(ca), hi2(ca)}, cf[2] = {lo2(co), hi2(co)};
#pragma unroll
            for (int t = 1; t < 4; t++) {
                f32x2 tr[2], ti[2];
#pragma unroll
                for (int st = 0; st < 2; st++) { tr[st] = bfma<1, 1>(a1[st], si[st][t - 1], sr[st][t]); ti[st] = bfma<1, 0>(a1[st], sr[st][t - 1], si[st][t]); }
                SB();
#pragma unroll
                for (int st = 0; st < 2; st++) { sr[st][t] = bfma<0, 0>(a1[st], sr[st][t - 1], tr[st]); si[st][t] = bfma<0, 0>(a1[st], si[st][t - 1], ti[st]); }
                SB();
            }
#pragma unroll
            for (int st = 0; st < 2; st++) {
                ev[2 * st] = sfma(c4[2 * st], sr[st][3].x, sfnma(c4[2 * st + 1], si[st][3].x, sr[st][3].y));
                ev[2 * st + 1] = sfma(c4[2 * st], si[st][3].x, sfma(c4[2 * st + 1], sr[st][3].x, si[st][3].y));
                eX[2 * st] = sr[st][3].x; eX[2 * st + 1] = si[st][3].x;
            }
            sts128f(L_SEG + w * 1024 + l16, ev);
            fence_c();
            f32x4 es[4];
#pragma unroll
            for (int s = 0; s < 4; s++) es[s] = lds128f(L_SEG + w * 1024 + s * 256 + (l16 & 0xf0));
            SB();
            unsigned ya = L_O + w * 4096 + l16;
            asm volatile("" : "+v"(ya), "+v"(eX));
#pragma unroll
            for (int st = 0; st < 2; st++) {
                f32x2 tr[4], ti[4];
#pragma unroll
                for (int t = 0; t < 4; t++) { tr[t] = bmul<1, 1>(cf[st], si[st][t]); ti[t] = bmul<1, 1>(cf[st], sr[st][t]); }
                SB();
#pragma unroll
                for (int t = 0; t < 4; t++) { tr[t] = bfma<0, 0>(cf[st], sr[st][t], tr[t]); ti[t] = bfma<0, 1>(cf[st], si[st][t], ti[t]); }
                SB();
                sts128(ya + st * 1024, v4u{pk2(tr[0]), pk2(tr[1]), pk2(tr[2]), pk2(tr[3])});
                sts128(ya + 2048 + st * 1024, v4u{pk2(ti[0]), pk2(ti[1]), pk2(ti[2]), pk2(ti[3])});
                SB();
                if (st == 0) {
#pragma unroll
                    for (int s = 1; s < 4; s++) { const f32x2 u0 = cmadp(lo2(c8), lo2(es[s - 1]), lo2(es[s])), u1 = cmadp(hi2(c8), hi2(es[s - 1]), hi2(es[s])); es[s] = f32x4{u0.x, u0.y, u1.x, u1.y}; }
                    sts128f(L_TOT + w * 256 + (l16 & 0xf0), es[3]);
                    post(fdone, 4);
                    f32x4 pin;
#pragma unroll
                    for (int k = 0; k < 4; k++) { const float lo = g == 1 ? es[0][k] : 0.0f, hi = g == 3 ? es[2][k] : es[1][k]; pin[k] = g >= 2 ? hi : lo; }
                    sts128f(L_SEG + w * 1024 + l16, pin);
                    SB();
                }
            }
        }
        STAMP(5);
        unsigned go = oG + q * 16384, so = oC + (q >> 1) * 16384 + (q & 1) * 2048;
        asm volatile("" : "+s"(go), "+s"(so));
        f32x4 acc[4][2][2];
        f32x2 r12;
        unsigned fc = 0;
        auto gpass = [&](auto pp, auto n0, auto n1) __attribute__((always_inline)) {
            constexpr int pass = decltype(pp)::value;
            unsigned fl = 0, dn = 0; bool lack = false;
            sfor<decltype(n0)::value, decltype(n1)::value>(LAMB(nn) {
                constexpr int n = decltype(nn)::value, m = n + PD;
                constexpr bool pre = pass == 0 || m < 128;
                constexpr unsigned off = wslot(m >> 4) + (m & 1) * 8192 + ((m >> 3) & 1) * 4096;
                const v4u cur = bf[(n + 64 + 128 * pass) % PD];
                mma<(n < 8)>(acc[(n >> 1) & 3][n & 1][0], cur, xa[n >> 3][0]);
                SB();
                if constexpr ((m & 15) == 0) post(fdone, dn);
                if constexpr ((m & 15) == 0) need_full(lack, ffl, 5 + 8 * pass + (m >> 4));
                v2u lo, hi;
                if constexpr (pre) lo = ldstr(tb[(m >> 1) & 3] + off); else lo = ldstr(tbs[m & 3]);
                if constexpr (pass == 1 && n >= 69 && n < 81 && (n & 1)) set2(IC<(n - 69) / 2>{});
                if constexpr ((m & 15) == 10) fl = peek(ffl);
                if constexpr ((m & 15) == 15) lack = any_lt(fl, m == 127 && pass == 0 ? N1 - 7 : 6 + 8 * pass + (m >> 4));
                if constexpr ((m & 15) == 13) { dn = 5 + 8 * pass + (m >> 4); asm volatile("" : "+v"(dn)); }
                if constexpr (n == EG - 8 && pass == 1) fc = peek(F_RDY);
                if constexpr (n == EG - 2 && pass == 1) { if (__builtin_expect(any_lt(fc, 1), 0)) { wait_ge(F_RDY, 1); } fence_c(); }
                if constexpr (n >= EG && n < EG + 24 && pass == 1 && (n - EG) % 3 == 0) { constexpr int f = (n - EG) / 3; reldA<(f & 3) * 1024>(AF(f >> 2, f & 3), l16, sws, uoff(f >> 2)); }
                if constexpr (pass == 1 && (n == 33 || n == 41 || n == 49 || n == 57)) try_raise(1);
                if constexpr (pass == 1 && n == 67) { if (raised < 1) { waitv<0>(); raise(1); } }
                if constexpr (n == 120 && pass == 0) fc = peek(ffl + (F_COMM - F_FULL));
                if constexpr (n == 121 && pass == 0) { const unsigned ra = L_RSTD + w * 128 + (((l16 >> 1) & 0x60) | ((l16 >> 2) & 0xc)); r12 = f32x2{lds32f(ra), lds32f(ra + 16)}; }
                SB();
                mma<(n < 8)>(acc[(n >> 1) & 3][n & 1][1], cur, xa[n >> 3][1]);
                SB();
                if constexpr (pre) hi = ldstr(tb[(m >> 1) & 3] + off + 512); else hi = ldstr(tbs[m & 3] + 2048);
                bf[n % PD] = v4u{lo.x, lo.y, hi.x, hi.y};
                if constexpr ((m & 57) == 57 && (pass == 0 || m < 64)) {
                    tb[(m >> 1) & 3] += (m & 64) ? L_P - L_W : L_W - L_P; asm volatile("" : "+v"(tb[(m >> 1) & 3]));
                }
                SB();
            });
        };
        auto gated = [&](auto ff) __attribute__((always_inline)) {
            constexpr int uu = decltype(ff)::value >> 1, bb = decltype(ff)::value & 1;
            const f32x4 h0 = acc[2 * uu][0][bb] * acc[2 * uu][1][bb], h1 = acc[2 * uu + 1][0][bb] * acc[2 * uu + 1][1][bb];
            return v4u{pk2(bmul<bb, 0>(r12, lo2(h0))), pk2(bmul<bb, 0>(r12, hi2(h0))), pk2(bmul<bb, 0>(r12, lo2(h1))), pk2(bmul<bb, 0>(r12, hi2(h1)))};
        };
        gpass(IC<0>{}, IC<0>{}, IC<128>{});
        {
            r12 = r12 * r12;
            mfma_done();
            STAMP(6); VAL(25, stall); stall = 0;
            sfor<0, 2>(LAMB(ff) { stA<decltype(ff)::value * 1024>(gated(ff), l16, sws, go); });
            SB();
            need_full(any_lt(fc, 4), ffl + (F_COMM - F_FULL), 4);
            const unsigned tg = peek(L_TAG);
            const unsigned cl = L_CST + (l16 & 0xf0);
            unsigned ya = L_O + w * 4096 + l16;
            asm volatile("" : "+v"(ya));
            const f32x4 h0 = lds128f(L_XCH + 256 + (l16 & 0xf0)), h1 = lds128f(L_XCH + 512 + (l16 & 0xf0));
            const f32x4 aw = lds128f(L_PW + 1024 + w * 256 + (l16 & 0xf0)), pb = lds128f(L_PRE + w * 256 + (l16 & 0xf0));
            const f32x4 ag = lds128f(L_PW + l16), pin = lds128f(L_SEG + w * 1024 + l16);
            const v4u sel[2] = {lds128(L_SEL + l16), lds128(L_SEL + 1024 + l16)};
            const v4u yr[2] = {lds128(ya), lds128(ya + 1024)}, yi[2] = {lds128(ya + 2048), lds128(ya + 3072)};
            const f32x4 c4 = lds128f(cl + 256);
            f32x4 cw[4];
            SB();
            sfor<2, 4>(LAMB(ff) { stA<decltype(ff)::value * 1024>(gated(ff), l16, sws, go); });
            tgs = __builtin_amdgcn_readfirstlane(tg);
#pragma unroll
            for (int t = 0; t < 4; t++) cw[t] = lds128f(cl + (3 + t) * 256);
            SB();
            f32x4 zr[2][2], zi[2][2];
#pragma unroll
            for (int st = 0; st < 2; st++)
#pragma unroll
                for (int bb = 0; bb < 2; bb++) { mma<true>(zr[st][bb], yr[st], sel[bb]); mma<true>(zi[st][bb], yi[st], sel[bb]); }
            SB();
            f32x2 hr[2], hi[2];
            {
                const f32x4 cy = h0 + h1;
                const f32x2 k0 = cmadp(lo2(ag), cmadp(lo2(aw), lo2(cy), lo2(pb)), lo2(pin)), k1 = cmadp(hi2(ag), cmadp(hi2(aw), hi2(cy), hi2(pb)), hi2(pin));
                const f32x4 hin = {k0.x, k0.y, k1.x, k1.y};
                float t0[2], t1[2];
#pragma unroll
                for (int st = 0; st < 2; st++) { t0[st] = sfnma(c4[2 * st + 1], hin[2 * st + 1], eX[2 * st]); t1[st] = sfma(c4[2 * st + 1], hin[2 * st], eX[2 * st + 1]); }
                SB();
#pragma unroll
                for (int st = 0; st < 2; st++) { hr[st] = f32x2{hin[2 * st], sfma(c4[2 * st], hin[2 * st], t0[st])}; hi[st] = f32x2{hin[2 * st + 1], sfma(c4[2 * st], hin[2 * st + 1], t1[st])}; }
                SB();
            }
#pragma unroll
            for (int st = 0; st < 2; st++) {
                f32x2 tr[4], ti[4];
#pragma unroll
                for (int t = 0; t < 4; t++) { const f32x2 wv = st ? hi2(cw[t]) : lo2(cw[t]); tr[t] = bmul<1, 1>(wv, hi[st]); ti[t] = bmul<1, 1>(wv, hr[st]); }
                SB();
#pragma unroll
                for (int t = 0; t < 4; t++) { const f32x2 wv = st ? hi2(cw[t]) : lo2(cw[t]); tr[t] = bfma<0, 0>(wv, hr[st], tr[t]); ti[t] = bfma<0, 1>(wv, hi[st], ti[t]); }
                SB();
                v4u fr = {pk2(tr[0]), pk2(tr[1]), pk2(tr[2]), pk2(tr[3])}, fi = {pk2(ti[0]), pk2(ti[1]), pk2(ti[2]), pk2(ti[3])};
                asm volatile("s_nop 1" : "+v"(fr), "+v"(fi));
#pragma unroll
                for (int bb = 0; bb < 2; bb++) { mma<false>(zr[st][bb], fr, sel[bb]); mma<false>(zi[st][bb], fi, sel[bb]); }
                SB();
            }
            mfma_done();
            stA<0>(pk8(zr[0][0], zr[1][0]), l16, sws, so); stA<0>(pk8(zi[0][0], zi[1][0]), l16, sws, so + 8 * 16384);
            stA<1024>(pk8(zr[0][1], zr[1][1]), l16, sws, so); stA<1024>(pk8(zi[0][1], zi[1][1]), l16, sws, so + 8 * 16384);
            SB();
            STAMP(7);
        }
        if (heavy) {
            gpass(IC<1>{}, IC<0>{}, IC<128>{});
            sfor<0, 4>(LAMB(ff) { constexpr int f = decltype(ff)::value; reldA<f * 1024>(AF(2, f), l16, sws, uoff(2)); });
            mfma_done();
            STAMP(8); VAL(26, stall); stall = 0;
            sfor<0, 4>(LAMB(ff) { stA<decltype(ff)::value * 1024>(gated(ff), l16, sws, go + 4 * 16384); });
            SB();
        }
        int q2 = q;
        asm volatile("" : "+s"(q2));
        if (q2 == 3) {
            waitv<0>(); raise(1); raise(2);
            sfor<0, 6>(set2);
            wait_ge(ffl, N1 + 1);
            sfor<0, PD>(prime2);
            wait_ge(F_RDY, 1);
            sfor<0, 12>(LAMB(ff) { constexpr int f = decltype(ff)::value; reldA<(f & 3) * 1024>(AF(f >> 2, f & 3), l16, sws, uoff(f >> 2)); });
#pragma unroll
            for (int u = 0; u < 3; u++) asm volatile("s_waitcnt vmcnt(0)" : "+v"(AF(u, 0)), "+v"(AF(u, 1)), "+v"(AF(u, 2)), "+v"(AF(u, 3))::"memory");
        }
    }
    STAMP(9);
    {
        f32x4 acc[8][2];
        v4u pk[2];
        unsigned fl = 0, fr = 0, dn = 0; bool lack = false;
        constexpr auto fv = [](int n) { return n < 48 ? n >> 4 : n < 108 ? 3 + (n - 48) / 12 : n < 144 ? 8 + ((n - 108) % 12) / 4 : 3 + (n - 144) / 4; };
        constexpr auto fk = [](int n) { return n < 48 ? (n >> 3) & 1 : n < 108 ? ((n - 48) % 12) / 6 : (n >> 1) & 1; };
        constexpr auto fd = [](int n) { return n < 48 ? n & 7 : n < 108 ? 2 + (n - 48) % 6 : n < 144 ? 2 * ((n - 108) / 12 + 1) + (n & 1) : n & 1; };
        constexpr auto first = [](int n) { return n >= 0 && (n < 48 ? (n & 15) == 0 : n < 108 ? (n - 48) % 12 == 0 : n < 120 && (n & 3) == 0); };
        constexpr int LG = LGATE;
        constexpr auto nmid = [](int n) { return n <= MG ? 0 : (n - MG + 2) / 3 < 20 ? (n - MG + 2) / 3 : 20; };
        static_assert(MG + 57 < LG && LG + 22 < 108, "load schedule");
        sfor<0, 176>(LAMB(nn) {
            constexpr int n = decltype(nn)::value, m = n + PD;
            constexpr int v = fv(n), ks = fk(n), dt = fd(n);
            constexpr int mv = fv(m), mk = fk(m), md = fd(m);
            constexpr bool mc = (mv & ~3) == 4;
            constexpr unsigned moff = (mv & 3) * SLOT + (mc ? md * 2048 : (md >> 2) * 8192 + mk * 4096);
            constexpr auto chk = [](int q) { return q == 16 ? 2 : q == 32 ? 4 : q == 60 ? 7 : q == 96 ? 11 : 0; };
            v4u cur; v2u lo, hi;
            if constexpr (n >= 0) {
                if constexpr (first(n)) {
                    constexpr int mid = nmid(n), late = n <= LG ? 0 : (n - LG + 1) / 2 < 12 ? (n - LG + 1) / 2 : 12;
                    constexpr int NW = v == 0 ? 12 : v == 1 ? 8 : v == 2 ? 63 : v < 8 ? mid - 4 * (v - 2) + late : late - 4 * (v - 7);
                    static_assert(NW >= 0, "load schedule");
                    asm volatile("s_waitcnt vmcnt(%4)" : "+v"(AF(v & 7, 0)), "+v"(AF(v & 7, 1)), "+v"(AF(v & 7, 2)), "+v"(AF(v & 7, 3)) : "n"(ABLW(v) ? 63 : NW) : "memory");
                }
                cur = bf[n % PD];
                mma<(n < 8)>(acc[dt][0], cur, AF(v & 7, 2 * ks));
                SB();
            }
            if constexpr (m > 0 && (m & 15) == 0 && m <= 48) post(fdone, dn);
            if constexpr (chk(m) > 0) need_full(lack, m == 96 ? ffl + (F_COMM - F_FULL) : ffl, N1 + chk(m));
            if constexpr (m < 176) { if constexpr (mc) bf[m % PD] = lds128(cbs[mk] + moff); else lo = ldstr(tbs[md & 3] + moff); }
            if constexpr (chk(m + 6) > 0) fl = peek(m == 90 ? ffl + (F_COMM - F_FULL) : ffl);
            if constexpr (chk(m + 1) > 0) lack = any_lt(fl, N1 + chk(m + 1));
            if constexpr ((m & 15) == 13 && m < 48) { dn = N1 + mv + 1; asm volatile("" : "+v"(dn)); }
            if constexpr (n == MG - 7) fr = peek(F_RDY);
            if constexpr (n == MG - 1) { if (__builtin_expect(any_lt(fr, 2), 0)) { if (raised < 2) { waitv<0>(); raise(2); } wait_ge(F_RDY, 2); } fence_c(); }
            if constexpr (n == RAISE2) { if (raised < 2) { if (!ABLW(99)) waitv<nmid(n)>(); raise(2); } }
            if constexpr (n >= MG && n < MG + 60 && (n - MG) % 3 == 0) { constexpr int f = (n - MG) / 3; reldA<(f & 3) * 1024>(AF(3 + (f >> 2), f & 3), l16, sws, uoff(3 + (f >> 2))); }
            if constexpr (n == LG - 6) fr = peek(F_RDY + 4);
            if constexpr (n == LG - 1) need_full(any_lt(fr, 1), F_RDY + 4, 1);
            if constexpr (n >= LG && n < LG + 24 && (n - LG) % 2 == 0) { constexpr int f = (n - LG) / 2; reldA<(f & 3) * 1024>(AF(f >> 2, f & 3), l16, sws, uoff(8 + (f >> 2))); }
            SB();
            if constexpr (n >= 0) { mma<(n < 8)>(acc[dt][1], cur, AF(v & 7, 2 * ks + 1)); SB(); }
            if constexpr (m < 176 && !mc) { hi = ldstr(tbs[md & 3] + moff + 2048); bf[m % PD] = v4u{lo.x, lo.y, hi.x, hi.y}; }
            if constexpr (n >= 120 && n < 156) {
                constexpr int sl = (n - 108) / 12, r = (n - 108) % 12 - 2;
                if constexpr (r >= 0 && r < 10 && r % 5 < 4) { const f32x4& a = acc[2 * sl + r / 5][(r % 5) >> 1]; pk[r / 5][r % 5] = cvtv(a[2 * (r % 5 & 1)], a[2 * (r % 5 & 1) + 1]); }
                if constexpr (r >= 0 && r % 5 == 4) stX<(r / 5) * 1024>(pk[r / 5], l16, sws, oX[sl - 1]);
            }
            if constexpr (n == 156) post(fdone + (F_SENT - F_DONE), 3);
            if constexpr (n == 107) { STAMP(10); VAL(28, stall); stall = 0; }
            SB();
        });
        mfma_done();
        STAMP(11); VAL(29, stall);
#pragma unroll
        for (int e = 0; e < 4; e++) sts128f(L_O + w * 4096 + e * 1024 + l16, acc[e >> 1][e & 1]);
        post(fdone + (F_SENT - F_DONE), 4);
    }
    STAMP(14); STAMP_DUMP();
}

extern "C" void kernel_launch(void* const* d_in, const int* in_sizes, int n_in, void* d_out, int out_size, void* d_ws, size_t ws_size,
                              hipStream_t stream) {
    Params p;
    p.x = (const u16*)d_in[0]; p.lre = (const u16*)d_in[1]; p.lim = (const u16*)d_in[2];
    p.Bre = (const u16*)d_in[3]; p.Bim = (const u16*)d_in[4]; p.Cre = (const u16*)d_in[5]; p.Cim = (const u16*)d_in[6];
    p.ldt = (const u16*)d_in[7]; p.Dsk = (const u16*)d_in[8];
    p.wg = (const u16*)d_in[10]; p.wu = (const u16*)d_in[11]; p.wd = (const u16*)d_in[12];
    p.out = (float*)d_out; p.ws = (unsigned char*)d_ws + WS_SHIFT;
    (void)hipFuncSetAttribute((const void*)fused, hipFuncAttributeMaxDynamicSharedMemorySize, L_TOTAL);
    fused<<<256, 512, L_TOTAL, stream>>>(p);
}
```

```cpp
#define NOXT2
#include <hip/hip_runtime.h>
#include <stdint.h>

typedef unsigned short u16;
typedef unsigned v4u __attribute__((ext_vector_type(4)));
typedef unsigned v2u __attribute__((ext_vector_type(2)));
typedef short s16x4 __attribute__((ext_vector_type(4)));
typedef __bf16 bf16x2 __attribute__((ext_vector_type(2)));
typedef __bf16 bf16x8 __attribute__((ext_vector_type(8)));
typedef float f32x2 __attribute__((ext_vector_type(2)));
typedef float f32x4 __attribute__((ext_vector_type(4)));
#define GAS __attribute__((address_space(1)))
#define LAS __attribute__((address_space(3)))
#define DEVI __device__ __forceinline__
#define NOUNROLL _Pragma("nounroll")

#define SPIN_SLEEP 0
constexpr int DM = 512, FF = 1792;
constexpr unsigned WS_A2 = 0;
constexpr unsigned WS_XB = 16u * 44u * 16384u;
constexpr unsigned WS_GRAN = WS_XB + 256u * 24576u;
constexpr unsigned WS_FLAG = WS_GRAN + 16u * 16u * 512u;
constexpr unsigned WS_EPOCH = WS_FLAG + 1024u * 128u;
constexpr unsigned WS_END = WS_EPOCH + 256u;
constexpr unsigned SLOT = 16384;
constexpr unsigned L_RSTD = 0, L_MEAN = 512;
constexpr unsigned F_DONE = 1024, F_SENT = F_DONE + 16, F_FULL = F_DONE + 32, F_COMM = F_DONE + 40, F_RDY = F_DONE + 48, F_OUT = F_DONE + 64, F_XI = F_DONE + 80, F_BS = F_DONE + 96, L_TAG = F_DONE + 128;
constexpr unsigned L_CST = 1280;
constexpr unsigned L_XCH = L_CST + 8 * 256;
constexpr unsigned L_SEG = 4096, L_PW = 8192, L_TOT = L_PW + 2048, L_PRE = L_TOT + 1024, L_SEL = 12288;
constexpr unsigned L_P = SLOT, L_O = 5 * SLOT, L_W = 6 * SLOT, L_TOTAL = 10 * SLOT;
constexpr int SPIN_MAX = 1 << 18;
constexpr int LSPIN_MAX = 1 << 22;

struct Params {
    const u16 *x, *lre, *lim, *Bre, *Bim, *Cre, *Cim, *ldt, *Dsk, *wg, *wu, *wd;
    float* out;
    unsigned char* ws;
};

DEVI float bf2f(u16 v) { return __uint_as_float(((unsigned)v) << 16); }
DEVI float bflo(unsigned v) { return __uint_as_float(v << 16); }
DEVI float bfhi(unsigned v) { return __uint_as_float(v & 0xffff0000u); }
DEVI unsigned pk2(float a, float b) { f32x2 p = {a, b}; return __builtin_bit_cast(unsigned, __builtin_convertvector(p, bf16x2)); }
DEVI unsigned pk2(f32x2 p) { return __builtin_bit_cast(unsigned, __builtin_convertvector(p, bf16x2)); }
DEVI v4u pk8(f32x4 a, f32x4 b) { return v4u{pk2(a.x, a.y), pk2(a.z, a.w), pk2(b.x, b.y), pk2(b.z, b.w)}; }
DEVI float dot2(unsigned a, unsigned b, float c) { return __builtin_amdgcn_fdot2_f32_bf16(__builtin_bit_cast(bf16x2, a), __builtin_bit_cast(bf16x2, b), c, false); }
template <int CTRL> DEVI float dppf(float v) { return __builtin_bit_cast(float, __builtin_amdgcn_update_dpp(0, __builtin_bit_cast(int, v), CTRL, 0xf, 0xf, true)); }
DEVI f32x4 mfma(v4u a, v4u b, f32x4 c) { return __builtin_amdgcn_mfma_f32_16x16x32_bf16(__builtin_bit_cast(bf16x8, a), __builtin_bit_cast(bf16x8, b), c, 0, 0, 0); }
template <bool FIRST> DEVI void mma(f32x4& c, const v4u& a, const v4u& b) {
    if constexpr (FIRST) asm volatile("v_mfma_f32_16x16x32_bf16 %0, %1, %2, 0" : "=&v"(c) : "v"(a), "v"(b));
    else asm volatile("v_mfma_f32_16x16x32_bf16 %0, %1, %2, %0" : "+v"(c) : "v"(a), "v"(b));
}
DEVI void mfma_done() { __builtin_amdgcn_sched_barrier(0); asm volatile("s_nop 7"); __builtin_amdgcn_sched_barrier(0); }
DEVI v4u lds128(unsigned a) { return *(LAS v4u*)(uintptr_t)a; }
DEVI f32x4 lds128f(unsigned a) { return *(LAS f32x4*)(uintptr_t)a; }
DEVI float lds32f(unsigned a) { return *(LAS float*)(uintptr_t)a; }
DEVI void sts32f(unsigned a, float v) { *(LAS float*)(uintptr_t)a = v; }
DEVI void sts128(unsigned a, v4u v) { *(LAS v4u*)(uintptr_t)a = v; }
DEVI void sts128f(unsigned a, f32x4 v) { *(LAS f32x4*)(uintptr_t)a = v; }
DEVI v2u ldstr(unsigned a) { return __builtin_bit_cast(v2u, __builtin_amdgcn_ds_read_tr16_b64_v4i16((LAS s16x4*)(uintptr_t)a)); }
DEVI void fence_c() { asm volatile("" ::: "memory"); }
DEVI void bar_l() { asm volatile("s_waitcnt lgkmcnt(0)\n\ts_barrier" ::: "memory"); }
template <int N> DEVI void waitv() { asm volatile("s_waitcnt vmcnt(%0)" ::"n"(N) : "memory"); }
DEVI void dma16(const void* sbase, unsigned voff, unsigned ldst) {
    asm volatile("s_mov_b32 m0, %2\n\ts_nop 0\n\tglobal_load_lds_dwordx4 %0, %1" ::"v"(voff), "s"(sbase), "s"(ldst) : "memory");
}
DEVI void post(unsigned a, unsigned v) { fence_c(); *(LAS unsigned*)(uintptr_t)a = v; fence_c(); }
DEVI unsigned peek(unsigned a) { fence_c(); return *(LAS unsigned*)(uintptr_t)a; }
DEVI bool any_lt(unsigned v, unsigned need) { return __builtin_amdgcn_ballot_w64(v < need) != 0; }
DEVI void wait_ge(unsigned a, unsigned need) {
    int s = LSPIN_MAX;
    NOUNROLL while (any_lt(peek(a), need)) { asm volatile("s_sub_i32 %0, %0, 1" : "+s"(s) :: "scc"); if (s < 0) break; if (SPIN_SLEEP) __builtin_amdgcn_s_sleep(SPIN_SLEEP); }
    fence_c();
}
template <int V> DEVI void postm(unsigned a, int lane) {
    asm volatile("s_mov_b64 exec, %2\n\tds_max_u32 %0, %1\n\ts_mov_b64 exec, -1" ::"v"(a), "v"(lane), "n"(1 << V) : "memory");
}
DEVI unsigned vm_outstanding() {
    unsigned v; asm volatile("s_getreg_b32 %0, hwreg(HW_REG_IB_STS)" : "=s"(v)::"memory");
    return (v & 15u) | ((v >> 18) & 0x30u);
}
DEVI void postv(unsigned a, int lane, unsigned v) {
    asm volatile("s_lshl_b64 exec, 1, %2\n\tds_max_u32 %0, %1\n\ts_mov_b64 exec, -1" ::"v"(a), "v"(lane), "s"(v) : "memory", "scc");
}
template <int V> DEVI void postc(unsigned a, int lane) {
    asm volatile("s_mov_b64 exec, %2\n\tds_write_b32 %0, %1\n\ts_mov_b64 exec, -1" ::"v"(a), "v"(lane), "n"(1 << V) : "memory");
}
DEVI void need_full(bool lack, unsigned a, unsigned need) { if (__builtin_expect(lack, 0)) wait_ge(a, need); fence_c(); }
#define VAL(id, v)
DEVI float sfma(float a, float b, float c) { float d; asm("v_fma_f32 %0, %1, %2, %3" : "=v"(d) : "v"(a), "v"(b), "v"(c)); return d; }
DEVI float sfms(float a, float b, float c) { float d; asm("v_fma_f32 %0, %1, %2, -%3" : "=v"(d) : "v"(a), "v"(b), "v"(c)); return d; }
DEVI float sfnma(float a, float b, float c) { float d; asm("v_fma_f32 %0, -%1, %2, %3" : "=v"(d) : "v"(a), "v"(b), "v"(c)); return d; }
DEVI float sadd(float a, float b) { float d; asm("v_add_f32 %0, %1, %2" : "=v"(d) : "v"(a), "v"(b)); return d; }
DEVI float smul(float a, float b) { float d; asm("v_mul_f32 %0, %1, %2" : "=v"(d) : "v"(a), "v"(b)); return d; }
DEVI float smul_t(float a, float b) { float d; asm("s_nop 0\n\tv_mul_f32 %0, %1, %2" : "=v"(d) : "v"(a), "v"(b)); return d; }
struct cpx { float x, y; };
DEVI cpx cmul(cpx a, cpx b) { return cpx{sfms(a.x, b.x, smul(a.y, b.y)), sfma(a.x, b.y, smul(a.y, b.x))}; }
DEVI float lmad(cpx a, float h, float u) {
    float d;
    asm("v_fma_f32 %0, %1, %3, %4\n\ts_nop 0\n\tv_fmac_f32_dpp %0, %3, %2 quad_perm:[1,0,3,2] row_mask:0xf bank_mask:0xf" : "=&v"(d) : "v"(a.x), "v"(a.y), "v"(h), "v"(u));
    return d;
}
DEVI f32x2 dup(float v) { f32x2 d = {v, v}; asm("" : "+v"(d)); return d; }
DEVI f32x2 pfma(f32x2 a, f32x2 b, f32x2 c) { return __builtin_elementwise_fma(a, b, c); }
DEVI f32x2 lo2(f32x4 v) { return f32x2{v.x, v.y}; }
DEVI f32x2 hi2(f32x4 v) { return f32x2{v.z, v.w}; }
#define SB() __builtin_amdgcn_sched_barrier(0)
template <int V> struct IC { static constexpr int value = V; };
template <int B, int E, class F> DEVI void sfor(F&& f) { if constexpr (B < E) { f(IC<B>{}); sfor<B + 1, E>(f); } }
#define LAMB(nn) [&](auto nn) __attribute__((always_inline))
#define STAMP(id)
#define STAMP_INIT()
#define STAMP_DUMP()
DEVI int fresh_lane() { int l; asm volatile("v_mbcnt_lo_u32_b32 %0, -1, 0\n\tv_mbcnt_hi_u32_b32 %0, -1, %0" : "=v"(l)); return l; }
DEVI v4u make_sel(int lane, int bb) {
    const int c = lane & 15, tk = 8 * (c >> 2) + 4 * bb + (c & 3) - 8 * (lane >> 4);
    v4u s;
#pragma unroll
    for (int e = 0; e < 4; e++) s[e] = (tk == 2 * e ? 0x3f80u : 0u) | (tk == 2 * e + 1 ? 0x3f800000u : 0u);
    return s;
}
#define LANE_VARS const int lane = fresh_lane(), c = lane & 15, g = lane >> 4, q8 = lane >> 3, p8 = lane & 7; (void)c; (void)g; (void)q8; (void)p8
constexpr unsigned wslot(int n) { return (unsigned)(n & 3) * SLOT; }

#define ROT1 ((i + 2 * kq) & 7)
#define ROT2 0
#define XI_STEP 6
#define XI2_STEP 15
#define TOUCH_GATE 1
#define LB_SLEEP 8
#define LB_FIRST 1
#define T2_SLEEP 1
#define FL_FIRST 20
#define FL_SLEEP 6
#define MIDV 64
#define X_FIRST 14
#define X_SLEEP 1
#define PDV 4
#define INFL 8
#define TSL 6
#define TW __builtin_amdgcn_s_sleep(TSL);
#define PACE 14
#define PACE2 8
#define XLV 3
#define LGATE 80
#define RAISE2 23
#define TGN 37
#define EGATE 102
#define X_LAST 6
#define CLAG 4
#define CRING 8
#define PF_SLEEP 1
#define WS_SHIFT 0
#define NOXT
#define LT_SLEEP 16
#define MG 18
#define ABLW(v) 0
#define WR_DEPTH 4
#define PFPOS 2
#define PF_N 6
#define WR_SLEEP 2
#define SLEEP0 12
constexpr unsigned CODE_LINES = 306;
template <int OFF> DEVI void reldA(v4u& d, unsigned voff, v4u srd, unsigned so) {
    asm volatile("buffer_load_dwordx4 %0, %1, %2, %3 offen offset:%4 sc1" : "+v"(d) : "v"(voff), "s"(srd), "s"(so), "n"(OFF) : "memory");
}
template <int OFF> DEVI void stA(const v4u& d, unsigned voff, v4u srd, unsigned so) {
    asm volatile("buffer_store_dwordx4 %0, %1, %2, %3 offen offset:%4 sc1\n\ts_nop 1" ::"v"(d), "v"(voff), "s"(srd), "s"(so), "n"(OFF) : "memory");
}
template <int OFF> DEVI void stX(const v4u& d, unsigned voff, v4u srd, unsigned so) {
    asm volatile("buffer_store_dwordx4 %0, %1, %2, %3 offen offset:%4 sc1" ::"v"(d), "v"(voff), "s"(srd), "s"(so), "n"(OFF) : "memory");
}
DEVI void ldW(v4u& d, unsigned voff, v4u srd, unsigned so) { asm volatile("buffer_load_dwordx4 %0, %1, %2, %3 offen" : "=v"(d) : "v"(voff), "s"(srd), "s"(so) : "memory"); }
DEVI void touch(unsigned& sink, const void* tp, unsigned to) { asm volatile("global_load_dword %0, %1, %2" : "+v"(sink) : "v"(to), "s"(tp) : "memory"); }

DEVI v4u make_srd(const void* base) { const unsigned long long a = (unsigned long long)base; return v4u{(unsigned)a, (unsigned)(a >> 32) & 0xffffu, 0x7fffffffu, 0x00020000u}; }
DEVI void dma4(v4u srd, unsigned ve, unsigned vo, unsigned s0, unsigned s1, unsigned s2, unsigned s3, unsigned ldst) {
    asm volatile("s_mov_b32 m0, %7\n\ts_nop 0\n\tbuffer_load_dwordx4 %0, %2, %3 offen lds\n\tbuffer_load_dwordx4 %1, %2, %4 offen offset:1024 lds\n\t"
                 "buffer_load_dwordx4 %0, %2, %5 offen offset:2048 lds\n\tbuffer_load_dwordx4 %1, %2, %6 offen offset:3072 lds"
                 ::"v"(ve), "v"(vo), "s"(srd), "s"(s0), "s"(s1), "s"(s2), "s"(s3), "s"(ldst) : "memory");
}
DEVI void dma1(v4u srd, unsigned v, unsigned so, unsigned ldst) {
    asm volatile("s_mov_b32 m0, %3\n\ts_nop 0\n\tbuffer_load_dwordx4 %0, %1, %2 offen lds" ::"v"(v), "s"(srd), "s"(so), "s"(ldst) : "memory");
}
template <int H, int NEG> DEVI f32x2 bmul(f32x2 a, f32x2 b) {
    f32x2 d; asm("v_pk_mul_f32 %0, %1, %2 op_sel:[%3,0] op_sel_hi:[%3,1] neg_lo:[%4,0] neg_hi:[%4,0]" : "=v"(d) : "v"(a), "v"(b), "n"(H), "n"(NEG)); return d;
}
template <int H, int NEG> DEVI f32x2 bfma(f32x2 a, f32x2 b, f32x2 c) {
    f32x2 d; asm("v_pk_fma_f32 %0, %1, %2, %3 op_sel:[%4,0,0] op_sel_hi:[%4,1,1] neg_lo:[%5,0,0] neg_hi:[%5,0,0]" : "=v"(d) : "v"(a), "v"(b), "v"(c), "n"(H), "n"(NEG)); return d;
}
DEVI f32x2 cmadp(f32x2 a, f32x2 h, f32x2 u) {
    f32x2 t, d;
    asm("v_pk_fma_f32 %0, %1, %2, %3 op_sel:[0,0,0] op_sel_hi:[0,1,1]" : "=v"(t) : "v"(h), "v"(a), "v"(u));
    asm("v_pk_fma_f32 %0, %1, %2, %3 op_sel:[1,0,0] op_sel_hi:[0,0,1] neg_lo:[1,0,0]" : "=v"(d) : "v"(a), "v"(f32x2{h.y, h.y}), "v"(t));
    return d;
}
DEVI void mma_c(f32x4& d, const v4u& a, const v4u& b, const f32x4& c) { asm volatile("v_mfma_f32_16x16x32_bf16 %0, %1, %2, %3" : "=&v"(d) : "v"(a), "v"(b), "v"(c)); }
DEVI void mma_t(f32x4& d, const v4u& a, const v4u& b, const f32x4& c) { asm volatile("v_mfma_f32_16x16x32_bf16 %0, %1, %2, %3" : "+v"(d) : "v"(a), "v"(b), "v"(c)); }
DEVI unsigned cvtv(float a, float b) { unsigned d; asm volatile("v_cvt_pk_bf16_f32 %0, %1, %2" : "=v"(d) : "v"(a), "v"(b)); return d; }

__global__ __launch_bounds__(512, 2) void fused(Params p) {
    const unsigned char* pc0;
    asm volatile("s_getpc_b64 %0" : "=s"(pc0));
    const int w = __builtin_amdgcn_readfirstlane(threadIdx.x >> 6);
    const int b = blockIdx.x, xcd = b & 7, idx = b >> 3;
    const int i = 8 * (xcd & 1) + (idx & 7), kq = xcd >> 1, q = idx >> 3, j = 4 * kq + q;
    const bool heavy = q != 3;
    const int N1 = heavy ? 20 : 12;
    const int lw = w & 1, pr = w & 3;
    const int rot = ROT1;
    const __amdgpu_buffer_rsrc_t rws = __builtin_amdgcn_make_buffer_rsrc(p.ws, 0, WS_END, 0x00020000);
    auto unit_of = [&](int v) { return v < 4 ? 7 * kq + v : v < 8 ? 28 + 8 * ((v >> 1) & 1) + 2 * kq + (v & 1) : 7 * kq + v - 4; };
    const unsigned sl_lo = L_P + (unsigned)pr * SLOT;
    const unsigned xo = WS_XB + (unsigned)((i * 4 + q) * 4) * 24576u + pr * 6144;
    const unsigned fo = WS_FLAG + (unsigned)(i * 16 + j) * 512u;

    if (w >= 4) {
        LANE_VARS;
        STAMP_INIT();
        STAMP(0);
        const unsigned swz = (unsigned)((p8 ^ (q8 >> 1)) << 4);
        const unsigned fa = F_DONE + (lane & 3) * 4;
        unsigned sink = 0, tg;
        auto poll16 = [&](int which) __attribute__((always_inline)) {
            wait_ge(F_OUT + (lane & 3) * 4, which + 1);
            STAMP(16);
            const unsigned pf = WS_FLAG + (unsigned)(i * 16 + 4 * kq + (lane & 3)) * 512u + which * 128 + ((lane >> 2) & 3) * 8;
            NOUNROLL for (int spin = 0; spin < SPIN_MAX; spin++) {
                fence_c();
                const v2u f = __builtin_amdgcn_raw_buffer_load_b64(rws, pf, 0, 16);
                const unsigned bad = (unsigned)__builtin_amdgcn_ballot_w64(f.x != tg || f.y != ~tg);
                if (bad == 0) break;
                if ((bad & 0x7777u) == 0) { if (which) break; postc<1>(F_RDY, lane); }
                __builtin_amdgcn_s_sleep(FL_SLEEP);
            }
            postc<2>(F_RDY + which * 4, lane);
            STAMP(17);
        };
        if (w < 6) {
            unsigned ffull = F_FULL + lw * 4;
            asm volatile("" : "+v"(ffull));
            touch(sink, lw ? p.Bim : p.Bre, (32 * j + 2 * i) * 1024 + c * 128);
            bar_l();
            {
                const unsigned voffS = (8 * (q8 >> 2) + (q8 & 3)) * 1024 + swz;
                const v4u srd = make_srd((const unsigned char*)(lw ? p.Bim : p.Bre) + (size_t)(32 * j) * 1024);
                wait_ge(F_XI + (lane & 3) * 4, 1);
                NOUNROLL for (int n = 0; n < 8; n++) {
                    const unsigned so = 128 * ((n + rot) & 7);
                    dma4(srd, voffS, voffS ^ 64u, so, so + 15360, so + 2048, so + 17408, L_W + (n >> 1) * SLOT + lw * 8192 + (n & 1) * 4096);
                    waitv<8>(); postv(ffull, lane, max(n - 1, 0) >> 1);
                }
                asm volatile("" ::"v"(sink));
            }
            STAMP(1);
            const unsigned voffG = q8 * (FF * 2) + (((p8 >> 1) ^ ((q8 >> 1) & 1)) << 5) + ((p8 & 1) << 4), voffG1 = voffG ^ 64u;
            const unsigned voffC = q8 * 1024 + swz, voffC1 = voffC ^ 64u;
            const unsigned voffD = q8 * 1024 + (((p8 >> 1) ^ (q8 >> 1) ^ (2 * (kq & 1))) << 5) + ((p8 & 1) << 4);
            const unsigned char* gb = (const unsigned char*)(lw ? p.wu : p.wg) + (7 * kq + q) * 128;
            unsigned thr = (lane >> 2) + 1;
            int credit = 0, cbase = 0;
            auto item = [&](int n, const unsigned char* b0, const unsigned char* b1, unsigned ve, unsigned vo, unsigned st) __attribute__((always_inline)) {
                v4u s0 = make_srd(b0), s1 = make_srd(b1);
                unsigned dst = ((n & 4) ? L_P : L_W) + (n & 3) * SLOT + lw * 8192, dst1 = dst + 4096;
                asm volatile("" : "+s"(s0), "+s"(s1), "+s"(dst), "+s"(dst1));
                int sp = LSPIN_MAX;
                NOUNROLL while (n - 7 > credit) {
                    const unsigned dn = peek(fa);
                    postv(ffull, lane, n - ((vm_outstanding() + 7) >> 3));
                    unsigned long long mk = __builtin_amdgcn_ballot_w64(dn >= thr);
                    mk &= mk >> 1; mk &= mk >> 2;
                    credit = __builtin_popcountll(mk & 0x1111111111111111ull) + cbase;
                    asm volatile("s_sub_i32 %0, %0, 1" : "+s"(sp)::"scc");
                    if (sp < 0) break;
                }
                waitv<INFL>();
                dma4(s0, ve, vo, 0, st - 1024, 2 * st - 2048, 3 * st - 3072, dst);
                postv(ffull, lane, n - (INFL + 7) / 8);
                waitv<INFL>();
                dma4(s1, ve, vo, 0, st - 1024, 2 * st - 2048, 3 * st - 3072, dst1);
            };
            wait_ge(F_XI + (lane & 3) * 4, 3);
            NOUNROLL for (int n = 4; n < N1; n++) {
                const int t = n - 4;
                const unsigned char* bp = gb + (size_t)(((t + rot) & 7) * 64) * (FF * 2) + (t >> 3) * 512;
                item(n, bp, bp + 32 * FF * 2, voffG, voffG1, 8 * FF * 2);
            }
            cbase = N1 - 8; thr += cbase;
            const unsigned char* dp = (const unsigned char*)p.wd + (size_t)(7 * kq) * 65536 + q * 256 + (lw ^ (kq >> 1)) * 128;
            NOUNROLL for (int v = 0; v < 7; v++) {
                if (v >= 4) {
                    const unsigned char* cbp = (const unsigned char*)((v & 2) ? p.Cim : p.Cre) + (size_t)q * 131072 + (2 * kq + (v & 1)) * 128;
                    item(N1 + v, cbp + ((2 * lw) ^ kq) * 32768, cbp + ((2 * lw + 1) ^ kq) * 32768, voffC, voffC1, 8192);
                } else {
                    item(N1 + v, dp, dp + 32768, voffD, voffD, 8192);
                    dp += 65536;
                }
            }
            waitv<16>(); postv(ffull, lane, N1 + 5);
            waitv<8>(); postv(ffull, lane, N1 + 6);
            waitv<0>(); postv(ffull, lane, N1 + 11);
            tg = peek(L_TAG);
            STAMP(7);
            if (lw) {
                const unsigned pf = WS_FLAG + (unsigned)(i * 16 + 4 * (lane & 3) + q) * 512u + 256 + ((lane >> 2) & 1) * 8;
                NOUNROLL for (int spin = 0; spin < SPIN_MAX; spin++) {
                    fence_c();
                    const v2u f = __builtin_amdgcn_raw_buffer_load_b64(rws, pf, 0, 16);
                    if (__builtin_amdgcn_ballot_w64(f.x != tg || f.y != ~tg) == 0) break;
                    __builtin_amdgcn_s_sleep(16);
                }
            }
            if (lw) poll16(1);
        } else {
            unsigned tag0, prm0, prm1, prm2;
            const unsigned cto = min(idx * 11 + 4 * lw + p8, (int)CODE_LINES - 1) * 128;
            touch(sink, pc0, cto);
            touch(sink, lw ? (const void*)p.ws : (const void*)p.Dsk, lw ? ((lane & 4) ? WS_GRAN + (j * 16 + i) * 512 : fo) + (lane & 3) * 128 : p8 * 128);
            if (!heavy) __builtin_amdgcn_s_sleep(LT_SLEEP);
            NOUNROLL for (int t = heavy ? 4 : 0; t < 4; t++)
                touch(sink, lw ? p.wu : p.wg, (32 * i + (lane & 31)) * (FF * 2) + (7 * kq + min(2 * t + (lane >> 5), 6)) * 128);
            const unsigned po = 2 * (32 * j + 8 * (lane >> 4) + 4 * ((lane >> 1) & 1) + ((lane >> 2) & 3));
            asm volatile("global_load_ushort %0, %4, %5\n\tglobal_load_ushort %1, %4, %6\n\tglobal_load_ushort %2, %4, %7\n\tglobal_load_dword %3, %8, %9 sc1"
                         : "=&v"(prm0), "=&v"(prm1), "=&v"(prm2), "=&v"(tag0) : "v"(po), "s"(p.lre), "s"(p.lim), "s"(p.ldt), "v"(WS_EPOCH), "s"(p.ws) : "memory");
            *(LAS unsigned*)(uintptr_t)(F_DONE + (lane & 31) * 4) = 0u;
            bar_l();
            sts128(L_SEL + lw * 1024 + lane * 16, make_sel(lane, lw));
            asm volatile("s_waitcnt vmcnt(0)" : "+v"(prm0), "+v"(prm1), "+v"(prm2), "+v"(tag0)::"memory");
            tg = tag0 + 1u;
            const bool odd = lane & 1;
            const unsigned la = lane * 4;
            const float lr = bflo(prm0), li = bflo(prm1);
            float dt = __expf(bflo(prm2));
            dt = bflo(pk2(dt, dt));
            const float ex = __expf(dt * lr);
            const float fr = __builtin_amdgcn_fractf(dt * li * 0.15915494309189535f);
            const float ar = smul_t(ex, __builtin_amdgcn_cosf(fr)), ai = smul_t(ex, __builtin_amdgcn_sinf(fr));
            const cpx a1 = {ar, ai};
            const cpx a2 = cmul(a1, a1), a4 = cmul(a2, a2);
            const unsigned sg = odd ? 0u : 0x80000000u;
            auto sgn = [&](cpx v) { return cpx{v.x, __uint_as_float(__float_as_uint(v.y) ^ sg)}; };
            const cpx r8 = cmul(a4, a4), r16 = cmul(r8, r8), r32 = cmul(r16, r16), r64 = cmul(r32, r32);
            const cpx a32 = sgn(r32);
            {
                const cpx r = lw ? r32 : r8, rs = sgn(r);
                float v = odd ? r.y : r.x;
                sts32f(L_PW + lw * 1024 + la, odd ? 0.0f : 1.0f);
#pragma unroll
                for (int s = 1; s < 4; s++) { sts32f(L_PW + lw * 1024 + s * 256 + la, v); if (s < 3) v = lmad(rs, v, 0.0f); }
            }
            if (lw == 0) {
                const float nr = ar - 1.0f, den = smul_t(__builtin_amdgcn_rcpf(sfma(lr, lr, smul(li, li))), 2.0f);
                float co = smul(odd ? sfms(ai, lr, smul(nr, li)) : sfma(nr, lr, smul(ai, li)), den);
                sts32f(L_CST + la, odd ? a1.y : a1.x); sts32f(L_CST + 256 + la, odd ? a4.y : a4.x);
                const cpx a1s = sgn(a1);
#pragma unroll
                for (int t = 0; t < 5; t++) { sts32f(L_CST + (2 + t) * 256 + la, co); if (t < 4) co = lmad(a1s, co, 0.0f); }
                *(LAS unsigned*)(uintptr_t)L_TAG = tg;
            }
            post(F_COMM + lw * 4, 1);
            STAMP(1);
            cpx wn[8];
            {
                const cpx r128 = cmul(r64, r64), r256 = cmul(r128, r128);
                wn[0] = lw ? r128 : cpx{1.0f, 0.0f};
#pragma unroll
                for (int n = 1; n < 8; n++) wn[n] = cmul(wn[n - 1], r256);
#pragma unroll
                for (int n = 0; n < 8; n++) { wn[n] = sgn(wn[n]); asm volatile("" : "+v"(wn[n].x), "+v"(wn[n].y)); }
            }
            unsigned gro = WS_GRAN + j * 8192 + lane * 8, gro2 = gro + i * 512;
            unsigned to0 = lw ? min((unsigned)(lane & 31) * 0x98000u + idx * 0x4000u, WS_END - 4) : (lane & 3) * 0x100000 + idx * 0x4000;
            unsigned to1 = (448 * kq + 28 * i + min(lane, 27)) * 1024 + (2 * q + lw) * 128, to2 = (128 * q + 8 * i + p8) * 1024 + (2 * kq + (q8 & 1)) * 128;
            unsigned po2 = WS_XB + (unsigned)((i * 4 + q) * 4 + kq) * 24576u + lw * 12288 + lane * 16;
            unsigned voffC = q8 * 1024 + swz, voffC1 = voffC ^ 64u;
            unsigned voffD = q8 * 1024 + (((p8 >> 1) ^ (q8 >> 1) ^ (2 * (kq & 1))) << 5) + ((p8 & 1) << 4);
            v4u none = {~0u, ~0u, ~0u, ~0u};
            asm volatile("" : "+v"(gro), "+v"(gro2), "+v"(to0), "+v"(to1), "+v"(to2), "+v"(po2), "+v"(voffC), "+v"(voffC1), "+v"(voffD), "+v"(none));
            wait_ge(F_XI + (lane & 3) * 4, heavy ? 2 : 0);
            touch(sink, p.wd, to1);
            touch(sink, lw ? p.Cim : p.Cre, to2);
            touch(sink, lw ? (const void*)p.ws : (const void*)p.out, to0);
            v4u wr[4][8];
            {
                const v4u sc = make_srd((const unsigned char*)p.Cim + (size_t)q * 131072 + (2 * kq + 1) * 128);
                const v4u sd = make_srd((const unsigned char*)p.wd + (size_t)(7 * kq + 4) * 65536 + q * 256 + (lw ^ (kq >> 1)) * 128);
                waitv<0>();
                sfor<0, 32>(LAMB(ff) {
                    constexpr int f = decltype(ff)::value, m = f & 7;
                    if constexpr (f < 8) ldW(wr[0][m], (m & 1) ? voffC1 : voffC, sc, ((2 * lw + (m >> 2)) ^ kq) * 32768 + (m & 3) * 8192);
                    else ldW(wr[f >> 3][m], voffD, sd, (f - 8) * 8192);
                    if constexpr (f % 3 == 2) __builtin_amdgcn_s_sleep(WR_SLEEP);
                });
            }
            auto clear = [&](int n0, int n1) __attribute__((always_inline)) {
                const v4u sws = make_srd(p.ws);
                NOUNROLL for (int n = n0; n < n1; n++) { stX<0>(none, po2, sws, n * 1024); __builtin_amdgcn_s_sleep(PF_SLEEP); }
            };
            STAMP(3);
            wait_ge(fa, 4);
            STAMP(4);
            if (lw) {
                float e[4];
#pragma unroll
                for (int s = 0; s < 4; s++) e[s] = lds32f(L_TOT + s * 256 + la);
                const float p2 = lmad(a32, e[0], e[1]), p3 = lmad(a32, p2, e[2]);
                __builtin_amdgcn_raw_buffer_store_b64(v2u{__float_as_uint(lmad(a32, p3, e[3])), tg}, rws, gro2, 0, 16);
                sts32f(L_PRE + la, 0.0f); sts32f(L_PRE + 256 + la, e[0]); sts32f(L_PRE + 512 + la, p2); sts32f(L_PRE + 768 + la, p3);
            }
            STAMP(5);
            clear(0, PF_N);
            __builtin_amdgcn_s_sleep(LB_FIRST);
            {
                const int top = i - 1 - lw;
                v2u gr[8] = {};
                NOUNROLL for (int spin = 0; spin < SPIN_MAX; spin++) {
                    fence_c();
#pragma unroll
                    for (int n = 0; n < 8; n++) gr[n] = __builtin_amdgcn_raw_buffer_load_b64(rws, gro, ((top - 2 * n) & 15) * 512, 16);
                    unsigned long long bad = 0;
#pragma unroll
                    for (int n = 0; n < 8; n++) if (top - 2 * n >= 0) bad |= __builtin_amdgcn_ballot_w64(gr[n].y != tg);
                    if (bad == 0) break;
                    __builtin_amdgcn_s_sleep(LB_SLEEP);
                }
                float h = 0;
#pragma unroll
                for (int n = 7; n >= 0; n--) if (top - 2 * n >= 0) h = lmad(wn[n], __uint_as_float(gr[n].x), h);
                sts32f(L_XCH + 256 + lw * 256 + la, h);
            }
            postc<4>(F_COMM + lw * 4, lane);
            STAMP(6);
            clear(PF_N, 12);
            waitv<0>();
            asm volatile("" ::"v"(sink));
            if (lane == 0) __builtin_amdgcn_raw_buffer_store_b64(v2u{tg, ~tg}, rws, fo + 256 + lw * 8, 0, 16);
            STAMP(7);
#pragma unroll
            for (int u = 0; u < 4; u++) asm volatile("" : "+v"(wr[u][0]), "+v"(wr[u][1]), "+v"(wr[u][2]), "+v"(wr[u][3]), "+v"(wr[u][4]), "+v"(wr[u][5]), "+v"(wr[u][6]), "+v"(wr[u][7]));
            STAMP(13);
            if (!lw) poll16(0);
            {
                unsigned wa0 = lw * 8192 + lane * 16 + L_P, wa1 = wa0 + (L_W + 3 * SLOT - L_P);
                asm volatile("" : "+v"(wa0), "+v"(wa1));
                sfor<0, 4>(LAMB(uu) {
                    constexpr int u = decltype(uu)::value;
                    wait_ge(fa, N1 + u);
#pragma unroll
                    for (int m = 0; m < 8; m++) sts128((u ? wa0 + (u - 1) * SLOT : wa1) + m * 1024, wr[u][m]);
                    postv(F_COMM + lw * 4, lane, N1 + 8 + u);
                });
            }
            STAMP(15);
        }
        f32x4 base[4];
        {
            const int d = 128 * q + 32 * kq + 4 * p8;
            const v2u dv = *(const GAS v2u*)(p.Dsk + d);
            const float df[4] = {bflo(dv.x), bfhi(dv.x), bflo(dv.y), bfhi(dv.y)};
            v2u xv[4];
#pragma unroll
            for (int e = 0; e < 4; e++) xv[e] = *(const GAS v2u*)(p.x + (size_t)(128 * i + 32 * pr + 8 * e + q8) * DM + d);
#pragma unroll
            for (int e = 0; e < 4; e++) {
                const unsigned tp = (32 * pr + 8 * e + 4 * ((q8 >> 1) & 1) + 2 * (q8 & 1) + (q8 >> 2)) * 4;
                const float rs = lds32f(L_RSTD + tp), nm = smul(lds32f(L_MEAN + tp), rs);
                const float xf[4] = {bflo(xv[e].x), bfhi(xv[e].x), bflo(xv[e].y), bfhi(xv[e].y)};
#pragma unroll
                for (int k = 0; k < 4; k++) base[e][k] = sfma(sfma(xf[k], rs, nm), df[k], xf[k]);
            }
        }
        STAMP(8);
        const unsigned xi = xo + kq * 24576u + lane * 16;
        const unsigned sc = L_O + pr * 4096;
        unsigned wa = sc + (8 * (c >> 2) + 4 * (c & 1) + ((c >> 1) & 1)) * 128 + ((g ^ (c & 7)) << 4), ra = sc + q8 * 128 + ((p8 ^ (2 * (q8 & 1) + (q8 >> 2))) << 4);
        GAS float* op = (GAS float*)p.out + (size_t)(128 * i + 32 * pr + q8) * DM + 128 * q + 32 * kq + 4 * p8;
        asm volatile("" : "+v"(wa), "+v"(ra), "+v"(op));
        wait_ge(F_SENT + pr * 4, 3);
        __builtin_amdgcn_s_sleep(X_FIRST);
        v4u in[6];
        f32x4 own[4];
        NOUNROLL for (int spin = 0; spin < SPIN_MAX; spin++) {
            fence_c();
#pragma unroll
            for (int n = 0; n < 6; n++) in[n] = __builtin_amdgcn_raw_buffer_load_b128(rws, xi + n * 1024, 0, 16);
            SB();
            wait_ge(F_SENT + pr * 4, 4);
#pragma unroll
            for (int e = 0; e < 4; e++) own[e] = lds128f(sc + e * 1024 + lane * 16);
            unsigned mx = 0;
#pragma unroll
            for (int n = 0; n < 6; n++) mx = max(mx, max(max(in[n].x, in[n].y), max(in[n].z, in[n].w)));
            if (__builtin_amdgcn_ballot_w64(mx == ~0u) == 0) break;
        }
        STAMP(11);
#pragma unroll
        for (int e = 0; e < 4; e++)
#pragma unroll
            for (int sr = 0; sr < 3; sr++) { const unsigned d0 = in[2 * sr + (e >> 1)][2 * (e & 1)], d1 = in[2 * sr + (e >> 1)][2 * (e & 1) + 1]; own[e] += f32x4{bflo(d0), bfhi(d0), bflo(d1), bfhi(d1)}; }
        asm volatile("" : "+v"(own[0]), "+v"(own[1]), "+v"(own[2]), "+v"(own[3]));
#pragma unroll
        for (int e = 0; e < 4; e++) sts128f((wa ^ (unsigned)((e >> 1) << 6)) + (e & 1) * 256, own[e]);
        fence_c();
        f32x4 ta[4];
#pragma unroll
        for (int e = 0; e < 4; e++) ta[e] = lds128f((ra ^ (unsigned)((e & 1) << 6)) + e * 1024);
        SB();
#pragma unroll
        for (int e = 0; e < 4; e++) *(GAS f32x4*)(op + e * 8 * DM) = base[e] + ta[e];
        if (b == 255 && w == 4 && lane == 0) __builtin_amdgcn_raw_buffer_store_b32(tg, rws, WS_EPOCH, 0, 16);
        STAMP(12); STAMP_DUMP();
        return;
    }
    constexpr int PD = PDV, XL = XLV, EG = EGATE;
    unsigned stall = 0; (void)stall;
    v4u xa[16][2];
#define AF(u, k) xa[2 * (u) + ((k) >> 1)][(k) & 1]
    v4u sws = make_srd(p.ws);
    unsigned oG = WS_A2 + (unsigned)(i * 44 + 7 * kq) * 16384u + w * 4096, oC = WS_A2 + (unsigned)(i * 44 + 28 + 2 * kq) * 16384u + w * 4096, oF = fo + w * 8;
    asm volatile("" : "+s"(sws), "+s"(oG), "+s"(oC), "+s"(oF));
    auto uoff = [&](int v) __attribute__((always_inline)) { return v < 4 ? oG + v * 16384 : v < 8 ? oC + (8 * ((v >> 1) & 1) + (v & 1)) * 16384 : oG + (v - 4) * 16384; };
    LANE_VARS;
    unsigned fdone = F_DONE + w * 4, l16 = lane * 16;
    asm volatile("" : "+v"(fdone), "+v"(l16));
    unsigned tgs = 0; int raised = 0;
    auto raise = [&](int level) __attribute__((always_inline)) {
        asm volatile("s_mov_b64 exec, 1\n\tbuffer_store_dwordx2 %0, off, %1, %2 offset:%3 sc1\n\ts_mov_b64 exec, -1" ::"v"(v2u{tgs, ~tgs}), "s"(sws), "s"(oF), "n"((level - 1) * 128) : "memory");
        post(fdone + (F_OUT - F_DONE), level);
        raised = level;

        if (level == 1) STAMP(17); else STAMP(18);
    };
    auto try_raise = [&](int level) __attribute__((always_inline)) {
        unsigned v; asm volatile("s_getreg_b32 %0, hwreg(HW_REG_IB_STS, 0, 4)" : "=s"(v)::"memory");
        if (__builtin_expect((int)(v + raised) == level - 1, 0)) raise(level);
    };
    unsigned oX[3];
#pragma unroll
    for (int sl = 1; sl < 4; sl++) { oX[sl - 1] = xo + (kq ^ sl) * 24576u + (sl - 1) * 2048; asm volatile("" : "+s"(oX[sl - 1])); }
    unsigned cbs[2], tbs[4];
    v4u bf[PD];
    const unsigned ffl = F_FULL + (lane & 1) * 4;
    auto set2 = [&](auto kk) __attribute__((always_inline)) {
        constexpr int k = decltype(kk)::value;
        if constexpr (k == 0) tbs[0] = L_P + ((l16 << 1) & 0x780) + ((l16 >> 1) & 0x18) + (((l16 >> 2) & 0x40) | ((l16 >> 2) & 0x20));
        else if constexpr (k < 4) tbs[k] = tbs[0] ^ (unsigned)(k << 5);
        else if constexpr (k == 4) cbs[0] = L_W + ((l16 << 3) & 0x780) + (((l16 >> 4) ^ (l16 >> 1)) & 0x70);
        else cbs[1] = cbs[0] ^ 64u;
        if constexpr (k < 4) asm volatile("" : "+v"(tbs[k])); else asm volatile("" : "+v"(cbs[k - 4]));
    };
    auto prime2 = [&](auto mm) __attribute__((always_inline)) { constexpr int m = decltype(mm)::value; const v2u lo = ldstr(tbs[m]), hi = ldstr(tbs[m] + 2048); bf[m] = v4u{lo.x, lo.y, hi.x, hi.y}; };
    {
        STAMP_INIT();
        STAMP(0);
        unsigned cb[2];
        cb[0] = (unsigned)c * 128u + (unsigned)((g ^ (c >> 1)) << 4); cb[1] = cb[0] ^ 64u;
        {
            const unsigned voffX = (8 * (q8 >> 2) + 4 * (q8 & 1) + ((q8 >> 1) & 1)) * 1024 + ((p8 ^ (q8 >> 1)) << 4), voffX1 = voffX ^ 64u;
            const v4u srd = make_srd((const unsigned char*)p.x + (size_t)(128 * i + 32 * w) * 1024 - 4096);
            auto issue_x = [&](int t) __attribute__((always_inline)) {
                const unsigned s0 = (t >> 3) * 2048 + ((t + rot) & 7) * 128, s1 = (t >> 3) * 2048 + ((t + 1 + rot) & 7) * 128 + 2048;
                dma4(srd, voffX, voffX1, s0 + 4096, s0 + 4096 + 15360, s1, s1 + 15360, sl_lo + (t & 7) * 2048);
            };
            issue_x(0); issue_x(2); issue_x(4); issue_x(6);
            bar_l();
            const v4u sel0 = make_sel(lane, 0), sel1 = make_sel(lane, 1);
            v4u ones = {0x3f803f80u, 0x3f803f80u, 0x3f803f80u, 0x3f803f80u};
            asm volatile("" : "+v"(ones));
            f32x4 sm[2], sq[2], nm4[2], ct[6];
            const unsigned xl0 = cb[0] + sl_lo, xl1 = cb[1] + sl_lo;
            unsigned sa = L_RSTD + (32 * w + 8 * g + (c & 3)) * 4;
            const unsigned long long dgm = __builtin_amdgcn_ballot_w64(g == (c >> 2));
            asm volatile("" : "+v"(sa));
            auto stats = [&](int bb) __attribute__((always_inline)) {
                float s01, s23, sd;
                asm("v_cndmask_b32 %0, %1, %2, %3" : "=v"(s01) : "v"(sq[bb].x), "v"(sq[bb].y), "s"(0xaaaaaaaaaaaaaaaaull));
                asm("v_cndmask_b32 %0, %1, %2, %3" : "=v"(s23) : "v"(sq[bb].z), "v"(sq[bb].w), "s"(0xaaaaaaaaaaaaaaaaull));
                asm("v_cndmask_b32 %0, %1, %2, %3" : "=v"(sd) : "v"(s01), "v"(s23), "s"(0xccccccccccccccccull));
                const float rstd = __builtin_amdgcn_rsqf(fmaxf(sd * (1.0f / DM) - nm4[bb].x * nm4[bb].x, 0.0f) + 1e-5f);
                asm volatile("s_mov_b64 exec, %3\n\tds_write2_b32 %0, %1, %2 offset0:%4 offset1:%5\n\ts_mov_b64 exec, -1" ::"v"(sa), "v"(rstd), "v"(nm4[bb].x), "s"(dgm), "n"(4 * bb), "n"(L_MEAN / 4 + 4 * bb) : "memory");
            };
            auto centre = [&](auto kk) __attribute__((always_inline)) {
                constexpr int k = decltype(kk)::value, e = k - 4;
                if constexpr (k < 64) mma_c(ct[k % 6], (k & 1) ? sel1 : sel0, xa[(k >> 1) & 15][k >> 5], nm4[k >> 5]);
                if constexpr (e >= 0) {
                    xa[(e >> 1) & 15][e >> 5][2 * (e & 1)] = cvtv(ct[e % 6].x, ct[e % 6].y);
                    xa[(e >> 1) & 15][e >> 5][2 * (e & 1) + 1] = cvtv(ct[e % 6].z, ct[e % 6].w);
                }
                SB();
            };
            SB();
            sfor<0, 17>(LAMB(tt) {
                constexpr int t = decltype(tt)::value, u = t - 1;
                if constexpr (t < 16) {
                    constexpr int t5 = ((t + 1) & ~1) + 5, top = t5 < 7 ? 7 : t5 > 15 ? 15 : t5;
                    waitv<2 * (top - t)>();
                    xa[2 * (t & 7)][t >> 3] = lds128(xl0 + (t & 7) * 2048); xa[2 * (t & 7) + 1][t >> 3] = lds128(xl1 + (t & 7) * 2048);
                }
                SB();
                if constexpr (t == 9) { asm volatile("s_nop 3"); SB(); nm4[0] = sm[0] * (-1.0f / DM); asm volatile("s_nop 1" : "+v"(nm4[0])); SB(); }
                if constexpr (t >= 9) { centre(IC<4 * (t - 9)>{}); centre(IC<4 * (t - 9) + 1>{}); centre(IC<4 * (t - 9) + 2>{}); centre(IC<4 * (t - 9) + 3>{}); }
                if constexpr (u >= 0) {
                    mma<(u & 7) == 0>(sm[u >> 3], ones, xa[2 * (u & 7)][u >> 3]); mma<(u & 7) == 0>(sq[u >> 3], xa[2 * (u & 7)][u >> 3], xa[2 * (u & 7)][u >> 3]);
                    mma<false>(sm[u >> 3], ones, xa[2 * (u & 7) + 1][u >> 3]); mma<false>(sq[u >> 3], xa[2 * (u & 7) + 1][u >> 3], xa[2 * (u & 7) + 1][u >> 3]);
                }
                if constexpr ((t & 1) == 0 && t >= 2 && t + 6 < 16) issue_x(t + 6);
                if constexpr (t == XI_STEP) post(fdone + (F_XI - F_DONE), 1);
                if constexpr (t == XI2_STEP) post(fdone + (F_XI - F_DONE), 2);
                SB();
            });
            post(fdone + (F_XI - F_DONE), 3);
            STAMP(1);
            stats(0);
            SB();
            nm4[1] = sm[1] * (-1.0f / DM); asm volatile("s_nop 1" : "+v"(nm4[1]));
            SB();
            sfor<32, 64>(LAMB(kk) { centre(kk); });
            stats(1);
            SB();
            sfor<64, 68>(LAMB(kk) { centre(kk); });
        }
        STAMP(2);
        f32x4 eX;
        unsigned tb[4];
        {
            cb[0] += L_W; cb[1] += L_W;
            asm volatile("" : "+v"(cb[0]), "+v"(cb[1]));
            f32x4 acc[4][2];
            unsigned fl = 0; bool lack = false;
            {
                const int q4 = c >> 2, p4 = c & 3;
                const unsigned tb1 = L_P + (unsigned)(8 * g + q4) * 128u + (unsigned)((((q4 >> 1) & 1) | ((g & 1) << 1)) << 5) + p4 * 8;
#pragma unroll
                for (int t = 0; t < 4; t++) { tb[t] = tb1 ^ (unsigned)(t << 5); asm volatile("" : "+v"(tb[t])); }
            }
            f32x4 ca, c4, co, c8, rs0, rs1;
            wait_ge(F_FULL + (lane & 3) * 4, 1);
            STAMP(3);
            sfor<-PD, 64>(LAMB(nn) {
                constexpr int n = decltype(nn)::value, m = n + PD;
                v4u cur;
                if constexpr (n >= 0) { cur = bf[n % PD]; mma<(n < 4)>(acc[n & 3][0], xa[n >> 2][0], cur); SB(); }
                if constexpr (m > 0 && (m & 15) == 0) need_full(lack, ffl, (m >> 4) + 1);
                if constexpr (m < 64) bf[m % PD] = lds128(cb[(m >> 2) & 1] + wslot(m >> 4) + (((m >> 1) & 1) * 4 + ((m >> 3) & 1) * 2 + (m & 1)) * 2048);
                else { const unsigned a = tb[(m >> 1) & 3] + wslot(0) + (m & 1) * 8192; const v2u lo = ldstr(a), hi = ldstr(a + 512); bf[m % PD] = v4u{lo.x, lo.y, hi.x, hi.y}; }
                SB();
                if constexpr (n >= 0) { mma<(n < 4)>(acc[n & 3][1], xa[n >> 2][1], cur); SB(); }
                if constexpr ((m & 15) == 10) fl = peek(ffl);
                if constexpr ((m & 15) == 14) lack = any_lt(fl, (m >> 4) + 2);
                if constexpr ((m & 15) == 15 && m < 63) post(fdone, (m >> 4) + 1);
                if constexpr (m == 52) ca = lds128f(L_CST + c * 16);
                if constexpr (m == 53) c4 = lds128f(L_CST + 256 + c * 16);
                if constexpr (m == 54) co = lds128f(L_CST + 512 + c * 16);
                if constexpr (m == 57) c8 = lds128f(L_PW + 256 + c * 16);
                if constexpr (m == 55) rs0 = lds128f(L_RSTD + (32 * w + 8 * g) * 4);
                if constexpr (m == 56) rs1 = lds128f(L_RSTD + (32 * w + 8 * g + 4) * 4);
                SB();
            });
            mfma_done();
            STAMP(4); VAL(24, stall); stall = 0;
            f32x4 ev;
            f32x2 sr[2][4], si[2][4];
#pragma unroll
            for (int st = 0; st < 2; st++) {
                const f32x4 r0 = acc[st][0] * rs0, r1 = acc[st][1] * rs1, i0 = acc[2 + st][0] * rs0, i1 = acc[2 + st][1] * rs1;
                sr[st][0] = lo2(r0); sr[st][1] = hi2(r0); sr[st][2] = lo2(r1); sr[st][3] = hi2(r1);
                si[st][0] = lo2(i0); si[st][1] = hi2(i0); si[st][2] = lo2(i1); si[st][3] = hi2(i1);
            }
            const f32x2 a1[2] = {lo2(ca), hi2(ca)}, cf[2] = {lo2(co), hi2(co)};
#pragma unroll
            for (int t = 1; t < 4; t++) {
                f32x2 tr[2], ti[2];
#pragma unroll
                for (int st = 0; st < 2; st++) { tr[st] = bfma<1, 1>(a1[st], si[st][t - 1], sr[st][t]); ti[st] = bfma<1, 0>(a1[st], sr[st][t - 1], si[st][t]); }
                SB();
#pragma unroll
                for (int st = 0; st < 2; st++) { sr[st][t] = bfma<0, 0>(a1[st], sr[st][t - 1], tr[st]); si[st][t] = bfma<0, 0>(a1[st], si[st][t - 1], ti[st]); }
                SB();
            }
#pragma unroll
            for (int st = 0; st < 2; st++) {
                ev[2 * st] = sfma(c4[2 * st], sr[st][3].x, sfnma(c4[2 * st + 1], si[st][3].x, sr[st][3].y));
                ev[2 * st + 1] = sfma(c4[2 * st], si[st][3].x, sfma(c4[2 * st + 1], sr[st][3].x, si[st][3].y));
                eX[2 * st] = sr[st][3].x; eX[2 * st + 1] = si[st][3].x;
            }
            sts128f(L_SEG + w * 1024 + l16, ev);
            fence_c();
            f32x4 es[4];
#pragma unroll
            for (int s = 0; s < 4; s++) es[s] = lds128f(L_SEG + w * 1024 + s * 256 + (l16 & 0xf0));
            SB();
            unsigned ya = L_O + w * 4096 + l16;
            asm volatile("" : "+v"(ya), "+v"(eX));
#pragma unroll
            for (int st = 0; st < 2; st++) {
                f32x2 tr[4], ti[4];
#pragma unroll
                for (int t = 0; t < 4; t++) { tr[t] = bmul<1, 1>(cf[st], si[st][t]); ti[t] = bmul<1, 1>(cf[st], sr[st][t]); }
                SB();
#pragma unroll
                for (int t = 0; t < 4; t++) { tr[t] = bfma<0, 0>(cf[st], sr[st][t], tr[t]); ti[t] = bfma<0, 1>(cf[st], si[st][t], ti[t]); }
                SB();
                if (st == 0) {
#pragma unroll
                    for (int s = 1; s < 4; s++) { const f32x2 u0 = cmadp(lo2(c8), lo2(es[s - 1]), lo2(es[s])), u1 = cmadp(hi2(c8), hi2(es[s - 1]), hi2(es[s])); es[s] = f32x4{u0.x, u0.y, u1.x, u1.y}; }
                    sts128f(L_TOT + w * 256 + (l16 & 0xf0), es[3]);
                    post(fdone, 4);
                    f32x4 pin;
#pragma unroll
                    for (int k = 0; k < 4; k++) { const float lo = g == 1 ? es[0][k] : 0.0f, hi = g == 3 ? es[2][k] : es[1][k]; pin[k] = g >= 2 ? hi : lo; }
                    sts128f(L_SEG + w * 1024 + l16, pin);
                    SB();
                }
                sts128(ya + st * 1024, v4u{pk2(tr[0]), pk2(tr[1]), pk2(tr[2]), pk2(tr[3])});
                sts128(ya + 2048 + st * 1024, v4u{pk2(ti[0]), pk2(ti[1]), pk2(ti[2]), pk2(ti[3])});
                SB();
            }
        }
        STAMP(5);
        unsigned go = oG + q * 16384, so = oC + (q >> 1) * 16384 + (q & 1) * 2048;
        asm volatile("" : "+s"(go), "+s"(so));
        f32x4 acc[4][2][2];
        f32x2 r12;
        unsigned fc = 0;
        auto gpass = [&](auto pp, auto n0, auto n1) __attribute__((always_inline)) {
            constexpr int pass = decltype(pp)::value;
            unsigned fl = 0, dn = 0; bool lack = false;
            sfor<decltype(n0)::value, decltype(n1)::value>(LAMB(nn) {
                constexpr int n = decltype(nn)::value, m = n + PD;
                constexpr bool pre = pass == 0 || m < 128;
                constexpr unsigned off = wslot(m >> 4) + (m & 1) * 8192 + ((m >> 3) & 1) * 4096;
                const v4u cur = bf[(n + 64 + 128 * pass) % PD];
                mma<(n < 8)>(acc[(n >> 1) & 3][n & 1][0], cur, xa[n >> 3][0]);
                SB();
                if constexpr ((m & 15) == 0) post(fdone, dn);
                if constexpr ((m & 15) == 0) need_full(lack, ffl, 5 + 8 * pass + (m >> 4));
                v2u lo, hi;
                if constexpr (pre) lo = ldstr(tb[(m >> 1) & 3] + off); else lo = ldstr(tbs[m & 3]);
                if constexpr (pass == 1 && n >= 69 && n < 81 && (n & 1)) set2(IC<(n - 69) / 2>{});
                if constexpr ((m & 15) == 10) fl = peek(ffl);
                if constexpr ((m & 15) == 15) lack = any_lt(fl, m == 127 && pass == 0 ? N1 - 7 : 6 + 8 * pass + (m >> 4));
                if constexpr ((m & 15) == 13) { dn = 5 + 8 * pass + (m >> 4); asm volatile("" : "+v"(dn)); }
                if constexpr (n == EG - 8 && pass == 1) fc = peek(F_RDY);
                if constexpr (n == EG - 2 && pass == 1) { if (__builtin_expect(any_lt(fc, 1), 0)) { wait_ge(F_RDY, 1); } fence_c(); }
                if constexpr (n >= EG && n < EG + 24 && pass == 1 && (n - EG) % 3 == 0) { constexpr int f = (n - EG) / 3; reldA<(f & 3) * 1024>(AF(f >> 2, f & 3), l16, sws, uoff(f >> 2)); }
                if constexpr (pass == 1 && (n == 33 || n == 41 || n == 49 || n == 57)) try_raise(1);
                if constexpr (pass == 1 && n == 67) { if (raised < 1) { waitv<0>(); raise(1); } }
                if constexpr (n == 120 && pass == 0) fc = peek(ffl + (F_COMM - F_FULL));
                if constexpr (n == 121 && pass == 0) { const unsigned ra = L_RSTD + w * 128 + (((l16 >> 1) & 0x60) | ((l16 >> 2) & 0xc)); r12 = f32x2{lds32f(ra), lds32f(ra + 16)}; }
                SB();
                mma<(n < 8)>(acc[(n >> 1) & 3][n & 1][1], cur, xa[n >> 3][1]);
                SB();
                if constexpr (pre) hi = ldstr(tb[(m >> 1) & 3] + off + 512); else hi = ldstr(tbs[m & 3] + 2048);
                bf[n % PD] = v4u{lo.x, lo.y, hi.x, hi.y};
                if constexpr ((m & 57) == 57 && (pass == 0 || m < 64)) {
                    tb[(m >> 1) & 3] += (m & 64) ? L_P - L_W : L_W - L_P; asm volatile("" : "+v"(tb[(m >> 1) & 3]));
                }
                SB();
            });
        };
        auto gated = [&](auto ff) __attribute__((always_inline)) {
            constexpr int uu = decltype(ff)::value >> 1, bb = decltype(ff)::value & 1;
            const f32x4 h0 = acc[2 * uu][0][bb] * acc[2 * uu][1][bb], h1 = acc[2 * uu + 1][0][bb] * acc[2 * uu + 1][1][bb];
            return v4u{pk2(bmul<bb, 0>(r12, lo2(h0))), pk2(bmul<bb, 0>(r12, hi2(h0))), pk2(bmul<bb, 0>(r12, lo2(h1))), pk2(bmul<bb, 0>(r12, hi2(h1)))};
        };
        gpass(IC<0>{}, IC<0>{}, IC<128>{});
        {
            r12 = r12 * r12;
            mfma_done();
            STAMP(6); VAL(25, stall); stall = 0;
            sfor<0, 2>(LAMB(ff) { stA<decltype(ff)::value * 1024>(gated(ff), l16, sws, go); });
            SB();
            need_full(any_lt(fc, 4), ffl + (F_COMM - F_FULL), 4);
            const unsigned tg = peek(L_TAG);
            const unsigned cl = L_CST + (l16 & 0xf0);
            unsigned ya = L_O + w * 4096 + l16;
            asm volatile("" : "+v"(ya));
            const f32x4 h0 = lds128f(L_XCH + 256 + (l16 & 0xf0)), h1 = lds128f(L_XCH + 512 + (l16 & 0xf0));
            const f32x4 aw = lds128f(L_PW + 1024 + w * 256 + (l16 & 0xf0)), pb = lds128f(L_PRE + w * 256 + (l16 & 0xf0));
            const f32x4 ag = lds128f(L_PW + l16), pin = lds128f(L_SEG + w * 1024 + l16);
            const v4u sel[2] = {lds128(L_SEL + l16), lds128(L_SEL + 1024 + l16)};
            const v4u yr[2] = {lds128(ya), lds128(ya + 1024)}, yi[2] = {lds128(ya + 2048), lds128(ya + 3072)};
            const f32x4 c4 = lds128f(cl + 256);
            f32x4 cw[4];
            SB();
            sfor<2, 4>(LAMB(ff) { stA<decltype(ff)::value * 1024>(gated(ff), l16, sws, go); });
            tgs = __builtin_amdgcn_readfirstlane(tg);
#pragma unroll
            for (int t = 0; t < 4; t++) cw[t] = lds128f(cl + (3 + t) * 256);
            SB();
            f32x4 zr[2][2], zi[2][2];
#pragma unroll
            for (int st = 0; st < 2; st++)
#pragma unroll
                for (int bb = 0; bb < 2; bb++) { mma<true>(zr[st][bb], yr[st], sel[bb]); mma<true>(zi[st][bb], yi[st], sel[bb]); }
            SB();
            f32x2 hr[2], hi[2];
            {
                const f32x4 cy = h0 + h1;
                const f32x2 k0 = cmadp(lo2(ag), cmadp(lo2(aw), lo2(cy), lo2(pb)), lo2(pin)), k1 = cmadp(hi2(ag), cmadp(hi2(aw), hi2(cy), hi2(pb)), hi2(pin));
                const f32x4 hin = {k0.x, k0.y, k1.x, k1.y};
                float t0[2], t1[2];
#pragma unroll
                for (int st = 0; st < 2; st++) { t0[st] = sfnma(c4[2 * st + 1], hin[2 * st + 1], eX[2 * st]); t1[st] = sfma(c4[2 * st + 1], hin[2 * st], eX[2 * st + 1]); }
                SB();
#pragma unroll
                for (int st = 0; st < 2; st++) { hr[st] = f32x2{hin[2 * st], sfma(c4[2 * st], hin[2 * st], t0[st])}; hi[st] = f32x2{hin[2 * st + 1], sfma(c4[2 * st], hin[2 * st + 1], t1[st])}; }
                SB();
            }
#pragma unroll
            for (int st = 0; st < 2; st++) {
                f32x2 tr[4], ti[4];
#pragma unroll
                for (int t = 0; t < 4; t++) { const f32x2 wv = st ? hi2(cw[t]) : lo2(cw[t]); tr[t] = bmul<1, 1>(wv, hi[st]); ti[t] = bmul<1, 1>(wv, hr[st]); }
                SB();
#pragma unroll
                for (int t = 0; t < 4; t++) { const f32x2 wv = st ? hi2(cw[t]) : lo2(cw[t]); tr[t] = bfma<0, 0>(wv, hr[st], tr[t]); ti[t] = bfma<0, 1>(wv, hi[st], ti[t]); }
                SB();
                v4u fr = {pk2(tr[0]), pk2(tr[1]), pk2(tr[2]), pk2(tr[3])}, fi = {pk2(ti[0]), pk2(ti[1]), pk2(ti[2]), pk2(ti[3])};
                asm volatile("s_nop 1" : "+v"(fr), "+v"(fi));
#pragma unroll
                for (int bb = 0; bb < 2; bb++) { mma<false>(zr[st][bb], fr, sel[bb]); mma<false>(zi[st][bb], fi, sel[bb]); }
                SB();
            }
            mfma_done();
            stA<0>(pk8(zr[0][0], zr[1][0]), l16, sws, so); stA<0>(pk8(zi[0][0], zi[1][0]), l16, sws, so + 8 * 16384);
            stA<1024>(pk8(zr[0][1], zr[1][1]), l16, sws, so); stA<1024>(pk8(zi[0][1], zi[1][1]), l16, sws, so + 8 * 16384);
            SB();
            STAMP(7);
        }
        if (heavy) {
            gpass(IC<1>{}, IC<0>{}, IC<128>{});
            sfor<0, 4>(LAMB(ff) { constexpr int f = decltype(ff)::value; reldA<f * 1024>(AF(2, f), l16, sws, uoff(2)); });
            mfma_done();
            STAMP(8); VAL(26, stall); stall = 0;
            sfor<0, 4>(LAMB(ff) { stA<decltype(ff)::value * 1024>(gated(ff), l16, sws, go + 4 * 16384); });
            SB();
        }
        int q2 = q;
        asm volatile("" : "+s"(q2));
        if (q2 == 3) {
            waitv<0>(); raise(1); raise(2);
            sfor<0, 6>(set2);
            wait_ge(ffl, N1 + 1);
            sfor<0, PD>(prime2);
            wait_ge(F_RDY, 1);
            sfor<0, 12>(LAMB(ff) { constexpr int f = decltype(ff)::value; reldA<(f & 3) * 1024>(AF(f >> 2, f & 3), l16, sws, uoff(f >> 2)); });
#pragma unroll
            for (int u = 0; u < 3; u++) asm volatile("s_waitcnt vmcnt(0)" : "+v"(AF(u, 0)), "+v"(AF(u, 1)), "+v"(AF(u, 2)), "+v"(AF(u, 3))::"memory");
        }
    }
    STAMP(9);
    {
        f32x4 acc[8][2];
        v4u pk[2];
        unsigned fl = 0, fr = 0, dn = 0; bool lack = false;
        constexpr auto fv = [](int n) { return n < 48 ? n >> 4 : n < 108 ? 3 + (n - 48) / 12 : n < 144 ? 8 + ((n - 108) % 12) / 4 : 3 + (n - 144) / 4; };
        constexpr auto fk = [](int n) { return n < 48 ? (n >> 3) & 1 : n < 108 ? ((n - 48) % 12) / 6 : (n >> 1) & 1; };
        constexpr auto fd = [](int n) { return n < 48 ? n & 7 : n < 108 ? 2 + (n - 48) % 6 : n < 144 ? 2 * ((n - 108) / 12 + 1) + (n & 1) : n & 1; };
        constexpr auto first = [](int n) { return n >= 0 && (n < 48 ? (n & 15) == 0 : n < 108 ? (n - 48) % 12 == 0 : n < 120 && (n & 3) == 0); };
        constexpr int LG = LGATE;
        constexpr auto nmid = [](int n) { return n <= MG ? 0 : (n - MG + 2) / 3 < 20 ? (n - MG + 2) / 3 : 20; };
        static_assert(MG + 57 < LG && LG + 22 < 108, "load schedule");
        sfor<0, 176>(LAMB(nn) {
            constexpr int n = decltype(nn)::value, m = n + PD;
            constexpr int v = fv(n), ks = fk(n), dt = fd(n);
            constexpr int mv = fv(m), mk = fk(m), md = fd(m);
            constexpr bool mc = (mv & ~3) == 4;
            constexpr unsigned moff = (mv & 3) * SLOT + (mc ? md * 2048 : (md >> 2) * 8192 + mk * 4096);
            constexpr auto chk = [](int q) { return q == 16 ? 2 : q == 32 ? 4 : q == 60 ? 7 : q == 96 ? 11 : 0; };
            v4u cur; v2u lo, hi;
            if constexpr (n >= 0) {
                if constexpr (first(n)) {
                    constexpr int mid = nmid(n), late = n <= LG ? 0 : (n - LG + 1) / 2 < 12 ? (n - LG + 1) / 2 : 12;
                    constexpr int NW = v == 0 ? 12 : v == 1 ? 8 : v == 2 ? 63 : v < 8 ? mid - 4 * (v - 2) + late : late - 4 * (v - 7);
                    static_assert(NW >= 0, "load schedule");
                    asm volatile("s_waitcnt vmcnt(%4)" : "+v"(AF(v & 7, 0)), "+v"(AF(v & 7, 1)), "+v"(AF(v & 7, 2)), "+v"(AF(v & 7, 3)) : "n"(ABLW(v) ? 63 : NW) : "memory");
                }
                cur = bf[n % PD];
                mma<(n < 8)>(acc[dt][0], cur, AF(v & 7, 2 * ks));
                SB();
            }
            if constexpr (m > 0 && (m & 15) == 0 && m <= 48) post(fdone, dn);
            if constexpr (chk(m) > 0) need_full(lack, m == 96 ? ffl + (F_COMM - F_FULL) : ffl, N1 + chk(m));
            if constexpr (m < 176) { if constexpr (mc) bf[m % PD] = lds128(cbs[mk] + moff); else lo = ldstr(tbs[md & 3] + moff); }
            if constexpr (chk(m + 6) > 0) fl = peek(m == 90 ? ffl + (F_COMM - F_FULL) : ffl);
            if constexpr (chk(m + 1) > 0) lack = any_lt(fl, N1 + chk(m + 1));
            if constexpr ((m & 15) == 13 && m < 48) { dn = N1 + mv + 1; asm volatile("" : "+v"(dn)); }
            if constexpr (n == MG - 7) fr = peek(F_RDY);
            if constexpr (n == MG - 1) { if (__builtin_expect(any_lt(fr, 2), 0)) { if (raised < 2) { waitv<0>(); raise(2); } wait_ge(F_RDY, 2); } fence_c(); }
            if constexpr (n == RAISE2) { if (raised < 2) { if (!ABLW(99)) waitv<nmid(n)>(); raise(2); } }
            if constexpr (n >= MG && n < MG + 60 && (n - MG) % 3 == 0) { constexpr int f = (n - MG) / 3; reldA<(f & 3) * 1024>(AF(3 + (f >> 2), f & 3), l16, sws, uoff(3 + (f >> 2))); }
            if constexpr (n == LG - 6) fr = peek(F_RDY + 4);
            if constexpr (n == LG - 1) need_full(any_lt(fr, 1), F_RDY + 4, 1);
            if constexpr (n >= LG && n < LG + 24 && (n - LG) % 2 == 0) { constexpr int f = (n - LG) / 2; reldA<(f & 3) * 1024>(AF(f >> 2, f & 3), l16, sws, uoff(8 + (f >> 2))); }
            SB();
            if constexpr (n >= 0) { mma<(n < 8)>(acc[dt][1], cur, AF(v & 7, 2 * ks + 1)); SB(); }
            if constexpr (m < 176 && !mc) { hi = ldstr(tbs[md & 3] + moff + 2048); bf[m % PD] = v4u{lo.x, lo.y, hi.x, hi.y}; }
            if constexpr (n >= 120 && n < 156) {
                constexpr int sl = (n - 108) / 12, r = (n - 108) % 12 - 2;
                if constexpr (r >= 0 && r < 10 && r % 5 < 4) { const f32x4& a = acc[2 * sl + r / 5][(r % 5) >> 1]; pk[r / 5][r % 5] = cvtv(a[2 * (r % 5 & 1)], a[2 * (r % 5 & 1) + 1]); }
                if constexpr (r >= 0 && r % 5 == 4) stX<(r / 5) * 1024>(pk[r / 5], l16, sws, oX[sl - 1]);
            }
            if constexpr (n == 156) post(fdone + (F_SENT - F_DONE), 3);
            if constexpr (n == 107) { STAMP(10); VAL(28, stall); stall = 0; }
            SB();
        });
        mfma_done();
        STAMP(11); VAL(29, stall);
#pragma unroll
        for (int e = 0; e < 4; e++) sts128f(L_O + w * 4096 + e * 1024 + l16, acc[e >> 1][e & 1]);
        post(fdone + (F_SENT - F_DONE), 4);
    }
    STAMP(14); STAMP_DUMP();
}

extern "C" void kernel_launch(void* const* d_in, const int* in_sizes, int n_in, void* d_out, int out_size, void* d_ws, size_t ws_size,
                              hipStream_t stream) {
    Params p;
    p.x = (const u16*)d_in[0]; p.lre = (const u16*)d_in[1]; p.lim = (const u16*)d_in[2];
    p.Bre = (const u16*)d_in[3]; p.Bim = (const u16*)d_in[4]; p.Cre = (const u16*)d_in[5]; p.Cim = (const u16*)d_in[6];
    p.ldt = (const u16*)d_in[7]; p.Dsk = (const u16*)d_in[8];
    p.wg = (const u16*)d_in[10]; p.wu = (const u16*)d_in[11]; p.wd = (const u16*)d_in[12];
    p.out = (float*)d_out; p.ws = (unsigned char*)d_ws + WS_SHIFT;
    (void)hipFuncSetAttribute((const void*)fused, hipFuncAttributeMaxDynamicSharedMemorySize, L_TOTAL);
    fused<<<256, 512, L_TOTAL, stream>>>(p);
}
```

```cpp
#define PROBE 0
#define PROBEW 0
#define PROBEC heavy
#include <hip/hip_runtime.h>
#include <stdint.h>

typedef unsigned short u16;
typedef unsigned v4u __attribute__((ext_vector_type(4)));
typedef unsigned v2u __attribute__((ext_vector_type(2)));
typedef short s16x4 __attribute__((ext_vector_type(4)));
typedef __bf16 bf16x2 __attribute__((ext_vector_type(2)));
typedef __bf16 bf16x8 __attribute__((ext_vector_type(8)));
typedef float f32x2 __attribute__((ext_vector_type(2)));
typedef float f32x4 __attribute__((ext_vector_type(4)));
#define GAS __attribute__((address_space(1)))
#define LAS __attribute__((address_space(3)))
#define DEVI __device__ __forceinline__
#define NOUNROLL _Pragma("nounroll")

#define SPIN_SLEEP 0
constexpr int DM = 512, FF = 1792;
constexpr unsigned WS_A2 = 0;
constexpr unsigned WS_XB = 16u * 44u * 16384u;
constexpr unsigned WS_GRAN = WS_XB + 256u * 24576u;
constexpr unsigned WS_FLAG = WS_GRAN + 16u * 16u * 512u;
constexpr unsigned WS_EPOCH = WS_FLAG + 1024u * 128u;
constexpr unsigned WS_END = WS_EPOCH + 256u;
constexpr unsigned SLOT = 16384;
constexpr unsigned L_RSTD = 0, L_MEAN = 512;
constexpr unsigned F_DONE = 1024, F_SENT = F_DONE + 16, F_FULL = F_DONE + 32, F_COMM = F_DONE + 40, F_RDY = F_DONE + 48, F_OUT = F_DONE + 64, F_XI = F_DONE + 80, F_BS = F_DONE + 96, L_TAG = F_DONE + 128;
constexpr unsigned L_CST = 1280;
constexpr unsigned L_XCH = L_CST + 8 * 256;
constexpr unsigned L_SEG = 4096, L_PW = 8192, L_TOT = L_PW + 2048, L_PRE = L_TOT + 1024, L_SEL = 12288;
constexpr unsigned L_P = SLOT, L_O = 5 * SLOT, L_W = 6 * SLOT, L_TOTAL = 10 * SLOT;
constexpr int SPIN_MAX = 1 << 18;
constexpr int LSPIN_MAX = 1 << 22;

struct Params {
    const u16 *x, *lre, *lim, *Bre, *Bim, *Cre, *Cim, *ldt, *Dsk, *wg, *wu, *wd;
    float* out;
    unsigned char* ws;
};

DEVI float bf2f(u16 v) { return __uint_as_float(((unsigned)v) << 16); }
DEVI float bflo(unsigned v) { return __uint_as_float(v << 16); }
DEVI float bfhi(unsigned v) { return __uint_as_float(v & 0xffff0000u); }
DEVI unsigned pk2(float a, float b) { f32x2 p = {a, b}; return __builtin_bit_cast(unsigned, __builtin_convertvector(p, bf16x2)); }
DEVI unsigned pk2(f32x2 p) { return __builtin_bit_cast(unsigned, __builtin_convertvector(p, bf16x2)); }
DEVI v4u pk8(f32x4 a, f32x4 b) { return v4u{pk2(a.x, a.y), pk2(a.z, a.w), pk2(b.x, b.y), pk2(b.z, b.w)}; }
DEVI float dot2(unsigned a, unsigned b, float c) { return __builtin_amdgcn_fdot2_f32_bf16(__builtin_bit_cast(bf16x2, a), __builtin_bit_cast(bf16x2, b), c, false); }
template <int CTRL> DEVI float dppf(float v) { return __builtin_bit_cast(float, __builtin_amdgcn_update_dpp(0, __builtin_bit_cast(int, v), CTRL, 0xf, 0xf, true)); }
DEVI f32x4 mfma(v4u a, v4u b, f32x4 c) { return __builtin_amdgcn_mfma_f32_16x16x32_bf16(__builtin_bit_cast(bf16x8, a), __builtin_bit_cast(bf16x8, b), c, 0, 0, 0); }
template <bool FIRST> DEVI void mma(f32x4& c, const v4u& a, const v4u& b) {
    if constexpr (FIRST) asm volatile("v_mfma_f32_16x16x32_bf16 %0, %1, %2, 0" : "=&v"(c) : "v"(a), "v"(b));
    else asm volatile("v_mfma_f32_16x16x32_bf16 %0, %1, %2, %0" : "+v"(c) : "v"(a), "v"(b));
}
DEVI void mfma_done() { __builtin_amdgcn_sched_barrier(0); asm volatile("s_nop 7"); __builtin_amdgcn_sched_barrier(0); }
DEVI v4u lds128(unsigned a) { return *(LAS v4u*)(uintptr_t)a; }
DEVI f32x4 lds128f(unsigned a) { return *(LAS f32x4*)(uintptr_t)a; }
DEVI float lds32f(unsigned a) { return *(LAS float*)(uintptr_t)a; }
DEVI void sts32f(unsigned a, float v) { *(LAS float*)(uintptr_t)a = v; }
DEVI void sts128(unsigned a, v4u v) { *(LAS v4u*)(uintptr_t)a = v; }
DEVI void sts128f(unsigned a, f32x4 v) { *(LAS f32x4*)(uintptr_t)a = v; }
DEVI v2u ldstr(unsigned a) { return __builtin_bit_cast(v2u, __builtin_amdgcn_ds_read_tr16_b64_v4i16((LAS s16x4*)(uintptr_t)a)); }
DEVI void fence_c() { asm volatile("" ::: "memory"); }
DEVI void bar_l() { asm volatile("s_waitcnt lgkmcnt(0)\n\ts_barrier" ::: "memory"); }
template <int N> DEVI void waitv() { asm volatile("s_waitcnt vmcnt(%0)" ::"n"(N) : "memory"); }
DEVI void dma16(const void* sbase, unsigned voff, unsigned ldst) {
    asm volatile("s_mov_b32 m0, %2\n\ts_nop 0\n\tglobal_load_lds_dwordx4 %0, %1" ::"v"(voff), "s"(sbase), "s"(ldst) : "memory");
}
DEVI void post(unsigned a, unsigned v) { fence_c(); *(LAS unsigned*)(uintptr_t)a = v; fence_c(); }
DEVI unsigned peek(unsigned a) { fence_c(); return *(LAS unsigned*)(uintptr_t)a; }
DEVI bool any_lt(unsigned v, unsigned need) { return __builtin_amdgcn_ballot_w64(v < need) != 0; }
DEVI void wait_ge(unsigned a, unsigned need) {
    int s = LSPIN_MAX;
    NOUNROLL while (any_lt(peek(a), need)) { asm volatile("s_sub_i32 %0, %0, 1" : "+s"(s) :: "scc"); if (s < 0) break; if (SPIN_SLEEP) __builtin_amdgcn_s_sleep(SPIN_SLEEP); }
    fence_c();
}
template <int V> DEVI void postm(unsigned a, int lane) {
    asm volatile("s_mov_b64 exec, %2\n\tds_max_u32 %0, %1\n\ts_mov_b64 exec, -1" ::"v"(a), "v"(lane), "n"(1 << V) : "memory");
}
DEVI unsigned vm_outstanding() {
    unsigned v; asm volatile("s_getreg_b32 %0, hwreg(HW_REG_IB_STS)" : "=s"(v)::"memory");
    return (v & 15u) | ((v >> 18) & 0x30u);
}
DEVI void postv(unsigned a, int lane, unsigned v) {
    asm volatile("s_lshl_b64 exec, 1, %2\n\tds_max_u32 %0, %1\n\ts_mov_b64 exec, -1" ::"v"(a), "v"(lane), "s"(v) : "memory", "scc");
}
template <int V> DEVI void postc(unsigned a, int lane) {
    asm volatile("s_mov_b64 exec, %2\n\tds_write_b32 %0, %1\n\ts_mov_b64 exec, -1" ::"v"(a), "v"(lane), "n"(1 << V) : "memory");
}
DEVI void need_full(bool lack, unsigned a, unsigned need) { if (__builtin_expect(lack, 0)) wait_ge(a, need); fence_c(); }
#define VAL(id, v)
DEVI float sfma(float a, float b, float c) { float d; asm("v_fma_f32 %0, %1, %2, %3" : "=v"(d) : "v"(a), "v"(b), "v"(c)); return d; }
DEVI float sfms(float a, float b, float c) { float d; asm("v_fma_f32 %0, %1, %2, -%3" : "=v"(d) : "v"(a), "v"(b), "v"(c)); return d; }
DEVI float sfnma(float a, float b, float c) { float d; asm("v_fma_f32 %0, -%1, %2, %3" : "=v"(d) : "v"(a), "v"(b), "v"(c)); return d; }
DEVI float sadd(float a, float b) { float d; asm("v_add_f32 %0, %1, %2" : "=v"(d) : "v"(a), "v"(b)); return d; }
DEVI float smul(float a, float b) { float d; asm("v_mul_f32 %0, %1, %2" : "=v"(d) : "v"(a), "v"(b)); return d; }
DEVI float smul_t(float a, float b) { float d; asm("s_nop 0\n\tv_mul_f32 %0, %1, %2" : "=v"(d) : "v"(a), "v"(b)); return d; }
struct cpx { float x, y; };
DEVI cpx cmul(cpx a, cpx b) { return cpx{sfms(a.x, b.x, smul(a.y, b.y)), sfma(a.x, b.y, smul(a.y, b.x))}; }
DEVI float lmad(cpx a, float h, float u) {
    float d;
    asm("v_fma_f32 %0, %1, %3, %4\n\ts_nop 0\n\tv_fmac_f32_dpp %0, %3, %2 quad_perm:[1,0,3,2] row_mask:0xf bank_mask:0xf" : "=&v"(d) : "v"(a.x), "v"(a.y), "v"(h), "v"(u));
    return d;
}
DEVI f32x2 dup(float v) { f32x2 d = {v, v}; asm("" : "+v"(d)); return d; }
DEVI f32x2 pfma(f32x2 a, f32x2 b, f32x2 c) { return __builtin_elementwise_fma(a, b, c); }
DEVI f32x2 lo2(f32x4 v) { return f32x2{v.x, v.y}; }
DEVI f32x2 hi2(f32x4 v) { return f32x2{v.z, v.w}; }
#define SB() __builtin_amdgcn_sched_barrier(0)
template <int V> struct IC { static constexpr int value = V; };
template <int B, int E, class F> DEVI void sfor(F&& f) { if constexpr (B < E) { f(IC<B>{}); sfor<B + 1, E>(f); } }
#define LAMB(nn) [&](auto nn) __attribute__((always_inline))
#define STAMP(id) do { if ((id) == PROBE && w == PROBEW) { SB(); unsigned long long t_; asm volatile("s_memrealtime %0\n\ts_waitcnt lgkmcnt(0)" : "=s"(t_) :: "memory"); *(LAS unsigned*)(uintptr_t)14336 = (unsigned)t_; SB(); } } while (0)
#define STAMP_INIT()
#define STAMP_DUMP() do { if (w == 4 && (PROBEC)) { const unsigned d_ = 4000u + __builtin_amdgcn_readfirstlane(peek(14336)) - (unsigned)tstart; \
    while ((unsigned)__builtin_amdgcn_s_memrealtime() - (unsigned)tstart < min(d_, 9000u)) __builtin_amdgcn_s_sleep(4); } } while (0)
DEVI int fresh_lane() { int l; asm volatile("v_mbcnt_lo_u32_b32 %0, -1, 0\n\tv_mbcnt_hi_u32_b32 %0, -1, %0" : "=v"(l)); return l; }
DEVI v4u make_sel(int lane, int bb) {
    const int c = lane & 15, tk = 8 * (c >> 2) + 4 * bb + (c & 3) - 8 * (lane >> 4);
    v4u s;
#pragma unroll
    for (int e = 0; e < 4; e++) s[e] = (tk == 2 * e ? 0x3f80u : 0u) | (tk == 2 * e + 1 ? 0x3f800000u : 0u);
    return s;
}
#define LANE_VARS const int lane = fresh_lane(), c = lane & 15, g = lane >> 4, q8 = lane >> 3, p8 = lane & 7; (void)c; (void)g; (void)q8; (void)p8
constexpr unsigned wslot(int n) { return (unsigned)(n & 3) * SLOT; }

#define ROT1 ((i + 2 * kq) & 7)
#define ROT2 0
#define XI_STEP 6
#define XI2_STEP 15
#define TOUCH_GATE 1
#define LB_SLEEP 8
#define LB_FIRST 1
#define T2_SLEEP 1
#define FL_FIRST 20
#define FL_SLEEP 6
#define MIDV 64
#define X_FIRST 14
#define X_SLEEP 1
#define PDV 4
#define INFL 8
#define TSL 6
#define TW __builtin_amdgcn_s_sleep(TSL);
#define PACE 14
#define PACE2 8
#define XLV 3
#define LGATE 80
#define RAISE2 23
#define TGN 37
#define EGATE 102
#define X_LAST 6
#define CLAG 4
#define CRING 8
#define PF_SLEEP 1
#define WS_SHIFT 0
#define NOXT
#define LT_SLEEP 16
#define MG 18
#define ABLW(v) 0
#define WR_DEPTH 4
#define PFPOS 2
#define PF_N 12
#define WR_SLEEP 2
#define SLEEP0 12
constexpr unsigned CODE_LINES = 306;
template <int OFF> DEVI void reldA(v4u& d, unsigned voff, v4u srd, unsigned so) {
    asm volatile("buffer_load_dwordx4 %0, %1, %2, %3 offen offset:%4 sc1" : "+v"(d) : "v"(voff), "s"(srd), "s"(so), "n"(OFF) : "memory");
}
template <int OFF> DEVI void stA(const v4u& d, unsigned voff, v4u srd, unsigned so) {
    asm volatile("buffer_store_dwordx4 %0, %1, %2, %3 offen offset:%4 sc1\n\ts_nop 1" ::"v"(d), "v"(voff), "s"(srd), "s"(so), "n"(OFF) : "memory");
}
template <int OFF> DEVI void stX(const v4u& d, unsigned voff, v4u srd, unsigned so) {
    asm volatile("buffer_store_dwordx4 %0, %1, %2, %3 offen offset:%4 sc1" ::"v"(d), "v"(voff), "s"(srd), "s"(so), "n"(OFF) : "memory");
}
DEVI void ldW(v4u& d, unsigned voff, v4u srd, unsigned so) { asm volatile("buffer_load_dwordx4 %0, %1, %2, %3 offen" : "=v"(d) : "v"(voff), "s"(srd), "s"(so) : "memory"); }
DEVI void touch(unsigned& sink, const void* tp, unsigned to) { asm volatile("global_load_dword %0, %1, %2" : "+v"(sink) : "v"(to), "s"(tp) : "memory"); }

DEVI v4u make_srd(const void* base) { const unsigned long long a = (unsigned long long)base; return v4u{(unsigned)a, (unsigned)(a >> 32) & 0xffffu, 0x7fffffffu, 0x00020000u}; }
DEVI void dma4(v4u srd, unsigned ve, unsigned vo, unsigned s0, unsigned s1, unsigned s2, unsigned s3, unsigned ldst) {
    asm volatile("s_mov_b32 m0, %7\n\ts_nop 0\n\tbuffer_load_dwordx4 %0, %2, %3 offen lds\n\tbuffer_load_dwordx4 %1, %2, %4 offen offset:1024 lds\n\t"
                 "buffer_load_dwordx4 %0, %2, %5 offen offset:2048 lds\n\tbuffer_load_dwordx4 %1, %2, %6 offen offset:3072 lds"
                 ::"v"(ve), "v"(vo), "s"(srd), "s"(s0), "s"(s1), "s"(s2), "s"(s3), "s"(ldst) : "memory");
}
DEVI void dma1(v4u srd, unsigned v, unsigned so, unsigned ldst) {
    asm volatile("s_mov_b32 m0, %3\n\ts_nop 0\n\tbuffer_load_dwordx4 %0, %1, %2 offen lds" ::"v"(v), "s"(srd), "s"(so), "s"(ldst) : "memory");
}
template <int H, int NEG> DEVI f32x2 bmul(f32x2 a, f32x2 b) {
    f32x2 d; asm("v_pk_mul_f32 %0, %1, %2 op_sel:[%3,0] op_sel_hi:[%3,1] neg_lo:[%4,0] neg_hi:[%4,0]" : "=v"(d) : "v"(a), "v"(b), "n"(H), "n"(NEG)); return d;
}
template <int H, int NEG> DEVI f32x2 bfma(f32x2 a, f32x2 b, f32x2 c) {
    f32x2 d; asm("v_pk_fma_f32 %0, %1, %2, %3 op_sel:[%4,0,0] op_sel_hi:[%4,1,1] neg_lo:[%5,0,0] neg_hi:[%5,0,0]" : "=v"(d) : "v"(a), "v"(b), "v"(c), "n"(H), "n"(NEG)); return d;
}
DEVI f32x2 cmadp(f32x2 a, f32x2 h, f32x2 u) {
    f32x2 t, d;
    asm("v_pk_fma_f32 %0, %1, %2, %3 op_sel:[0,0,0] op_sel_hi:[0,1,1]" : "=v"(t) : "v"(h), "v"(a), "v"(u));
    asm("v_pk_fma_f32 %0, %1, %2, %3 op_sel:[1,0,0] op_sel_hi:[0,0,1] neg_lo:[1,0,0]" : "=v"(d) : "v"(a), "v"(f32x2{h.y, h.y}), "v"(t));
    return d;
}
DEVI void mma_c(f32x4& d, const v4u& a, const v4u& b, const f32x4& c) { asm volatile("v_mfma_f32_16x16x32_bf16 %0, %1, %2, %3" : "=&v"(d) : "v"(a), "v"(b), "v"(c)); }
DEVI void mma_t(f32x4& d, const v4u& a, const v4u& b, const f32x4& c) { asm volatile("v_mfma_f32_16x16x32_bf16 %0, %1, %2, %3" : "+v"(d) : "v"(a), "v"(b), "v"(c)); }
DEVI unsigned cvtv(float a, float b) { unsigned d; asm volatile("v_cvt_pk_bf16_f32 %0, %1, %2" : "=v"(d) : "v"(a), "v"(b)); return d; }

__global__ __launch_bounds__(512, 2) void fused(Params p) {
    const unsigned char* pc0;
    asm volatile("s_getpc_b64 %0" : "=s"(pc0));
    const unsigned long long tstart = __builtin_amdgcn_s_memrealtime();
    const int w = __builtin_amdgcn_readfirstlane(threadIdx.x >> 6);
    const int b = blockIdx.x, xcd = b & 7, idx = b >> 3;
    const int i = 8 * (xcd & 1) + (idx & 7), kq = xcd >> 1, q = idx >> 3, j = 4 * kq + q;
    const bool heavy = q != 3;
    const int N1 = heavy ? 20 : 12;
    const int lw = w & 1, pr = w & 3;
    const int rot = ROT1;
    const __amdgpu_buffer_rsrc_t rws = __builtin_amdgcn_make_buffer_rsrc(p.ws, 0, WS_END, 0x00020000);
    auto unit_of = [&](int v) { return v < 4 ? 7 * kq + v : v < 8 ? 28 + 8 * ((v >> 1) & 1) + 2 * kq + (v & 1) : 7 * kq + v - 4; };
    const unsigned sl_lo = L_P + (unsigned)pr * SLOT;
    const unsigned xo = WS_XB + (unsigned)((i * 4 + q) * 4) * 24576u + pr * 6144;
    const unsigned fo = WS_FLAG + (unsigned)(i * 16 + j) * 512u;

    if (w >= 4) {
        LANE_VARS;
        STAMP_INIT();
        STAMP(0);
        const unsigned swz = (unsigned)((p8 ^ (q8 >> 1)) << 4);
        const unsigned fa = F_DONE + (lane & 3) * 4;
        unsigned sink = 0, tg;
        auto poll16 = [&](int which) __attribute__((always_inline)) {
            wait_ge(F_OUT + (lane & 3) * 4, which + 1);
            STAMP(16);
            const unsigned pf = WS_FLAG + (unsigned)(i * 16 + 4 * kq + (lane & 3)) * 512u + which * 128 + ((lane >> 2) & 3) * 8;
            NOUNROLL for (int spin = 0; spin < SPIN_MAX; spin++) {
                fence_c();
                const v2u f = __builtin_amdgcn_raw_buffer_load_b64(rws, pf, 0, 16);
                const unsigned bad = (unsigned)__builtin_amdgcn_ballot_w64(f.x != tg || f.y != ~tg);
                if (bad == 0) break;
                if ((bad & 0x7777u) == 0) { if (which) break; postc<1>(F_RDY, lane); }
                __builtin_amdgcn_s_sleep(FL_SLEEP);
            }
            postc<2>(F_RDY + which * 4, lane);
            STAMP(17);
        };
        if (w < 6) {
            unsigned ffull = F_FULL + lw * 4;
            asm volatile("" : "+v"(ffull));
            touch(sink, lw ? p.Bim : p.Bre, (32 * j + 2 * i) * 1024 + c * 128);
            bar_l();
            {
                const unsigned voffS = (8 * (q8 >> 2) + (q8 & 3)) * 1024 + swz;
                const v4u srd = make_srd((const unsigned char*)(lw ? p.Bim : p.Bre) + (size_t)(32 * j) * 1024);
                wait_ge(F_XI + (lane & 3) * 4, 1);
                NOUNROLL for (int n = 0; n < 8; n++) {
                    const unsigned so = 128 * ((n + rot) & 7);
                    dma4(srd, voffS, voffS ^ 64u, so, so + 15360, so + 2048, so + 17408, L_W + (n >> 1) * SLOT + lw * 8192 + (n & 1) * 4096);
                    waitv<8>(); postv(ffull, lane, max(n - 1, 0) >> 1);
                }
                asm volatile("" ::"v"(sink));
            }
            STAMP(1);
            const unsigned voffG = q8 * (FF * 2) + (((p8 >> 1) ^ ((q8 >> 1) & 1)) << 5) + ((p8 & 1) << 4), voffG1 = voffG ^ 64u;
            const unsigned voffC = q8 * 1024 + swz, voffC1 = voffC ^ 64u;
            const unsigned voffD = q8 * 1024 + (((p8 >> 1) ^ (q8 >> 1) ^ (2 * (kq & 1))) << 5) + ((p8 & 1) << 4);
            const unsigned char* gb = (const unsigned char*)(lw ? p.wu : p.wg) + (7 * kq + q) * 128;
            unsigned thr = (lane >> 2) + 1;
            int credit = 0, cbase = 0;
            auto item = [&](int n, const unsigned char* b0, const unsigned char* b1, unsigned ve, unsigned vo, unsigned st) __attribute__((always_inline)) {
                v4u s0 = make_srd(b0), s1 = make_srd(b1);
                unsigned dst = ((n & 4) ? L_P : L_W) + (n & 3) * SLOT + lw * 8192, dst1 = dst + 4096;
                asm volatile("" : "+s"(s0), "+s"(s1), "+s"(dst), "+s"(dst1));
                int sp = LSPIN_MAX;
                NOUNROLL while (n - 7 > credit) {
                    const unsigned dn = peek(fa);
                    postv(ffull, lane, n - ((vm_outstanding() + 7) >> 3));
                    unsigned long long mk = __builtin_amdgcn_ballot_w64(dn >= thr);
                    mk &= mk >> 1; mk &= mk >> 2;
                    credit = __builtin_popcountll(mk & 0x1111111111111111ull) + cbase;
                    asm volatile("s_sub_i32 %0, %0, 1" : "+s"(sp)::"scc");
                    if (sp < 0) break;
                }
                waitv<INFL>();
                dma4(s0, ve, vo, 0, st - 1024, 2 * st - 2048, 3 * st - 3072, dst);
                postv(ffull, lane, n - (INFL + 7) / 8);
                waitv<INFL>();
                dma4(s1, ve, vo, 0, st - 1024, 2 * st - 2048, 3 * st - 3072, dst1);
            };
            wait_ge(F_XI + (lane & 3) * 4, 3);
            NOUNROLL for (int n = 4; n < N1; n++) {
                const int t = n - 4;
                const unsigned char* bp = gb + (size_t)(((t + rot) & 7) * 64) * (FF * 2) + (t >> 3) * 512;
                item(n, bp, bp + 32 * FF * 2, voffG, voffG1, 8 * FF * 2);
            }
            cbase = N1 - 8; thr += cbase;
            const unsigned char* dp = (const unsigned char*)p.wd + (size_t)(7 * kq) * 65536 + q * 256 + (lw ^ (kq >> 1)) * 128;
            NOUNROLL for (int v = 0; v < 7; v++) {
                if (v >= 4) {
                    const unsigned char* cbp = (const unsigned char*)((v & 2) ? p.Cim : p.Cre) + (size_t)q * 131072 + (2 * kq + (v & 1)) * 128;
                    item(N1 + v, cbp + ((2 * lw) ^ kq) * 32768, cbp + ((2 * lw + 1) ^ kq) * 32768, voffC, voffC1, 8192);
                } else {
                    item(N1 + v, dp, dp + 32768, voffD, voffD, 8192);
                    dp += 65536;
                }
            }
            waitv<16>(); postv(ffull, lane, N1 + 5);
            waitv<8>(); postv(ffull, lane, N1 + 6);
            waitv<0>(); postv(ffull, lane, N1 + 11);
            tg = peek(L_TAG);
            STAMP(7);
            if (lw) {
                const unsigned pf = WS_FLAG + (unsigned)(i * 16 + 4 * (lane & 3) + q) * 512u + 256 + ((lane >> 2) & 1) * 8;
                NOUNROLL for (int spin = 0; spin < SPIN_MAX; spin++) {
                    fence_c();
                    const v2u f = __builtin_amdgcn_raw_buffer_load_b64(rws, pf, 0, 16);
                    if (__builtin_amdgcn_ballot_w64(f.x != tg || f.y != ~tg) == 0) break;
                    __builtin_amdgcn_s_sleep(16);
                }
            }
            if (lw) poll16(1);
        } else {
            unsigned tag0, prm0, prm1, prm2;
            const unsigned cto = min(idx * 11 + 4 * lw + p8, (int)CODE_LINES - 1) * 128;
            touch(sink, pc0, cto);
            touch(sink, lw ? (const void*)p.ws : (const void*)p.Dsk, lw ? ((lane & 4) ? WS_GRAN + (j * 16 + i) * 512 : fo) + (lane & 3) * 128 : p8 * 128);
            if (!heavy) __builtin_amdgcn_s_sleep(LT_SLEEP);
            NOUNROLL for (int t = heavy ? 4 : 0; t < 4; t++)
                touch(sink, lw ? p.wu : p.wg, (32 * i + (lane & 31)) * (FF * 2) + (7 * kq + min(2 * t + (lane >> 5), 6)) * 128);
            const unsigned po = 2 * (32 * j + 8 * (lane >> 4) + 4 * ((lane >> 1) & 1) + ((lane >> 2) & 3));
            asm volatile("global_load_ushort %0, %4, %5\n\tglobal_load_ushort %1, %4, %6\n\tglobal_load_ushort %2, %4, %7\n\tglobal_load_dword %3, %8, %9 sc1"
                         : "=&v"(prm0), "=&v"(prm1), "=&v"(prm2), "=&v"(tag0) : "v"(po), "s"(p.lre), "s"(p.lim), "s"(p.ldt), "v"(WS_EPOCH), "s"(p.ws) : "memory");
            *(LAS unsigned*)(uintptr_t)(F_DONE + (lane & 31) * 4) = 0u;
            bar_l();
            sts128(L_SEL + lw * 1024 + lane * 16, make_sel(lane, lw));
            asm volatile("s_waitcnt vmcnt(0)" : "+v"(prm0), "+v"(prm1), "+v"(prm2), "+v"(tag0)::"memory");
            tg = tag0 + 1u;
            const bool odd = lane & 1;
            const unsigned la = lane * 4;
            const float lr = bflo(prm0), li = bflo(prm1);
            float dt = __expf(bflo(prm2));
            dt = bflo(pk2(dt, dt));
            const float ex = __expf(dt * lr);
            const float fr = __builtin_amdgcn_fractf(dt * li * 0.15915494309189535f);
            const float ar = smul_t(ex, __builtin_amdgcn_cosf(fr)), ai = smul_t(ex, __builtin_amdgcn_sinf(fr));
            const cpx a1 = {ar, ai};
            const cpx a2 = cmul(a1, a1), a4 = cmul(a2, a2);
            const unsigned sg = odd ? 0u : 0x80000000u;
            auto sgn = [&](cpx v) { return cpx{v.x, __uint_as_float(__float_as_uint(v.y) ^ sg)}; };
            const cpx r8 = cmul(a4, a4), r16 = cmul(r8, r8), r32 = cmul(r16, r16), r64 = cmul(r32, r32);
            const cpx a32 = sgn(r32);
            {
                const cpx r = lw ? r32 : r8, rs = sgn(r);
                float v = odd ? r.y : r.x;
                sts32f(L_PW + lw * 1024 + la, odd ? 0.0f : 1.0f);
#pragma unroll
                for (int s = 1; s < 4; s++) { sts32f(L_PW + lw * 1024 + s * 256 + la, v); if (s < 3) v = lmad(rs, v, 0.0f); }
            }
            if (lw == 0) {
                const float nr = ar - 1.0f, den = smul_t(__builtin_amdgcn_rcpf(sfma(lr, lr, smul(li, li))), 2.0f);
                float co = smul(odd ? sfms(ai, lr, smul(nr, li)) : sfma(nr, lr, smul(ai, li)), den);
                sts32f(L_CST + la, odd ? a1.y : a1.x); sts32f(L_CST + 256 + la, odd ? a4.y : a4.x);
                const cpx a1s = sgn(a1);
#pragma unroll
                for (int t = 0; t < 5; t++) { sts32f(L_CST + (2 + t) * 256 + la, co); if (t < 4) co = lmad(a1s, co, 0.0f); }
                *(LAS unsigned*)(uintptr_t)L_TAG = tg;
            }
            post(F_COMM + lw * 4, 1);
            STAMP(1);
            cpx wn[8];
            {
                const cpx r128 = cmul(r64, r64), r256 = cmul(r128, r128);
                wn[0] = lw ? r128 : cpx{1.0f, 0.0f};
#pragma unroll
                for (int n = 1; n < 8; n++) wn[n] = cmul(wn[n - 1], r256);
#pragma unroll
                for (int n = 0; n < 8; n++) { wn[n] = sgn(wn[n]); asm volatile("" : "+v"(wn[n].x), "+v"(wn[n].y)); }
            }
            unsigned gro = WS_GRAN + j * 8192 + lane * 8, gro2 = gro + i * 512;
            unsigned to0 = lw ? min((unsigned)(lane & 31) * 0x98000u + idx * 0x4000u, WS_END - 4) : (lane & 3) * 0x100000 + idx * 0x4000;
            unsigned to1 = (448 * kq + 28 * i + min(lane, 27)) * 1024 + (2 * q + lw) * 128, to2 = (128 * q + 8 * i + p8) * 1024 + (2 * kq + (q8 & 1)) * 128;
            unsigned po2 = WS_XB + (unsigned)((i * 4 + q) * 4 + kq) * 24576u + lw * 12288 + lane * 16;
            unsigned voffC = q8 * 1024 + swz, voffC1 = voffC ^ 64u;
            unsigned voffD = q8 * 1024 + (((p8 >> 1) ^ (q8 >> 1) ^ (2 * (kq & 1))) << 5) + ((p8 & 1) << 4);
            v4u none = {~0u, ~0u, ~0u, ~0u};
            asm volatile("" : "+v"(gro), "+v"(gro2), "+v"(to0), "+v"(to1), "+v"(to2), "+v"(po2), "+v"(voffC), "+v"(voffC1), "+v"(voffD), "+v"(none));
            wait_ge(F_XI + (lane & 3) * 4, heavy ? 2 : 0);
            touch(sink, p.wd, to1);
            touch(sink, lw ? p.Cim : p.Cre, to2);
            touch(sink, lw ? (const void*)p.ws : (const void*)p.out, to0);
            v4u wr[4][8];
            {
                const v4u sc = make_srd((const unsigned char*)p.Cim + (size_t)q * 131072 + (2 * kq + 1) * 128);
                const v4u sd = make_srd((const unsigned char*)p.wd + (size_t)(7 * kq + 4) * 65536 + q * 256 + (lw ^ (kq >> 1)) * 128);
                waitv<0>();
                sfor<0, 32>(LAMB(ff) {
                    constexpr int f = decltype(ff)::value, m = f & 7;
                    if constexpr (f < 8) ldW(wr[0][m], (m & 1) ? voffC1 : voffC, sc, ((2 * lw + (m >> 2)) ^ kq) * 32768 + (m & 3) * 8192);
                    else ldW(wr[f >> 3][m], voffD, sd, (f - 8) * 8192);
                    if constexpr (f % 3 == 2) __builtin_amdgcn_s_sleep(WR_SLEEP);
                });
            }
            auto clear = [&](int n0, int n1) __attribute__((always_inline)) {
                const v4u sws = make_srd(p.ws);
                NOUNROLL for (int n = n0; n < n1; n++) { stX<0>(none, po2, sws, n * 1024); __builtin_amdgcn_s_sleep(PF_SLEEP); }
            };
            STAMP(3);
            wait_ge(fa, 4);
            STAMP(4);
            if (lw) {
                float e[4];
#pragma unroll
                for (int s = 0; s < 4; s++) e[s] = lds32f(L_TOT + s * 256 + la);
                const float p2 = lmad(a32, e[0], e[1]), p3 = lmad(a32, p2, e[2]);
                __builtin_amdgcn_raw_buffer_store_b64(v2u{__float_as_uint(lmad(a32, p3, e[3])), tg}, rws, gro2, 0, 16);
                sts32f(L_PRE + la, 0.0f); sts32f(L_PRE + 256 + la, e[0]); sts32f(L_PRE + 512 + la, p2); sts32f(L_PRE + 768 + la, p3);
            }
            STAMP(5);
            clear(0, PF_N);
            __builtin_amdgcn_s_sleep(LB_FIRST);
            {
                const int top = i - 1 - lw;
                v2u gr[8] = {};
                NOUNROLL for (int spin = 0; spin < SPIN_MAX; spin++) {
                    fence_c();
#pragma unroll
                    for (int n = 0; n < 8; n++) gr[n] = __builtin_amdgcn_raw_buffer_load_b64(rws, gro, ((top - 2 * n) & 15) * 512, 16);
                    unsigned long long bad = 0;
#pragma unroll
                    for (int n = 0; n < 8; n++) if (top - 2 * n >= 0) bad |= __builtin_amdgcn_ballot_w64(gr[n].y != tg);
                    if (bad == 0) break;
                    __builtin_amdgcn_s_sleep(LB_SLEEP);
                }
                float h = 0;
#pragma unroll
                for (int n = 7; n >= 0; n--) if (top - 2 * n >= 0) h = lmad(wn[n], __uint_as_float(gr[n].x), h);
                sts32f(L_XCH + 256 + lw * 256 + la, h);
            }
            postc<4>(F_COMM + lw * 4, lane);
            STAMP(6);
            clear(PF_N, 12);
            waitv<0>();
            asm volatile("" ::"v"(sink));
            if (lane == 0) __builtin_amdgcn_raw_buffer_store_b64(v2u{tg, ~tg}, rws, fo + 256 + lw * 8, 0, 16);
            STAMP(7);
#pragma unroll
            for (int u = 0; u < 4; u++) asm volatile("" : "+v"(wr[u][0]), "+v"(wr[u][1]), "+v"(wr[u][2]), "+v"(wr[u][3]), "+v"(wr[u][4]), "+v"(wr[u][5]), "+v"(wr[u][6]), "+v"(wr[u][7]));
            STAMP(13);
            if (!lw) poll16(0);
            {
                unsigned wa0 = lw * 8192 + lane * 16 + L_P, wa1 = wa0 + (L_W + 3 * SLOT - L_P);
                asm volatile("" : "+v"(wa0), "+v"(wa1));
                sfor<0, 4>(LAMB(uu) {
                    constexpr int u = decltype(uu)::value;
                    wait_ge(fa, N1 + u);
#pragma unroll
                    for (int m = 0; m < 8; m++) sts128((u ? wa0 + (u - 1) * SLOT : wa1) + m * 1024, wr[u][m]);
                    postv(F_COMM + lw * 4, lane, N1 + 8 + u);
                });
            }
            STAMP(15);
        }
        f32x4 base[4];
        {
            const int d = 128 * q + 32 * kq + 4 * p8;
            const v2u dv = *(const GAS v2u*)(p.Dsk + d);
            const float df[4] = {bflo(dv.x), bfhi(dv.x), bflo(dv.y), bfhi(dv.y)};
            v2u xv[4];
#pragma unroll
            for (int e = 0; e < 4; e++) xv[e] = *(const GAS v2u*)(p.x + (size_t)(128 * i + 32 * pr + 8 * e + q8) * DM + d);
#pragma unroll
            for (int e = 0; e < 4; e++) {
                const unsigned tp = (32 * pr + 8 * e + 4 * ((q8 >> 1) & 1) + 2 * (q8 & 1) + (q8 >> 2)) * 4;
                const float rs = lds32f(L_RSTD + tp), nm = smul(lds32f(L_MEAN + tp), rs);
                const float xf[4] = {bflo(xv[e].x), bfhi(xv[e].x), bflo(xv[e].y), bfhi(xv[e].y)};
#pragma unroll
                for (int k = 0; k < 4; k++) base[e][k] = sfma(sfma(xf[k], rs, nm), df[k], xf[k]);
            }
        }
        STAMP(8);
        const unsigned xi = xo + kq * 24576u + lane * 16;
        const unsigned sc = L_O + pr * 4096;
        unsigned wa = sc + (8 * (c >> 2) + 4 * (c & 1) + ((c >> 1) & 1)) * 128 + ((g ^ (c & 7)) << 4), ra = sc + q8 * 128 + ((p8 ^ (2 * (q8 & 1) + (q8 >> 2))) << 4);
        GAS float* op = (GAS float*)p.out + (size_t)(128 * i + 32 * pr + q8) * DM + 128 * q + 32 * kq + 4 * p8;
        asm volatile("" : "+v"(wa), "+v"(ra), "+v"(op));
        wait_ge(F_SENT + pr * 4, 3);
        __builtin_amdgcn_s_sleep(X_FIRST);
        v4u in[6];
        f32x4 own[4];
        NOUNROLL for (int spin = 0; spin < SPIN_MAX; spin++) {
            fence_c();
#pragma unroll
            for (int n = 0; n < 6; n++) in[n] = __builtin_amdgcn_raw_buffer_load_b128(rws, xi + n * 1024, 0, 16);
            SB();
            wait_ge(F_SENT + pr * 4, 4);
#pragma unroll
            for (int e = 0; e < 4; e++) own[e] = lds128f(sc + e * 1024 + lane * 16);
            unsigned mx = 0;
#pragma unroll
            for (int n = 0; n < 6; n++) mx = max(mx, max(max(in[n].x, in[n].y), max(in[n].z, in[n].w)));
            if (__builtin_amdgcn_ballot_w64(mx == ~0u) == 0) break;
        }
        STAMP(11);
#pragma unroll
        for (int e = 0; e < 4; e++)
#pragma unroll
            for (int sr = 0; sr < 3; sr++) { const unsigned d0 = in[2 * sr + (e >> 1)][2 * (e & 1)], d1 = in[2 * sr + (e >> 1)][2 * (e & 1) + 1]; own[e] += f32x4{bflo(d0), bfhi(d0), bflo(d1), bfhi(d1)}; }
        asm volatile("" : "+v"(own[0]), "+v"(own[1]), "+v"(own[2]), "+v"(own[3]));
#pragma unroll
        for (int e = 0; e < 4; e++) sts128f((wa ^ (unsigned)((e >> 1) << 6)) + (e & 1) * 256, own[e]);
        fence_c();
        f32x4 ta[4];
#pragma unroll
        for (int e = 0; e < 4; e++) ta[e] = lds128f((ra ^ (unsigned)((e & 1) << 6)) + e * 1024);
        SB();
#pragma unroll
        for (int e = 0; e < 4; e++) *(GAS f32x4*)(op + e * 8 * DM) = base[e] + ta[e];
        if (b == 255 && w == 4 && lane == 0) __builtin_amdgcn_raw_buffer_store_b32(tg, rws, WS_EPOCH, 0, 16);
        STAMP(12); STAMP_DUMP();
        return;
    }
    constexpr int PD = PDV, XL = XLV, EG = EGATE;
    unsigned stall = 0; (void)stall;
    v4u xa[16][2];
#define AF(u, k) xa[2 * (u) + ((k) >> 1)][(k) & 1]
    v4u sws = make_srd(p.ws);
    unsigned oG = WS_A2 + (unsigned)(i * 44 + 7 * kq) * 16384u + w * 4096, oC = WS_A2 + (unsigned)(i * 44 + 28 + 2 * kq) * 16384u + w * 4096, oF = fo + w * 8;
    asm volatile("" : "+s"(sws), "+s"(oG), "+s"(oC), "+s"(oF));
    auto uoff = [&](int v) __attribute__((always_inline)) { return v < 4 ? oG + v * 16384 : v < 8 ? oC + (8 * ((v >> 1) & 1) + (v & 1)) * 16384 : oG + (v - 4) * 16384; };
    LANE_VARS;
    unsigned fdone = F_DONE + w * 4, l16 = lane * 16;
    asm volatile("" : "+v"(fdone), "+v"(l16));
    unsigned tgs = 0; int raised = 0;
    auto raise = [&](int level) __attribute__((always_inline)) {
        asm volatile("s_mov_b64 exec, 1\n\tbuffer_store_dwordx2 %0, off, %1, %2 offset:%3 sc1\n\ts_mov_b64 exec, -1" ::"v"(v2u{tgs, ~tgs}), "s"(sws), "s"(oF), "n"((level - 1) * 128) : "memory");
        post(fdone + (F_OUT - F_DONE), level);
        raised = level;

        if (level == 1) STAMP(17); else STAMP(18);
    };
    auto try_raise = [&](int level) __attribute__((always_inline)) {
        unsigned v; asm volatile("s_getreg_b32 %0, hwreg(HW_REG_IB_STS, 0, 4)" : "=s"(v)::"memory");
        if (__builtin_expect((int)(v + raised) == level - 1, 0)) raise(level);
    };
    unsigned oX[3];
#pragma unroll
    for (int sl = 1; sl < 4; sl++) { oX[sl - 1] = xo + (kq ^ sl) * 24576u + (sl - 1) * 2048; asm volatile("" : "+s"(oX[sl - 1])); }
    unsigned cbs[2], tbs[4];
    v4u bf[PD];
    const unsigned ffl = F_FULL + (lane & 1) * 4;
    auto set2 = [&](auto kk) __attribute__((always_inline)) {
        constexpr int k = decltype(kk)::value;
        if constexpr (k == 0) tbs[0] = L_P + ((l16 << 1) & 0x780) + ((l16 >> 1) & 0x18) + (((l16 >> 2) & 0x40) | ((l16 >> 2) & 0x20));
        else if constexpr (k < 4) tbs[k] = tbs[0] ^ (unsigned)(k << 5);
        else if constexpr (k == 4) cbs[0] = L_W + ((l16 << 3) & 0x780) + (((l16 >> 4) ^ (l16 >> 1)) & 0x70);
        else cbs[1] = cbs[0] ^ 64u;
        if constexpr (k < 4) asm volatile("" : "+v"(tbs[k])); else asm volatile("" : "+v"(cbs[k - 4]));
    };
    auto prime2 = [&](auto mm) __attribute__((always_inline)) { constexpr int m = decltype(mm)::value; const v2u lo = ldstr(tbs[m]), hi = ldstr(tbs[m] + 2048); bf[m] = v4u{lo.x, lo.y, hi.x, hi.y}; };
    {
        STAMP_INIT();
        STAMP(0);
        unsigned cb[2];
        cb[0] = (unsigned)c * 128u + (unsigned)((g ^ (c >> 1)) << 4); cb[1] = cb[0] ^ 64u;
        {
            const unsigned voffX = (8 * (q8 >> 2) + 4 * (q8 & 1) + ((q8 >> 1) & 1)) * 1024 + ((p8 ^ (q8 >> 1)) << 4), voffX1 = voffX ^ 64u;
            const v4u srd = make_srd((const unsigned char*)p.x + (size_t)(128 * i + 32 * w) * 1024 - 4096);
            auto issue_x = [&](int t) __attribute__((always_inline)) {
                const unsigned s0 = (t >> 3) * 2048 + ((t + rot) & 7) * 128, s1 = (t >> 3) * 2048 + ((t + 1 + rot) & 7) * 128 + 2048;
                dma4(srd, voffX, voffX1, s0 + 4096, s0 + 4096 + 15360, s1, s1 + 15360, sl_lo + (t & 7) * 2048);
            };
            asm volatile("s_mov_b32 m0, %3\n\ts_nop 0\n\tbuffer_load_dword %0, %1, %2 offen lds" ::"v"(p8 * 128), "s"(srd), "s"(4096 + (4 * (j >> 1) + 2 + (j & 1)) * 1024), "s"(L_O + w * 256) : "memory");
            issue_x(0); issue_x(2); issue_x(4); issue_x(6);
            bar_l();
            const v4u sel0 = make_sel(lane, 0), sel1 = make_sel(lane, 1);
            v4u ones = {0x3f803f80u, 0x3f803f80u, 0x3f803f80u, 0x3f803f80u};
            asm volatile("" : "+v"(ones));
            f32x4 sm[2], sq[2], nm4[2], ct[6];
            const unsigned xl0 = cb[0] + sl_lo, xl1 = cb[1] + sl_lo;
            unsigned sa = L_RSTD + (32 * w + 8 * g + (c & 3)) * 4;
            const unsigned long long dgm = __builtin_amdgcn_ballot_w64(g == (c >> 2));
            asm volatile("" : "+v"(sa));
            auto stats = [&](int bb) __attribute__((always_inline)) {
                float s01, s23, sd;
                asm("v_cndmask_b32 %0, %1, %2, %3" : "=v"(s01) : "v"(sq[bb].x), "v"(sq[bb].y), "s"(0xaaaaaaaaaaaaaaaaull));
                asm("v_cndmask_b32 %0, %1, %2, %3" : "=v"(s23) : "v"(sq[bb].z), "v"(sq[bb].w), "s"(0xaaaaaaaaaaaaaaaaull));
                asm("v_cndmask_b32 %0, %1, %2, %3" : "=v"(sd) : "v"(s01), "v"(s23), "s"(0xccccccccccccccccull));
                const float rstd = __builtin_amdgcn_rsqf(fmaxf(sd * (1.0f / DM) - nm4[bb].x * nm4[bb].x, 0.0f) + 1e-5f);
                asm volatile("s_mov_b64 exec, %3\n\tds_write2_b32 %0, %1, %2 offset0:%4 offset1:%5\n\ts_mov_b64 exec, -1" ::"v"(sa), "v"(rstd), "v"(nm4[bb].x), "s"(dgm), "n"(4 * bb), "n"(L_MEAN / 4 + 4 * bb) : "memory");
            };
            auto centre = [&](auto kk) __attribute__((always_inline)) {
                constexpr int k = decltype(kk)::value, e = k - 4;
                if constexpr (k < 64) mma_c(ct[k % 6], (k & 1) ? sel1 : sel0, xa[(k >> 1) & 15][k >> 5], nm4[k >> 5]);
                if constexpr (e >= 0) {
                    xa[(e >> 1) & 15][e >> 5][2 * (e & 1)] = cvtv(ct[e % 6].x, ct[e % 6].y);
                    xa[(e >> 1) & 15][e >> 5][2 * (e & 1) + 1] = cvtv(ct[e % 6].z, ct[e % 6].w);
                }
                SB();
            };
            SB();
            sfor<0, 17>(LAMB(tt) {
                constexpr int t = decltype(tt)::value, u = t - 1;
                if constexpr (t < 16) {
                    constexpr int t5 = ((t + 1) & ~1) + 5, top = t5 < 7 ? 7 : t5 > 15 ? 15 : t5;
                    waitv<2 * (top - t)>();
                    xa[2 * (t & 7)][t >> 3] = lds128(xl0 + (t & 7) * 2048); xa[2 * (t & 7) + 1][t >> 3] = lds128(xl1 + (t & 7) * 2048);
                }
                SB();
                if constexpr (t == 9) { asm volatile("s_nop 3"); SB(); nm4[0] = sm[0] * (-1.0f / DM); asm volatile("s_nop 1" : "+v"(nm4[0])); SB(); }
                if constexpr (t >= 9) { centre(IC<4 * (t - 9)>{}); centre(IC<4 * (t - 9) + 1>{}); centre(IC<4 * (t - 9) + 2>{}); centre(IC<4 * (t - 9) + 3>{}); }
                if constexpr (u >= 0) {
                    mma<(u & 7) == 0>(sm[u >> 3], ones, xa[2 * (u & 7)][u >> 3]); mma<(u & 7) == 0>(sq[u >> 3], xa[2 * (u & 7)][u >> 3], xa[2 * (u & 7)][u >> 3]);
                    mma<false>(sm[u >> 3], ones, xa[2 * (u & 7) + 1][u >> 3]); mma<false>(sq[u >> 3], xa[2 * (u & 7) + 1][u >> 3], xa[2 * (u & 7) + 1][u >> 3]);
                }
                if constexpr ((t & 1) == 0 && t >= 2 && t + 6 < 16) issue_x(t + 6);
                if constexpr (t == XI_STEP) post(fdone + (F_XI - F_DONE), 1);
                if constexpr (t == XI2_STEP) post(fdone + (F_XI - F_DONE), 2);
                SB();
            });
            post(fdone + (F_XI - F_DONE), 3);
            STAMP(1);
            stats(0);
            SB();
            nm4[1] = sm[1] * (-1.0f / DM); asm volatile("s_nop 1" : "+v"(nm4[1]));
            SB();
            sfor<32, 64>(LAMB(kk) { centre(kk); });
            stats(1);
            SB();
            sfor<64, 68>(LAMB(kk) { centre(kk); });
        }
        STAMP(2);
        f32x4 eX;
        unsigned tb[4];
        {
            cb[0] += L_W; cb[1] += L_W;
            asm volatile("" : "+v"(cb[0]), "+v"(cb[1]));
            f32x4 acc[4][2];
            unsigned fl = 0; bool lack = false;
            {
                const int q4 = c >> 2, p4 = c & 3;
                const unsigned tb1 = L_P + (unsigned)(8 * g + q4) * 128u + (unsigned)((((q4 >> 1) & 1) | ((g & 1) << 1)) << 5) + p4 * 8;
#pragma unroll
                for (int t = 0; t < 4; t++) { tb[t] = tb1 ^ (unsigned)(t << 5); asm volatile("" : "+v"(tb[t])); }
            }
            f32x4 ca, c4, co, c8, rs0, rs1;
            wait_ge(F_FULL + (lane & 3) * 4, 1);
            STAMP(3);
            sfor<-PD, 64>(LAMB(nn) {
                constexpr int n = decltype(nn)::value, m = n + PD;
                v4u cur;
                if constexpr (n >= 0) { cur = bf[n % PD]; mma<(n < 4)>(acc[n & 3][0], xa[n >> 2][0], cur); SB(); }
                if constexpr (m > 0 && (m & 15) == 0) need_full(lack, ffl, (m >> 4) + 1);
                if constexpr (m < 64) bf[m % PD] = lds128(cb[(m >> 2) & 1] + wslot(m >> 4) + (((m >> 1) & 1) * 4 + ((m >> 3) & 1) * 2 + (m & 1)) * 2048);
                else { const unsigned a = tb[(m >> 1) & 3] + wslot(0) + (m & 1) * 8192; const v2u lo = ldstr(a), hi = ldstr(a + 512); bf[m % PD] = v4u{lo.x, lo.y, hi.x, hi.y}; }
                SB();
                if constexpr (n >= 0) { mma<(n < 4)>(acc[n & 3][1], xa[n >> 2][1], cur); SB(); }
                if constexpr ((m & 15) == 10) fl = peek(ffl);
                if constexpr ((m & 15) == 14) lack = any_lt(fl, (m >> 4) + 2);
                if constexpr ((m & 15) == 15 && m < 63) post(fdone, (m >> 4) + 1);
                if constexpr (m == 52) ca = lds128f(L_CST + c * 16);
                if constexpr (m == 53) c4 = lds128f(L_CST + 256 + c * 16);
                if constexpr (m == 54) co = lds128f(L_CST + 512 + c * 16);
                if constexpr (m == 57) c8 = lds128f(L_PW + 256 + c * 16);
                if constexpr (m == 55) rs0 = lds128f(L_RSTD + (32 * w + 8 * g) * 4);
                if constexpr (m == 56) rs1 = lds128f(L_RSTD + (32 * w + 8 * g + 4) * 4);
                SB();
            });
            mfma_done();
            STAMP(4); VAL(24, stall); stall = 0;
            f32x4 ev;
            f32x2 sr[2][4], si[2][4];
#pragma unroll
            for (int st = 0; st < 2; st++) {
                const f32x4 r0 = acc[st][0] * rs0, r1 = acc[st][1] * rs1, i0 = acc[2 + st][0] * rs0, i1 = acc[2 + st][1] * rs1;
                sr[st][0] = lo2(r0); sr[st][1] = hi2(r0); sr[st][2] = lo2(r1); sr[st][3] = hi2(r1);
                si[st][0] = lo2(i0); si[st][1] = hi2(i0); si[st][2] = lo2(i1); si[st][3] = hi2(i1);
            }
            const f32x2 a1[2] = {lo2(ca), hi2(ca)}, cf[2] = {lo2(co), hi2(co)};
#pragma unroll
            for (int t = 1; t < 4; t++) {
                f32x2 tr[2], ti[2];
#pragma unroll
                for (int st = 0; st < 2; st++) { tr[st] = bfma<1, 1>(a1[st], si[st][t - 1], sr[st][t]); ti[st] = bfma<1, 0>(a1[st], sr[st][t - 1], si[st][t]); }
                SB();
#pragma unroll
                for (int st = 0; st < 2; st++) { sr[st][t] = bfma<0, 0>(a1[st], sr[st][t - 1], tr[st]); si[st][t] = bfma<0, 0>(a1[st], si[st][t - 1], ti[st]); }
                SB();
            }
#pragma unroll
            for (int st = 0; st < 2; st++) {
                ev[2 * st] = sfma(c4[2 * st], sr[st][3].x, sfnma(c4[2 * st + 1], si[st][3].x, sr[st][3].y));
                ev[2 * st + 1] = sfma(c4[2 * st], si[st][3].x, sfma(c4[2 * st + 1], sr[st][3].x, si[st][3].y));
                eX[2 * st] = sr[st][3].x; eX[2 * st + 1] = si[st][3].x;
            }
            sts128f(L_SEG + w * 1024 + l16, ev);
            fence_c();
            f32x4 es[4];
#pragma unroll
            for (int s = 0; s < 4; s++) es[s] = lds128f(L_SEG + w * 1024 + s * 256 + (l16 & 0xf0));
            SB();
            unsigned ya = L_O + w * 4096 + l16;
            asm volatile("" : "+v"(ya), "+v"(eX));
#pragma unroll
            for (int st = 0; st < 2; st++) {
                f32x2 tr[4], ti[4];
#pragma unroll
                for (int t = 0; t < 4; t++) { tr[t] = bmul<1, 1>(cf[st], si[st][t]); ti[t] = bmul<1, 1>(cf[st], sr[st][t]); }
                SB();
#pragma unroll
                for (int t = 0; t < 4; t++) { tr[t] = bfma<0, 0>(cf[st], sr[st][t], tr[t]); ti[t] = bfma<0, 1>(cf[st], si[st][t], ti[t]); }
                SB();
                if (st == 0) {
#pragma unroll
                    for (int s = 1; s < 4; s++) { const f32x2 u0 = cmadp(lo2(c8), lo2(es[s - 1]), lo2(es[s])), u1 = cmadp(hi2(c8), hi2(es[s - 1]), hi2(es[s])); es[s] = f32x4{u0.x, u0.y, u1.x, u1.y}; }
                    sts128f(L_TOT + w * 256 + (l16 & 0xf0), es[3]);
                    post(fdone, 4);
                    f32x4 pin;
#pragma unroll
                    for (int k = 0; k < 4; k++) { const float lo = g == 1 ? es[0][k] : 0.0f, hi = g == 3 ? es[2][k] : es[1][k]; pin[k] = g >= 2 ? hi : lo; }
                    sts128f(L_SEG + w * 1024 + l16, pin);
                    SB();
                }
                sts128(ya + st * 1024, v4u{pk2(tr[0]), pk2(tr[1]), pk2(tr[2]), pk2(tr[3])});
                sts128(ya + 2048 + st * 1024, v4u{pk2(ti[0]), pk2(ti[1]), pk2(ti[2]), pk2(ti[3])});
                SB();
            }
        }
        STAMP(5);
        unsigned go = oG + q * 16384, so = oC + (q >> 1) * 16384 + (q & 1) * 2048;
        asm volatile("" : "+s"(go), "+s"(so));
        f32x4 acc[4][2][2];
        f32x2 r12;
        unsigned fc = 0;
        auto gpass = [&](auto pp, auto n0, auto n1) __attribute__((always_inline)) {
            constexpr int pass = decltype(pp)::value;
            unsigned fl = 0, dn = 0; bool lack = false;
            sfor<decltype(n0)::value, decltype(n1)::value>(LAMB(nn) {
                constexpr int n = decltype(nn)::value, m = n + PD;
                constexpr bool pre = pass == 0 || m < 128;
                constexpr unsigned off = wslot(m >> 4) + (m & 1) * 8192 + ((m >> 3) & 1) * 4096;
                const v4u cur = bf[(n + 64 + 128 * pass) % PD];
                mma<(n < 8)>(acc[(n >> 1) & 3][n & 1][0], cur, xa[n >> 3][0]);
                SB();
                if constexpr ((m & 15) == 0) post(fdone, dn);
                if constexpr ((m & 15) == 0) need_full(lack, ffl, 5 + 8 * pass + (m >> 4));
                v2u lo, hi;
                if constexpr (pre) lo = ldstr(tb[(m >> 1) & 3] + off); else lo = ldstr(tbs[m & 3]);
                if constexpr (pass == 1 && n >= 69 && n < 81 && (n & 1)) set2(IC<(n - 69) / 2>{});
                if constexpr ((m & 15) == 10) fl = peek(ffl);
                if constexpr ((m & 15) == 15) lack = any_lt(fl, m == 127 && pass == 0 ? N1 - 7 : 6 + 8 * pass + (m >> 4));
                if constexpr ((m & 15) == 13) { dn = 5 + 8 * pass + (m >> 4); asm volatile("" : "+v"(dn)); }
                if constexpr (n == EG - 8 && pass == 1) fc = peek(F_RDY);
                if constexpr (n == EG - 2 && pass == 1) { if (__builtin_expect(any_lt(fc, 1), 0)) { wait_ge(F_RDY, 1); } fence_c(); }
                if constexpr (n >= EG && n < EG + 24 && pass == 1 && (n - EG) % 3 == 0) { constexpr int f = (n - EG) / 3; reldA<(f & 3) * 1024>(AF(f >> 2, f & 3), l16, sws, uoff(f >> 2)); }
                if constexpr (pass == 1 && (n == 33 || n == 41 || n == 49 || n == 57)) try_raise(1);
                if constexpr (pass == 1 && n == 67) { if (raised < 1) { waitv<0>(); raise(1); } }
                if constexpr (n == 120 && pass == 0) fc = peek(ffl + (F_COMM - F_FULL));
                if constexpr (n == 121 && pass == 0) { const unsigned ra = L_RSTD + w * 128 + (((l16 >> 1) & 0x60) | ((l16 >> 2) & 0xc)); r12 = f32x2{lds32f(ra), lds32f(ra + 16)}; }
                SB();
                mma<(n < 8)>(acc[(n >> 1) & 3][n & 1][1], cur, xa[n >> 3][1]);
                SB();
                if constexpr (pre) hi = ldstr(tb[(m >> 1) & 3] + off + 512); else hi = ldstr(tbs[m & 3] + 2048);
                bf[n % PD] = v4u{lo.x, lo.y, hi.x, hi.y};
                if constexpr ((m & 57) == 57 && (pass == 0 || m < 64)) {
                    tb[(m >> 1) & 3] += (m & 64) ? L_P - L_W : L_W - L_P; asm volatile("" : "+v"(tb[(m >> 1) & 3]));
                }
                SB();
            });
        };
        auto gated = [&](auto ff) __attribute__((always_inline)) {
            constexpr int uu = decltype(ff)::value >> 1, bb = decltype(ff)::value & 1;
            const f32x4 h0 = acc[2 * uu][0][bb] * acc[2 * uu][1][bb], h1 = acc[2 * uu + 1][0][bb] * acc[2 * uu + 1][1][bb];
            return v4u{pk2(bmul<bb, 0>(r12, lo2(h0))), pk2(bmul<bb, 0>(r12, hi2(h0))), pk2(bmul<bb, 0>(r12, lo2(h1))), pk2(bmul<bb, 0>(r12, hi2(h1)))};
        };
        gpass(IC<0>{}, IC<0>{}, IC<128>{});
        {
            r12 = r12 * r12;
            mfma_done();
            STAMP(6); VAL(25, stall); stall = 0;
            sfor<0, 2>(LAMB(ff) { stA<decltype(ff)::value * 1024>(gated(ff), l16, sws, go); });
            SB();
            need_full(any_lt(fc, 4), ffl + (F_COMM - F_FULL), 4);
            const unsigned tg = peek(L_TAG);
            const unsigned cl = L_CST + (l16 & 0xf0);
            unsigned ya = L_O + w * 4096 + l16;
            asm volatile("" : "+v"(ya));
            const f32x4 h0 = lds128f(L_XCH + 256 + (l16 & 0xf0)), h1 = lds128f(L_XCH + 512 + (l16 & 0xf0));
            const f32x4 aw = lds128f(L_PW + 1024 + w * 256 + (l16 & 0xf0)), pb = lds128f(L_PRE + w * 256 + (l16 & 0xf0));
            const f32x4 ag = lds128f(L_PW + l16), pin = lds128f(L_SEG + w * 1024 + l16);
            const v4u sel[2] = {lds128(L_SEL + l16), lds128(L_SEL + 1024 + l16)};
            const v4u yr[2] = {lds128(ya), lds128(ya + 1024)}, yi[2] = {lds128(ya + 2048), lds128(ya + 3072)};
            const f32x4 c4 = lds128f(cl + 256);
            f32x4 cw[4];
            SB();
            sfor<2, 4>(LAMB(ff) { stA<decltype(ff)::value * 1024>(gated(ff), l16, sws, go); });
            tgs = __builtin_amdgcn_readfirstlane(tg);
#pragma unroll
            for (int t = 0; t < 4; t++) cw[t] = lds128f(cl + (3 + t) * 256);
            SB();
            f32x4 zr[2][2], zi[2][2];
#pragma unroll
            for (int st = 0; st < 2; st++)
#pragma unroll
                for (int bb = 0; bb < 2; bb++) { mma<true>(zr[st][bb], yr[st], sel[bb]); mma<true>(zi[st][bb], yi[st], sel[bb]); }
            SB();
            f32x2 hr[2], hi[2];
            {
                const f32x4 cy = h0 + h1;
                const f32x2 k0 = cmadp(lo2(ag), cmadp(lo2(aw), lo2(cy), lo2(pb)), lo2(pin)), k1 = cmadp(hi2(ag), cmadp(hi2(aw), hi2(cy), hi2(pb)), hi2(pin));
                const f32x4 hin = {k0.x, k0.y, k1.x, k1.y};
                float t0[2], t1[2];
#pragma unroll
                for (int st = 0; st < 2; st++) { t0[st] = sfnma(c4[2 * st + 1], hin[2 * st + 1], eX[2 * st]); t1[st] = sfma(c4[2 * st + 1], hin[2 * st], eX[2 * st + 1]); }
                SB();
#pragma unroll
                for (int st = 0; st < 2; st++) { hr[st] = f32x2{hin[2 * st], sfma(c4[2 * st], hin[2 * st], t0[st])}; hi[st] = f32x2{hin[2 * st + 1], sfma(c4[2 * st], hin[2 * st + 1], t1[st])}; }
                SB();
            }
#pragma unroll
            for (int st = 0; st < 2; st++) {
                f32x2 tr[4], ti[4];
#pragma unroll
                for (int t = 0; t < 4; t++) { const f32x2 wv = st ? hi2(cw[t]) : lo2(cw[t]); tr[t] = bmul<1, 1>(wv, hi[st]); ti[t] = bmul<1, 1>(wv, hr[st]); }
                SB();
#pragma unroll
                for (int t = 0; t < 4; t++) { const f32x2 wv = st ? hi2(cw[t]) : lo2(cw[t]); tr[t] = bfma<0, 0>(wv, hr[st], tr[t]); ti[t] = bfma<0, 1>(wv, hi[st], ti[t]); }
                SB();
                v4u fr = {pk2(tr[0]), pk2(tr[1]), pk2(tr[2]), pk2(tr[3])}, fi = {pk2(ti[0]), pk2(ti[1]), pk2(ti[2]), pk2(ti[3])};
                asm volatile("s_nop 1" : "+v"(fr), "+v"(fi));
#pragma unroll
                for (int bb = 0; bb < 2; bb++) { mma<false>(zr[st][bb], fr, sel[bb]); mma<false>(zi[st][bb], fi, sel[bb]); }
                SB();
            }
            mfma_done();
            stA<0>(pk8(zr[0][0], zr[1][0]), l16, sws, so); stA<0>(pk8(zi[0][0], zi[1][0]), l16, sws, so + 8 * 16384);
            stA<1024>(pk8(zr[0][1], zr[1][1]), l16, sws, so); stA<1024>(pk8(zi[0][1], zi[1][1]), l16, sws, so + 8 * 16384);
            SB();
            STAMP(7);
        }
        if (heavy) {
            gpass(IC<1>{}, IC<0>{}, IC<128>{});
            sfor<0, 4>(LAMB(ff) { constexpr int f = decltype(ff)::value; reldA<f * 1024>(AF(2, f), l16, sws, uoff(2)); });
            mfma_done();
            STAMP(8); VAL(26, stall); stall = 0;
            sfor<0, 4>(LAMB(ff) { stA<decltype(ff)::value * 1024>(gated(ff), l16, sws, go + 4 * 16384); });
            SB();
        }
        int q2 = q;
        asm volatile("" : "+s"(q2));
        if (q2 == 3) {
            waitv<0>(); raise(1); raise(2);
            sfor<0, 6>(set2);
            wait_ge(ffl, N1 + 1);
            sfor<0, PD>(prime2);
            wait_ge(F_RDY, 1);
            sfor<0, 12>(LAMB(ff) { constexpr int f = decltype(ff)::value; reldA<(f & 3) * 1024>(AF(f >> 2, f & 3), l16, sws, uoff(f >> 2)); });
#pragma unroll
            for (int u = 0; u < 3; u++) asm volatile("s_waitcnt vmcnt(0)" : "+v"(AF(u, 0)), "+v"(AF(u, 1)), "+v"(AF(u, 2)), "+v"(AF(u, 3))::"memory");
        }
    }
    STAMP(9);
    {
        f32x4 acc[8][2];
        v4u pk[2];
        unsigned fl = 0, fr = 0, dn = 0; bool lack = false;
        constexpr auto fv = [](int n) { return n < 48 ? n >> 4 : n < 108 ? 3 + (n - 48) / 12 : n < 144 ? 8 + ((n - 108) % 12) / 4 : 3 + (n - 144) / 4; };
        constexpr auto fk = [](int n) { return n < 48 ? (n >> 3) & 1 : n < 108 ? ((n - 48) % 12) / 6 : (n >> 1) & 1; };
        constexpr auto fd = [](int n) { return n < 48 ? n & 7 : n < 108 ? 2 + (n - 48) % 6 : n < 144 ? 2 * ((n - 108) / 12 + 1) + (n & 1) : n & 1; };
        constexpr auto first = [](int n) { return n >= 0 && (n < 48 ? (n & 15) == 0 : n < 108 ? (n - 48) % 12 == 0 : n < 120 && (n & 3) == 0); };
        constexpr int LG = LGATE;
        constexpr auto nmid = [](int n) { return n <= MG ? 0 : (n - MG + 2) / 3 < 20 ? (n - MG + 2) / 3 : 20; };
        static_assert(MG + 57 < LG && LG + 22 < 108, "load schedule");
        sfor<0, 176>(LAMB(nn) {
            constexpr int n = decltype(nn)::value, m = n + PD;
            constexpr int v = fv(n), ks = fk(n), dt = fd(n);
            constexpr int mv = fv(m), mk = fk(m), md = fd(m);
            constexpr bool mc = (mv & ~3) == 4;
            constexpr unsigned moff = (mv & 3) * SLOT + (mc ? md * 2048 : (md >> 2) * 8192 + mk * 4096);
            constexpr auto chk = [](int q) { return q == 16 ? 2 : q == 32 ? 4 : q == 60 ? 7 : q == 96 ? 11 : 0; };
            v4u cur; v2u lo, hi;
            if constexpr (n >= 0) {
                if constexpr (first(n)) {
                    constexpr int mid = nmid(n), late = n <= LG ? 0 : (n - LG + 1) / 2 < 12 ? (n - LG + 1) / 2 : 12;
                    constexpr int NW = v == 0 ? 12 : v == 1 ? 8 : v == 2 ? 63 : v < 8 ? mid - 4 * (v - 2) + late : late - 4 * (v - 7);
                    static_assert(NW >= 0, "load schedule");
                    asm volatile("s_waitcnt vmcnt(%4)" : "+v"(AF(v & 7, 0)), "+v"(AF(v & 7, 1)), "+v"(AF(v & 7, 2)), "+v"(AF(v & 7, 3)) : "n"(ABLW(v) ? 63 : NW) : "memory");
                }
                cur = bf[n % PD];
                mma<(n < 8)>(acc[dt][0], cur, AF(v & 7, 2 * ks));
                SB();
            }
            if constexpr (m > 0 && (m & 15) == 0 && m <= 48) post(fdone, dn);
            if constexpr (chk(m) > 0) need_full(lack, m == 96 ? ffl + (F_COMM - F_FULL) : ffl, N1 + chk(m));
            if constexpr (m < 176) { if constexpr (mc) bf[m % PD] = lds128(cbs[mk] + moff); else lo = ldstr(tbs[md & 3] + moff); }
            if constexpr (chk(m + 6) > 0) fl = peek(m == 90 ? ffl + (F_COMM - F_FULL) : ffl);
            if constexpr (chk(m + 1) > 0) lack = any_lt(fl, N1 + chk(m + 1));
            if constexpr ((m & 15) == 13 && m < 48) { dn = N1 + mv + 1; asm volatile("" : "+v"(dn)); }
            if constexpr (n == MG - 7) fr = peek(F_RDY);
            if constexpr (n == MG - 1) { if (__builtin_expect(any_lt(fr, 2), 0)) { if (raised < 2) { waitv<0>(); raise(2); } wait_ge(F_RDY, 2); } fence_c(); }
            if constexpr (n == RAISE2) { if (raised < 2) { if (!ABLW(99)) waitv<nmid(n)>(); raise(2); } }
            if constexpr (n >= MG && n < MG + 60 && (n - MG) % 3 == 0) { constexpr int f = (n - MG) / 3; reldA<(f & 3) * 1024>(AF(3 + (f >> 2), f & 3), l16, sws, uoff(3 + (f >> 2))); }
            if constexpr (n == LG - 6) fr = peek(F_RDY + 4);
            if constexpr (n == LG - 1) need_full(any_lt(fr, 1), F_RDY + 4, 1);
            if constexpr (n >= LG && n < LG + 24 && (n - LG) % 2 == 0) { constexpr int f = (n - LG) / 2; reldA<(f & 3) * 1024>(AF(f >> 2, f & 3), l16, sws, uoff(8 + (f >> 2))); }
            SB();
            if constexpr (n >= 0) { mma<(n < 8)>(acc[dt][1], cur, AF(v & 7, 2 * ks + 1)); SB(); }
            if constexpr (m < 176 && !mc) { hi = ldstr(tbs[md & 3] + moff + 2048); bf[m % PD] = v4u{lo.x, lo.y, hi.x, hi.y}; }
            if constexpr (n >= 120 && n < 156) {
                constexpr int sl = (n - 108) / 12, r = (n - 108) % 12 - 2;
                if constexpr (r >= 0 && r < 10 && r % 5 < 4) { const f32x4& a = acc[2 * sl + r / 5][(r % 5) >> 1]; pk[r / 5][r % 5] = cvtv(a[2 * (r % 5 & 1)], a[2 * (r % 5 & 1) + 1]); }
                if constexpr (r >= 0 && r % 5 == 4) stX<(r / 5) * 1024>(pk[r / 5], l16, sws, oX[sl - 1]);
            }
            if constexpr (n == 156) post(fdone + (F_SENT - F_DONE), 3);
            if constexpr (n == 107) { STAMP(10); VAL(28, stall); stall = 0; }
            SB();
        });
        mfma_done();
        STAMP(11); VAL(29, stall);
#pragma unroll
        for (int e = 0; e < 4; e++) sts128f(L_O + w * 4096 + e * 1024 + l16, acc[e >> 1][e & 1]);
        post(fdone + (F_SENT - F_DONE), 4);
    }
    STAMP(14); STAMP_DUMP();
}

extern "C" void kernel_launch(void* const* d_in, const int* in_sizes, int n_in, void* d_out, int out_size, void* d_ws, size_t ws_size,
                              hipStream_t stream) {
    Params p;
    p.x = (const u16*)d_in[0]; p.lre = (const u16*)d_in[1]; p.lim = (const u16*)d_in[2];
    p.Bre = (const u16*)d_in[3]; p.Bim = (const u16*)d_in[4]; p.Cre = (const u16*)d_in[5]; p.Cim = (const u16*)d_in[6];
    p.ldt = (const u16*)d_in[7]; p.Dsk = (const u16*)d_in[8];
    p.wg = (const u16*)d_in[10]; p.wu = (const u16*)d_in[11]; p.wd = (const u16*)d_in[12];
    p.out = (float*)d_out; p.ws = (unsigned char*)d_ws + WS_SHIFT;
    (void)hipFuncSetAttribute((const void*)fused, hipFuncAttributeMaxDynamicSharedMemorySize, L_TOTAL);
    fused<<<256, 512, L_TOTAL, stream>>>(p);
}
```

```cpp
#include <hip/hip_runtime.h>
#include <stdint.h>

typedef unsigned short u16;
typedef unsigned v4u __attribute__((ext_vector_type(4)));
typedef unsigned v2u __attribute__((ext_vector_type(2)));
typedef short s16x4 __attribute__((ext_vector_type(4)));
typedef __bf16 bf16x2 __attribute__((ext_vector_type(2)));
typedef __bf16 bf16x8 __attribute__((ext_vector_type(8)));
typedef float f32x2 __attribute__((ext_vector_type(2)));
typedef float f32x4 __attribute__((ext_vector_type(4)));
#define GAS __attribute__((address_space(1)))
#define LAS __attribute__((address_space(3)))
#define DEVI __device__ __forceinline__
#define NOUNROLL _Pragma("nounroll")

#define SPIN_SLEEP 0
constexpr int DM = 512, FF = 1792;
constexpr unsigned WS_A2 = 0;
constexpr unsigned WS_XB = 16u * 44u * 16384u;
constexpr unsigned WS_GRAN = WS_XB + 256u * 24576u;
constexpr unsigned WS_FLAG = WS_GRAN + 16u * 16u * 512u;
constexpr unsigned WS_EPOCH = WS_FLAG + 1024u * 128u;
constexpr unsigned WS_END = WS_EPOCH + 256u;
constexpr unsigned SLOT = 16384;
constexpr unsigned L_RSTD = 0, L_MEAN = 512;
constexpr unsigned F_DONE = 1024, F_SENT = F_DONE + 16, F_FULL = F_DONE + 32, F_COMM = F_DONE + 40, F_RDY = F_DONE + 48, F_OUT = F_DONE + 64, F_XI = F_DONE + 80, F_BS = F_DONE + 96, L_TAG = F_DONE + 128;
constexpr unsigned L_CST = 1280;
constexpr unsigned L_XCH = L_CST + 8 * 256;
constexpr unsigned L_SEG = 4096, L_PW = 8192, L_TOT = L_PW + 2048, L_PRE = L_TOT + 1024, L_SEL = 12288;
constexpr unsigned L_P = SLOT, L_O = 5 * SLOT, L_W = 6 * SLOT, L_TOTAL = 10 * SLOT;
constexpr int SPIN_MAX = 1 << 18;
constexpr int LSPIN_MAX = 1 << 22;

struct Params {
    const u16 *x, *lre, *lim, *Bre, *Bim, *Cre, *Cim, *ldt, *Dsk, *wg, *wu, *wd;
    float* out;
    unsigned char* ws;
};

DEVI float bf2f(u16 v) { return __uint_as_float(((unsigned)v) << 16); }
DEVI float bflo(unsigned v) { return __uint_as_float(v << 16); }
DEVI float bfhi(unsigned v) { return __uint_as_float(v & 0xffff0000u); }
DEVI unsigned pk2(float a, float b) { f32x2 p = {a, b}; return __builtin_bit_cast(unsigned, __builtin_convertvector(p, bf16x2)); }
DEVI unsigned pk2(f32x2 p) { return __builtin_bit_cast(unsigned, __builtin_convertvector(p, bf16x2)); }
DEVI v4u pk8(f32x4 a, f32x4 b) { return v4u{pk2(a.x, a.y), pk2(a.z, a.w), pk2(b.x, b.y), pk2(b.z, b.w)}; }
DEVI float dot2(unsigned a, unsigned b, float c) { return __builtin_amdgcn_fdot2_f32_bf16(__builtin_bit_cast(bf16x2, a), __builtin_bit_cast(bf16x2, b), c, false); }
template <int CTRL> DEVI float dppf(float v) { return __builtin_bit_cast(float, __builtin_amdgcn_update_dpp(0, __builtin_bit_cast(int, v), CTRL, 0xf, 0xf, true)); }
DEVI f32x4 mfma(v4u a, v4u b, f32x4 c) { return __builtin_amdgcn_mfma_f32_16x16x32_bf16(__builtin_bit_cast(bf16x8, a), __builtin_bit_cast(bf16x8, b), c, 0, 0, 0); }
template <bool FIRST> DEVI void mma(f32x4& c, const v4u& a, const v4u& b) {
    if constexpr (FIRST) asm volatile("v_mfma_f32_16x16x32_bf16 %0, %1, %2, 0" : "=&v"(c) : "v"(a), "v"(b));
    else asm volatile("v_mfma_f32_16x16x32_bf16 %0, %1, %2, %0" : "+v"(c) : "v"(a), "v"(b));
}
DEVI void mfma_done() { __builtin_amdgcn_sched_barrier(0); asm volatile("s_nop 7"); __builtin_amdgcn_sched_barrier(0); }
DEVI v4u lds128(unsigned a) { return *(LAS v4u*)(uintptr_t)a; }
DEVI f32x4 lds128f(unsigned a) { return *(LAS f32x4*)(uintptr_t)a; }
DEVI float lds32f(unsigned a) { return *(LAS float*)(uintptr_t)a; }
DEVI void sts32f(unsigned a, float v) { *(LAS float*)(uintptr_t)a = v; }
DEVI void sts128(unsigned a, v4u v) { *(LAS v4u*)(uintptr_t)a = v; }
DEVI void sts128f(unsigned a, f32x4 v) { *(LAS f32x4*)(uintptr_t)a = v; }
DEVI v2u ldstr(unsigned a) { return __builtin_bit_cast(v2u, __builtin_amdgcn_ds_read_tr16_b64_v4i16((LAS s16x4*)(uintptr_t)a)); }
DEVI void fence_c() { asm volatile("" ::: "memory"); }
DEVI void bar_l() { asm volatile("s_waitcnt lgkmcnt(0)\n\ts_barrier" ::: "memory"); }
template <int N> DEVI void waitv() { asm volatile("s_waitcnt vmcnt(%0)" ::"n"(N) : "memory"); }
DEVI void dma16(const void* sbase, unsigned voff, unsigned ldst) {
    asm volatile("s_mov_b32 m0, %2\n\ts_nop 0\n\tglobal_load_lds_dwordx4 %0, %1" ::"v"(voff), "s"(sbase), "s"(ldst) : "memory");
}
DEVI void post(unsigned a, unsigned v) { fence_c(); *(LAS unsigned*)(uintptr_t)a = v; fence_c(); }
DEVI unsigned peek(unsigned a) { fence_c(); return *(LAS unsigned*)(uintptr_t)a; }
DEVI bool any_lt(unsigned v, unsigned need) { return __builtin_amdgcn_ballot_w64(v < need) != 0; }
DEVI void wait_ge(unsigned a, unsigned need) {
    int s = LSPIN_MAX;
    NOUNROLL while (any_lt(peek(a), need)) { asm volatile("s_sub_i32 %0, %0, 1" : "+s"(s) :: "scc"); if (s < 0) break; if (SPIN_SLEEP) __builtin_amdgcn_s_sleep(SPIN_SLEEP); }
    fence_c();
}
template <int V> DEVI void postm(unsigned a, int lane) {
    asm volatile("s_mov_b64 exec, %2\n\tds_max_u32 %0, %1\n\ts_mov_b64 exec, -1" ::"v"(a), "v"(lane), "n"(1 << V) : "memory");
}
DEVI unsigned vm_outstanding() {
    unsigned v; asm volatile("s_getreg_b32 %0, hwreg(HW_REG_IB_STS)" : "=s"(v)::"memory");
    return (v & 15u) | ((v >> 18) & 0x30u);
}
DEVI void postv(unsigned a, int lane, unsigned v) {
    asm volatile("s_lshl_b64 exec, 1, %2\n\tds_max_u32 %0, %1\n\ts_mov_b64 exec, -1" ::"v"(a), "v"(lane), "s"(v) : "memory", "scc");
}
template <int V> DEVI void postc(unsigned a, int lane) {
    asm volatile("s_mov_b64 exec, %2\n\tds_write_b32 %0, %1\n\ts_mov_b64 exec, -1" ::"v"(a), "v"(lane), "n"(1 << V) : "memory");
}
DEVI void need_full(bool lack, unsigned a, unsigned need) { if (__builtin_expect(lack, 0)) wait_ge(a, need); fence_c(); }
#define VAL(id, v)
DEVI float sfma(float a, float b, float c) { float d; asm("v_fma_f32 %0, %1, %2, %3" : "=v"(d) : "v"(a), "v"(b), "v"(c)); return d; }
DEVI float sfms(float a, float b, float c) { float d; asm("v_fma_f32 %0, %1, %2, -%3" : "=v"(d) : "v"(a), "v"(b), "v"(c)); return d; }
DEVI float sfnma(float a, float b, float c) { float d; asm("v_fma_f32 %0, -%1, %2, %3" : "=v"(d) : "v"(a), "v"(b), "v"(c)); return d; }
DEVI float sadd(float a, float b) { float d; asm("v_add_f32 %0, %1, %2" : "=v"(d) : "v"(a), "v"(b)); return d; }
DEVI float smul(float a, float b) { float d; asm("v_mul_f32 %0, %1, %2" : "=v"(d) : "v"(a), "v"(b)); return d; }
DEVI float smul_t(float a, float b) { float d; asm("s_nop 0\n\tv_mul_f32 %0, %1, %2" : "=v"(d) : "v"(a), "v"(b)); return d; }
struct cpx { float x, y; };
DEVI cpx cmul(cpx a, cpx b) { return cpx{sfms(a.x, b.x, smul(a.y, b.y)), sfma(a.x, b.y, smul(a.y, b.x))}; }
DEVI float lmad(cpx a, float h, float u) {
    float d;
    asm("v_fma_f32 %0, %1, %3, %4\n\ts_nop 0\n\tv_fmac_f32_dpp %0, %3, %2 quad_perm:[1,0,3,2] row_mask:0xf bank_mask:0xf" : "=&v"(d) : "v"(a.x), "v"(a.y), "v"(h), "v"(u));
    return d;
}
DEVI f32x2 dup(float v) { f32x2 d = {v, v}; asm("" : "+v"(d)); return d; }
DEVI f32x2 pfma(f32x2 a, f32x2 b, f32x2 c) { return __builtin_elementwise_fma(a, b, c); }
DEVI f32x2 lo2(f32x4 v) { return f32x2{v.x, v.y}; }
DEVI f32x2 hi2(f32x4 v) { return f32x2{v.z, v.w}; }
#define SB() __builtin_amdgcn_sched_barrier(0)
template <int V> struct IC { static constexpr int value = V; };
template <int B, int E, class F> DEVI void sfor(F&& f) { if constexpr (B < E) { f(IC<B>{}); sfor<B + 1, E>(f); } }
#define LAMB(nn) [&](auto nn) __attribute__((always_inline))
#define STAMP(id)
#define STAMP_INIT()
#define STAMP_DUMP()
DEVI int fresh_lane() { int l; asm volatile("v_mbcnt_lo_u32_b32 %0, -1, 0\n\tv_mbcnt_hi_u32_b32 %0, -1, %0" : "=v"(l)); return l; }
DEVI v4u make_sel(int lane, int bb) {
    const int c = lane & 15, tk = 8 * (c >> 2) + 4 * bb + (c & 3) - 8 * (lane >> 4);
    v4u s;
#pragma unroll
    for (int e = 0; e < 4; e++) s[e] = (tk == 2 * e ? 0x3f80u : 0u) | (tk == 2 * e + 1 ? 0x3f800000u : 0u);
    return s;
}
#define LANE_VARS const int lane = fresh_lane(), c = lane & 15, g = lane >> 4, q8 = lane >> 3, p8 = lane & 7; (void)c; (void)g; (void)q8; (void)p8
constexpr unsigned wslot(int n) { return (unsigned)(n & 3) * SLOT; }

#define ROT1 ((i + 2 * kq) & 7)
#define ROT2 0
#define XI_STEP 6
#define XI2_STEP 15
#define TOUCH_GATE 1
#define LB_SLEEP 8
#define LB_FIRST 1
#define T2_SLEEP 1
#define FL_FIRST 20
#define FL_SLEEP 6
#define MIDV 64
#define X_FIRST 14
#define X_SLEEP 1
#define PDV 4
#define INFL 8
#define TSL 6
#define TW __builtin_amdgcn_s_sleep(TSL);
#define PACE 14
#define PACE2 8
#define XLV 3
#define LGATE 80
#define RAISE2 23
#define TGN 37
#define EGATE 102
#define X_LAST 6
#define CLAG 4
#define CRING 8
#define PF_SLEEP 1
#define WS_SHIFT 0
#define NOXT
#define LT_SLEEP 16
#define MG 18
#define ABLW(v) 0
#define WR_DEPTH 4
#define PFPOS 2
#define PF_N 12
#define WR_SLEEP 2
#define SLEEP0 12
constexpr unsigned CODE_LINES = 306;
template <int OFF> DEVI void reldA(v4u& d, unsigned voff, v4u srd, unsigned so) {
    asm volatile("buffer_load_dwordx4 %0, %1, %2, %3 offen offset:%4 sc1" : "+v"(d) : "v"(voff), "s"(srd), "s"(so), "n"(OFF) : "memory");
}
template <int OFF> DEVI void stA(const v4u& d, unsigned voff, v4u srd, unsigned so) {
    asm volatile("buffer_store_dwordx4 %0, %1, %2, %3 offen offset:%4 sc1\n\ts_nop 1" ::"v"(d), "v"(voff), "s"(srd), "s"(so), "n"(OFF) : "memory");
}
template <int OFF> DEVI void stX(const v4u& d, unsigned voff, v4u srd, unsigned so) {
    asm volatile("buffer_store_dwordx4 %0, %1, %2, %3 offen offset:%4 sc1" ::"v"(d), "v"(voff), "s"(srd), "s"(so), "n"(OFF) : "memory");
}
DEVI void ldW(v4u& d, unsigned voff, v4u srd, unsigned so) { asm volatile("buffer_load_dwordx4 %0, %1, %2, %3 offen" : "=v"(d) : "v"(voff), "s"(srd), "s"(so) : "memory"); }
DEVI void touch(unsigned& sink, const void* tp, unsigned to) { asm volatile("global_load_dword %0, %1, %2" : "+v"(sink) : "v"(to), "s"(tp) : "memory"); }

DEVI v4u make_srd(const void* base) { const unsigned long long a = (unsigned long long)base; return v4u{(unsigned)a, (unsigned)(a >> 32) & 0xffffu, 0x7fffffffu, 0x00020000u}; }
DEVI void dma4(v4u srd, unsigned ve, unsigned vo, unsigned s0, unsigned s1, unsigned s2, unsigned s3, unsigned ldst) {
    asm volatile("s_mov_b32 m0, %7\n\ts_nop 0\n\tbuffer_load_dwordx4 %0, %2, %3 offen lds\n\tbuffer_load_dwordx4 %1, %2, %4 offen offset:1024 lds\n\t"
                 "buffer_load_dwordx4 %0, %2, %5 offen offset:2048 lds\n\tbuffer_load_dwordx4 %1, %2, %6 offen offset:3072 lds"
                 ::"v"(ve), "v"(vo), "s"(srd), "s"(s0), "s"(s1), "s"(s2), "s"(s3), "s"(ldst) : "memory");
}
DEVI void dma1(v4u srd, unsigned v, unsigned so, unsigned ldst) {
    asm volatile("s_mov_b32 m0, %3\n\ts_nop 0\n\tbuffer_load_dwordx4 %0, %1, %2 offen lds" ::"v"(v), "s"(srd), "s"(so), "s"(ldst) : "memory");
}
template <int H, int NEG> DEVI f32x2 bmul(f32x2 a, f32x2 b) {
    f32x2 d; asm("v_pk_mul_f32 %0, %1, %2 op_sel:[%3,0] op_sel_hi:[%3,1] neg_lo:[%4,0] neg_hi:[%4,0]" : "=v"(d) : "v"(a), "v"(b), "n"(H), "n"(NEG)); return d;
}
template <int H, int NEG> DEVI f32x2 bfma(f32x2 a, f32x2 b, f32x2 c) {
    f32x2 d; asm("v_pk_fma_f32 %0, %1, %2, %3 op_sel:[%4,0,0] op_sel_hi:[%4,1,1] neg_lo:[%5,0,0] neg_hi:[%5,0,0]" : "=v"(d) : "v"(a), "v"(b), "v"(c), "n"(H), "n"(NEG)); return d;
}
DEVI f32x2 cmadp(f32x2 a, f32x2 h, f32x2 u) {
    f32x2 t, d;
    asm("v_pk_fma_f32 %0, %1, %2, %3 op_sel:[0,0,0] op_sel_hi:[0,1,1]" : "=v"(t) : "v"(h), "v"(a), "v"(u));
    asm("v_pk_fma_f32 %0, %1, %2, %3 op_sel:[1,0,0] op_sel_hi:[0,0,1] neg_lo:[1,0,0]" : "=v"(d) : "v"(a), "v"(f32x2{h.y, h.y}), "v"(t));
    return d;
}
DEVI void mma_c(f32x4& d, const v4u& a, const v4u& b, const f32x4& c) { asm volatile("v_mfma_f32_16x16x32_bf16 %0, %1, %2, %3" : "=&v"(d) : "v"(a), "v"(b), "v"(c)); }
DEVI void mma_t(f32x4& d, const v4u& a, const v4u& b, const f32x4& c) { asm volatile("v_mfma_f32_16x16x32_bf16 %0, %1, %2, %3" : "+v"(d) : "v"(a), "v"(b), "v"(c)); }
DEVI unsigned cvtv(float a, float b) { unsigned d; asm volatile("v_cvt_pk_bf16_f32 %0, %1, %2" : "=v"(d) : "v"(a), "v"(b)); return d; }

__global__ __launch_bounds__(512, 2) void fused(Params p) {
    const unsigned char* pc0;
    asm volatile("s_getpc_b64 %0" : "=s"(pc0));
    const int w = __builtin_amdgcn_readfirstlane(threadIdx.x >> 6);
    const int b = blockIdx.x, xcd = b & 7, idx = b >> 3;
    const int i = 8 * (xcd & 1) + (idx & 7), kq = xcd >> 1, q = idx >> 3, j = 4 * kq + q;
    const bool heavy = q != 3;
    const int N1 = heavy ? 20 : 12;
    const int lw = w & 1, pr = w & 3;
    const int rot = ROT1;
    const __amdgpu_buffer_rsrc_t rws = __builtin_amdgcn_make_buffer_rsrc(p.ws, 0, WS_END, 0x00020000);
    auto unit_of = [&](int v) { return v < 4 ? 7 * kq + v : v < 8 ? 28 + 8 * ((v >> 1) & 1) + 2 * kq + (v & 1) : 7 * kq + v - 4; };
    const unsigned sl_lo = L_P + (unsigned)pr * SLOT;
    const unsigned xo = WS_XB + (unsigned)((i * 4 + q) * 4) * 24576u + pr * 6144;
    const unsigned fo = WS_FLAG + (unsigned)(i * 16 + j) * 512u;

    if (w >= 4) {
        LANE_VARS;
        STAMP_INIT();
        STAMP(0);
        const unsigned swz = (unsigned)((p8 ^ (q8 >> 1)) << 4);
        const unsigned fa = F_DONE + (lane & 3) * 4;
        unsigned sink = 0, tg;
        auto poll16 = [&](int which) __attribute__((always_inline)) {
            wait_ge(F_OUT + (lane & 3) * 4, which + 1);
            STAMP(16);
            const unsigned pf = WS_FLAG + (unsigned)(i * 16 + 4 * kq + (lane & 3)) * 512u + which * 128 + ((lane >> 2) & 3) * 8;
            NOUNROLL for (int spin = 0; spin < SPIN_MAX; spin++) {
                fence_c();
                const v2u f = __builtin_amdgcn_raw_buffer_load_b64(rws, pf, 0, 16);
                const unsigned bad = (unsigned)__builtin_amdgcn_ballot_w64(f.x != tg || f.y != ~tg);
                if (bad == 0) break;
                if ((bad & 0x7777u) == 0) { if (which) break; postc<1>(F_RDY, lane); }
                __builtin_amdgcn_s_sleep(FL_SLEEP);
            }
            postc<2>(F_RDY + which * 4, lane);
            STAMP(17);
        };
        if (w < 6) {
            unsigned ffull = F_FULL + lw * 4;
            asm volatile("" : "+v"(ffull));
            touch(sink, lw ? p.Bim : p.Bre, (32 * j + 2 * i) * 1024 + c * 128);
            bar_l();
            {
                const unsigned voffS = (8 * (q8 >> 2) + (q8 & 3)) * 1024 + swz;
                const v4u srd = make_srd((const unsigned char*)(lw ? p.Bim : p.Bre) + (size_t)(32 * j) * 1024);
                wait_ge(F_XI + (lane & 3) * 4, 1);
                NOUNROLL for (int n = 0; n < 8; n++) {
                    const unsigned so = 128 * ((n + rot) & 7);
                    dma4(srd, voffS, voffS ^ 64u, so, so + 15360, so + 2048, so + 17408, L_W + (n >> 1) * SLOT + lw * 8192 + (n & 1) * 4096);
                    waitv<8>(); postv(ffull, lane, max(n - 1, 0) >> 1);
                }
                asm volatile("" ::"v"(sink));
            }
            STAMP(1);
            const unsigned voffG = q8 * (FF * 2) + (((p8 >> 1) ^ ((q8 >> 1) & 1)) << 5) + ((p8 & 1) << 4), voffG1 = voffG ^ 64u;
            const unsigned voffC = q8 * 1024 + swz, voffC1 = voffC ^ 64u;
            const unsigned voffD = q8 * 1024 + (((p8 >> 1) ^ (q8 >> 1) ^ (2 * (kq & 1))) << 5) + ((p8 & 1) << 4);
            const unsigned char* gb = (const unsigned char*)(lw ? p.wu : p.wg) + (7 * kq + q) * 128;
            unsigned thr = (lane >> 2) + 1;
            int credit = 0, cbase = 0;
            auto item = [&](int n, const unsigned char* b0, const unsigned char* b1, unsigned ve, unsigned vo, unsigned st) __attribute__((always_inline)) {
                v4u s0 = make_srd(b0), s1 = make_srd(b1);
                unsigned dst = ((n & 4) ? L_P : L_W) + (n & 3) * SLOT + lw * 8192, dst1 = dst + 4096;
                asm volatile("" : "+s"(s0), "+s"(s1), "+s"(dst), "+s"(dst1));
                int sp = LSPIN_MAX;
                NOUNROLL while (n - 7 > credit) {
                    const unsigned dn = peek(fa);
                    postv(ffull, lane, n - ((vm_outstanding() + 7) >> 3));
                    unsigned long long mk = __builtin_amdgcn_ballot_w64(dn >= thr);
                    mk &= mk >> 1; mk &= mk >> 2;
                    credit = __builtin_popcountll(mk & 0x1111111111111111ull) + cbase;
                    asm volatile("s_sub_i32 %0, %0, 1" : "+s"(sp)::"scc");
                    if (sp < 0) break;
                }
                waitv<INFL>();
                dma4(s0, ve, vo, 0, st - 1024, 2 * st - 2048, 3 * st - 3072, dst);
                postv(ffull, lane, n - (INFL + 7) / 8);
                waitv<INFL>();
                dma4(s1, ve, vo, 0, st - 1024, 2 * st - 2048, 3 * st - 3072, dst1);
            };
            wait_ge(F_XI + (lane & 3) * 4, 3);
            NOUNROLL for (int n = 4; n < N1; n++) {
                const int t = n - 4;
                const unsigned char* bp = gb + (size_t)(((t + rot) & 7) * 64) * (FF * 2) + (t >> 3) * 512;
                item(n, bp, bp + 32 * FF * 2, voffG, voffG1, 8 * FF * 2);
            }
            cbase = N1 - 8; thr += cbase;
            const unsigned char* dp = (const unsigned char*)p.wd + (size_t)(7 * kq) * 65536 + q * 256 + (lw ^ (kq >> 1)) * 128;
            NOUNROLL for (int v = 0; v < 7; v++) {
                if (v >= 4) {
                    const unsigned char* cbp = (const unsigned char*)((v & 2) ? p.Cim : p.Cre) + (size_t)q * 131072 + (2 * kq + (v & 1)) * 128;
                    item(N1 + v, cbp + ((2 * lw) ^ kq) * 32768, cbp + ((2 * lw + 1) ^ kq) * 32768, voffC, voffC1, 8192);
                } else {
                    item(N1 + v, dp, dp + 32768, voffD, voffD, 8192);
                    dp += 65536;
                }
            }
            waitv<16>(); postv(ffull, lane, N1 + 5);
            waitv<8>(); postv(ffull, lane, N1 + 6);
            waitv<0>(); postv(ffull, lane, N1 + 11);
            tg = peek(L_TAG);
            STAMP(7);
            if (lw) {
                const unsigned pf = WS_FLAG + (unsigned)(i * 16 + 4 * (lane & 3) + q) * 512u + 256 + ((lane >> 2) & 1) * 8;
                NOUNROLL for (int spin = 0; spin < SPIN_MAX; spin++) {
                    fence_c();
                    const v2u f = __builtin_amdgcn_raw_buffer_load_b64(rws, pf, 0, 16);
                    if (__builtin_amdgcn_ballot_w64(f.x != tg || f.y != ~tg) == 0) break;
                    __builtin_amdgcn_s_sleep(16);
                }
            }
            if (lw) poll16(1);
        } else {
            unsigned tag0, prm0, prm1, prm2;
            const unsigned cto = min(idx * 11 + 4 * lw + p8, (int)CODE_LINES - 1) * 128;
            touch(sink, pc0, cto);
            touch(sink, lw ? (const void*)p.ws : (const void*)p.Dsk, lw ? ((lane & 4) ? WS_GRAN + (j * 16 + i) * 512 : fo) + (lane & 3) * 128 : p8 * 128);
            if (!heavy) __builtin_amdgcn_s_sleep(LT_SLEEP);
            NOUNROLL for (int t = heavy ? 4 : 0; t < 4; t++)
                touch(sink, lw ? p.wu : p.wg, (32 * i + (lane & 31)) * (FF * 2) + (7 * kq + min(2 * t + (lane >> 5), 6)) * 128);
            const unsigned po = 2 * (32 * j + 8 * (lane >> 4) + 4 * ((lane >> 1) & 1) + ((lane >> 2) & 3));
            asm volatile("global_load_ushort %0, %4, %5\n\tglobal_load_ushort %1, %4, %6\n\tglobal_load_ushort %2, %4, %7\n\tglobal_load_dword %3, %8, %9 sc1"
                         : "=&v"(prm0), "=&v"(prm1), "=&v"(prm2), "=&v"(tag0) : "v"(po), "s"(p.lre), "s"(p.lim), "s"(p.ldt), "v"(WS_EPOCH), "s"(p.ws) : "memory");
            *(LAS unsigned*)(uintptr_t)(F_DONE + (lane & 31) * 4) = 0u;
            bar_l();
            sts128(L_SEL + lw * 1024 + lane * 16, make_sel(lane, lw));
            asm volatile("s_waitcnt vmcnt(0)" : "+v"(prm0), "+v"(prm1), "+v"(prm2), "+v"(tag0)::"memory");
            tg = tag0 + 1u;
            const bool odd = lane & 1;
            const unsigned la = lane * 4;
            const float lr = bflo(prm0), li = bflo(prm1);
            float dt = __expf(bflo(prm2));
            dt = bflo(pk2(dt, dt));
            const float ex = __expf(dt * lr);
            const float fr = __builtin_amdgcn_fractf(dt * li * 0.15915494309189535f);
            const float ar = smul_t(ex, __builtin_amdgcn_cosf(fr)), ai = smul_t(ex, __builtin_amdgcn_sinf(fr));
            const cpx a1 = {ar, ai};
            const cpx a2 = cmul(a1, a1), a4 = cmul(a2, a2);
            const unsigned sg = odd ? 0u : 0x80000000u;
            auto sgn = [&](cpx v) { return cpx{v.x, __uint_as_float(__float_as_uint(v.y) ^ sg)}; };
            const cpx r8 = cmul(a4, a4), r16 = cmul(r8, r8), r32 = cmul(r16, r16), r64 = cmul(r32, r32);
            const cpx a32 = sgn(r32);
            {
                const cpx r = lw ? r32 : r8, rs = sgn(r);
                float v = odd ? r.y : r.x;
                sts32f(L_PW + lw * 1024 + la, odd ? 0.0f : 1.0f);
#pragma unroll
                for (int s = 1; s < 4; s++) { sts32f(L_PW + lw * 1024 + s * 256 + la, v); if (s < 3) v = lmad(rs, v, 0.0f); }
            }
            if (lw == 0) {
                const float nr = ar - 1.0f, den = smul_t(__builtin_amdgcn_rcpf(sfma(lr, lr, smul(li, li))), 2.0f);
                float co = smul(odd ? sfms(ai, lr, smul(nr, li)) : sfma(nr, lr, smul(ai, li)), den);
                sts32f(L_CST + la, odd ? a1.y : a1.x); sts32f(L_CST + 256 + la, odd ? a4.y : a4.x);
                const cpx a1s = sgn(a1);
#pragma unroll
                for (int t = 0; t < 5; t++) { sts32f(L_CST + (2 + t) * 256 + la, co); if (t < 4) co = lmad(a1s, co, 0.0f); }
                *(LAS unsigned*)(uintptr_t)L_TAG = tg;
            }
            post(F_COMM + lw * 4, 1);
            STAMP(1);
            cpx wn[8];
            {
                const cpx r128 = cmul(r64, r64), r256 = cmul(r128, r128);
                wn[0] = lw ? r128 : cpx{1.0f, 0.0f};
#pragma unroll
                for (int n = 1; n < 8; n++) wn[n] = cmul(wn[n - 1], r256);
#pragma unroll
                for (int n = 0; n < 8; n++) { wn[n] = sgn(wn[n]); asm volatile("" : "+v"(wn[n].x), "+v"(wn[n].y)); }
            }
            unsigned gro = WS_GRAN + j * 8192 + lane * 8, gro2 = gro + i * 512;
            unsigned to0 = lw ? min((unsigned)(lane & 31) * 0x98000u + idx * 0x4000u, WS_END - 4) : (lane & 3) * 0x100000 + idx * 0x4000;
            unsigned to1 = (448 * kq + 28 * i + min(lane, 27)) * 1024 + (2 * q + lw) * 128, to2 = (128 * q + 8 * i + p8) * 1024 + (2 * kq + (q8 & 1)) * 128;
            unsigned po2 = WS_XB + (unsigned)((i * 4 + q) * 4 + kq) * 24576u + lw * 12288 + lane * 16;
            unsigned voffC = q8 * 1024 + swz, voffC1 = voffC ^ 64u;
            unsigned voffD = q8 * 1024 + (((p8 >> 1) ^ (q8 >> 1) ^ (2 * (kq & 1))) << 5) + ((p8 & 1) << 4);
            v4u none = {~0u, ~0u, ~0u, ~0u};
            asm volatile("" : "+v"(gro), "+v"(gro2), "+v"(to0), "+v"(to1), "+v"(to2), "+v"(po2), "+v"(voffC), "+v"(voffC1), "+v"(voffD), "+v"(none));
            wait_ge(F_XI + (lane & 3) * 4, heavy ? 2 : 0);
            touch(sink, p.wd, to1);
            touch(sink, lw ? p.Cim : p.Cre, to2);
            touch(sink, lw ? (const void*)p.ws : (const void*)p.out, to0);
            v4u wr[4][8];
            {
                const v4u sc = make_srd((const unsigned char*)p.Cim + (size_t)q * 131072 + (2 * kq + 1) * 128);
                const v4u sd = make_srd((const unsigned char*)p.wd + (size_t)(7 * kq + 4) * 65536 + q * 256 + (lw ^ (kq >> 1)) * 128);
                waitv<0>();
                sfor<0, 32>(LAMB(ff) {
                    constexpr int f = decltype(ff)::value, m = f & 7;
                    if constexpr (f < 8) ldW(wr[0][m], (m & 1) ? voffC1 : voffC, sc, ((2 * lw + (m >> 2)) ^ kq) * 32768 + (m & 3) * 8192);
                    else ldW(wr[f >> 3][m], voffD, sd, (f - 8) * 8192);
                    if constexpr (f % 3 == 2) __builtin_amdgcn_s_sleep(WR_SLEEP);
                });
            }
            auto clear = [&](int n0, int n1) __attribute__((always_inline)) {
                const v4u sws = make_srd(p.ws);
                NOUNROLL for (int n = n0; n < n1; n++) { stX<0>(none, po2, sws, n * 1024); __builtin_amdgcn_s_sleep(PF_SLEEP); }
            };
            STAMP(3);
            wait_ge(fa, 4);
            STAMP(4);
            if (lw) {
                float e[4];
#pragma unroll
                for (int s = 0; s < 4; s++) e[s] = lds32f(L_TOT + s * 256 + la);
                const float p2 = lmad(a32, e[0], e[1]), p3 = lmad(a32, p2, e[2]);
                __builtin_amdgcn_raw_buffer_store_b64(v2u{__float_as_uint(lmad(a32, p3, e[3])), tg}, rws, gro2, 0, 16);
                sts32f(L_PRE + la, 0.0f); sts32f(L_PRE + 256 + la, e[0]); sts32f(L_PRE + 512 + la, p2); sts32f(L_PRE + 768 + la, p3);
            }
            STAMP(5);
            clear(0, PF_N);
            __builtin_amdgcn_s_sleep(LB_FIRST);
            {
                const int top = i - 1 - lw;
                v2u gr[8] = {};
                NOUNROLL for (int spin = 0; spin < SPIN_MAX; spin++) {
                    fence_c();
#pragma unroll
                    for (int n = 0; n < 8; n++) gr[n] = __builtin_amdgcn_raw_buffer_load_b64(rws, gro, ((top - 2 * n) & 15) * 512, 16);
                    unsigned long long bad = 0;
#pragma unroll
                    for (int n = 0; n < 8; n++) if (top - 2 * n >= 0) bad |= __builtin_amdgcn_ballot_w64(gr[n].y != tg);
                    if (bad == 0) break;
                    __builtin_amdgcn_s_sleep(LB_SLEEP);
                }
                float h = 0;
#pragma unroll
                for (int n = 7; n >= 0; n--) if (top - 2 * n >= 0) h = lmad(wn[n], __uint_as_float(gr[n].x), h);
                sts32f(L_XCH + 256 + lw * 256 + la, h);
            }
            postc<4>(F_COMM + lw * 4, lane);
            STAMP(6);
            clear(PF_N, 12);
            waitv<0>();
            asm volatile("" ::"v"(sink));
            if (lane == 0) __builtin_amdgcn_raw_buffer_store_b64(v2u{tg, ~tg}, rws, fo + 256 + lw * 8, 0, 16);
            STAMP(7);
#pragma unroll
            for (int u = 0; u < 4; u++) asm volatile("" : "+v"(wr[u][0]), "+v"(wr[u][1]), "+v"(wr[u][2]), "+v"(wr[u][3]), "+v"(wr[u][4]), "+v"(wr[u][5]), "+v"(wr[u][6]), "+v"(wr[u][7]));
            STAMP(13);
            if (!lw) poll16(0);
            {
                unsigned wa0 = lw * 8192 + lane * 16 + L_P, wa1 = wa0 + (L_W + 3 * SLOT - L_P);
                asm volatile("" : "+v"(wa0), "+v"(wa1));
                sfor<0, 4>(LAMB(uu) {
                    constexpr int u = decltype(uu)::value;
                    wait_ge(fa, N1 + u);
#pragma unroll
                    for (int m = 0; m < 8; m++) sts128((u ? wa0 + (u - 1) * SLOT : wa1) + m * 1024, wr[u][m]);
                    postv(F_COMM + lw * 4, lane, N1 + 8 + u);
                });
            }
            STAMP(15);
        }
        f32x4 base[4];
        {
            const int d = 128 * q + 32 * kq + 4 * p8;
            const v2u dv = *(const GAS v2u*)(p.Dsk + d);
            const float df[4] = {bflo(dv.x), bfhi(dv.x), bflo(dv.y), bfhi(dv.y)};
            v2u xv[4];
#pragma unroll
            for (int e = 0; e < 4; e++) xv[e] = *(const GAS v2u*)(p.x + (size_t)(128 * i + 32 * pr + 8 * e + q8) * DM + d);
#pragma unroll
            for (int e = 0; e < 4; e++) {
                const unsigned tp = (32 * pr + 8 * e + 4 * ((q8 >> 1) & 1) + 2 * (q8 & 1) + (q8 >> 2)) * 4;
                const float rs = lds32f(L_RSTD + tp), nm = smul(lds32f(L_MEAN + tp), rs);
                const float xf[4] = {bflo(xv[e].x), bfhi(xv[e].x), bflo(xv[e].y), bfhi(xv[e].y)};
#pragma unroll
                for (int k = 0; k < 4; k++) base[e][k] = sfma(sfma(xf[k], rs, nm), df[k], xf[k]);
            }
        }
        STAMP(8);
        const unsigned xi = xo + kq * 24576u + lane * 16;
        const unsigned sc = L_O + pr * 4096;
        unsigned wa = sc + (8 * (c >> 2) + 4 * (c & 1) + ((c >> 1) & 1)) * 128 + ((g ^ (c & 7)) << 4), ra = sc + q8 * 128 + ((p8 ^ (2 * (q8 & 1) + (q8 >> 2))) << 4);
        GAS float* op = (GAS float*)p.out + (size_t)(128 * i + 32 * pr + q8) * DM + 128 * q + 32 * kq + 4 * p8;
        asm volatile("" : "+v"(wa), "+v"(ra), "+v"(op));
        wait_ge(F_SENT + pr * 4, 3);
        __builtin_amdgcn_s_sleep(X_FIRST);
        v4u in[6];
        f32x4 own[4];
        NOUNROLL for (int spin = 0; spin < SPIN_MAX; spin++) {
            fence_c();
#pragma unroll
            for (int n = 0; n < 6; n++) in[n] = __builtin_amdgcn_raw_buffer_load_b128(rws, xi + n * 1024, 0, 16);
            SB();
            wait_ge(F_SENT + pr * 4, 4);
#pragma unroll
            for (int e = 0; e < 4; e++) own[e] = lds128f(sc + e * 1024 + lane * 16);
            unsigned mx = 0;
#pragma unroll
            for (int n = 0; n < 6; n++) mx = max(mx, max(max(in[n].x, in[n].y), max(in[n].z, in[n].w)));
            if (__builtin_amdgcn_ballot_w64(mx == ~0u) == 0) break;
        }
        STAMP(11);
#pragma unroll
        for (int e = 0; e < 4; e++)
#pragma unroll
            for (int sr = 0; sr < 3; sr++) { const unsigned d0 = in[2 * sr + (e >> 1)][2 * (e & 1)], d1 = in[2 * sr + (e >> 1)][2 * (e & 1) + 1]; own[e] += f32x4{bflo(d0), bfhi(d0), bflo(d1), bfhi(d1)}; }
        asm volatile("" : "+v"(own[0]), "+v"(own[1]), "+v"(own[2]), "+v"(own[3]));
#pragma unroll
        for (int e = 0; e < 4; e++) sts128f((wa ^ (unsigned)((e >> 1) << 6)) + (e & 1) * 256, own[e]);
        fence_c();
        f32x4 ta[4];
#pragma unroll
        for (int e = 0; e < 4; e++) ta[e] = lds128f((ra ^ (unsigned)((e & 1) << 6)) + e * 1024);
        SB();
#pragma unroll
        for (int e = 0; e < 4; e++) *(GAS f32x4*)(op + e * 8 * DM) = base[e] + ta[e];
        if (b == 255 && w == 4 && lane == 0) __builtin_amdgcn_raw_buffer_store_b32(tg, rws, WS_EPOCH, 0, 16);
        STAMP(12); STAMP_DUMP();
        return;
    }
    constexpr int PD = PDV, XL = XLV, EG = EGATE;
    unsigned stall = 0; (void)stall;
    v4u xa[16][2];
#define AF(u, k) xa[2 * (u) + ((k) >> 1)][(k) & 1]
    v4u sws = make_srd(p.ws);
    unsigned oG = WS_A2 + (unsigned)(i * 44 + 7 * kq) * 16384u + w * 4096, oC = WS_A2 + (unsigned)(i * 44 + 28 + 2 * kq) * 16384u + w * 4096, oF = fo + w * 8;
    asm volatile("" : "+s"(sws), "+s"(oG), "+s"(oC), "+s"(oF));
    auto uoff = [&](int v) __attribute__((always_inline)) { return v < 4 ? oG + v * 16384 : v < 8 ? oC + (8 * ((v >> 1) & 1) + (v & 1)) * 16384 : oG + (v - 4) * 16384; };
    LANE_VARS;
    unsigned fdone = F_DONE + w * 4, l16 = lane * 16;
    asm volatile("" : "+v"(fdone), "+v"(l16));
    unsigned tgs = 0; int raised = 0;
    auto raise = [&](int level) __attribute__((always_inline)) {
        asm volatile("s_mov_b64 exec, 1\n\tbuffer_store_dwordx2 %0, off, %1, %2 offset:%3 sc1\n\ts_mov_b64 exec, -1" ::"v"(v2u{tgs, ~tgs}), "s"(sws), "s"(oF), "n"((level - 1) * 128) : "memory");
        post(fdone + (F_OUT - F_DONE), level);
        raised = level;

        if (level == 1) STAMP(17); else STAMP(18);
    };
    auto try_raise = [&](int level) __attribute__((always_inline)) {
        unsigned v; asm volatile("s_getreg_b32 %0, hwreg(HW_REG_IB_STS, 0, 4)" : "=s"(v)::"memory");
        if (__builtin_expect((int)(v + raised) == level - 1, 0)) raise(level);
    };
    unsigned oX[3];
#pragma unroll
    for (int sl = 1; sl < 4; sl++) { oX[sl - 1] = xo + (kq ^ sl) * 24576u + (sl - 1) * 2048; asm volatile("" : "+s"(oX[sl - 1])); }
    unsigned cbs[2], tbs[4];
    v4u bf[PD];
    const unsigned ffl = F_FULL + (lane & 1) * 4;
    auto set2 = [&](auto kk) __attribute__((always_inline)) {
        constexpr int k = decltype(kk)::value;
        if constexpr (k == 0) tbs[0] = L_P + ((l16 << 1) & 0x780) + ((l16 >> 1) & 0x18) + (((l16 >> 2) & 0x40) | ((l16 >> 2) & 0x20));
        else if constexpr (k < 4) tbs[k] = tbs[0] ^ (unsigned)(k << 5);
        else if constexpr (k == 4) cbs[0] = L_W + ((l16 << 3) & 0x780) + (((l16 >> 4) ^ (l16 >> 1)) & 0x70);
        else cbs[1] = cbs[0] ^ 64u;
        if constexpr (k < 4) asm volatile("" : "+v"(tbs[k])); else asm volatile("" : "+v"(cbs[k - 4]));
    };
    auto prime2 = [&](auto mm) __attribute__((always_inline)) { constexpr int m = decltype(mm)::value; const v2u lo = ldstr(tbs[m]), hi = ldstr(tbs[m] + 2048); bf[m] = v4u{lo.x, lo.y, hi.x, hi.y}; };
    {
        STAMP_INIT();
        STAMP(0);
        unsigned cb[2];
        cb[0] = (unsigned)c * 128u + (unsigned)((g ^ (c >> 1)) << 4); cb[1] = cb[0] ^ 64u;
        {
            const unsigned voffX = (8 * (q8 >> 2) + 4 * (q8 & 1) + ((q8 >> 1) & 1)) * 1024 + ((p8 ^ (q8 >> 1)) << 4), voffX1 = voffX ^ 64u;
            const v4u srd = make_srd((const unsigned char*)p.x + (size_t)(128 * i + 32 * w) * 1024 - 4096);
            auto issue_x = [&](int t) __attribute__((always_inline)) {
                const unsigned s0 = (t >> 3) * 2048 + ((t + rot) & 7) * 128, s1 = (t >> 3) * 2048 + ((t + 1 + rot) & 7) * 128 + 2048;
                dma4(srd, voffX, voffX1, s0 + 4096, s0 + 4096 + 15360, s1, s1 + 15360, sl_lo + (t & 7) * 2048);
            };
            asm volatile("s_mov_b32 m0, %3\n\ts_nop 0\n\tbuffer_load_dword %0, %1, %2 offen lds" ::"v"(p8 * 128), "s"(srd), "s"(4096 + (4 * (j >> 1) + 2 + (j & 1)) * 1024), "s"(L_O + w * 256) : "memory");
            issue_x(0); issue_x(2); issue_x(4); issue_x(6);
            bar_l();
            const v4u sel0 = make_sel(lane, 0), sel1 = make_sel(lane, 1);
            v4u ones = {0x3f803f80u, 0x3f803f80u, 0x3f803f80u, 0x3f803f80u};
            asm volatile("" : "+v"(ones));
            f32x4 sm[2], sq[2], nm4[2], ct[6];
            const unsigned xl0 = cb[0] + sl_lo, xl1 = cb[1] + sl_lo;
            unsigned sa = L_RSTD + (32 * w + 8 * g + (c & 3)) * 4;
            const unsigned long long dgm = __builtin_amdgcn_ballot_w64(g == (c >> 2));
            asm volatile("" : "+v"(sa));
            auto stats = [&](int bb) __attribute__((always_inline)) {
                float s01, s23, sd;
                asm("v_cndmask_b32 %0, %1, %2, %3" : "=v"(s01) : "v"(sq[bb].x), "v"(sq[bb].y), "s"(0xaaaaaaaaaaaaaaaaull));
                asm("v_cndmask_b32 %0, %1, %2, %3" : "=v"(s23) : "v"(sq[bb].z), "v"(sq[bb].w), "s"(0xaaaaaaaaaaaaaaaaull));
                asm("v_cndmask_b32 %0, %1, %2, %3" : "=v"(sd) : "v"(s01), "v"(s23), "s"(0xccccccccccccccccull));
                const float rstd = __builtin_amdgcn_rsqf(fmaxf(sd * (1.0f / DM) - nm4[bb].x * nm4[bb].x, 0.0f) + 1e-5f);
                asm volatile("s_mov_b64 exec, %3\n\tds_write2_b32 %0, %1, %2 offset0:%4 offset1:%5\n\ts_mov_b64 exec, -1" ::"v"(sa), "v"(rstd), "v"(nm4[bb].x), "s"(dgm), "n"(4 * bb), "n"(L_MEAN / 4 + 4 * bb) : "memory");
            };
            auto centre = [&](auto kk) __attribute__((always_inline)) {
                constexpr int k = decltype(kk)::value, e = k - 4;
                if constexpr (k < 64) mma_c(ct[k % 6], (k & 1) ? sel1 : sel0, xa[(k >> 1) & 15][k >> 5], nm4[k >> 5]);
                if constexpr (e >= 0) {
                    xa[(e >> 1) & 15][e >> 5][2 * (e & 1)] = cvtv(ct[e % 6].x, ct[e % 6].y);
                    xa[(e >> 1) & 15][e >> 5][2 * (e & 1) + 1] = cvtv(ct[e % 6].z, ct[e % 6].w);
                }
                SB();
            };
            SB();
            sfor<0, 17>(LAMB(tt) {
                constexpr int t = decltype(tt)::value, u = t - 1;
                if constexpr (t < 16) {
                    constexpr int t5 = ((t + 1) & ~1) + 5, top = t5 < 7 ? 7 : t5 > 15 ? 15 : t5;
                    waitv<2 * (top - t)>();
                    xa[2 * (t & 7)][t >> 3] = lds128(xl0 + (t & 7) * 2048); xa[2 * (t & 7) + 1][t >> 3] = lds128(xl1 + (t & 7) * 2048);
                }
                SB();
                if constexpr (t == 9) { asm volatile("s_nop 3"); SB(); nm4[0] = sm[0] * (-1.0f / DM); asm volatile("s_nop 1" : "+v"(nm4[0])); SB(); }
                if constexpr (t >= 9) { centre(IC<4 * (t - 9)>{}); centre(IC<4 * (t - 9) + 1>{}); centre(IC<4 * (t - 9) + 2>{}); centre(IC<4 * (t - 9) + 3>{}); }
                if constexpr (u >= 0) {
                    mma<(u & 7) == 0>(sm[u >> 3], ones, xa[2 * (u & 7)][u >> 3]); mma<(u & 7) == 0>(sq[u >> 3], xa[2 * (u & 7)][u >> 3], xa[2 * (u & 7)][u >> 3]);
                    mma<false>(sm[u >> 3], ones, xa[2 * (u & 7) + 1][u >> 3]); mma<false>(sq[u >> 3], xa[2 * (u & 7) + 1][u >> 3], xa[2 * (u & 7) + 1][u >> 3]);
                }
                if constexpr ((t & 1) == 0 && t >= 2 && t + 6 < 16) issue_x(t + 6);
                if constexpr (t == XI_STEP) post(fdone + (F_XI - F_DONE), 1);
                if constexpr (t == XI2_STEP) post(fdone + (F_XI - F_DONE), 2);
                SB();
            });
            post(fdone + (F_XI - F_DONE), 3);
            STAMP(1);
            stats(0);
            SB();
            nm4[1] = sm[1] * (-1.0f / DM); asm volatile("s_nop 1" : "+v"(nm4[1]));
            SB();
            sfor<32, 64>(LAMB(kk) { centre(kk); });
            stats(1);
            SB();
            sfor<64, 68>(LAMB(kk) { centre(kk); });
        }
        STAMP(2);
        f32x4 eX;
        unsigned tb[4];
        {
            cb[0] += L_W; cb[1] += L_W;
            asm volatile("" : "+v"(cb[0]), "+v"(cb[1]));
            f32x4 acc[4][2];
            unsigned fl = 0; bool lack = false;
            {
                const int q4 = c >> 2, p4 = c & 3;
                const unsigned tb1 = L_P + (unsigned)(8 * g + q4) * 128u + (unsigned)((((q4 >> 1) & 1) | ((g & 1) << 1)) << 5) + p4 * 8;
#pragma unroll
                for (int t = 0; t < 4; t++) { tb[t] = tb1 ^ (unsigned)(t << 5); asm volatile("" : "+v"(tb[t])); }
            }
            f32x4 ca, c4, co, c8, rs0, rs1;
            wait_ge(F_FULL + (lane & 3) * 4, 1);
            STAMP(3);
            sfor<-PD, 64>(LAMB(nn) {
                constexpr int n = decltype(nn)::value, m = n + PD;
                v4u cur;
                if constexpr (n >= 0) { cur = bf[n % PD]; mma<(n < 4)>(acc[n & 3][0], xa[n >> 2][0], cur); SB(); }
                if constexpr (m > 0 && (m & 15) == 0) need_full(lack, ffl, (m >> 4) + 1);
                if constexpr (m < 64) bf[m % PD] = lds128(cb[(m >> 2) & 1] + wslot(m >> 4) + (((m >> 1) & 1) * 4 + ((m >> 3) & 1) * 2 + (m & 1)) * 2048);
                else { const unsigned a = tb[(m >> 1) & 3] + wslot(0) + (m & 1) * 8192; const v2u lo = ldstr(a), hi = ldstr(a + 512); bf[m % PD] = v4u{lo.x, lo.y, hi.x, hi.y}; }
                SB();
                if constexpr (n >= 0) { mma<(n < 4)>(acc[n & 3][1], xa[n >> 2][1], cur); SB(); }
                if constexpr ((m & 15) == 10) fl = peek(ffl);
                if constexpr ((m & 15) == 14) lack = any_lt(fl, (m >> 4) + 2);
                if constexpr ((m & 15) == 15 && m < 63) post(fdone, (m >> 4) + 1);
                if constexpr (m == 52) ca = lds128f(L_CST + c * 16);
                if constexpr (m == 53) c4 = lds128f(L_CST + 256 + c * 16);
                if constexpr (m == 54) co = lds128f(L_CST + 512 + c * 16);
                if constexpr (m == 57) c8 = lds128f(L_PW + 256 + c * 16);
                if constexpr (m == 55) rs0 = lds128f(L_RSTD + (32 * w + 8 * g) * 4);
                if constexpr (m == 56) rs1 = lds128f(L_RSTD + (32 * w + 8 * g + 4) * 4);
                SB();
            });
            mfma_done();
            STAMP(4); VAL(24, stall); stall = 0;
            f32x4 ev;
            f32x2 sr[2][4], si[2][4];
#pragma unroll
            for (int st = 0; st < 2; st++) {
                const f32x4 r0 = acc[st][0] * rs0, r1 = acc[st][1] * rs1, i0 = acc[2 + st][0] * rs0, i1 = acc[2 + st][1] * rs1;
                sr[st][0] = lo2(r0); sr[st][1] = hi2(r0); sr[st][2] = lo2(r1); sr[st][3] = hi2(r1);
                si[st][0] = lo2(i0); si[st][1] = hi2(i0); si[st][2] = lo2(i1); si[st][3] = hi2(i1);
            }
            const f32x2 a1[2] = {lo2(ca), hi2(ca)}, cf[2] = {lo2(co), hi2(co)};
#pragma unroll
            for (int t = 1; t < 4; t++) {
                f32x2 tr[2], ti[2];
#pragma unroll
                for (int st = 0; st < 2; st++) { tr[st] = bfma<1, 1>(a1[st], si[st][t - 1], sr[st][t]); ti[st] = bfma<1, 0>(a1[st], sr[st][t - 1], si[st][t]); }
                SB();
#pragma unroll
                for (int st = 0; st < 2; st++) { sr[st][t] = bfma<0, 0>(a1[st], sr[st][t - 1], tr[st]); si[st][t] = bfma<0, 0>(a1[st], si[st][t - 1], ti[st]); }
                SB();
            }
#pragma unroll
            for (int st = 0; st < 2; st++) {
                ev[2 * st] = sfma(c4[2 * st], sr[st][3].x, sfnma(c4[2 * st + 1], si[st][3].x, sr[st][3].y));
                ev[2 * st + 1] = sfma(c4[2 * st], si[st][3].x, sfma(c4[2 * st + 1], sr[st][3].x, si[st][3].y));
                eX[2 * st] = sr[st][3].x; eX[2 * st + 1] = si[st][3].x;
            }
            sts128f(L_SEG + w * 1024 + l16, ev);
            fence_c();
            f32x4 es[4];
#pragma unroll
            for (int s = 0; s < 4; s++) es[s] = lds128f(L_SEG + w * 1024 + s * 256 + (l16 & 0xf0));
            SB();
            unsigned ya = L_O + w * 4096 + l16;
            asm volatile("" : "+v"(ya), "+v"(eX));
#pragma unroll
            for (int st = 0; st < 2; st++) {
                f32x2 tr[4], ti[4];
#pragma unroll
                for (int t = 0; t < 4; t++) { tr[t] = bmul<1, 1>(cf[st], si[st][t]); ti[t] = bmul<1, 1>(cf[st], sr[st][t]); }
                SB();
#pragma unroll
                for (int t = 0; t < 4; t++) { tr[t] = bfma<0, 0>(cf[st], sr[st][t], tr[t]); ti[t] = bfma<0, 1>(cf[st], si[st][t], ti[t]); }
                SB();
                if (st == 0) {
#pragma unroll
                    for (int s = 1; s < 4; s++) { const f32x2 u0 = cmadp(lo2(c8), lo2(es[s - 1]), lo2(es[s])), u1 = cmadp(hi2(c8), hi2(es[s - 1]), hi2(es[s])); es[s] = f32x4{u0.x, u0.y, u1.x, u1.y}; }
                    sts128f(L_TOT + w * 256 + (l16 & 0xf0), es[3]);
                    post(fdone, 4);
                    f32x4 pin;
#pragma unroll
                    for (int k = 0; k < 4; k++) { const float lo = g == 1 ? es[0][k] : 0.0f, hi = g == 3 ? es[2][k] : es[1][k]; pin[k] = g >= 2 ? hi : lo; }
                    sts128f(L_SEG + w * 1024 + l16, pin);
                    SB();
                }
                sts128(ya + st * 1024, v4u{pk2(tr[0]), pk2(tr[1]), pk2(tr[2]), pk2(tr[3])});
                sts128(ya + 2048 + st * 1024, v4u{pk2(ti[0]), pk2(ti[1]), pk2(ti[2]), pk2(ti[3])});
                SB();
            }
        }
        STAMP(5);
        unsigned go = oG + q * 16384, so = oC + (q >> 1) * 16384 + (q & 1) * 2048;
        asm volatile("" : "+s"(go), "+s"(so));
        f32x4 acc[4][2][2];
        f32x2 r12;
        unsigned fc = 0;
        auto gpass = [&](auto pp, auto n0, auto n1) __attribute__((always_inline)) {
            constexpr int pass = decltype(pp)::value;
            unsigned fl = 0, dn = 0; bool lack = false;
            sfor<decltype(n0)::value, decltype(n1)::value>(LAMB(nn) {
                constexpr int n = decltype(nn)::value, m = n + PD;
                constexpr bool pre = pass == 0 || m < 128;
                constexpr unsigned off = wslot(m >> 4) + (m & 1) * 8192 + ((m >> 3) & 1) * 4096;
                const v4u cur = bf[(n + 64 + 128 * pass) % PD];
                mma<(n < 8)>(acc[(n >> 1) & 3][n & 1][0], cur, xa[n >> 3][0]);
                SB();
                if constexpr ((m & 15) == 0) post(fdone, dn);
                if constexpr ((m & 15) == 0) need_full(lack, ffl, 5 + 8 * pass + (m >> 4));
                v2u lo, hi;
                if constexpr (pre) lo = ldstr(tb[(m >> 1) & 3] + off); else lo = ldstr(tbs[m & 3]);
                if constexpr (pass == 1 && n >= 69 && n < 81 && (n & 1)) set2(IC<(n - 69) / 2>{});
                if constexpr ((m & 15) == 10) fl = peek(ffl);
                if constexpr ((m & 15) == 15) lack = any_lt(fl, m == 127 && pass == 0 ? N1 - 7 : 6 + 8 * pass + (m >> 4));
                if constexpr ((m & 15) == 13) { dn = 5 + 8 * pass + (m >> 4); asm volatile("" : "+v"(dn)); }
                if constexpr (n == EG - 8 && pass == 1) fc = peek(F_RDY);
                if constexpr (n == EG - 2 && pass == 1) { if (__builtin_expect(any_lt(fc, 1), 0)) { wait_ge(F_RDY, 1); } fence_c(); }
                if constexpr (n >= EG && n < EG + 24 && pass == 1 && (n - EG) % 3 == 0) { constexpr int f = (n - EG) / 3; reldA<(f & 3) * 1024>(AF(f >> 2, f & 3), l16, sws, uoff(f >> 2)); }
                if constexpr (pass == 1 && (n == 33 || n == 41 || n == 49 || n == 57)) try_raise(1);
                if constexpr (pass == 1 && n == 67) { if (raised < 1) { waitv<0>(); raise(1); } }
                if constexpr (n == 120 && pass == 0) fc = peek(ffl + (F_COMM - F_FULL));
                if constexpr (n == 121 && pass == 0) { const unsigned ra = L_RSTD + w * 128 + (((l16 >> 1) & 0x60) | ((l16 >> 2) & 0xc)); r12 = f32x2{lds32f(ra), lds32f(ra + 16)}; }
                SB();
                mma<(n < 8)>(acc[(n >> 1) & 3][n & 1][1], cur, xa[n >> 3][1]);
                SB();
                if constexpr (pre) hi = ldstr(tb[(m >> 1) & 3] + off + 512); else hi = ldstr(tbs[m & 3] + 2048);
                bf[n % PD] = v4u{lo.x, lo.y, hi.x, hi.y};
                if constexpr ((m & 57) == 57 && (pass == 0 || m < 64)) {
                    tb[(m >> 1) & 3] += (m & 64) ? L_P - L_W : L_W - L_P; asm volatile("" : "+v"(tb[(m >> 1) & 3]));
                }
                SB();
            });
        };
        auto gated = [&](auto ff) __attribute__((always_inline)) {
            constexpr int uu = decltype(ff)::value >> 1, bb = decltype(ff)::value & 1;
            const f32x4 h0 = acc[2 * uu][0][bb] * acc[2 * uu][1][bb], h1 = acc[2 * uu + 1][0][bb] * acc[2 * uu + 1][1][bb];
            return v4u{pk2(bmul<bb, 0>(r12, lo2(h0))), pk2(bmul<bb, 0>(r12, hi2(h0))), pk2(bmul<bb, 0>(r12, lo2(h1))), pk2(bmul<bb, 0>(r12, hi2(h1)))};
        };
        gpass(IC<0>{}, IC<0>{}, IC<128>{});
        {
            r12 = r12 * r12;
            mfma_done();
            STAMP(6); VAL(25, stall); stall = 0;
            sfor<0, 2>(LAMB(ff) { stA<decltype(ff)::value * 1024>(gated(ff), l16, sws, go); });
            SB();
            need_full(any_lt(fc, 4), ffl + (F_COMM - F_FULL), 4);
            const unsigned tg = peek(L_TAG);
            const unsigned cl = L_CST + (l16 & 0xf0);
            unsigned ya = L_O + w * 4096 + l16;
            asm volatile("" : "+v"(ya));
            const f32x4 h0 = lds128f(L_XCH + 256 + (l16 & 0xf0)), h1 = lds128f(L_XCH + 512 + (l16 & 0xf0));
            const f32x4 aw = lds128f(L_PW + 1024 + w * 256 + (l16 & 0xf0)), pb = lds128f(L_PRE + w * 256 + (l16 & 0xf0));
            const f32x4 ag = lds128f(L_PW + l16), pin = lds128f(L_SEG + w * 1024 + l16);
            const v4u sel[2] = {lds128(L_SEL + l16), lds128(L_SEL + 1024 + l16)};
            const v4u yr[2] = {lds128(ya), lds128(ya + 1024)}, yi[2] = {lds128(ya + 2048), lds128(ya + 3072)};
            const f32x4 c4 = lds128f(cl + 256);
            f32x4 cw[4];
            SB();
            sfor<2, 4>(LAMB(ff) { stA<decltype(ff)::value * 1024>(gated(ff), l16, sws, go); });
            tgs = __builtin_amdgcn_readfirstlane(tg);
#pragma unroll
            for (int t = 0; t < 4; t++) cw[t] = lds128f(cl + (3 + t) * 256);
            SB();
            f32x4 zr[2][2], zi[2][2];
#pragma unroll
            for (int st = 0; st < 2; st++)
#pragma unroll
                for (int bb = 0; bb < 2; bb++) { mma<true>(zr[st][bb], yr[st], sel[bb]); mma<true>(zi[st][bb], yi[st], sel[bb]); }
            SB();
            f32x2 hr[2], hi[2];
            {
                const f32x4 cy = h0 + h1;
                const f32x2 k0 = cmadp(lo2(ag), cmadp(lo2(aw), lo2(cy), lo2(pb)), lo2(pin)), k1 = cmadp(hi2(ag), cmadp(hi2(aw), hi2(cy), hi2(pb)), hi2(pin));
                const f32x4 hin = {k0.x, k0.y, k1.x, k1.y};
                float t0[2], t1[2];
#pragma unroll
                for (int st = 0; st < 2; st++) { t0[st] = sfnma(c4[2 * st + 1], hin[2 * st + 1], eX[2 * st]); t1[st] = sfma(c4[2 * st + 1], hin[2 * st], eX[2 * st + 1]); }
                SB();
#pragma unroll
                for (int st = 0; st < 2; st++) { hr[st] = f32x2{hin[2 * st], sfma(c4[2 * st], hin[2 * st], t0[st])}; hi[st] = f32x2{hin[2 * st + 1], sfma(c4[2 * st], hin[2 * st + 1], t1[st])}; }
                SB();
            }
#pragma unroll
            for (int st = 0; st < 2; st++) {
                f32x2 tr[4], ti[4];
#pragma unroll
                for (int t = 0; t < 4; t++) { const f32x2 wv = st ? hi2(cw[t]) : lo2(cw[t]); tr[t] = bmul<1, 1>(wv, hi[st]); ti[t] = bmul<1, 1>(wv, hr[st]); }
                SB();
#pragma unroll
                for (int t = 0; t < 4; t++) { const f32x2 wv = st ? hi2(cw[t]) : lo2(cw[t]); tr[t] = bfma<0, 0>(wv, hr[st], tr[t]); ti[t] = bfma<0, 1>(wv, hi[st], ti[t]); }
                SB();
                v4u fr = {pk2(tr[0]), pk2(tr[1]), pk2(tr[2]), pk2(tr[3])}, fi = {pk2(ti[0]), pk2(ti[1]), pk2(ti[2]), pk2(ti[3])};
                asm volatile("s_nop 1" : "+v"(fr), "+v"(fi));
#pragma unroll
                for (int bb = 0; bb < 2; bb++) { mma<false>(zr[st][bb], fr, sel[bb]); mma<false>(zi[st][bb], fi, sel[bb]); }
                SB();
            }
            mfma_done();
            stA<0>(pk8(zr[0][0], zr[1][0]), l16, sws, so); stA<0>(pk8(zi[0][0], zi[1][0]), l16, sws, so + 8 * 16384);
            stA<1024>(pk8(zr[0][1], zr[1][1]), l16, sws, so); stA<1024>(pk8(zi[0][1], zi[1][1]), l16, sws, so + 8 * 16384);
            SB();
            STAMP(7);
        }
        if (heavy) {
            gpass(IC<1>{}, IC<0>{}, IC<128>{});
            sfor<0, 4>(LAMB(ff) { constexpr int f = decltype(ff)::value; reldA<f * 1024>(AF(2, f), l16, sws, uoff(2)); });
            mfma_done();
            STAMP(8); VAL(26, stall); stall = 0;
            sfor<0, 4>(LAMB(ff) { stA<decltype(ff)::value * 1024>(gated(ff), l16, sws, go + 4 * 16384); });
            SB();
        }
        int q2 = q;
        asm volatile("" : "+s"(q2));
        if (q2 == 3) {
            waitv<0>(); raise(1); raise(2);
            sfor<0, 6>(set2);
            wait_ge(ffl, N1 + 1);
            sfor<0, PD>(prime2);
            wait_ge(F_RDY, 1);
            sfor<0, 12>(LAMB(ff) { constexpr int f = decltype(ff)::value; reldA<(f & 3) * 1024>(AF(f >> 2, f & 3), l16, sws, uoff(f >> 2)); });
#pragma unroll
            for (int u = 0; u < 3; u++) asm volatile("s_waitcnt vmcnt(0)" : "+v"(AF(u, 0)), "+v"(AF(u, 1)), "+v"(AF(u, 2)), "+v"(AF(u, 3))::"memory");
        }
    }
    STAMP(9);
    {
        f32x4 acc[8][2];
        v4u pk[2];
        unsigned fl = 0, fr = 0, dn = 0; bool lack = false;
        constexpr auto fv = [](int n) { return n < 48 ? n >> 4 : n < 108 ? 3 + (n - 48) / 12 : n < 144 ? 8 + ((n - 108) % 12) / 4 : 3 + (n - 144) / 4; };
        constexpr auto fk = [](int n) { return n < 48 ? (n >> 3) & 1 : n < 108 ? ((n - 48) % 12) / 6 : (n >> 1) & 1; };
        constexpr auto fd = [](int n) { return n < 48 ? n & 7 : n < 108 ? 2 + (n - 48) % 6 : n < 144 ? 2 * ((n - 108) / 12 + 1) + (n & 1) : n & 1; };
        constexpr auto first = [](int n) { return n >= 0 && (n < 48 ? (n & 15) == 0 : n < 108 ? (n - 48) % 12 == 0 : n < 120 && (n & 3) == 0); };
        constexpr int LG = LGATE;
        constexpr auto nmid = [](int n) { return n <= MG ? 0 : (n - MG + 2) / 3 < 20 ? (n - MG + 2) / 3 : 20; };
        static_assert(MG + 57 < LG && LG + 22 < 108, "load schedule");
        sfor<0, 176>(LAMB(nn) {
            constexpr int n = decltype(nn)::value, m = n + PD;
            constexpr int v = fv(n), ks = fk(n), dt = fd(n);
            constexpr int mv = fv(m), mk = fk(m), md = fd(m);
            constexpr bool mc = (mv & ~3) == 4;
            constexpr unsigned moff = (mv & 3) * SLOT + (mc ? md * 2048 : (md >> 2) * 8192 + mk * 4096);
            constexpr auto chk = [](int q) { return q == 16 ? 2 : q == 32 ? 4 : q == 60 ? 7 : q == 96 ? 11 : 0; };
            v4u cur; v2u lo, hi;
            if constexpr (n >= 0) {
                if constexpr (first(n)) {
                    constexpr int mid = nmid(n), late = n <= LG ? 0 : (n - LG + 1) / 2 < 12 ? (n - LG + 1) / 2 : 12;
                    constexpr int NW = v == 0 ? 12 : v == 1 ? 8 : v == 2 ? 63 : v < 8 ? mid - 4 * (v - 2) + late : late - 4 * (v - 7);
                    static_assert(NW >= 0, "load schedule");
                    asm volatile("s_waitcnt vmcnt(%4)" : "+v"(AF(v & 7, 0)), "+v"(AF(v & 7, 1)), "+v"(AF(v & 7, 2)), "+v"(AF(v & 7, 3)) : "n"(ABLW(v) ? 63 : NW) : "memory");
                }
                cur = bf[n % PD];
                mma<(n < 8)>(acc[dt][0], cur, AF(v & 7, 2 * ks));
                SB();
            }
            if constexpr (m > 0 && (m & 15) == 0 && m <= 48) post(fdone, dn);
            if constexpr (chk(m) > 0) need_full(lack, m == 96 ? ffl + (F_COMM - F_FULL) : ffl, N1 + chk(m));
            if constexpr (m < 176) { if constexpr (mc) bf[m % PD] = lds128(cbs[mk] + moff); else lo = ldstr(tbs[md & 3] + moff); }
            if constexpr (chk(m + 6) > 0) fl = peek(m == 90 ? ffl + (F_COMM - F_FULL) : ffl);
            if constexpr (chk(m + 1) > 0) lack = any_lt(fl, N1 + chk(m + 1));
            if constexpr ((m & 15) == 13 && m < 48) { dn = N1 + mv + 1; asm volatile("" : "+v"(dn)); }
            if constexpr (n == MG - 7) fr = peek(F_RDY);
            if constexpr (n == MG - 1) { if (__builtin_expect(any_lt(fr, 2), 0)) { if (raised < 2) { waitv<0>(); raise(2); } wait_ge(F_RDY, 2); } fence_c(); }
            if constexpr (n == RAISE2) { if (raised < 2) { if (!ABLW(99)) waitv<nmid(n)>(); raise(2); } }
            if constexpr (n >= MG && n < MG + 60 && (n - MG) % 3 == 0) { constexpr int f = (n - MG) / 3; reldA<(f & 3) * 1024>(AF(3 + (f >> 2), f & 3), l16, sws, uoff(3 + (f >> 2))); }
            if constexpr (n == LG - 6) fr = peek(F_RDY + 4);
            if constexpr (n == LG - 1) need_full(any_lt(fr, 1), F_RDY + 4, 1);
            if constexpr (n >= LG && n < LG + 24 && (n - LG) % 2 == 0) { constexpr int f = (n - LG) / 2; reldA<(f & 3) * 1024>(AF(f >> 2, f & 3), l16, sws, uoff(8 + (f >> 2))); }
            SB();
            if constexpr (n >= 0) { mma<(n < 8)>(acc[dt][1], cur, AF(v & 7, 2 * ks + 1)); SB(); }
            if constexpr (m < 176 && !mc) { hi = ldstr(tbs[md & 3] + moff + 2048); bf[m % PD] = v4u{lo.x, lo.y, hi.x, hi.y}; }
            if constexpr (n >= 120 && n < 156) {
                constexpr int sl = (n - 108) / 12, r = (n - 108) % 12 - 2;
                if constexpr (r >= 0 && r < 10 && r % 5 < 4) { const f32x4& a = acc[2 * sl + r / 5][(r % 5) >> 1]; pk[r / 5][r % 5] = cvtv(a[2 * (r % 5 & 1)], a[2 * (r % 5 & 1) + 1]); }
                if constexpr (r >= 0 && r % 5 == 4) stX<(r / 5) * 1024>(pk[r / 5], l16, sws, oX[sl - 1]);
            }
            if constexpr (n == 156) post(fdone + (F_SENT - F_DONE), 3);
            if constexpr (n == 107) { STAMP(10); VAL(28, stall); stall = 0; }
            SB();
        });
        mfma_done();
        STAMP(11); VAL(29, stall);
#pragma unroll
        for (int e = 0; e < 4; e++) sts128f(L_O + w * 4096 + e * 1024 + l16, acc[e >> 1][e & 1]);
        post(fdone + (F_SENT - F_DONE), 4);
    }
    STAMP(14); STAMP_DUMP();
}

extern "C" void kernel_launch(void* const* d_in, const int* in_sizes, int n_in, void* d_out, int out_size, void* d_ws, size_t ws_size,
                              hipStream_t stream) {
    Params p;
    p.x = (const u16*)d_in[0]; p.lre = (const u16*)d_in[1]; p.lim = (const u16*)d_in[2];
    p.Bre = (const u16*)d_in[3]; p.Bim = (const u16*)d_in[4]; p.Cre = (const u16*)d_in[5]; p.Cim = (const u16*)d_in[6];
    p.ldt = (const u16*)d_in[7]; p.Dsk = (const u16*)d_in[8];
    p.wg = (const u16*)d_in[10]; p.wu = (const u16*)d_in[11]; p.wd = (const u16*)d_in[12];
    p.out = (float*)d_out; p.ws = (unsigned char*)d_ws + WS_SHIFT;
    (void)hipFuncSetAttribute((const void*)fused, hipFuncAttributeMaxDynamicSharedMemorySize, L_TOTAL);
    fused<<<256, 512, L_TOTAL, stream>>>(p);
}
```

```cpp
#define BGATE 3
#include <hip/hip_runtime.h>
#include <stdint.h>

typedef unsigned short u16;
typedef unsigned v4u __attribute__((ext_vector_type(4)));
typedef unsigned v2u __attribute__((ext_vector_type(2)));
typedef short s16x4 __attribute__((ext_vector_type(4)));
typedef __bf16 bf16x2 __attribute__((ext_vector_type(2)));
typedef __bf16 bf16x8 __attribute__((ext_vector_type(8)));
typedef float f32x2 __attribute__((ext_vector_type(2)));
typedef float f32x4 __attribute__((ext_vector_type(4)));
#define GAS __attribute__((address_space(1)))
#define LAS __attribute__((address_space(3)))
#define DEVI __device__ __forceinline__
#define NOUNROLL _Pragma("nounroll")

#define SPIN_SLEEP 0
constexpr int DM = 512, FF = 1792;
constexpr unsigned WS_A2 = 0;
constexpr unsigned WS_XB = 16u * 44u * 16384u;
constexpr unsigned WS_GRAN = WS_XB + 256u * 24576u;
constexpr unsigned WS_FLAG = WS_GRAN + 16u * 16u * 512u;
constexpr unsigned WS_EPOCH = WS_FLAG + 1024u * 128u;
constexpr unsigned WS_END = WS_EPOCH + 256u;
constexpr unsigned SLOT = 16384;
constexpr unsigned L_RSTD = 0, L_MEAN = 512;
constexpr unsigned F_DONE = 1024, F_SENT = F_DONE + 16, F_FULL = F_DONE + 32, F_COMM = F_DONE + 40, F_RDY = F_DONE + 48, F_OUT = F_DONE + 64, F_XI = F_DONE + 80, F_BS = F_DONE + 96, L_TAG = F_DONE + 128;
constexpr unsigned L_CST = 1280;
constexpr unsigned L_XCH = L_CST + 8 * 256;
constexpr unsigned L_SEG = 4096, L_PW = 8192, L_TOT = L_PW + 2048, L_PRE = L_TOT + 1024, L_SEL = 12288;
constexpr unsigned L_P = SLOT, L_O = 5 * SLOT, L_W = 6 * SLOT, L_TOTAL = 10 * SLOT;
constexpr int SPIN_MAX = 1 << 18;
constexpr int LSPIN_MAX = 1 << 22;

struct Params {
    const u16 *x, *lre, *lim, *Bre, *Bim, *Cre, *Cim, *ldt, *Dsk, *wg, *wu, *wd;
    float* out;
    unsigned char* ws;
};

DEVI float bf2f(u16 v) { return __uint_as_float(((unsigned)v) << 16); }
DEVI float bflo(unsigned v) { return __uint_as_float(v << 16); }
DEVI float bfhi(unsigned v) { return __uint_as_float(v & 0xffff0000u); }
DEVI unsigned pk2(float a, float b) { f32x2 p = {a, b}; return __builtin_bit_cast(unsigned, __builtin_convertvector(p, bf16x2)); }
DEVI unsigned pk2(f32x2 p) { return __builtin_bit_cast(unsigned, __builtin_convertvector(p, bf16x2)); }
DEVI v4u pk8(f32x4 a, f32x4 b) { return v4u{pk2(a.x, a.y), pk2(a.z, a.w), pk2(b.x, b.y), pk2(b.z, b.w)}; }
DEVI float dot2(unsigned a, unsigned b, float c) { return __builtin_amdgcn_fdot2_f32_bf16(__builtin_bit_cast(bf16x2, a), __builtin_bit_cast(bf16x2, b), c, false); }
template <int CTRL> DEVI float dppf(float v) { return __builtin_bit_cast(float, __builtin_amdgcn_update_dpp(0, __builtin_bit_cast(int, v), CTRL, 0xf, 0xf, true)); }
DEVI f32x4 mfma(v4u a, v4u b, f32x4 c) { return __builtin_amdgcn_mfma_f32_16x16x32_bf16(__builtin_bit_cast(bf16x8, a), __builtin_bit_cast(bf16x8, b), c, 0, 0, 0); }
template <bool FIRST> DEVI void mma(f32x4& c, const v4u& a, const v4u& b) {
    if constexpr (FIRST) asm volatile("v_mfma_f32_16x16x32_bf16 %0, %1, %2, 0" : "=&v"(c) : "v"(a), "v"(b));
    else asm volatile("v_mfma_f32_16x16x32_bf16 %0, %1, %2, %0" : "+v"(c) : "v"(a), "v"(b));
}
DEVI void mfma_done() { __builtin_amdgcn_sched_barrier(0); asm volatile("s_nop 7"); __builtin_amdgcn_sched_barrier(0); }
DEVI v4u lds128(unsigned a) { return *(LAS v4u*)(uintptr_t)a; }
DEVI f32x4 lds128f(unsigned a) { return *(LAS f32x4*)(uintptr_t)a; }
DEVI float lds32f(unsigned a) { return *(LAS float*)(uintptr_t)a; }
DEVI void sts32f(unsigned a, float v) { *(LAS float*)(uintptr_t)a = v; }
DEVI void sts128(unsigned a, v4u v) { *(LAS v4u*)(uintptr_t)a = v; }
DEVI void sts128f(unsigned a, f32x4 v) { *(LAS f32x4*)(uintptr_t)a = v; }
DEVI v2u ldstr(unsigned a) { return __builtin_bit_cast(v2u, __builtin_amdgcn_ds_read_tr16_b64_v4i16((LAS s16x4*)(uintptr_t)a)); }
DEVI void fence_c() { asm volatile("" ::: "memory"); }
DEVI void bar_l() { asm volatile("s_waitcnt lgkmcnt(0)\n\ts_barrier" ::: "memory"); }
template <int N> DEVI void waitv() { asm volatile("s_waitcnt vmcnt(%0)" ::"n"(N) : "memory"); }
DEVI void dma16(const void* sbase, unsigned voff, unsigned ldst) {
    asm volatile("s_mov_b32 m0, %2\n\ts_nop 0\n\tglobal_load_lds_dwordx4 %0, %1" ::"v"(voff), "s"(sbase), "s"(ldst) : "memory");
}
DEVI void post(unsigned a, unsigned v) { fence_c(); *(LAS unsigned*)(uintptr_t)a = v; fence_c(); }
DEVI unsigned peek(unsigned a) { fence_c(); return *(LAS unsigned*)(uintptr_t)a; }
DEVI bool any_lt(unsigned v, unsigned need) { return __builtin_amdgcn_ballot_w64(v < need) != 0; }
DEVI void wait_ge(unsigned a, unsigned need) {
    int s = LSPIN_MAX;
    NOUNROLL while (any_lt(peek(a), need)) { asm volatile("s_sub_i32 %0, %0, 1" : "+s"(s) :: "scc"); if (s < 0) break; if (SPIN_SLEEP) __builtin_amdgcn_s_sleep(SPIN_SLEEP); }
    fence_c();
}
template <int V> DEVI void postm(unsigned a, int lane) {
    asm volatile("s_mov_b64 exec, %2\n\tds_max_u32 %0, %1\n\ts_mov_b64 exec, -1" ::"v"(a), "v"(lane), "n"(1 << V) : "memory");
}
DEVI unsigned vm_outstanding() {
    unsigned v; asm volatile("s_getreg_b32 %0, hwreg(HW_REG_IB_STS)" : "=s"(v)::"memory");
    return (v & 15u) | ((v >> 18) & 0x30u);
}
DEVI void postv(unsigned a, int lane, unsigned v) {
    asm volatile("s_lshl_b64 exec, 1, %2\n\tds_max_u32 %0, %1\n\ts_mov_b64 exec, -1" ::"v"(a), "v"(lane), "s"(v) : "memory", "scc");
}
template <int V> DEVI void postc(unsigned a, int lane) {
    asm volatile("s_mov_b64 exec, %2\n\tds_write_b32 %0, %1\n\ts_mov_b64 exec, -1" ::"v"(a), "v"(lane), "n"(1 << V) : "memory");
}
DEVI void need_full(bool lack, unsigned a, unsigned need) { if (__builtin_expect(lack, 0)) wait_ge(a, need); fence_c(); }
#define VAL(id, v)
DEVI float sfma(float a, float b, float c) { float d; asm("v_fma_f32 %0, %1, %2, %3" : "=v"(d) : "v"(a), "v"(b), "v"(c)); return d; }
DEVI float sfms(float a, float b, float c) { float d; asm("v_fma_f32 %0, %1, %2, -%3" : "=v"(d) : "v"(a), "v"(b), "v"(c)); return d; }
DEVI float sfnma(float a, float b, float c) { float d; asm("v_fma_f32 %0, -%1, %2, %3" : "=v"(d) : "v"(a), "v"(b), "v"(c)); return d; }
DEVI float sadd(float a, float b) { float d; asm("v_add_f32 %0, %1, %2" : "=v"(d) : "v"(a), "v"(b)); return d; }
DEVI float smul(float a, float b) { float d; asm("v_mul_f32 %0, %1, %2" : "=v"(d) : "v"(a), "v"(b)); return d; }
DEVI float smul_t(float a, float b) { float d; asm("s_nop 0\n\tv_mul_f32 %0, %1, %2" : "=v"(d) : "v"(a), "v"(b)); return d; }
struct cpx { float x, y; };
DEVI cpx cmul(cpx a, cpx b) { return cpx{sfms(a.x, b.x, smul(a.y, b.y)), sfma(a.x, b.y, smul(a.y, b.x))}; }
DEVI float lmad(cpx a, float h, float u) {
    float d;
    asm("v_fma_f32 %0, %1, %3, %4\n\ts_nop 0\n\tv_fmac_f32_dpp %0, %3, %2 quad_perm:[1,0,3,2] row_mask:0xf bank_mask:0xf" : "=&v"(d) : "v"(a.x), "v"(a.y), "v"(h), "v"(u));
    return d;
}
DEVI f32x2 dup(float v) { f32x2 d = {v, v}; asm("" : "+v"(d)); return d; }
DEVI f32x2 pfma(f32x2 a, f32x2 b, f32x2 c) { return __builtin_elementwise_fma(a, b, c); }
DEVI f32x2 lo2(f32x4 v) { return f32x2{v.x, v.y}; }
DEVI f32x2 hi2(f32x4 v) { return f32x2{v.z, v.w}; }
#define SB() __builtin_amdgcn_sched_barrier(0)
template <int V> struct IC { static constexpr int value = V; };
template <int B, int E, class F> DEVI void sfor(F&& f) { if constexpr (B < E) { f(IC<B>{}); sfor<B + 1, E>(f); } }
#define LAMB(nn) [&](auto nn) __attribute__((always_inline))
#define STAMP(id)
#define STAMP_INIT()
#define STAMP_DUMP()
DEVI int fresh_lane() { int l; asm volatile("v_mbcnt_lo_u32_b32 %0, -1, 0\n\tv_mbcnt_hi_u32_b32 %0, -1, %0" : "=v"(l)); return l; }
DEVI v4u make_sel(int lane, int bb) {
    const int c = lane & 15, tk = 8 * (c >> 2) + 4 * bb + (c & 3) - 8 * (lane >> 4);
    v4u s;
#pragma unroll
    for (int e = 0; e < 4; e++) s[e] = (tk == 2 * e ? 0x3f80u : 0u) | (tk == 2 * e + 1 ? 0x3f800000u : 0u);
    return s;
}
#define LANE_VARS const int lane = fresh_lane(), c = lane & 15, g = lane >> 4, q8 = lane >> 3, p8 = lane & 7; (void)c; (void)g; (void)q8; (void)p8
constexpr unsigned wslot(int n) { return (unsigned)(n & 3) * SLOT; }

#define ROT1 ((i + 2 * kq) & 7)
#define ROT2 0
#define XI_STEP 6
#define XI2_STEP 15
#define TOUCH_GATE 1
#define LB_SLEEP 8
#define LB_FIRST 1
#define T2_SLEEP 1
#define FL_FIRST 20
#define FL_SLEEP 6
#define MIDV 64
#define X_FIRST 14
#define X_SLEEP 1
#define PDV 4
#define INFL 8
#define TSL 6
#define TW __builtin_amdgcn_s_sleep(TSL);
#define PACE 14
#define PACE2 8
#define XLV 3
#define LGATE 80
#define RAISE2 23
#define TGN 37
#define EGATE 102
#define X_LAST 6
#define CLAG 4
#define CRING 8
#define PF_SLEEP 1
#define WS_SHIFT 0
#define NOXT
#define LT_SLEEP 16
#define MG 18
#define ABLW(v) 0
#define WR_DEPTH 4
#define PFPOS 2
#define PF_N 12
#define WR_SLEEP 2
#define LT_POS 0
#define G1T 64
#define SLEEP0 12
constexpr unsigned CODE_LINES = 306;
template <int OFF> DEVI void reldA(v4u& d, unsigned voff, v4u srd, unsigned so) {
    asm volatile("buffer_load_dwordx4 %0, %1, %2, %3 offen offset:%4 sc1" : "+v"(d) : "v"(voff), "s"(srd), "s"(so), "n"(OFF) : "memory");
}
template <int OFF> DEVI void stA(const v4u& d, unsigned voff, v4u srd, unsigned so) {
    asm volatile("buffer_store_dwordx4 %0, %1, %2, %3 offen offset:%4 sc1\n\ts_nop 1" ::"v"(d), "v"(voff), "s"(srd), "s"(so), "n"(OFF) : "memory");
}
template <int OFF> DEVI void stX(const v4u& d, unsigned voff, v4u srd, unsigned so) {
    asm volatile("buffer_store_dwordx4 %0, %1, %2, %3 offen offset:%4 sc1" ::"v"(d), "v"(voff), "s"(srd), "s"(so), "n"(OFF) : "memory");
}
DEVI void ldW(v4u& d, unsigned voff, v4u srd, unsigned so) { asm volatile("buffer_load_dwordx4 %0, %1, %2, %3 offen" : "=v"(d) : "v"(voff), "s"(srd), "s"(so) : "memory"); }
DEVI void touch(unsigned& sink, const void* tp, unsigned to) { asm volatile("global_load_dword %0, %1, %2" : "+v"(sink) : "v"(to), "s"(tp) : "memory"); }

DEVI v4u make_srd(const void* base) { const unsigned long long a = (unsigned long long)base; return v4u{(unsigned)a, (unsigned)(a >> 32) & 0xffffu, 0x7fffffffu, 0x00020000u}; }
DEVI void dma4(v4u srd, unsigned ve, unsigned vo, unsigned s0, unsigned s1, unsigned s2, unsigned s3, unsigned ldst) {
    asm volatile("s_mov_b32 m0, %7\n\ts_nop 0\n\tbuffer_load_dwordx4 %0, %2, %3 offen lds\n\tbuffer_load_dwordx4 %1, %2, %4 offen offset:1024 lds\n\t"
                 "buffer_load_dwordx4 %0, %2, %5 offen offset:2048 lds\n\tbuffer_load_dwordx4 %1, %2, %6 offen offset:3072 lds"
                 ::"v"(ve), "v"(vo), "s"(srd), "s"(s0), "s"(s1), "s"(s2), "s"(s3), "s"(ldst) : "memory");
}
DEVI void dma1(v4u srd, unsigned v, unsigned so, unsigned ldst) {
    asm volatile("s_mov_b32 m0, %3\n\ts_nop 0\n\tbuffer_load_dwordx4 %0, %1, %2 offen lds" ::"v"(v), "s"(srd), "s"(so), "s"(ldst) : "memory");
}
template <int H, int NEG> DEVI f32x2 bmul(f32x2 a, f32x2 b) {
    f32x2 d; asm("v_pk_mul_f32 %0, %1, %2 op_sel:[%3,0] op_sel_hi:[%3,1] neg_lo:[%4,0] neg_hi:[%4,0]" : "=v"(d) : "v"(a), "v"(b), "n"(H), "n"(NEG)); return d;
}
template <int H, int NEG> DEVI f32x2 bfma(f32x2 a, f32x2 b, f32x2 c) {
    f32x2 d; asm("v_pk_fma_f32 %0, %1, %2, %3 op_sel:[%4,0,0] op_sel_hi:[%4,1,1] neg_lo:[%5,0,0] neg_hi:[%5,0,0]" : "=v"(d) : "v"(a), "v"(b), "v"(c), "n"(H), "n"(NEG)); return d;
}
DEVI f32x2 cmadp(f32x2 a, f32x2 h, f32x2 u) {
    f32x2 t, d;
    asm("v_pk_fma_f32 %0, %1, %2, %3 op_sel:[0,0,0] op_sel_hi:[0,1,1]" : "=v"(t) : "v"(h), "v"(a), "v"(u));
    asm("v_pk_fma_f32 %0, %1, %2, %3 op_sel:[1,0,0] op_sel_hi:[0,0,1] neg_lo:[1,0,0]" : "=v"(d) : "v"(a), "v"(f32x2{h.y, h.y}), "v"(t));
    return d;
}
DEVI void mma_c(f32x4& d, const v4u& a, const v4u& b, const f32x4& c) { asm volatile("v_mfma_f32_16x16x32_bf16 %0, %1, %2, %3" : "=&v"(d) : "v"(a), "v"(b), "v"(c)); }
DEVI void mma_t(f32x4& d, const v4u& a, const v4u& b, const f32x4& c) { asm volatile("v_mfma_f32_16x16x32_bf16 %0, %1, %2, %3" : "+v"(d) : "v"(a), "v"(b), "v"(c)); }
DEVI unsigned cvtv(float a, float b) { unsigned d; asm volatile("v_cvt_pk_bf16_f32 %0, %1, %2" : "=v"(d) : "v"(a), "v"(b)); return d; }

__global__ __launch_bounds__(512, 2) void fused(Params p) {
    const unsigned char* pc0;
    asm volatile("s_getpc_b64 %0" : "=s"(pc0));
    const int w = __builtin_amdgcn_readfirstlane(threadIdx.x >> 6);
    const int b = blockIdx.x, xcd = b & 7, idx = b >> 3;
    const int i = 8 * (xcd & 1) + (idx & 7), kq = xcd >> 1, q = idx >> 3, j = 4 * kq + q;
    const bool heavy = q != 3;
    const int N1 = heavy ? 20 : 12;
    const int lw = w & 1, pr = w & 3;
    const int rot = ROT1;
    const __amdgpu_buffer_rsrc_t rws = __builtin_amdgcn_make_buffer_rsrc(p.ws, 0, WS_END, 0x00020000);
    auto unit_of = [&](int v) { return v < 4 ? 7 * kq + v : v < 8 ? 28 + 8 * ((v >> 1) & 1) + 2 * kq + (v & 1) : 7 * kq + v - 4; };
    const unsigned sl_lo = L_P + (unsigned)pr * SLOT;
    const unsigned xo = WS_XB + (unsigned)((i * 4 + q) * 4) * 24576u + pr * 6144;
    const unsigned fo = WS_FLAG + (unsigned)(i * 16 + j) * 512u;

    if (w >= 4) {
        LANE_VARS;
        STAMP_INIT();
        STAMP(0);
        const unsigned swz = (unsigned)((p8 ^ (q8 >> 1)) << 4);
        const unsigned fa = F_DONE + (lane & 3) * 4;
        unsigned sink = 0, tg;
        auto poll16 = [&](int which) __attribute__((always_inline)) {
            wait_ge(F_OUT + (lane & 3) * 4, which + 1);
            STAMP(16);
            const unsigned pf = WS_FLAG + (unsigned)(i * 16 + 4 * kq + (lane & 3)) * 512u + which * 128 + ((lane >> 2) & 3) * 8;
            NOUNROLL for (int spin = 0; spin < SPIN_MAX; spin++) {
                fence_c();
                const v2u f = __builtin_amdgcn_raw_buffer_load_b64(rws, pf, 0, 16);
                const unsigned bad = (unsigned)__builtin_amdgcn_ballot_w64(f.x != tg || f.y != ~tg);
                if (bad == 0) break;
                if ((bad & 0x7777u) == 0) { if (which) break; postc<1>(F_RDY, lane); }
                __builtin_amdgcn_s_sleep(FL_SLEEP);
            }
            postc<2>(F_RDY + which * 4, lane);
            STAMP(17);
        };
        if (w < 6) {
            unsigned ffull = F_FULL + lw * 4;
            asm volatile("" : "+v"(ffull));
            touch(sink, lw ? p.Bim : p.Bre, (32 * j + 2 * i) * 1024 + c * 128);
            bar_l();
            {
                const unsigned voffS = (8 * (q8 >> 2) + (q8 & 3)) * 1024 + swz;
                const v4u srd = make_srd((const unsigned char*)(lw ? p.Bim : p.Bre) + (size_t)(32 * j) * 1024);
                wait_ge(F_XI + (lane & 3) * 4, 1);
                NOUNROLL for (int n = 0; n < 8; n++) {
                    const unsigned so = 128 * ((n + rot) & 7);
                    dma4(srd, voffS, voffS ^ 64u, so, so + 15360, so + 2048, so + 17408, L_W + (n >> 1) * SLOT + lw * 8192 + (n & 1) * 4096);
                    waitv<8>(); postv(ffull, lane, max(n - 1, 0) >> 1);
                }
                asm volatile("" ::"v"(sink));
            }
            STAMP(1);
            const unsigned voffG = q8 * (FF * 2) + (((p8 >> 1) ^ ((q8 >> 1) & 1)) << 5) + ((p8 & 1) << 4), voffG1 = voffG ^ 64u;
            const unsigned voffC = q8 * 1024 + swz, voffC1 = voffC ^ 64u;
            const unsigned voffD = q8 * 1024 + (((p8 >> 1) ^ (q8 >> 1) ^ (2 * (kq & 1))) << 5) + ((p8 & 1) << 4);
            const unsigned char* gb = (const unsigned char*)(lw ? p.wu : p.wg) + (7 * kq + q) * 128;
            unsigned thr = (lane >> 2) + 1;
            int credit = 0, cbase = 0;
            auto item = [&](int n, const unsigned char* b0, const unsigned char* b1, unsigned ve, unsigned vo, unsigned st) __attribute__((always_inline)) {
                v4u s0 = make_srd(b0), s1 = make_srd(b1);
                unsigned dst = ((n & 4) ? L_P : L_W) + (n & 3) * SLOT + lw * 8192, dst1 = dst + 4096;
                asm volatile("" : "+s"(s0), "+s"(s1), "+s"(dst), "+s"(dst1));
                int sp = LSPIN_MAX;
                NOUNROLL while (n - 7 > credit) {
                    const unsigned dn = peek(fa);
                    postv(ffull, lane, n - ((vm_outstanding() + 7) >> 3));
                    unsigned long long mk = __builtin_amdgcn_ballot_w64(dn >= thr);
                    mk &= mk >> 1; mk &= mk >> 2;
                    credit = __builtin_popcountll(mk & 0x1111111111111111ull) + cbase;
                    asm volatile("s_sub_i32 %0, %0, 1" : "+s"(sp)::"scc");
                    if (sp < 0) break;
                }
                waitv<INFL>();
                dma4(s0, ve, vo, 0, st - 1024, 2 * st - 2048, 3 * st - 3072, dst);
                postv(ffull, lane, n - (INFL + 7) / 8);
                waitv<INFL>();
                dma4(s1, ve, vo, 0, st - 1024, 2 * st - 2048, 3 * st - 3072, dst1);
            };
            wait_ge(F_XI + (lane & 3) * 4, 3);
            NOUNROLL for (int n = 4; n < N1; n++) {
                const int t = n - 4;
                const unsigned char* bp = gb + (size_t)(((t + rot) & 7) * 64) * (FF * 2) + (t >> 3) * 512;
                item(n, bp, bp + 32 * FF * 2, voffG, voffG1, 8 * FF * 2);
            }
            cbase = N1 - 8; thr += cbase;
            const unsigned char* dp = (const unsigned char*)p.wd + (size_t)(7 * kq) * 65536 + q * 256 + (lw ^ (kq >> 1)) * 128;
            NOUNROLL for (int v = 0; v < 7; v++) {
                if (v >= 4) {
                    const unsigned char* cbp = (const unsigned char*)((v & 2) ? p.Cim : p.Cre) + (size_t)q * 131072 + (2 * kq + (v & 1)) * 128;
                    item(N1 + v, cbp + ((2 * lw) ^ kq) * 32768, cbp + ((2 * lw + 1) ^ kq) * 32768, voffC, voffC1, 8192);
                } else {
                    item(N1 + v, dp, dp + 32768, voffD, voffD, 8192);
                    dp += 65536;
                }
            }
            waitv<16>(); postv(ffull, lane, N1 + 5);
            waitv<8>(); postv(ffull, lane, N1 + 6);
            waitv<0>(); postv(ffull, lane, N1 + 11);
            tg = peek(L_TAG);
            STAMP(7);
            if (lw) {
                const unsigned pf = WS_FLAG + (unsigned)(i * 16 + 4 * (lane & 3) + q) * 512u + 256 + ((lane >> 2) & 1) * 8;
                NOUNROLL for (int spin = 0; spin < SPIN_MAX; spin++) {
                    fence_c();
                    const v2u f = __builtin_amdgcn_raw_buffer_load_b64(rws, pf, 0, 16);
                    if (__builtin_amdgcn_ballot_w64(f.x != tg || f.y != ~tg) == 0) break;
                    __builtin_amdgcn_s_sleep(16);
                }
            }
            if (lw) poll16(1);
        } else {
            unsigned tag0, prm0, prm1, prm2;
            const unsigned cto = min(idx * 11 + 4 * lw + p8, (int)CODE_LINES - 1) * 128;
            touch(sink, pc0, cto);
            touch(sink, lw ? (const void*)p.ws : (const void*)p.Dsk, lw ? ((lane & 4) ? WS_GRAN + (j * 16 + i) * 512 : fo) + (lane & 3) * 128 : p8 * 128);
            if (!heavy) __builtin_amdgcn_s_sleep(LT_SLEEP);
            NOUNROLL for (int t = heavy ? 3 : 0; t < 3; t++)
                touch(sink, t < 2 ? p.wd : lw ? p.Cim : p.Cre, t < 2 ? (448 * kq + 224 * (1 - t) + 14 * i + 7 * lw + min(q8, 6)) * 1024 + p8 * 128 : (32 * i + (lane >> 1)) * 1024 + (2 * kq + (lane & 1)) * 128);
            const unsigned po = 2 * (32 * j + 8 * (lane >> 4) + 4 * ((lane >> 1) & 1) + ((lane >> 2) & 3));
            asm volatile("global_load_ushort %0, %4, %5\n\tglobal_load_ushort %1, %4, %6\n\tglobal_load_ushort %2, %4, %7\n\tglobal_load_dword %3, %8, %9 sc1"
                         : "=&v"(prm0), "=&v"(prm1), "=&v"(prm2), "=&v"(tag0) : "v"(po), "s"(p.lre), "s"(p.lim), "s"(p.ldt), "v"(WS_EPOCH), "s"(p.ws) : "memory");
            *(LAS unsigned*)(uintptr_t)(F_DONE + (lane & 31) * 4) = 0u;
            bar_l();
            sts128(L_SEL + lw * 1024 + lane * 16, make_sel(lane, lw));
            asm volatile("s_waitcnt vmcnt(0)" : "+v"(prm0), "+v"(prm1), "+v"(prm2), "+v"(tag0)::"memory");
            tg = tag0 + 1u;
            const bool odd = lane & 1;
            const unsigned la = lane * 4;
            const float lr = bflo(prm0), li = bflo(prm1);
            float dt = __expf(bflo(prm2));
            dt = bflo(pk2(dt, dt));
            const float ex = __expf(dt * lr);
            const float fr = __builtin_amdgcn_fractf(dt * li * 0.15915494309189535f);
            const float ar = smul_t(ex, __builtin_amdgcn_cosf(fr)), ai = smul_t(ex, __builtin_amdgcn_sinf(fr));
            const cpx a1 = {ar, ai};
            const cpx a2 = cmul(a1, a1), a4 = cmul(a2, a2);
            const unsigned sg = odd ? 0u : 0x80000000u;
            auto sgn = [&](cpx v) { return cpx{v.x, __uint_as_float(__float_as_uint(v.y) ^ sg)}; };
            const cpx r8 = cmul(a4, a4), r16 = cmul(r8, r8), r32 = cmul(r16, r16), r64 = cmul(r32, r32);
            const cpx a32 = sgn(r32);
            {
                const cpx r = lw ? r32 : r8, rs = sgn(r);
                float v = odd ? r.y : r.x;
                sts32f(L_PW + lw * 1024 + la, odd ? 0.0f : 1.0f);
#pragma unroll
                for (int s = 1; s < 4; s++) { sts32f(L_PW + lw * 1024 + s * 256 + la, v); if (s < 3) v = lmad(rs, v, 0.0f); }
            }
            if (lw == 0) {
                const float nr = ar - 1.0f, den = smul_t(__builtin_amdgcn_rcpf(sfma(lr, lr, smul(li, li))), 2.0f);
                float co = smul(odd ? sfms(ai, lr, smul(nr, li)) : sfma(nr, lr, smul(ai, li)), den);
                sts32f(L_CST + la, odd ? a1.y : a1.x); sts32f(L_CST + 256 + la, odd ? a4.y : a4.x);
                const cpx a1s = sgn(a1);
#pragma unroll
                for (int t = 0; t < 5; t++) { sts32f(L_CST + (2 + t) * 256 + la, co); if (t < 4) co = lmad(a1s, co, 0.0f); }
                *(LAS unsigned*)(uintptr_t)L_TAG = tg;
            }
            post(F_COMM + lw * 4, 1);
            STAMP(1);
            cpx wn[8];
            {
                const cpx r128 = cmul(r64, r64), r256 = cmul(r128, r128);
                wn[0] = lw ? r128 : cpx{1.0f, 0.0f};
#pragma unroll
                for (int n = 1; n < 8; n++) wn[n] = cmul(wn[n - 1], r256);
#pragma unroll
                for (int n = 0; n < 8; n++) { wn[n] = sgn(wn[n]); asm volatile("" : "+v"(wn[n].x), "+v"(wn[n].y)); }
            }
            unsigned gro = WS_GRAN + j * 8192 + lane * 8, gro2 = gro + i * 512;
            unsigned to0 = lw ? min((unsigned)(lane & 31) * 0x98000u + idx * 0x4000u, WS_END - 4) : (lane & 3) * 0x100000 + idx * 0x4000;
            unsigned po2 = WS_XB + (unsigned)((i * 4 + q) * 4 + kq) * 24576u + lw * 12288 + lane * 16;
            unsigned voffC = q8 * 1024 + swz, voffC1 = voffC ^ 64u;
            unsigned voffD = q8 * 1024 + (((p8 >> 1) ^ (q8 >> 1) ^ (2 * (kq & 1))) << 5) + ((p8 & 1) << 4);
            v4u none = {~0u, ~0u, ~0u, ~0u};
            asm volatile("" : "+v"(gro), "+v"(gro2), "+v"(to0), "+v"(po2), "+v"(voffC), "+v"(voffC1), "+v"(voffD), "+v"(none));
            wait_ge(F_XI + (lane & 3) * 4, heavy ? 2 : LT_POS == 2 ? 1 : 0);
            wait_ge(F_FULL + (lane & 1) * 4, BGATE);
            touch(sink, lw ? p.wu : p.wg, (32 * i + (lane & 31)) * (FF * 2) + (7 * kq + q + ((heavy && lane < G1T) ? 4 : 0)) * 128);
            touch(sink, lw ? (const void*)p.ws : (const void*)p.out, to0);
            v4u wr[4][8];
            {
                const v4u sc = make_srd((const unsigned char*)p.Cim + (size_t)q * 131072 + (2 * kq + 1) * 128);
                const v4u sd = make_srd((const unsigned char*)p.wd + (size_t)(7 * kq + 4) * 65536 + q * 256 + (lw ^ (kq >> 1)) * 128);
                waitv<0>();
                __builtin_amdgcn_s_sleep(T2_SLEEP);
                sfor<0, 32>(LAMB(ff) {
                    constexpr int f = decltype(ff)::value, m = f & 7;
                    if constexpr (f < 8) ldW(wr[0][m], (m & 1) ? voffC1 : voffC, sc, ((2 * lw + (m >> 2)) ^ kq) * 32768 + (m & 3) * 8192);
                    else ldW(wr[f >> 3][m], voffD, sd, (f - 8) * 8192);
                    if constexpr (f % 3 == 2) __builtin_amdgcn_s_sleep(WR_SLEEP);
                });
            }
            auto clear = [&](int n0, int n1) __attribute__((always_inline)) {
                const v4u sws = make_srd(p.ws);
                NOUNROLL for (int n = n0; n < n1; n++) { stX<0>(none, po2, sws, n * 1024); __builtin_amdgcn_s_sleep(PF_SLEEP); }
            };
            STAMP(3);
            wait_ge(fa, 4);
            STAMP(4);
            if (lw) {
                float e[4];
#pragma unroll
                for (int s = 0; s < 4; s++) e[s] = lds32f(L_TOT + s * 256 + la);
                const float p2 = lmad(a32, e[0], e[1]), p3 = lmad(a32, p2, e[2]);
                __builtin_amdgcn_raw_buffer_store_b64(v2u{__float_as_uint(lmad(a32, p3, e[3])), tg}, rws, gro2, 0, 16);
                sts32f(L_PRE + la, 0.0f); sts32f(L_PRE + 256 + la, e[0]); sts32f(L_PRE + 512 + la, p2); sts32f(L_PRE + 768 + la, p3);
            }
            STAMP(5);
            clear(0, PF_N);
            __builtin_amdgcn_s_sleep(LB_FIRST);
            {
                const int top = i - 1 - lw;
                v2u gr[8] = {};
                NOUNROLL for (int spin = 0; spin < SPIN_MAX; spin++) {
                    fence_c();
#pragma unroll
                    for (int n = 0; n < 8; n++) gr[n] = __builtin_amdgcn_raw_buffer_load_b64(rws, gro, ((top - 2 * n) & 15) * 512, 16);
                    unsigned long long bad = 0;
#pragma unroll
                    for (int n = 0; n < 8; n++) if (top - 2 * n >= 0) bad |= __builtin_amdgcn_ballot_w64(gr[n].y != tg);
                    if (bad == 0) break;
                    __builtin_amdgcn_s_sleep(LB_SLEEP);
                }
                float h = 0;
#pragma unroll
                for (int n = 7; n >= 0; n--) if (top - 2 * n >= 0) h = lmad(wn[n], __uint_as_float(gr[n].x), h);
                sts32f(L_XCH + 256 + lw * 256 + la, h);
            }
            postc<4>(F_COMM + lw * 4, lane);
            STAMP(6);
            clear(PF_N, 12);
            waitv<0>();
            asm volatile("" ::"v"(sink));
            if (lane == 0) __builtin_amdgcn_raw_buffer_store_b64(v2u{tg, ~tg}, rws, fo + 256 + lw * 8, 0, 16);
            STAMP(7);
#pragma unroll
            for (int u = 0; u < 4; u++) asm volatile("" : "+v"(wr[u][0]), "+v"(wr[u][1]), "+v"(wr[u][2]), "+v"(wr[u][3]), "+v"(wr[u][4]), "+v"(wr[u][5]), "+v"(wr[u][6]), "+v"(wr[u][7]));
            STAMP(13);
            if (!lw) poll16(0);
            {
                unsigned wa0 = lw * 8192 + lane * 16 + L_P, wa1 = wa0 + (L_W + 3 * SLOT - L_P);
                asm volatile("" : "+v"(wa0), "+v"(wa1));
                sfor<0, 4>(LAMB(uu) {
                    constexpr int u = decltype(uu)::value;
                    wait_ge(fa, N1 + u);
#pragma unroll
                    for (int m = 0; m < 8; m++) sts128((u ? wa0 + (u - 1) * SLOT : wa1) + m * 1024, wr[u][m]);
                    postv(F_COMM + lw * 4, lane, N1 + 8 + u);
                });
            }
            STAMP(15);
        }
        f32x4 base[4];
        {
            const int d = 128 * q + 32 * kq + 4 * p8;
            const v2u dv = *(const GAS v2u*)(p.Dsk + d);
            const float df[4] = {bflo(dv.x), bfhi(dv.x), bflo(dv.y), bfhi(dv.y)};
            v2u xv[4];
#pragma unroll
            for (int e = 0; e < 4; e++) xv[e] = *(const GAS v2u*)(p.x + (size_t)(128 * i + 32 * pr + 8 * e + q8) * DM + d);
#pragma unroll
            for (int e = 0; e < 4; e++) {
                const unsigned tp = (32 * pr + 8 * e + 4 * ((q8 >> 1) & 1) + 2 * (q8 & 1) + (q8 >> 2)) * 4;
                const float rs = lds32f(L_RSTD + tp), nm = smul(lds32f(L_MEAN + tp), rs);
                const float xf[4] = {bflo(xv[e].x), bfhi(xv[e].x), bflo(xv[e].y), bfhi(xv[e].y)};
#pragma unroll
                for (int k = 0; k < 4; k++) base[e][k] = sfma(sfma(xf[k], rs, nm), df[k], xf[k]);
            }
        }
        STAMP(8);
        const unsigned xi = xo + kq * 24576u + lane * 16;
        const unsigned sc = L_O + pr * 4096;
        unsigned wa = sc + (8 * (c >> 2) + 4 * (c & 1) + ((c >> 1) & 1)) * 128 + ((g ^ (c & 7)) << 4), ra = sc + q8 * 128 + ((p8 ^ (2 * (q8 & 1) + (q8 >> 2))) << 4);
        GAS float* op = (GAS float*)p.out + (size_t)(128 * i + 32 * pr + q8) * DM + 128 * q + 32 * kq + 4 * p8;
        asm volatile("" : "+v"(wa), "+v"(ra), "+v"(op));
        wait_ge(F_SENT + pr * 4, 3);
        __builtin_amdgcn_s_sleep(X_FIRST);
        v4u in[6];
        f32x4 own[4];
        NOUNROLL for (int spin = 0; spin < SPIN_MAX; spin++) {
            fence_c();
#pragma unroll
            for (int n = 0; n < 6; n++) in[n] = __builtin_amdgcn_raw_buffer_load_b128(rws, xi + n * 1024, 0, 16);
            SB();
            wait_ge(F_SENT + pr * 4, 4);
#pragma unroll
            for (int e = 0; e < 4; e++) own[e] = lds128f(sc + e * 1024 + lane * 16);
            unsigned mx = 0;
#pragma unroll
            for (int n = 0; n < 6; n++) mx = max(mx, max(max(in[n].x, in[n].y), max(in[n].z, in[n].w)));
            if (__builtin_amdgcn_ballot_w64(mx == ~0u) == 0) break;
        }
        STAMP(11);
#pragma unroll
        for (int e = 0; e < 4; e++)
#pragma unroll
            for (int sr = 0; sr < 3; sr++) { const unsigned d0 = in[2 * sr + (e >> 1)][2 * (e & 1)], d1 = in[2 * sr + (e >> 1)][2 * (e & 1) + 1]; own[e] += f32x4{bflo(d0), bfhi(d0), bflo(d1), bfhi(d1)}; }
        asm volatile("" : "+v"(own[0]), "+v"(own[1]), "+v"(own[2]), "+v"(own[3]));
#pragma unroll
        for (int e = 0; e < 4; e++) sts128f((wa ^ (unsigned)((e >> 1) << 6)) + (e & 1) * 256, own[e]);
        fence_c();
        f32x4 ta[4];
#pragma unroll
        for (int e = 0; e < 4; e++) ta[e] = lds128f((ra ^ (unsigned)((e & 1) << 6)) + e * 1024);
        SB();
#pragma unroll
        for (int e = 0; e < 4; e++) *(GAS f32x4*)(op + e * 8 * DM) = base[e] + ta[e];
        if (b == 255 && w == 4 && lane == 0) __builtin_amdgcn_raw_buffer_store_b32(tg, rws, WS_EPOCH, 0, 16);
        STAMP(12); STAMP_DUMP();
        return;
    }
    constexpr int PD = PDV, XL = XLV, EG = EGATE;
    unsigned stall = 0; (void)stall;
    v4u xa[16][2];
#define AF(u, k) xa[2 * (u) + ((k) >> 1)][(k) & 1]
    v4u sws = make_srd(p.ws);
    unsigned oG = WS_A2 + (unsigned)(i * 44 + 7 * kq) * 16384u + w * 4096, oC = WS_A2 + (unsigned)(i * 44 + 28 + 2 * kq) * 16384u + w * 4096, oF = fo + w * 8;
    asm volatile("" : "+s"(sws), "+s"(oG), "+s"(oC), "+s"(oF));
    auto uoff = [&](int v) __attribute__((always_inline)) { return v < 4 ? oG + v * 16384 : v < 8 ? oC + (8 * ((v >> 1) & 1) + (v & 1)) * 16384 : oG + (v - 4) * 16384; };
    LANE_VARS;
    unsigned fdone = F_DONE + w * 4, l16 = lane * 16;
    asm volatile("" : "+v"(fdone), "+v"(l16));
    unsigned tgs = 0; int raised = 0;
    auto raise = [&](int level) __attribute__((always_inline)) {
        asm volatile("s_mov_b64 exec, 1\n\tbuffer_store_dwordx2 %0, off, %1, %2 offset:%3 sc1\n\ts_mov_b64 exec, -1" ::"v"(v2u{tgs, ~tgs}), "s"(sws), "s"(oF), "n"((level - 1) * 128) : "memory");
        post(fdone + (F_OUT - F_DONE), level);
        raised = level;

        if (level == 1) STAMP(17); else STAMP(18);
    };
    auto try_raise = [&](int level) __attribute__((always_inline)) {
        unsigned v; asm volatile("s_getreg_b32 %0, hwreg(HW_REG_IB_STS, 0, 4)" : "=s"(v)::"memory");
        if (__builtin_expect((int)(v + raised) == level - 1, 0)) raise(level);
    };
    unsigned oX[3];
#pragma unroll
    for (int sl = 1; sl < 4; sl++) { oX[sl - 1] = xo + (kq ^ sl) * 24576u + (sl - 1) * 2048; asm volatile("" : "+s"(oX[sl - 1])); }
    unsigned cbs[2], tbs[4];
    v4u bf[PD];
    const unsigned ffl = F_FULL + (lane & 1) * 4;
    auto set2 = [&](auto kk) __attribute__((always_inline)) {
        constexpr int k = decltype(kk)::value;
        if constexpr (k == 0) tbs[0] = L_P + ((l16 << 1) & 0x780) + ((l16 >> 1) & 0x18) + (((l16 >> 2) & 0x40) | ((l16 >> 2) & 0x20));
        else if constexpr (k < 4) tbs[k] = tbs[0] ^ (unsigned)(k << 5);
        else if constexpr (k == 4) cbs[0] = L_W + ((l16 << 3) & 0x780) + (((l16 >> 4) ^ (l16 >> 1)) & 0x70);
        else cbs[1] = cbs[0] ^ 64u;
        if constexpr (k < 4) asm volatile("" : "+v"(tbs[k])); else asm volatile("" : "+v"(cbs[k - 4]));
    };
    auto prime2 = [&](auto mm) __attribute__((always_inline)) { constexpr int m = decltype(mm)::value; const v2u lo = ldstr(tbs[m]), hi = ldstr(tbs[m] + 2048); bf[m] = v4u{lo.x, lo.y, hi.x, hi.y}; };
    {
        STAMP_INIT();
        STAMP(0);
        unsigned cb[2];
        cb[0] = (unsigned)c * 128u + (unsigned)((g ^ (c >> 1)) << 4); cb[1] = cb[0] ^ 64u;
        {
            const unsigned voffX = (8 * (q8 >> 2) + 4 * (q8 & 1) + ((q8 >> 1) & 1)) * 1024 + ((p8 ^ (q8 >> 1)) << 4), voffX1 = voffX ^ 64u;
            const v4u srd = make_srd((const unsigned char*)p.x + (size_t)(128 * i + 32 * w) * 1024 - 4096);
            auto issue_x = [&](int t) __attribute__((always_inline)) {
                const unsigned s0 = (t >> 3) * 2048 + ((t + rot) & 7) * 128, s1 = (t >> 3) * 2048 + ((t + 1 + rot) & 7) * 128 + 2048;
                dma4(srd, voffX, voffX1, s0 + 4096, s0 + 4096 + 15360, s1, s1 + 15360, sl_lo + (t & 7) * 2048);
            };
            asm volatile("s_mov_b32 m0, %3\n\ts_nop 0\n\tbuffer_load_dword %0, %1, %2 offen lds" ::"v"(p8 * 128), "s"(srd), "s"(4096 + (4 * (j >> 1) + 2 + (j & 1)) * 1024), "s"(L_O + w * 256) : "memory");
            issue_x(0); issue_x(2); issue_x(4); issue_x(6);
            bar_l();
            const v4u sel0 = make_sel(lane, 0), sel1 = make_sel(lane, 1);
            v4u ones = {0x3f803f80u, 0x3f803f80u, 0x3f803f80u, 0x3f803f80u};
            asm volatile("" : "+v"(ones));
            f32x4 sm[2], sq[2], nm4[2], ct[6];
            const unsigned xl0 = cb[0] + sl_lo, xl1 = cb[1] + sl_lo;
            unsigned sa = L_RSTD + (32 * w + 8 * g + (c & 3)) * 4;
            const unsigned long long dgm = __builtin_amdgcn_ballot_w64(g == (c >> 2));
            asm volatile("" : "+v"(sa));
            auto stats = [&](int bb) __attribute__((always_inline)) {
                float s01, s23, sd;
                asm("v_cndmask_b32 %0, %1, %2, %3" : "=v"(s01) : "v"(sq[bb].x), "v"(sq[bb].y), "s"(0xaaaaaaaaaaaaaaaaull));
                asm("v_cndmask_b32 %0, %1, %2, %3" : "=v"(s23) : "v"(sq[bb].z), "v"(sq[bb].w), "s"(0xaaaaaaaaaaaaaaaaull));
                asm("v_cndmask_b32 %0, %1, %2, %3" : "=v"(sd) : "v"(s01), "v"(s23), "s"(0xccccccccccccccccull));
                const float rstd = __builtin_amdgcn_rsqf(fmaxf(sd * (1.0f / DM) - nm4[bb].x * nm4[bb].x, 0.0f) + 1e-5f);
                asm volatile("s_mov_b64 exec, %3\n\tds_write2_b32 %0, %1, %2 offset0:%4 offset1:%5\n\ts_mov_b64 exec, -1" ::"v"(sa), "v"(rstd), "v"(nm4[bb].x), "s"(dgm), "n"(4 * bb), "n"(L_MEAN / 4 + 4 * bb) : "memory");
            };
            auto centre = [&](auto kk) __attribute__((always_inline)) {
                constexpr int k = decltype(kk)::value, e = k - 4;
                if constexpr (k < 64) mma_c(ct[k % 6], (k & 1) ? sel1 : sel0, xa[(k >> 1) & 15][k >> 5], nm4[k >> 5]);
                if constexpr (e >= 0) {
                    xa[(e >> 1) & 15][e >> 5][2 * (e & 1)] = cvtv(ct[e % 6].x, ct[e % 6].y);
                    xa[(e >> 1) & 15][e >> 5][2 * (e & 1) + 1] = cvtv(ct[e % 6].z, ct[e % 6].w);
                }
                SB();
            };
            SB();
            sfor<0, 17>(LAMB(tt) {
                constexpr int t = decltype(tt)::value, u = t - 1;
                if constexpr (t < 16) {
                    constexpr int t5 = ((t + 1) & ~1) + 5, top = t5 < 7 ? 7 : t5 > 15 ? 15 : t5;
                    waitv<2 * (top - t)>();
                    xa[2 * (t & 7)][t >> 3] = lds128(xl0 + (t & 7) * 2048); xa[2 * (t & 7) + 1][t >> 3] = lds128(xl1 + (t & 7) * 2048);
                }
                SB();
                if constexpr (t == 9) { asm volatile("s_nop 3"); SB(); nm4[0] = sm[0] * (-1.0f / DM); asm volatile("s_nop 1" : "+v"(nm4[0])); SB(); }
                if constexpr (t >= 9) { centre(IC<4 * (t - 9)>{}); centre(IC<4 * (t - 9) + 1>{}); centre(IC<4 * (t - 9) + 2>{}); centre(IC<4 * (t - 9) + 3>{}); }
                if constexpr (u >= 0) {
                    mma<(u & 7) == 0>(sm[u >> 3], ones, xa[2 * (u & 7)][u >> 3]); mma<(u & 7) == 0>(sq[u >> 3], xa[2 * (u & 7)][u >> 3], xa[2 * (u & 7)][u >> 3]);
                    mma<false>(sm[u >> 3], ones, xa[2 * (u & 7) + 1][u >> 3]); mma<false>(sq[u >> 3], xa[2 * (u & 7) + 1][u >> 3], xa[2 * (u & 7) + 1][u >> 3]);
                }
                if constexpr ((t & 1) == 0 && t >= 2 && t + 6 < 16) issue_x(t + 6);
                if constexpr (t == XI_STEP) post(fdone + (F_XI - F_DONE), 1);
                if constexpr (t == XI2_STEP) post(fdone + (F_XI - F_DONE), 2);
                SB();
            });
            post(fdone + (F_XI - F_DONE), 3);
            STAMP(1);
            stats(0);
            SB();
            nm4[1] = sm[1] * (-1.0f / DM); asm volatile("s_nop 1" : "+v"(nm4[1]));
            SB();
            sfor<32, 64>(LAMB(kk) { centre(kk); });
            stats(1);
            SB();
            sfor<64, 68>(LAMB(kk) { centre(kk); });
        }
        STAMP(2);
        f32x4 eX;
        unsigned tb[4];
        {
            cb[0] += L_W; cb[1] += L_W;
            asm volatile("" : "+v"(cb[0]), "+v"(cb[1]));
            f32x4 acc[4][2];
            unsigned fl = 0; bool lack = false;
            {
                const int q4 = c >> 2, p4 = c & 3;
                const unsigned tb1 = L_P + (unsigned)(8 * g + q4) * 128u + (unsigned)((((q4 >> 1) & 1) | ((g & 1) << 1)) << 5) + p4 * 8;
#pragma unroll
                for (int t = 0; t < 4; t++) { tb[t] = tb1 ^ (unsigned)(t << 5); asm volatile("" : "+v"(tb[t])); }
            }
            f32x4 ca, c4, co, c8, rs0, rs1;
            wait_ge(F_FULL + (lane & 3) * 4, 1);
            STAMP(3);
            sfor<-PD, 64>(LAMB(nn) {
                constexpr int n = decltype(nn)::value, m = n + PD;
                v4u cur;
                if constexpr (n >= 0) { cur = bf[n % PD]; mma<(n < 4)>(acc[n & 3][0], xa[n >> 2][0], cur); SB(); }
                if constexpr (m > 0 && (m & 15) == 0) need_full(lack, ffl, (m >> 4) + 1);
                if constexpr (m < 64) bf[m % PD] = lds128(cb[(m >> 2) & 1] + wslot(m >> 4) + (((m >> 1) & 1) * 4 + ((m >> 3) & 1) * 2 + (m & 1)) * 2048);
                else { const unsigned a = tb[(m >> 1) & 3] + wslot(0) + (m & 1) * 8192; const v2u lo = ldstr(a), hi = ldstr(a + 512); bf[m % PD] = v4u{lo.x, lo.y, hi.x, hi.y}; }
                SB();
                if constexpr (n >= 0) { mma<(n < 4)>(acc[n & 3][1], xa[n >> 2][1], cur); SB(); }
                if constexpr ((m & 15) == 10) fl = peek(ffl);
                if constexpr ((m & 15) == 14) lack = any_lt(fl, (m >> 4) + 2);
                if constexpr ((m & 15) == 15 && m < 63) post(fdone, (m >> 4) + 1);
                if constexpr (m == 52) ca = lds128f(L_CST + c * 16);
                if constexpr (m == 53) c4 = lds128f(L_CST + 256 + c * 16);
                if constexpr (m == 54) co = lds128f(L_CST + 512 + c * 16);
                if constexpr (m == 57) c8 = lds128f(L_PW + 256 + c * 16);
                if constexpr (m == 55) rs0 = lds128f(L_RSTD + (32 * w + 8 * g) * 4);
                if constexpr (m == 56) rs1 = lds128f(L_RSTD + (32 * w + 8 * g + 4) * 4);
                SB();
            });
            mfma_done();
            STAMP(4); VAL(24, stall); stall = 0;
            f32x4 ev;
            f32x2 sr[2][4], si[2][4];
#pragma unroll
            for (int st = 0; st < 2; st++) {
                const f32x4 r0 = acc[st][0] * rs0, r1 = acc[st][1] * rs1, i0 = acc[2 + st][0] * rs0, i1 = acc[2 + st][1] * rs1;
                sr[st][0] = lo2(r0); sr[st][1] = hi2(r0); sr[st][2] = lo2(r1); sr[st][3] = hi2(r1);
                si[st][0] = lo2(i0); si[st][1] = hi2(i0); si[st][2] = lo2(i1); si[st][3] = hi2(i1);
            }
            const f32x2 a1[2] = {lo2(ca), hi2(ca)}, cf[2] = {lo2(co), hi2(co)};
#pragma unroll
            for (int t = 1; t < 4; t++) {
                f32x2 tr[2], ti[2];
#pragma unroll
                for (int st = 0; st < 2; st++) { tr[st] = bfma<1, 1>(a1[st], si[st][t - 1], sr[st][t]); ti[st] = bfma<1, 0>(a1[st], sr[st][t - 1], si[st][t]); }
                SB();
#pragma unroll
                for (int st = 0; st < 2; st++) { sr[st][t] = bfma<0, 0>(a1[st], sr[st][t - 1], tr[st]); si[st][t] = bfma<0, 0>(a1[st], si[st][t - 1], ti[st]); }
                SB();
            }
#pragma unroll
            for (int st = 0; st < 2; st++) {
                ev[2 * st] = sfma(c4[2 * st], sr[st][3].x, sfnma(c4[2 * st + 1], si[st][3].x, sr[st][3].y));
                ev[2 * st + 1] = sfma(c4[2 * st], si[st][3].x, sfma(c4[2 * st + 1], sr[st][3].x, si[st][3].y));
                eX[2 * st] = sr[st][3].x; eX[2 * st + 1] = si[st][3].x;
            }
            sts128f(L_SEG + w * 1024 + l16, ev);
            fence_c();
            f32x4 es[4];
#pragma unroll
            for (int s = 0; s < 4; s++) es[s] = lds128f(L_SEG + w * 1024 + s * 256 + (l16 & 0xf0));
            SB();
            unsigned ya = L_O + w * 4096 + l16;
            asm volatile("" : "+v"(ya), "+v"(eX));
#pragma unroll
            for (int st = 0; st < 2; st++) {
                f32x2 tr[4], ti[4];
#pragma unroll
                for (int t = 0; t < 4; t++) { tr[t] = bmul<1, 1>(cf[st], si[st][t]); ti[t] = bmul<1, 1>(cf[st], sr[st][t]); }
                SB();
#pragma unroll
                for (int t = 0; t < 4; t++) { tr[t] = bfma<0, 0>(cf[st], sr[st][t], tr[t]); ti[t] = bfma<0, 1>(cf[st], si[st][t], ti[t]); }
                SB();
                if (st == 0) {
#pragma unroll
                    for (int s = 1; s < 4; s++) { const f32x2 u0 = cmadp(lo2(c8), lo2(es[s - 1]), lo2(es[s])), u1 = cmadp(hi2(c8), hi2(es[s - 1]), hi2(es[s])); es[s] = f32x4{u0.x, u0.y, u1.x, u1.y}; }
                    sts128f(L_TOT + w * 256 + (l16 & 0xf0), es[3]);
                    post(fdone, 4);
                    f32x4 pin;
#pragma unroll
                    for (int k = 0; k < 4; k++) { const float lo = g == 1 ? es[0][k] : 0.0f, hi = g == 3 ? es[2][k] : es[1][k]; pin[k] = g >= 2 ? hi : lo; }
                    sts128f(L_SEG + w * 1024 + l16, pin);
                    SB();
                }
                sts128(ya + st * 1024, v4u{pk2(tr[0]), pk2(tr[1]), pk2(tr[2]), pk2(tr[3])});
                sts128(ya + 2048 + st * 1024, v4u{pk2(ti[0]), pk2(ti[1]), pk2(ti[2]), pk2(ti[3])});
                SB();
            }
        }
        STAMP(5);
        unsigned go = oG + q * 16384, so = oC + (q >> 1) * 16384 + (q & 1) * 2048;
        asm volatile("" : "+s"(go), "+s"(so));
        f32x4 acc[4][2][2];
        f32x2 r12;
        unsigned fc = 0;
        auto gpass = [&](auto pp, auto n0, auto n1) __attribute__((always_inline)) {
            constexpr int pass = decltype(pp)::value;
            unsigned fl = 0, dn = 0; bool lack = false;
            sfor<decltype(n0)::value, decltype(n1)::value>(LAMB(nn) {
                constexpr int n = decltype(nn)::value, m = n + PD;
                constexpr bool pre = pass == 0 || m < 128;
                constexpr unsigned off = wslot(m >> 4) + (m & 1) * 8192 + ((m >> 3) & 1) * 4096;
                const v4u cur = bf[(n + 64 + 128 * pass) % PD];
                mma<(n < 8)>(acc[(n >> 1) & 3][n & 1][0], cur, xa[n >> 3][0]);
                SB();
                if constexpr ((m & 15) == 0) post(fdone, dn);
                if constexpr ((m & 15) == 0) need_full(lack, ffl, 5 + 8 * pass + (m >> 4));
                v2u lo, hi;
                if constexpr (pre) lo = ldstr(tb[(m >> 1) & 3] + off); else lo = ldstr(tbs[m & 3]);
                if constexpr (pass == 1 && n >= 69 && n < 81 && (n & 1)) set2(IC<(n - 69) / 2>{});
                if constexpr ((m & 15) == 10) fl = peek(ffl);
                if constexpr ((m & 15) == 15) lack = any_lt(fl, m == 127 && pass == 0 ? N1 - 7 : 6 + 8 * pass + (m >> 4));
                if constexpr ((m & 15) == 13) { dn = 5 + 8 * pass + (m >> 4); asm volatile("" : "+v"(dn)); }
                if constexpr (n == EG - 8 && pass == 1) fc = peek(F_RDY);
                if constexpr (n == EG - 2 && pass == 1) { if (__builtin_expect(any_lt(fc, 1), 0)) { wait_ge(F_RDY, 1); } fence_c(); }
                if constexpr (n >= EG && n < EG + 24 && pass == 1 && (n - EG) % 3 == 0) { constexpr int f = (n - EG) / 3; reldA<(f & 3) * 1024>(AF(f >> 2, f & 3), l16, sws, uoff(f >> 2)); }
                if constexpr (pass == 1 && (n == 33 || n == 41 || n == 49 || n == 57)) try_raise(1);
                if constexpr (pass == 1 && n == 67) { if (raised < 1) { waitv<0>(); raise(1); } }
                if constexpr (n == 120 && pass == 0) fc = peek(ffl + (F_COMM - F_FULL));
                if constexpr (n == 121 && pass == 0) { const unsigned ra = L_RSTD + w * 128 + (((l16 >> 1) & 0x60) | ((l16 >> 2) & 0xc)); r12 = f32x2{lds32f(ra), lds32f(ra + 16)}; }
                SB();
                mma<(n < 8)>(acc[(n >> 1) & 3][n & 1][1], cur, xa[n >> 3][1]);
                SB();
                if constexpr (pre) hi = ldstr(tb[(m >> 1) & 3] + off + 512); else hi = ldstr(tbs[m & 3] + 2048);
                bf[n % PD] = v4u{lo.x, lo.y, hi.x, hi.y};
                if constexpr ((m & 57) == 57 && (pass == 0 || m < 64)) {
                    tb[(m >> 1) & 3] += (m & 64) ? L_P - L_W : L_W - L_P; asm volatile("" : "+v"(tb[(m >> 1) & 3]));
                }
                SB();
            });
        };
        auto gated = [&](auto ff) __attribute__((always_inline)) {
            constexpr int uu = decltype(ff)::value >> 1, bb = decltype(ff)::value & 1;
            const f32x4 h0 = acc[2 * uu][0][bb] * acc[2 * uu][1][bb], h1 = acc[2 * uu + 1][0][bb] * acc[2 * uu + 1][1][bb];
            return v4u{pk2(bmul<bb, 0>(r12, lo2(h0))), pk2(bmul<bb, 0>(r12, hi2(h0))), pk2(bmul<bb, 0>(r12, lo2(h1))), pk2(bmul<bb, 0>(r12, hi2(h1)))};
        };
        gpass(IC<0>{}, IC<0>{}, IC<128>{});
        {
            r12 = r12 * r12;
            mfma_done();
            STAMP(6); VAL(25, stall); stall = 0;
            sfor<0, 2>(LAMB(ff) { stA<decltype(ff)::value * 1024>(gated(ff), l16, sws, go); });
            SB();
            need_full(any_lt(fc, 4), ffl + (F_COMM - F_FULL), 4);
            const unsigned tg = peek(L_TAG);
            const unsigned cl = L_CST + (l16 & 0xf0);
            unsigned ya = L_O + w * 4096 + l16;
            asm volatile("" : "+v"(ya));
            const f32x4 h0 = lds128f(L_XCH + 256 + (l16 & 0xf0)), h1 = lds128f(L_XCH + 512 + (l16 & 0xf0));
            const f32x4 aw = lds128f(L_PW + 1024 + w * 256 + (l16 & 0xf0)), pb = lds128f(L_PRE + w * 256 + (l16 & 0xf0));
            const f32x4 ag = lds128f(L_PW + l16), pin = lds128f(L_SEG + w * 1024 + l16);
            const v4u sel[2] = {lds128(L_SEL + l16), lds128(L_SEL + 1024 + l16)};
            const v4u yr[2] = {lds128(ya), lds128(ya + 1024)}, yi[2] = {lds128(ya + 2048), lds128(ya + 3072)};
            const f32x4 c4 = lds128f(cl + 256);
            f32x4 cw[4];
            SB();
            sfor<2, 4>(LAMB(ff) { stA<decltype(ff)::value * 1024>(gated(ff), l16, sws, go); });
            tgs = __builtin_amdgcn_readfirstlane(tg);
#pragma unroll
            for (int t = 0; t < 4; t++) cw[t] = lds128f(cl + (3 + t) * 256);
            SB();
            f32x4 zr[2][2], zi[2][2];
#pragma unroll
            for (int st = 0; st < 2; st++)
#pragma unroll
                for (int bb = 0; bb < 2; bb++) { mma<true>(zr[st][bb], yr[st], sel[bb]); mma<true>(zi[st][bb], yi[st], sel[bb]); }
            SB();
            f32x2 hr[2], hi[2];
            {
                const f32x4 cy = h0 + h1;
                const f32x2 k0 = cmadp(lo2(ag), cmadp(lo2(aw), lo2(cy), lo2(pb)), lo2(pin)), k1 = cmadp(hi2(ag), cmadp(hi2(aw), hi2(cy), hi2(pb)), hi2(pin));
                const f32x4 hin = {k0.x, k0.y, k1.x, k1.y};
                float t0[2], t1[2];
#pragma unroll
                for (int st = 0; st < 2; st++) { t0[st] = sfnma(c4[2 * st + 1], hin[2 * st + 1], eX[2 * st]); t1[st] = sfma(c4[2 * st + 1], hin[2 * st], eX[2 * st + 1]); }
                SB();
#pragma unroll
                for (int st = 0; st < 2; st++) { hr[st] = f32x2{hin[2 * st], sfma(c4[2 * st], hin[2 * st], t0[st])}; hi[st] = f32x2{hin[2 * st + 1], sfma(c4[2 * st], hin[2 * st + 1], t1[st])}; }
                SB();
            }
#pragma unroll
            for (int st = 0; st < 2; st++) {
                f32x2 tr[4], ti[4];
#pragma unroll
                for (int t = 0; t < 4; t++) { const f32x2 wv = st ? hi2(cw[t]) : lo2(cw[t]); tr[t] = bmul<1, 1>(wv, hi[st]); ti[t] = bmul<1, 1>(wv, hr[st]); }
                SB();
#pragma unroll
                for (int t = 0; t < 4; t++) { const f32x2 wv = st ? hi2(cw[t]) : lo2(cw[t]); tr[t] = bfma<0, 0>(wv, hr[st], tr[t]); ti[t] = bfma<0, 1>(wv, hi[st], ti[t]); }
                SB();
                v4u fr = {pk2(tr[0]), pk2(tr[1]), pk2(tr[2]), pk2(tr[3])}, fi = {pk2(ti[0]), pk2(ti[1]), pk2(ti[2]), pk2(ti[3])};
                asm volatile("s_nop 1" : "+v"(fr), "+v"(fi));
#pragma unroll
                for (int bb = 0; bb < 2; bb++) { mma<false>(zr[st][bb], fr, sel[bb]); mma<false>(zi[st][bb], fi, sel[bb]); }
                SB();
            }
            mfma_done();
            stA<0>(pk8(zr[0][0], zr[1][0]), l16, sws, so); stA<0>(pk8(zi[0][0], zi[1][0]), l16, sws, so + 8 * 16384);
            stA<1024>(pk8(zr[0][1], zr[1][1]), l16, sws, so); stA<1024>(pk8(zi[0][1], zi[1][1]), l16, sws, so + 8 * 16384);
            SB();
            STAMP(7);
        }
        if (heavy) {
            gpass(IC<1>{}, IC<0>{}, IC<128>{});
            sfor<0, 4>(LAMB(ff) { constexpr int f = decltype(ff)::value; reldA<f * 1024>(AF(2, f), l16, sws, uoff(2)); });
            mfma_done();
            STAMP(8); VAL(26, stall); stall = 0;
            sfor<0, 4>(LAMB(ff) { stA<decltype(ff)::value * 1024>(gated(ff), l16, sws, go + 4 * 16384); });
            SB();
        }
        int q2 = q;
        asm volatile("" : "+s"(q2));
        if (q2 == 3) {
            waitv<0>(); raise(1); raise(2);
            sfor<0, 6>(set2);
            wait_ge(ffl, N1 + 1);
            sfor<0, PD>(prime2);
            wait_ge(F_RDY, 1);
            sfor<0, 12>(LAMB(ff) { constexpr int f = decltype(ff)::value; reldA<(f & 3) * 1024>(AF(f >> 2, f & 3), l16, sws, uoff(f >> 2)); });
#pragma unroll
            for (int u = 0; u < 3; u++) asm volatile("s_waitcnt vmcnt(0)" : "+v"(AF(u, 0)), "+v"(AF(u, 1)), "+v"(AF(u, 2)), "+v"(AF(u, 3))::"memory");
        }
    }
    STAMP(9);
    {
        f32x4 acc[8][2];
        v4u pk[2];
        unsigned fl = 0, fr = 0, dn = 0; bool lack = false;
        constexpr auto fv = [](int n) { return n < 48 ? n >> 4 : n < 108 ? 3 + (n - 48) / 12 : n < 144 ? 8 + ((n - 108) % 12) / 4 : 3 + (n - 144) / 4; };
        constexpr auto fk = [](int n) { return n < 48 ? (n >> 3) & 1 : n < 108 ? ((n - 48) % 12) / 6 : (n >> 1) & 1; };
        constexpr auto fd = [](int n) { return n < 48 ? n & 7 : n < 108 ? 2 + (n - 48) % 6 : n < 144 ? 2 * ((n - 108) / 12 + 1) + (n & 1) : n & 1; };
        constexpr auto first = [](int n) { return n >= 0 && (n < 48 ? (n & 15) == 0 : n < 108 ? (n - 48) % 12 == 0 : n < 120 && (n & 3) == 0); };
        constexpr int LG = LGATE;
        constexpr auto nmid = [](int n) { return n <= MG ? 0 : (n - MG + 2) / 3 < 20 ? (n - MG + 2) / 3 : 20; };
        static_assert(MG + 57 < LG && LG + 22 < 108, "load schedule");
        sfor<0, 176>(LAMB(nn) {
            constexpr int n = decltype(nn)::value, m = n + PD;
            constexpr int v = fv(n), ks = fk(n), dt = fd(n);
            constexpr int mv = fv(m), mk = fk(m), md = fd(m);
            constexpr bool mc = (mv & ~3) == 4;
            constexpr unsigned moff = (mv & 3) * SLOT + (mc ? md * 2048 : (md >> 2) * 8192 + mk * 4096);
            constexpr auto chk = [](int q) { return q == 16 ? 2 : q == 32 ? 4 : q == 60 ? 7 : q == 96 ? 11 : 0; };
            v4u cur; v2u lo, hi;
            if constexpr (n >= 0) {
                if constexpr (first(n)) {
                    constexpr int mid = nmid(n), late = n <= LG ? 0 : (n - LG + 1) / 2 < 12 ? (n - LG + 1) / 2 : 12;
                    constexpr int NW = v == 0 ? 12 : v == 1 ? 8 : v == 2 ? 63 : v < 8 ? mid - 4 * (v - 2) + late : late - 4 * (v - 7);
                    static_assert(NW >= 0, "load schedule");
                    asm volatile("s_waitcnt vmcnt(%4)" : "+v"(AF(v & 7, 0)), "+v"(AF(v & 7, 1)), "+v"(AF(v & 7, 2)), "+v"(AF(v & 7, 3)) : "n"(ABLW(v) ? 63 : NW) : "memory");
                }
                cur = bf[n % PD];
                mma<(n < 8)>(acc[dt][0], cur, AF(v & 7, 2 * ks));
                SB();
            }
            if constexpr (m > 0 && (m & 15) == 0 && m <= 48) post(fdone, dn);
            if constexpr (chk(m) > 0) need_full(lack, m == 96 ? ffl + (F_COMM - F_FULL) : ffl, N1 + chk(m));
            if constexpr (m < 176) { if constexpr (mc) bf[m % PD] = lds128(cbs[mk] + moff); else lo = ldstr(tbs[md & 3] + moff); }
            if constexpr (chk(m + 6) > 0) fl = peek(m == 90 ? ffl + (F_COMM - F_FULL) : ffl);
            if constexpr (chk(m + 1) > 0) lack = any_lt(fl, N1 + chk(m + 1));
            if constexpr ((m & 15) == 13 && m < 48) { dn = N1 + mv + 1; asm volatile("" : "+v"(dn)); }
            if constexpr (n == MG - 7) fr = peek(F_RDY);
            if constexpr (n == MG - 1) { if (__builtin_expect(any_lt(fr, 2), 0)) { if (raised < 2) { waitv<0>(); raise(2); } wait_ge(F_RDY, 2); } fence_c(); }
            if constexpr (n == RAISE2) { if (raised < 2) { if (!ABLW(99)) waitv<nmid(n)>(); raise(2); } }
            if constexpr (n >= MG && n < MG + 60 && (n - MG) % 3 == 0) { constexpr int f = (n - MG) / 3; reldA<(f & 3) * 1024>(AF(3 + (f >> 2), f & 3), l16, sws, uoff(3 + (f >> 2))); }
            if constexpr (n == LG - 6) fr = peek(F_RDY + 4);
            if constexpr (n == LG - 1) need_full(any_lt(fr, 1), F_RDY + 4, 1);
            if constexpr (n >= LG && n < LG + 24 && (n - LG) % 2 == 0) { constexpr int f = (n - LG) / 2; reldA<(f & 3) * 1024>(AF(f >> 2, f & 3), l16, sws, uoff(8 + (f >> 2))); }
            SB();
            if constexpr (n >= 0) { mma<(n < 8)>(acc[dt][1], cur, AF(v & 7, 2 * ks + 1)); SB(); }
            if constexpr (m < 176 && !mc) { hi = ldstr(tbs[md & 3] + moff + 2048); bf[m % PD] = v4u{lo.x, lo.y, hi.x, hi.y}; }
            if constexpr (n >= 120 && n < 156) {
                constexpr int sl = (n - 108) / 12, r = (n - 108) % 12 - 2;
                if constexpr (r >= 0 && r < 10 && r % 5 < 4) { const f32x4& a = acc[2 * sl + r / 5][(r % 5) >> 1]; pk[r / 5][r % 5] = cvtv(a[2 * (r % 5 & 1)], a[2 * (r % 5 & 1) + 1]); }
                if constexpr (r >= 0 && r % 5 == 4) stX<(r / 5) * 1024>(pk[r / 5], l16, sws, oX[sl - 1]);
            }
            if constexpr (n == 156) post(fdone + (F_SENT - F_DONE), 3);
            if constexpr (n == 107) { STAMP(10); VAL(28, stall); stall = 0; }
            SB();
        });
        mfma_done();
        STAMP(11); VAL(29, stall);
#pragma unroll
        for (int e = 0; e < 4; e++) sts128f(L_O + w * 4096 + e * 1024 + l16, acc[e >> 1][e & 1]);
        post(fdone + (F_SENT - F_DONE), 4);
    }
    STAMP(14); STAMP_DUMP();
}

extern "C" void kernel_launch(void* const* d_in, const int* in_sizes, int n_in, void* d_out, int out_size, void* d_ws, size_t ws_size,
                              hipStream_t stream) {
    Params p;
    p.x = (const u16*)d_in[0]; p.lre = (const u16*)d_in[1]; p.lim = (const u16*)d_in[2];
    p.Bre = (const u16*)d_in[3]; p.Bim = (const u16*)d_in[4]; p.Cre = (const u16*)d_in[5]; p.Cim = (const u16*)d_in[6];
    p.ldt = (const u16*)d_in[7]; p.Dsk = (const u16*)d_in[8];
    p.wg = (const u16*)d_in[10]; p.wu = (const u16*)d_in[11]; p.wd = (const u16*)d_in[12];
    p.out = (float*)d_out; p.ws = (unsigned char*)d_ws + WS_SHIFT;
    (void)hipFuncSetAttribute((const void*)fused, hipFuncAttributeMaxDynamicSharedMemorySize, L_TOTAL);
    fused<<<256, 512, L_TOTAL, stream>>>(p);
}
```
